```python
import torch

def _softmax(x, axis=-1):
    return torch.softmax(x, dim=axis)
def _gelu(x):
    return torch.nn.functional.gelu(x, approximate="tanh")
def _silu(x):
    return torch.nn.functional.silu(x)
def _sigmoid(x):
    return torch.sigmoid(x)
def _layernorm(x, axis=-1, eps=1e-5):
    m = x.mean(dim=axis, keepdim=True)
    v = x.var(dim=axis, keepdim=True, unbiased=False)
    return (x - m) / torch.sqrt(v + eps)
def _scatter_add(tgt, idx, src, axis=0):
    return tgt.clone().index_add_(axis, idx, src)
def _segment_sum(src, seg, n):
    out = torch.zeros((n,) + src.shape[1:], dtype=src.dtype)
    return out.index_add_(0, seg, src)
def _pad(x, pw):
    flat = tuple(v for pair in reversed(pw) for v in pair)
    return torch.nn.functional.pad(x, flat)

kernel_name = 'interleave_layers__sliding_local_dilated__mixer_token_mlp__prompt'

def setup_inputs():

    _w = (lambda a: (lambda t: t.to(torch.bfloat16) if t.is_floating_point() else t)(torch.as_tensor(a)))
    def _A_setup():
        B, S, D = 1, 2048, 512
        return {
            "x": _w((lambda *s: torch.randn(*s, dtype=torch.bfloat16) * 0.05)(B, S, D)),
            "wqkv": _w((lambda *s: torch.randn(*s, dtype=torch.bfloat16) * 0.05)(D, 3 * D)),
            "wo": _w((lambda *s: torch.randn(*s, dtype=torch.bfloat16) * 0.05)(D, D)),
        }
    _A_in = _A_setup()

    def _B_setup():
        B, S, D, F = 1, 2048, 512, 1792
        return {
            "x": _w((lambda *s: torch.randn(*s, dtype=torch.bfloat16) * 0.05)(B, S, D)),
            "w1": _w((lambda *s: torch.randn(*s, dtype=torch.bfloat16) * 0.8)(S, F) * (S ** -0.5)),
            "b1": _w((lambda *s: torch.randn(*s, dtype=torch.bfloat16) * 0.8)(F) * 0.02),
            "w2": _w((lambda *s: torch.randn(*s, dtype=torch.bfloat16) * 0.8)(F, S) * (F ** -0.5)),
            "b2": _w((lambda *s: torch.randn(*s, dtype=torch.bfloat16) * 0.8)(S) * 0.02),
        }
    _B_in = _B_setup()

    return {
        "A_x": _A_in["x"],
        "A_wqkv": _A_in["wqkv"],
        "A_wo": _A_in["wo"],
        "B_x": _B_in["x"],
        "B_w1": _B_in["w1"],
        "B_b1": _B_in["b1"],
        "B_w2": _B_in["w2"],
        "B_b2": _B_in["b2"],
    }

def reference(A_x, A_wqkv, A_wo, B_x, B_w1, B_b1, B_w2, B_b2):
    def _A_ref(x, wqkv, wo):
        B, S, D = x.shape
        H = 32; Dh = D // H
        W, dil = 8, 2
        qkv = (lambda x, *d: x.permute(*d))((x @ wqkv).reshape(B, S, 3, H, Dh), 2, 0, 3, 1, 4)
        q, k, v = qkv[0], qkv[1], qkv[2]
        scale = Dh ** -0.5
        pos = torch.arange(S)
        off = torch.arange(W) * dil
        idx = pos[:, None] - off[None, :]
        mask = idx >= 0
        idx_c = torch.where(mask, idx, 0).reshape(-1)
        kg = k[:, :, idx_c, :].reshape(B, H, S, W, Dh)
        vg = v[:, :, idx_c, :].reshape(B, H, S, W, Dh)
        logits = torch.einsum("bhsd,bhswd->bhsw", q, kg) * scale
        logits = torch.where(mask[None, None, :, :], logits, -1e9)
        attn = _softmax(logits, axis=-1)
        o = torch.einsum("bhsw,bhswd->bhsd", attn, vg)
        return (lambda x, *d: x.permute(*d))(o, 0, 2, 1, 3).reshape(B, S, D) @ wo

    def _B_ref(x, w1, b1, w2, b2):
        B, S, D = x.shape
        h = _layernorm(x, axis=-1)
        u = torch.einsum("bsd,sf->bdf", h, w1) + b1
        u = _gelu(u)
        y = torch.einsum("bdf,fs->bds", u, w2) + b2
        return (lambda x, *d: x.permute(*d))(y, 0, 2, 1)

    x = A_x
    x = x + _A_ref(_layernorm(x, axis=-1), A_wqkv, A_wo)
    x = x + _B_ref(_layernorm(x, axis=-1), B_w1, B_b1, B_w2, B_b2)
    x = x + _A_ref(_layernorm(x, axis=-1), A_wqkv, A_wo)
    x = x + _B_ref(_layernorm(x, axis=-1), B_w1, B_b1, B_w2, B_b2)
    return x
```

```cpp
#include <hip/hip_runtime.h>
#include <hip/hip_bf16.h>
typedef unsigned short u16;
__device__ __forceinline__ float bf2f(u16 b){ return __builtin_bit_cast(float, (unsigned)b << 16); }
__device__ __forceinline__ u16 f2bf(float f){ return __builtin_bit_cast(u16, (__bf16)f); }
constexpr int S=2048, D=512, F=1792, H=32, DH=16;

__global__ void ln_k(const u16* x, u16* y, int times){
  int row = blockIdx.x; int l = threadIdx.x;
  float v[8];
  for(int i=0;i<8;i++) v[i]=bf2f(x[row*D + l*8+i]);
  for(int t=0;t<times;t++){
    float s=0; for(int i=0;i<8;i++) s+=v[i];
    for(int o=32;o;o>>=1) s+=__shfl_xor(s,o);
    float m=s/D; float q=0; for(int i=0;i<8;i++){ float d=v[i]-m; q+=d*d; }
    for(int o=32;o;o>>=1) q+=__shfl_xor(q,o);
    float r=rsqrtf(q/D+1e-5f);
    for(int i=0;i<8;i++) v[i]=bf2f(f2bf((v[i]-m)*r));
  }
  for(int i=0;i<8;i++) y[row*D+l*8+i]=f2bf(v[i]);
}
__global__ void gemm_k(const u16* A, int sam, int sak, const u16* B, int N, int K, u16* C, int scm, int scn, const u16* bias, const u16* res, int mode){
  __shared__ float As[16][17], Bs[16][17];
  int tx=threadIdx.x, ty=threadIdx.y; int m=blockIdx.y*16+ty, n=blockIdx.x*16+tx;
  float acc=0;
  for(int k0=0;k0<K;k0+=16){
    As[ty][tx]=bf2f(A[(size_t)m*sam+(size_t)(k0+tx)*sak]);
    Bs[ty][tx]=bf2f(B[(size_t)(k0+ty)*N+n]);
    __syncthreads();
    for(int k=0;k<16;k++) acc+=As[ty][k]*Bs[k][tx];
    __syncthreads();
  }
  size_t o=(size_t)m*scm+(size_t)n*scn;
  if(mode==1){ acc+=bf2f(bias[n]); float u=acc; acc=0.5f*u*(1.f+tanhf(0.7978845608f*(u+0.044715f*u*u*u))); }
  if(mode==2){ acc+=bf2f(bias[n]); acc+=bf2f(res[o]); }
  if(mode==3){ acc+=bf2f(res[o]); }
  C[o]=f2bf(acc);
}
__global__ void attn_k(const u16* qkv, u16* o){
  int t=blockIdx.x*blockDim.x+threadIdx.x; int s=t/H, h=t%H;
  float q[16]; for(int i=0;i<16;i++) q[i]=bf2f(qkv[s*1536+h*16+i]);
  float lg[8]; float mx=-3e38f;
  for(int w=0;w<8;w++){ int j=s-2*w; float a=0; if(j>=0){ for(int i=0;i<16;i++) a+=q[i]*bf2f(qkv[j*1536+512+h*16+i]); a*=0.25f;} else a=-1e9f; lg[w]=a; mx=fmaxf(mx,a);}
  float sum=0; for(int w=0;w<8;w++){ lg[w]=__expf(lg[w]-mx); sum+=lg[w]; }
  float acc[16]; for(int i=0;i<16;i++) acc[i]=0;
  for(int w=0;w<8;w++){ int j=s-2*w; if(j<0) j=0; float p=lg[w]/sum; for(int i=0;i<16;i++) acc[i]+=p*bf2f(qkv[j*1536+1024+h*16+i]); }
  for(int i=0;i<16;i++) o[s*512+h*16+i]=f2bf(acc[i]);
}
extern "C" void kernel_launch(void* const* d_in, const int* in_sizes, int n_in, void* d_out, int out_size, void* d_ws, size_t ws_size, hipStream_t stream){
  const u16* x0=(const u16*)d_in[0]; const u16* wqkv=(const u16*)d_in[1]; const u16* wo=(const u16*)d_in[2];
  const u16* w1=(const u16*)d_in[4]; const u16* b1=(const u16*)d_in[5]; const u16* w2=(const u16*)d_in[6]; const u16* b2=(const u16*)d_in[7];
  u16* ws=(u16*)d_ws; u16* xn=ws; u16* qkv=xn+S*D; u16* o=qkv+S*1536; u16* x1=o+S*D; u16* u=x1+S*D; u16* x2=u+D*F;
  const u16* xin=x0;
  for(int L=0;L<2;L++){
    u16* xout = L==0? x2 : (u16*)d_out;
    ln_k<<<S,64,0,stream>>>(xin,xn,1);
    gemm_k<<<dim3(1536/16,S/16),dim3(16,16),0,stream>>>(xn,D,1,wqkv,1536,D,qkv,1536,1,nullptr,nullptr,0);
    attn_k<<<S*H/256,256,0,stream>>>(qkv,o);
    gemm_k<<<dim3(512/16,S/16),dim3(16,16),0,stream>>>(o,D,1,wo,512,D,x1,512,1,nullptr,xin,3);
    ln_k<<<S,64,0,stream>>>(x1,xn,2);
    gemm_k<<<dim3(F/16,D/16),dim3(16,16),0,stream>>>(xn,1,D,w1,F,S,u,F,1,b1,nullptr,1);
    gemm_k<<<dim3(S/16,D/16),dim3(16,16),0,stream>>>(u,F,1,w2,S,F,xout,1,D,b2,x1,2);
    xin=xout;
  }
}
```

```cpp
#include <hip/hip_runtime.h>
#include <hip/hip_bf16.h>

typedef unsigned short u16;
typedef unsigned long long u64;
typedef unsigned v4u __attribute__((ext_vector_type(4)));
typedef unsigned v2u __attribute__((ext_vector_type(2)));
typedef float f32x2 __attribute__((ext_vector_type(2)));
typedef float f32x4 __attribute__((ext_vector_type(4)));
typedef float f32x16 __attribute__((ext_vector_type(16)));
typedef __bf16 bf16x8 __attribute__((ext_vector_type(8)));
typedef __bf16 bf16x2 __attribute__((ext_vector_type(2)));
typedef short s16x4 __attribute__((ext_vector_type(4)));
#define GAS __attribute__((address_space(1)))
#define LAS __attribute__((address_space(3)))
#define DEV __device__ __forceinline__
typedef __amdgpu_buffer_rsrc_t rsrc_t;

#define STAMPS 0

constexpr int OFF_FLAG = 4096;
constexpr int OFF_DBG = 32768;
constexpr int OFF_ST1 = 131072;
constexpr int OFF_ST2 = OFF_ST1 + 2 * 131072;
constexpr int OFF_OF = 1 << 20;
constexpr int OFF_HF = OFF_OF + (4 << 20);
constexpr int OFF_UF = OFF_HF + (4 << 20);
constexpr int OFF_XF = OFF_UF + (4 << 20);
constexpr int OFF_W1F = OFF_XF + (2 << 20);
constexpr int OFF_W2F = OFF_W1F + (7 << 20);
constexpr int WS_BYTES = OFF_W2F + (7 << 20);
constexpr int OOB = 0x40000000;
constexpr int SC1 = 16;
constexpr int LDS_WO = 98304;
constexpr int LDS_BYTES = 163840;
constexpr unsigned SPIN_CAP = 1u << 17;
constexpr unsigned SENT = 0xFFFFFFFFu;
constexpr float EPS = 1e-5f;
constexpr float NEG = -1e30f;

DEV float asf(unsigned u) { return __builtin_bit_cast(float, u); }
DEV unsigned asu(float f) { return __builtin_bit_cast(unsigned, f); }
DEV unsigned pk(float a, float b) { f32x2 p = {a, b}; return __builtin_bit_cast(unsigned, __builtin_convertvector(p, bf16x2)); }
DEV float lo(unsigned u) { return asf(u << 16); }
DEV float hi(unsigned u) { return asf(u & 0xffff0000u); }
DEV f32x4 mfma16(v4u a, v4u b, f32x4 c) { return __builtin_amdgcn_mfma_f32_16x16x32_bf16(__builtin_bit_cast(bf16x8, a), __builtin_bit_cast(bf16x8, b), c, 0, 0, 0); }
DEV f32x4 mfma16k16(v2u a, v2u b, f32x4 c) { return __builtin_amdgcn_mfma_f32_16x16x16bf16_1k(__builtin_bit_cast(s16x4, a), __builtin_bit_cast(s16x4, b), c, 0, 0, 0); }
DEV f32x4 mfmaf(float a, float b) { f32x4 z = {0.f, 0.f, 0.f, 0.f}; return __builtin_amdgcn_mfma_f32_16x16x4f32(a, b, z, 0, 0, 0); }
DEV f32x16 mfma32(v4u a, v4u b, f32x16 c) { return __builtin_amdgcn_mfma_f32_32x32x16_bf16(__builtin_bit_cast(bf16x8, a), __builtin_bit_cast(bf16x8, b), c, 0, 0, 0); }
DEV v4u trfrag(LAS char* p0, LAS char* p1) {
  v2u a = __builtin_bit_cast(v2u, __builtin_amdgcn_ds_read_tr16_b64_v4i16((LAS s16x4*)p0));
  v2u b = __builtin_bit_cast(v2u, __builtin_amdgcn_ds_read_tr16_b64_v4i16((LAS s16x4*)p1));
  return v4u{a.x, a.y, b.x, b.y};
}
template <int AUX> DEV v4u ld128(rsrc_t r, int voff, int soff) { return __builtin_amdgcn_raw_buffer_load_b128(r, voff, soff, AUX); }
template <int AUX> DEV void st128(v4u v, rsrc_t r, int voff, int soff) { __builtin_amdgcn_raw_buffer_store_b128(v, r, voff, soff, AUX); }
#define SCHED() __builtin_amdgcn_sched_barrier(0)
#define DRAIN() asm volatile("s_waitcnt vmcnt(0)" ::: "memory")
template <int CTRL> DEV float dpp_add(float v) { return v + asf((unsigned)__builtin_amdgcn_update_dpp(0, (int)asu(v), CTRL, 0xf, 0xf, true)); }
DEV float rowsum16(float v) { v = dpp_add<0x128>(v); v = dpp_add<0x124>(v); v = dpp_add<0x122>(v); return dpp_add<0x121>(v); }
DEV float xmax16(float v) { auto r = __builtin_amdgcn_permlane16_swap(asu(v), asu(v), false, false); unsigned a = r[0], b = r[1]; return fmaxf(asf(a), asf(b)); }
DEV float xmax32(float v) { auto r = __builtin_amdgcn_permlane32_swap(asu(v), asu(v), false, false); unsigned a = r[0], b = r[1]; return fmaxf(asf(a), asf(b)); }
DEV float xadd32(float v) { auto r = __builtin_amdgcn_permlane32_swap(asu(v), asu(v), false, false); unsigned a = r[0], b = r[1]; return asf(a) + asf(b); }

DEV void poll_flags(const unsigned char* ws, int off, u64 tag) {
  const GAS u64* p = (const GAS u64*)(ws + off);
  for (unsigned s = 0; s < SPIN_CAP; ++s) {
    u64 v = __hip_atomic_load(p, __ATOMIC_RELAXED, __HIP_MEMORY_SCOPE_AGENT);
    if (__all(v == tag)) break;
  }
  __builtin_amdgcn_fence(__ATOMIC_ACQUIRE, "wavefront");
}
DEV void publish(unsigned char* ws, int off, u64 tag) {
  DRAIN();
  __builtin_amdgcn_s_barrier();
  if (threadIdx.x == 0) __hip_atomic_store((GAS u64*)(ws + off), tag, __ATOMIC_RELAXED, __HIP_MEMORY_SCOPE_AGENT);
  asm volatile("" ::: "memory");
}
#define STAMP(n)

DEV void tile_store(const unsigned (&raw)[8], rsrc_t rws, int voff) {
  v4u f0, f1;
#pragma unroll
  for (int i = 0; i < 4; ++i) {
    f0[i] = __builtin_amdgcn_perm(raw[2 * i + 1], raw[2 * i], 0x05040100u);
    f1[i] = __builtin_amdgcn_perm(raw[2 * i + 1], raw[2 * i], 0x07060302u);
  }
  st128<SC1>(f0, rws, voff, 0);
  st128<SC1>(f1, rws, voff + 1024, 0);
}

__global__ void __launch_bounds__(256, 1)
fused_kernel(const u16* __restrict__ x0, const u16* __restrict__ wqkv, const u16* __restrict__ wo, const u16* __restrict__ w1,
             const u16* __restrict__ b1, const u16* __restrict__ w2, const u16* __restrict__ b2, u16* out, unsigned char* ws) {
  extern __shared__ __attribute__((aligned(16))) char smem[];
  LAS char* lds = (LAS char*)smem;
  const int tid = threadIdx.x, w = __builtin_amdgcn_readfirstlane(tid >> 6);
  const int b = blockIdx.x, xa = (b & 7) >> 1, xb = b & 1, bi = b >> 3;
  const int T = 4 * xa + (bi >> 3), loc = bi & 7, G = 8 * xb + loc;
  const int TT = 2 * T + (loc >> 2), C = 4 * xb + (loc & 3);
  const int DT = 4 * xb + (bi & 3), FT = 7 * xa + (bi >> 2);
  const bool p3act = bi < 28;
#define LANEVARS int l = __builtin_amdgcn_mbcnt_hi(~0u, __builtin_amdgcn_mbcnt_lo(~0u, 0u)); asm volatile("" : "+v"(l)); \
  const int r = l & 15, g = l >> 4, j32 = l & 31, hh = l >> 5; (void)r; (void)g; (void)j32; (void)hh;
  const int wa = w >> 1, wb = w & 1;
  const rsrc_t rws = __builtin_amdgcn_make_buffer_rsrc(ws, 0, WS_BYTES, 0x00020000);
  const rsrc_t rx0 = __builtin_amdgcn_make_buffer_rsrc((void*)x0, 0, 2048 * 512 * 2, 0x00020000);
  const rsrc_t rwq = __builtin_amdgcn_make_buffer_rsrc((void*)wqkv, 0, 512 * 1536 * 2, 0x00020000);
  const rsrc_t rwo = __builtin_amdgcn_make_buffer_rsrc((void*)wo, 0, 512 * 512 * 2, 0x00020000);
  const rsrc_t rw1 = __builtin_amdgcn_make_buffer_rsrc((void*)w1, 0, 2048 * 1792 * 2, 0x00020000);
  const rsrc_t rw2 = __builtin_amdgcn_make_buffer_rsrc((void*)w2, 0, 2048 * 1792 * 2, 0x00020000);
  const rsrc_t rb1 = __builtin_amdgcn_make_buffer_rsrc((void*)b1, 0, 1792 * 2, 0x00020000);
  const rsrc_t rout = __builtin_amdgcn_make_buffer_rsrc((void*)out, 0, 2048 * 512 * 2, 0x00020000);
  STAMP(0);
  const u64 tag = *(const GAS u64*)ws + 1ull;
  const v4u ones = {0x3F803F80u, 0x3F803F80u, 0x3F803F80u, 0x3F803F80u};
  const f32x4 z4 = {0.f, 0.f, 0.f, 0.f};

  const int RT1 = 8 * T + 4 * wa + wb;
  const int RT2 = 4 * TT + w;
  v4u X[3][16];
  v4u wst[16];
  unsigned raw1[4][8], raw2[4][8];
  f32x4 cq[2], ck[2];
  float cv[2];
  v4u res1[2];
  v4u W[64];

#pragma unroll
  for (int L = 0; L < 2; ++L) {
    const int fbase = OFF_FLAG + L * 8192;
    v4u xres[2];
    {
    LANEVARS
    LAS char* const wq_rd = lds + (8 * g + (r >> 2)) * 64 + (l & 3) * 8;
    const int hsw = g & 1;
    v2u st2[3][4];
    if (L == 0) {
      v4u q[24];
      {
        const int vo = (128 * w + (l >> 2)) * 3072 + G * 64 + (l & 3) * 16;
#pragma unroll
        for (int i = 0; i < 24; ++i) q[i] = ld128<0>(rwq, vo + (i >> 3) * 1024, (i & 7) * 16 * 3072);
      }
      int xo[3];
#pragma unroll
      for (int j = 0; j < 3; ++j) { const int RT = RT1 + 2 * (j - 1); xo[j] = (32 * (RT >> 1) + 2 * r + wb) * 1024 + g * 16; }
      if (r < 9 || RT1 < 2) xo[0] = OOB;
#pragma unroll
      for (int ks = 0; ks < 12; ++ks)
#pragma unroll
        for (int j = 0; j < 3; ++j) X[j][ks] = ld128<0>(rx0, xo[j] + ks * 64, 0);
      SCHED();
      {
        const int row = 128 * w + (l >> 2);
        LAS char* d = lds + row * 64 + (((l & 3) ^ (2 * ((row >> 3) & 1))) * 16);
#pragma unroll
        for (int i = 0; i < 24; ++i) *(LAS v4u*)(d + (i >> 3) * 32768 + (i & 7) * 1024) = q[i];
      }
      SCHED();
#pragma unroll
      for (int ks = 12; ks < 16; ++ks)
#pragma unroll
        for (int j = 0; j < 3; ++j) X[j][ks] = ld128<0>(rx0, xo[j] + ks * 64, 0);
#pragma unroll
      for (int a = 0; a < 2; ++a) xres[a] = ld128<0>(rx0, (32 * (RT2 >> 1) + 2 * r + wb) * 1024 + g * 16 + (2 * C + (a ^ wa)) * 64, 0);
      if (l < 32) __builtin_amdgcn_raw_buffer_store_b64(v2u{SENT, SENT}, rws, OFF_ST1 + (l >> 4) * 131072 + (RT2 * 8 + C) * 128 + r * 8, 0, SC1);
      SCHED();
      __syncthreads();
      f32x4 cvv[2] = {z4, z4};
      cq[0] = cq[1] = ck[0] = ck[1] = z4;
#pragma unroll
      for (int ks = 0; ks < 16; ++ks)
#pragma unroll
        for (int hd = 0; hd < 2; ++hd) {
          LAS char* p = wq_rd + (hd ^ hsw) * 32 + ks * 2048;
          cq[hd] = mfma16(trfrag(p, p + 256), ones, cq[hd]);
          ck[hd] = mfma16(trfrag(p + 32768, p + 32768 + 256), ones, ck[hd]);
          cvv[hd] = mfma16(ones, trfrag(p + 65536, p + 65536 + 256), cvv[hd]);
        }
      cv[0] = cvv[0][0]; cv[1] = cvv[1][0];
      SCHED();
      {
        const int vo = (128 * w + (l >> 3)) * 1024 + C * 128 + (l & 7) * 16;
#pragma unroll
        for (int i = 0; i < 16; ++i) wst[i] = ld128<0>(rwo, vo, i * 8192);
      }
      {
        const int n1 = 16 * T + G, set = n1 >> 1, ft = set >> 2, rt0 = (set & 3) * 32 + (n1 & 1) * 16 + 4 * w;
#pragma unroll
        for (int k = 0; k < 4; ++k) {
          const int RT = rt0 + k;
          int vo = (32 * (RT >> 1) + 16 * hh + (RT & 1)) * 3584 + ft * 128 + j32 * 4;
          if (set >= 112) vo = OOB;
#pragma unroll
          for (int e = 0; e < 8; ++e) raw1[k][e] = __builtin_amdgcn_raw_buffer_load_b32(rw1, vo, e * 7168, 0);
        }
      }
      SCHED();
    } else {
      {
        const int tt = 2 * T + wa;
        const int t2 = (l & 8) ? (tt > 0 ? tt - 1 : 0) : tt;
        poll_flags(ws, OFF_FLAG + 3 * 2048 + (t2 * 8 + (l & 7)) * 8, tag);
      }
      STAMP(20);
      int xo[3];
#pragma unroll
      for (int j = 0; j < 3; ++j) xo[j] = OFF_XF + (RT1 + 2 * (j - 1)) * 16384 + l * 16;
      if (r < 9 || RT1 < 2) xo[0] = OOB;
#pragma unroll
      for (int j = 0; j < 3; ++j) {
        int so = OFF_ST2 + (RT1 + 2 * (j - 1)) * 2048 + g * 512 + r * 8;
        if (j == 0 && RT1 < 2) so = OOB;
#pragma unroll
        for (int q = 0; q < 4; ++q) st2[j][q] = __builtin_amdgcn_raw_buffer_load_b64(rws, so + q * 128, 0, SC1);
      }
#pragma unroll
      for (int ks = 0; ks < 16; ++ks)
#pragma unroll
        for (int j = 0; j < 3; ++j) X[j][ks] = ld128<SC1>(rws, xo[j], ks * 1024);
      SCHED();
    }
    f32x4 aq[2][2], ak[3][2], av[3][2];
#pragma unroll
    for (int hd = 0; hd < 2; ++hd) { aq[0][hd] = aq[1][hd] = z4; ak[0][hd] = ak[1][hd] = ak[2][hd] = z4; av[0][hd] = av[1][hd] = av[2][hd] = z4; }
#pragma unroll
    for (int ks = 0; ks < 16; ++ks) {
#pragma unroll
      for (int hd = 0; hd < 2; ++hd) {
        LAS char* p = wq_rd + (hd ^ hsw) * 32 + ks * 2048;
        const v4u fq = trfrag(p, p + 256), fk = trfrag(p + 32768, p + 32768 + 256), fv = trfrag(p + 65536, p + 65536 + 256);
#pragma unroll
        for (int j = 0; j < 3; ++j) {
          if (j > 0) aq[j - 1][hd] = mfma16(fq, X[j][ks], aq[j - 1][hd]);
          ak[j][hd] = mfma16(fk, X[j][ks], ak[j][hd]);
          av[j][hd] = mfma16(X[j][ks], fv, av[j][hd]);
        }
      }
    }
    SCHED();
    STAMP(1 + 20 * L);
    float sm[3], sq[3];
    if (L == 0) {
      {
        const int n2 = 32 * C + TT, set = n2 >> 1, tt = set >> 2, k0 = (set & 3) * 28 + (n2 & 1) * 14;
#pragma unroll
        for (int k = 0; k < 4; ++k) {
          const int kk = 4 * k + w, kap = k0 + kk;
          int vo = (64 * (kap >> 2) + ((kap >> 1) & 1) + 32 * (kap & 1) + 8 * hh) * 4096 + tt * 128 + j32 * 4;
          if (kk >= 14) vo = OOB;
#pragma unroll
          for (int e = 0; e < 8; ++e) raw2[k][e] = __builtin_amdgcn_raw_buffer_load_b32(rw2, vo, (16 * (e >> 2) + 2 * (e & 3)) * 4096, 0);
        }
      }
      SCHED();
#pragma unroll
      for (int j = 0; j < 3; ++j) {
        f32x4 s1 = z4, s2 = z4;
#pragma unroll
        for (int ks = 0; ks < 16; ++ks) { s1 = mfma16(ones, X[j][ks], s1); s2 = mfma16(X[j][ks], X[j][ks], s2); }
        const int rr = r & 3;
        float d = rr == 0 ? s2[0] : rr == 1 ? s2[1] : rr == 2 ? s2[2] : s2[3];
        d = (g == (r >> 2)) ? d : 0.f;
        sm[j] = s1[0];
        sq[j] = mfmaf(1.f, d)[0];
      }
    } else {
#pragma unroll
      for (int j = 0; j < 3; ++j) {
        const float a = (asf(st2[j][0].x) + asf(st2[j][1].x)) + (asf(st2[j][2].x) + asf(st2[j][3].x));
        const float c = (asf(st2[j][0].y) + asf(st2[j][1].y)) + (asf(st2[j][2].y) + asf(st2[j][3].y));
        sm[j] = mfmaf(1.f, a)[0];
        sq[j] = mfmaf(1.f, c)[0];
      }
    }
    {
      const int row = 128 * w + (l >> 3), c = l & 7;
      LAS char* d = lds + LDS_WO + row * 128 + (((c >> 1) ^ ((row >> 1) & 3)) * 32) + (c & 1) * 16;
#pragma unroll
      for (int i = 0; i < 16; ++i) *(LAS v4u*)(d + i * 1024) = wst[i];
    }
    v2u Qb[2][2], Kb[3][2], Vb[3][2];
    const float onehot = g == 0 ? 1.f : 0.f;
#pragma unroll
    for (int j = 0; j < 3; ++j) {
      const float m = sm[j] * (1.f / 512.f);
      const float var = fmaxf(sq[j] * (1.f / 512.f) - m * m, 0.f);
      const float rs = __builtin_amdgcn_rsqf(var + EPS);
      const float rq = rs * (0.25f * 1.44269504089f);
      const f32x4 mR = mfmaf(m, onehot), rR = mfmaf(rs, onehot);
#pragma unroll
      for (int hd = 0; hd < 2; ++hd) {
        const f32x4 kk = (ak[j][hd] - m * ck[hd]) * rs;
        Kb[j][hd] = v2u{pk(kk[0], kk[1]), pk(kk[2], kk[3])};
        const f32x4 vv = (av[j][hd] - mR * cv[hd]) * rR;
        Vb[j][hd] = v2u{pk(vv[0], vv[1]), pk(vv[2], vv[3])};
        if (j > 0) {
          const f32x4 qq = (aq[j - 1][hd] - m * cq[hd]) * rq;
          Qb[j - 1][hd] = v2u{pk(qq[0], qq[1]), pk(qq[2], qq[3])};
        }
      }
    }
    f32x4 mcur, mprev, mprev0;
    unsigned mc0, mc1;
    {
      bool vc[4];
#pragma unroll
      for (int e = 0; e < 4; ++e) {
        const int key = 4 * g + e;
        vc[e] = key <= r;
        mcur[e] = (key <= r && key >= r - 7) ? 0.f : NEG;
        mprev[e] = (key >= r + 9) ? 0.f : NEG;
        mprev0[e] = RT1 < 2 ? NEG : mprev[e];
      }
      mc0 = (vc[0] ? 0xffffu : 0u) | (vc[1] ? 0xffff0000u : 0u);
      mc1 = (vc[2] ? 0xffffu : 0u) | (vc[3] ? 0xffff0000u : 0u);
    }
    v4u of[2];
#pragma unroll
    for (int j = 1; j < 3; ++j)
#pragma unroll
      for (int hd = 0; hd < 2; ++hd) {
        const f32x4 sp = mfma16k16(Kb[j - 1][hd], Qb[j - 1][hd], j == 1 ? mprev0 : mprev);
        const f32x4 sc = mfma16k16(Kb[j][hd], Qb[j - 1][hd], mcur);
        const float s0 = fmaxf(sp[0], sc[0]), s1 = fmaxf(sp[1], sc[1]), s2 = fmaxf(sp[2], sc[2]), s3 = fmaxf(sp[3], sc[3]);
        const float mx = xmax32(xmax16(fmaxf(fmaxf(s0, s1), fmaxf(s2, s3))));
        const unsigned p0 = pk(__builtin_amdgcn_exp2f(s0 - mx), __builtin_amdgcn_exp2f(s1 - mx));
        const unsigned p1 = pk(__builtin_amdgcn_exp2f(s2 - mx), __builtin_amdgcn_exp2f(s3 - mx));
        const v4u P = {p0 & ~mc0, p1 & ~mc1, p0 & mc0, p1 & mc1};
        const v4u V = {Vb[j - 1][hd].x, Vb[j - 1][hd].y, Vb[j][hd].x, Vb[j][hd].y};
        const f32x4 o = mfma16(V, P, z4);
        const f32x4 ls = mfma16(ones, P, z4);
        const float inv = __builtin_amdgcn_rcpf(ls[0]);
        of[j - 1][2 * hd] = pk(o[0] * inv, o[1] * inv);
        of[j - 1][2 * hd + 1] = pk(o[2] * inv, o[3] * inv);
      }
    if (L == 0) {
      const int n1 = 16 * T + G, set = n1 >> 1, ft = set >> 2, rt0 = (set & 3) * 32 + (n1 & 1) * 16 + 4 * w;
      const int vo = set < 112 ? OFF_W1F + (ft * 128 + rt0) * 2048 + l * 16 : OOB;
#pragma unroll
      for (int k = 0; k < 4; ++k) tile_store(raw1[k], rws, vo + k * 2048);
    }
    st128<SC1>(of[0], rws, OFF_OF + L * (2 << 20) + (RT1 * 16 + G) * 1024 + l * 16, 0);
    st128<SC1>(of[1], rws, OFF_OF + L * (2 << 20) + ((RT1 + 2) * 16 + G) * 1024 + l * 16, 0);
    STAMP(2 + 20 * L);
    asm volatile("s_waitcnt lgkmcnt(0)" ::: "memory");
    publish(ws, fbase + (16 * T + G) * 8, tag);
    STAMP(3 + 20 * L);
    }
    {
    LANEVARS
    const float onehot = g == 0 ? 1.f : 0.f;
    if (L == 1) {
#pragma unroll
      for (int a = 0; a < 2; ++a) xres[a] = ld128<SC1>(rws, OFF_XF + RT2 * 16384 + (2 * C + (a ^ wa)) * 1024 + l * 16, 0);
    }
    {
      const int fi = (l & 16) && p3act ? 2 * (4 * FT + w) + (l & 1) : 16 * T + r;
      poll_flags(ws, fbase + fi * 8, tag);
    }
    STAMP(4 + 20 * L);
    v4u O[16];
#pragma unroll
    for (int k = 0; k < 16; ++k) O[k] = ld128<SC1>(rws, OFF_OF + L * (2 << 20) + RT2 * 16384 + l * 16, k * 1024);
    SCHED();
    const int w1o = p3act ? OFF_W1F + (FT * 128 + 32 * w) * 2048 + l * 16 : OOB;
    const int w1a = w1o + wa * 1024, w1b = w1o + (1 - wa) * 1024;
#define W1LOAD(lo_, hi_) _Pragma("unroll") for (int k = lo_; k < hi_; ++k) { W[2 * k] = ld128<SC1>(rws, w1a, k * 2048); W[2 * k + 1] = ld128<SC1>(rws, w1b, k * 2048); } SCHED();
    W1LOAD(0, 8)
    f32x4 a2[4];
    {
      v4u id[2];
#pragma unroll
      for (int u = 0; u < 2; ++u) {
        const int e0 = 16 * u + r - 8 * g;
#pragma unroll
        for (int i = 0; i < 4; ++i) id[u][i] = e0 == 2 * i ? 0x3F80u : e0 == 2 * i + 1 ? 0x3F800000u : 0u;
      }
#pragma unroll
      for (int ct = 0; ct < 4; ++ct) a2[ct] = mfma16(xres[ct >> 1], id[ct & 1], z4);
      const int rowl = 4 * g + (r >> 2), sw = (rowl >> 1) & 3;
      LAS char* base = lds + LDS_WO + rowl * 128 + (l & 3) * 8;
#pragma unroll
      for (int k = 0; k < 16; ++k)
#pragma unroll
        for (int ct = 0; ct < 4; ++ct) {
          LAS char* p = base + (((ct ^ (2 * wa)) ^ sw) * 32) + k * 4096;
          a2[ct] = mfma16(O[k], trfrag(p, p + 2048), a2[ct]);
        }
    }
    STAMP(5 + 20 * L);
    unsigned xp[4][2];
    float xr[4][4];
    {
      float s1[4], s2[4];
#pragma unroll
      for (int ct = 0; ct < 4; ++ct) {
        xp[ct][0] = pk(a2[ct][0], a2[ct][1]); xp[ct][1] = pk(a2[ct][2], a2[ct][3]);
        xr[ct][0] = lo(xp[ct][0]); xr[ct][1] = hi(xp[ct][0]); xr[ct][2] = lo(xp[ct][1]); xr[ct][3] = hi(xp[ct][1]);
      }
#pragma unroll
      for (int e = 0; e < 4; ++e) {
        s1[e] = rowsum16((xr[0][e] + xr[1][e]) + (xr[2][e] + xr[3][e]));
        s2[e] = rowsum16((xr[0][e] * xr[0][e] + xr[1][e] * xr[1][e]) + (xr[2][e] * xr[2][e] + xr[3][e] * xr[3][e]));
      }
      unsigned u1 = asu(r == 0 ? s1[0] : r == 1 ? s1[1] : r == 2 ? s1[2] : s1[3]);
      unsigned u2 = asu(r == 0 ? s2[0] : r == 1 ? s2[1] : r == 2 ? s2[2] : s2[3]);
      u1 = u1 == SENT ? 0x7FC00000u : u1; u2 = u2 == SENT ? 0x7FC00000u : u2;
      if (r < 4) __builtin_amdgcn_raw_buffer_store_b64(v2u{u1, u2}, rws, OFF_ST1 + L * 131072 + (RT2 * 8 + C) * 128 + (4 * g + r) * 8, 0, SC1);
    }
    SCHED();
    if (L == 0) {
      const int n2 = 32 * C + TT, set = n2 >> 1, tt = set >> 2, k0 = (set & 3) * 28 + (n2 & 1) * 14;
#pragma unroll
      for (int k = 0; k < 4; ++k) {
        const int kk = 4 * k + w;
        tile_store(raw2[k], rws, kk < 14 ? OFF_W2F + (tt * 112 + k0 + kk) * 2048 + l * 16 : OOB);
      }
      SCHED();
    }
    W1LOAD(8, 16)
#pragma unroll
    for (int a = 0; a < 2; ++a) {
      auto r0 = __builtin_amdgcn_permlane16_swap(xp[2 * a][0], xp[2 * a + 1][0], false, false);
      auto r1 = __builtin_amdgcn_permlane16_swap(xp[2 * a][1], xp[2 * a + 1][1], false, false);
      res1[a] = v4u{r0[0], r1[0], r0[1], r1[1]};
    }
    {
      const GAS u64* sp = (const GAS u64*)(ws + OFF_ST1 + L * 131072 + (RT2 * 8 + 2 * g) * 128 + r * 8);
      u64 v0, v1;
      for (unsigned s = 0; s < SPIN_CAP; ++s) {
        v0 = __hip_atomic_load(sp, __ATOMIC_RELAXED, __HIP_MEMORY_SCOPE_AGENT);
        v1 = __hip_atomic_load(sp + 16, __ATOMIC_RELAXED, __HIP_MEMORY_SCOPE_AGENT);
        const bool ok = (unsigned)v0 != SENT && (unsigned)(v0 >> 32) != SENT && (unsigned)v1 != SENT && (unsigned)(v1 >> 32) != SENT;
        if (__all(ok)) break;
      }
      STAMP(6 + 20 * L);
      const float t1 = mfmaf(1.f, asf((unsigned)v0) + asf((unsigned)v1))[0];
      const float t2 = mfmaf(1.f, asf((unsigned)(v0 >> 32)) + asf((unsigned)(v1 >> 32)))[0];
      const float m = t1 * (1.f / 512.f);
      const float var = fmaxf(t2 * (1.f / 512.f) - m * m, 0.f);
      const float r1 = __builtin_amdgcn_rsqf(var + EPS);
      const float rho = r1 * __builtin_amdgcn_rsqf(var * r1 * r1 + EPS);
      const f32x4 mR = mfmaf(m, onehot), rR = mfmaf(rho, onehot);
      unsigned hp[4][2];
#pragma unroll
      for (int ct = 0; ct < 4; ++ct) {
        hp[ct][0] = pk((xr[ct][0] - mR[0]) * rR[0], (xr[ct][1] - mR[1]) * rR[1]);
        hp[ct][1] = pk((xr[ct][2] - mR[2]) * rR[2], (xr[ct][3] - mR[3]) * rR[3]);
      }
#pragma unroll
      for (int a = 0; a < 2; ++a) {
        auto r0 = __builtin_amdgcn_permlane16_swap(hp[2 * a][0], hp[2 * a + 1][0], false, false);
        auto r1_ = __builtin_amdgcn_permlane16_swap(hp[2 * a][1], hp[2 * a + 1][1], false, false);
        st128<SC1>(v4u{r0[0], r1_[0], r0[1], r1_[1]}, rws, OFF_HF + L * (2 << 20) + ((C * 128 + RT2) * 2 + (a ^ wa)) * 1024 + l * 16, 0);
      }
    }
    STAMP(7 + 20 * L);
    publish(ws, fbase + 2048 + (32 * C + TT) * 8, tag);
    STAMP(8 + 20 * L);
    W1LOAD(16, 32)
    }
    LANEVARS
    const int w2o = OFF_W2F + (TT * 112 + 28 * w) * 2048 + l * 16;
    const int w2a = w2o + wb * 1024, w2b = w2o + (1 - wb) * 1024;
#define W2LOAD(lo_, hi_) _Pragma("unroll") for (int k = lo_; k < hi_; ++k) { W[2 * k] = ld128<SC1>(rws, w2a, k * 2048); W[2 * k + 1] = ld128<SC1>(rws, w2b, k * 2048); } SCHED();
    LAS char* const red = lds + LDS_WO;
    {
      v4u bb[4];
#pragma unroll
      for (int q = 0; q < 4; ++q) bb[q] = ld128<0>(rb1, p3act ? (64 * FT + 16 * q + 8 * hh) * 2 : OOB, 0);
      {
        const int fi = (l & 8) || !p3act ? 2 * (4 * TT + w) + (l & 1) : 32 * DT + 8 * w + (l & 7);
        poll_flags(ws, fbase + 2048 + fi * 8, tag);
      }
      STAMP(9 + 20 * L);
      if (p3act) {
        constexpr int WIN = 16;
        v4u A[2 * WIN];
        const int ho = OFF_HF + L * (2 << 20) + (DT * 128 + 32 * w) * 2048 + l * 16;
        const int ha = ho + wb * 1024, hb = ho + (1 - wb) * 1024;
#pragma unroll
        for (int k = 0; k < WIN; ++k) { A[2 * k] = ld128<SC1>(rws, ha, k * 2048); A[2 * k + 1] = ld128<SC1>(rws, hb, k * 2048); }
        SCHED();
        f32x16 acc[4];
#pragma unroll
        for (int i = 0; i < 16; ++i) { const unsigned d = bb[i >> 2][i & 3]; acc[0][i] = asf((d >> (16 * wa)) << 16); acc[1][i] = 0.f; acc[2][i] = 0.f; acc[3][i] = 0.f; }
#pragma unroll
        for (int k = 0; k < 32; ++k) {
          const v4u h0 = A[2 * (k % WIN)], h1 = A[2 * (k % WIN) + 1];
          acc[0] = mfma32(W[2 * k], h0, acc[0]);
          acc[2] = mfma32(W[2 * k + 1], h0, acc[2]);
          acc[1] = mfma32(W[2 * k], h1, acc[1]);
          acc[3] = mfma32(W[2 * k + 1], h1, acc[3]);
          if (k + WIN < 32) { A[2 * (k % WIN)] = ld128<SC1>(rws, ha, (k + WIN) * 2048); A[2 * (k % WIN) + 1] = ld128<SC1>(rws, hb, (k + WIN) * 2048); }
          SCHED();
        }
        STAMP(10 + 20 * L);
        W2LOAD(0, 6)
#pragma unroll
        for (int n = 1; n < 4; ++n)
#pragma unroll
          for (int q = 0; q < 4; ++q)
            *(LAS f32x4*)(red + ((n ^ w) * 3 + n - 1) * 4096 + q * 1024 + l * 16) = f32x4{acc[n][4 * q], acc[n][4 * q + 1], acc[n][4 * q + 2], acc[n][4 * q + 3]};
        __syncthreads();
        float y[16];
#pragma unroll
        for (int q = 0; q < 4; ++q) {
          const f32x4 p0 = *(LAS f32x4*)(red + (w * 3 + 0) * 4096 + q * 1024 + l * 16);
          const f32x4 p1 = *(LAS f32x4*)(red + (w * 3 + 1) * 4096 + q * 1024 + l * 16);
          const f32x4 p2 = *(LAS f32x4*)(red + (w * 3 + 2) * 4096 + q * 1024 + l * 16);
#pragma unroll
          for (int i = 0; i < 4; ++i) {
            const float u = (acc[0][4 * q + i] + p0[i]) + (p1[i] + p2[i]);
            const float a = u * (-2.302208198f - 0.1029432397f * u * u);
            y[4 * q + i] = u * __builtin_amdgcn_rcpf(1.f + __builtin_amdgcn_exp2f(a));
          }
        }
#pragma unroll
        for (int s2 = 0; s2 < 2; ++s2)
          st128<SC1>(v4u{pk(y[8 * s2], y[8 * s2 + 1]), pk(y[8 * s2 + 2], y[8 * s2 + 3]), pk(y[8 * s2 + 4], y[8 * s2 + 5]), pk(y[8 * s2 + 6], y[8 * s2 + 7])},
                     rws, OFF_UF + L * (2 << 20) + ((DT * 112 + 4 * FT + 2 * wa + s2) * 2 + wb) * 1024 + l * 16, 0);
        STAMP(11 + 20 * L);
        publish(ws, fbase + 4096 + (32 * DT + 8 * xa + (bi >> 2)) * 8, tag);
        STAMP(12 + 20 * L);
      } else {
        W2LOAD(0, 6)
      }
      W2LOAD(6, 28)
    }

    {
      const float bias = lo((unsigned)((const GAS u16*)b2)[64 * TT + 2 * j32 + wb]);
      poll_flags(ws, fbase + 4096 + (32 * C + 8 * w + ((l & 7) < 7 ? (l & 7) : 0)) * 8, tag);
      STAMP(13 + 20 * L);
      constexpr int WIN = 16;
      v4u A[2 * WIN];
      const int uo = OFF_UF + L * (2 << 20) + (C * 112 + 28 * w) * 2048 + l * 16;
      const int ua = uo + wa * 1024, ub = uo + (1 - wa) * 1024;
#pragma unroll
      for (int k = 0; k < WIN; ++k) { A[2 * k] = ld128<SC1>(rws, ua, k * 2048); A[2 * k + 1] = ld128<SC1>(rws, ub, k * 2048); }
      SCHED();
      f32x16 acc[4];
#pragma unroll
      for (int i = 0; i < 16; ++i) { acc[0][i] = bias; acc[1][i] = 0.f; acc[2][i] = 0.f; acc[3][i] = 0.f; }
      {
        v4u id;
        const int e0 = j32 - 16 * wa - 8 * hh;
#pragma unroll
        for (int i = 0; i < 4; ++i) id[i] = e0 == 2 * i ? 0x3F80u : e0 == 2 * i + 1 ? 0x3F800000u : 0u;
        acc[0] = mfma32(res1[0], id, acc[0]);
        acc[2] = mfma32(res1[1], id, acc[2]);
      }
#pragma unroll
      for (int k = 0; k < 28; ++k) {
        const v4u u0 = A[2 * (k % WIN)], u1 = A[2 * (k % WIN) + 1];
        acc[0] = mfma32(u0, W[2 * k], acc[0]);
        acc[1] = mfma32(u0, W[2 * k + 1], acc[1]);
        acc[2] = mfma32(u1, W[2 * k], acc[2]);
        acc[3] = mfma32(u1, W[2 * k + 1], acc[3]);
        if (k + WIN < 28) { A[2 * (k % WIN)] = ld128<SC1>(rws, ua, (k + WIN) * 2048); A[2 * (k % WIN) + 1] = ld128<SC1>(rws, ub, (k + WIN) * 2048); }
        SCHED();
      }
      STAMP(14 + 20 * L);
#pragma unroll
      for (int n = 1; n < 4; ++n)
#pragma unroll
        for (int q = 0; q < 4; ++q)
          *(LAS f32x4*)(red + ((n ^ w) * 3 + n - 1) * 4096 + q * 1024 + l * 16) = f32x4{acc[n][4 * q], acc[n][4 * q + 1], acc[n][4 * q + 2], acc[n][4 * q + 3]};
      __syncthreads();
      unsigned dw[8];
#pragma unroll
      for (int q = 0; q < 4; ++q) {
        const f32x4 p0 = *(LAS f32x4*)(red + (w * 3 + 0) * 4096 + q * 1024 + l * 16);
        const f32x4 p1 = *(LAS f32x4*)(red + (w * 3 + 1) * 4096 + q * 1024 + l * 16);
        const f32x4 p2 = *(LAS f32x4*)(red + (w * 3 + 2) * 4096 + q * 1024 + l * 16);
        float y[4];
#pragma unroll
        for (int i = 0; i < 4; ++i) y[i] = (acc[0][4 * q + i] + p0[i]) + (p1[i] + p2[i]);
        dw[2 * q] = pk(y[0], y[1]); dw[2 * q + 1] = pk(y[2], y[3]);
      }
      const int RTo = 4 * TT + 2 * (j32 >> 4) + wb;
#pragma unroll
      for (int s2 = 0; s2 < 2; ++s2) {
        auto r0 = __builtin_amdgcn_permlane32_swap(dw[4 * s2], dw[4 * s2 + 2], false, false);
        auto r1 = __builtin_amdgcn_permlane32_swap(dw[4 * s2 + 1], dw[4 * s2 + 3], false, false);
        const v4u v = {r0[0], r1[0], r0[1], r1[1]};
        if (L == 0) st128<SC1>(v, rws, OFF_XF + (RTo * 16 + 2 * C + wa) * 1024 + (2 * s2 + hh) * 256 + r * 16, 0);
        else st128<0>(v, rout, (64 * TT + 2 * j32 + wb) * 1024 + (64 * C + 32 * wa + 8 * (2 * s2 + hh)) * 2, 0);
      }
      if (L == 0) {
        float s1 = 0.f, s2 = 0.f;
#pragma unroll
        for (int i = 0; i < 8; ++i) { const float a = lo(dw[i]), c = hi(dw[i]); s1 += a + c; s2 += a * a + c * c; }
        s1 = xadd32(s1); s2 = xadd32(s2);
        if (l < 32) __builtin_amdgcn_raw_buffer_store_b64(v2u{asu(s1), asu(s2)}, rws, OFF_ST2 + (RTo * 16 + 2 * C + wa) * 128 + r * 8, 0, SC1);
        STAMP(15);
        publish(ws, fbase + 6144 + (TT * 8 + C) * 8, tag);
        STAMP(16);
        const int vo = (128 * w + (l >> 3)) * 1024 + C * 128 + (l & 7) * 16;
#pragma unroll
        for (int i = 0; i < 16; ++i) wst[i] = ld128<0>(rwo, vo, i * 8192);
        SCHED();
      }
    }
  }
  if (b == 0 && tid == 0) *(GAS u64*)ws = tag;
  STAMP(35);
}

extern "C" void kernel_launch(void* const* d_in, const int* in_sizes, int n_in, void* d_out, int out_size, void* d_ws, size_t ws_size,
                              hipStream_t stream) {
  (void)hipFuncSetAttribute((const void*)fused_kernel, hipFuncAttributeMaxDynamicSharedMemorySize, LDS_BYTES);
  fused_kernel<<<256, 256, LDS_BYTES, stream>>>((const u16*)d_in[0], (const u16*)d_in[1], (const u16*)d_in[2], (const u16*)d_in[4],
                                                (const u16*)d_in[5], (const u16*)d_in[6], (const u16*)d_in[7], (u16*)d_out, (unsigned char*)d_ws);
}
```

```cpp
#include <hip/hip_runtime.h>
#include <hip/hip_bf16.h>

typedef unsigned short u16;
constexpr int LDS_BYTES = 163840;

__global__ void __launch_bounds__(256, 1)
fused_kernel(const u16* x0, const u16* wqkv, const u16* wo, const u16* w1, const u16* b1, const u16* w2, const u16* b2, u16* out,
             unsigned char* ws) {
  const int wave = __builtin_amdgcn_readfirstlane(threadIdx.x >> 6);
  const int bx = blockIdx.x;
  const unsigned long long ka = (unsigned long long)__builtin_amdgcn_kernarg_segment_ptr();
  asm volatile(
  ".Lbeg_%=:\n"
  "s_mov_b64 s[96:97], %0\n"
  "s_mov_b32 s99, %1\n"
  "s_mov_b32 s58, %2\n"
  "s_load_dwordx2 s[24:25], s[96:97], 0\n"
  "s_load_dwordx2 s[28:29], s[96:97], 8\n"
  "s_load_dwordx2 s[20:21], s[96:97], 64\n"
  "s_load_dwordx2 s[32:33], s[96:97], 16\n"
  "s_load_dwordx2 s[36:37], s[96:97], 24\n"
  "s_load_dwordx2 s[44:45], s[96:97], 32\n"
  "s_load_dwordx2 s[40:41], s[96:97], 40\n"
  "s_load_dwordx2 s[52:53], s[96:97], 48\n"
  "s_load_dwordx2 s[48:49], s[96:97], 56\n"
  "s_getpc_b64 s[88:89]\n"
  "s_and_b32 s88, s88, 0xffffff00\n"
  "s_mov_b32 s90, .Lend_%=-.Lbeg_%=\n"
  "s_mov_b32 s91, 0x20000\n"
  "v_mbcnt_lo_u32_b32 v0, -1, 0\n"
  "v_mbcnt_hi_u32_b32 v0, -1, v0\n"
  "s_lshr_b32 s68, s99, 3\n"
  "s_lshl2_add_u32 s87, s68, s58\n"
  "s_lshl_b32 s87, s87, 12\n"
  "v_lshl_add_u32 v252, v0, 6, s87\n"
  "buffer_load_dword v252, v252, s[88:91], 0 offen\n"
  "s_mov_b32 s22, 18874368\n"
  "s_mov_b32 s23, 0x20000\n"
  "s_mov_b32 s26, 2097152\n"
  "s_mov_b32 s27, 0x20000\n"
  "s_mov_b32 s30, 1572864\n"
  "s_mov_b32 s31, 0x20000\n"
  "s_mov_b32 s34, 524288\n"
  "s_mov_b32 s35, 0x20000\n"
  "s_mov_b32 s38, 7340032\n"
  "s_mov_b32 s39, 0x20000\n"
  "s_mov_b32 s42, 7340032\n"
  "s_mov_b32 s43, 0x20000\n"
  "s_mov_b32 s46, 3584\n"
  "s_mov_b32 s47, 0x20000\n"
  "s_mov_b32 s50, 2097152\n"
  "s_mov_b32 s51, 0x20000\n"
  "s_mov_b32 s54, 4096\n"
  "s_mov_b32 s55, 0x20000\n"
  "s_bfe_u32 s69, s99, 0x20001\n"
  "s_and_b32 s92, s99, 1\n"
  "s_lshr_b32 s87, s68, 3\n"
  "s_lshl2_add_u32 s61, s69, s87\n"
  "s_and_b32 s87, s68, 7\n"
  "s_lshl3_add_u32 s62, s92, s87\n"
  "s_lshr_b32 s87, s87, 2\n"
  "s_lshl1_add_u32 s63, s61, s87\n"
  "s_and_b32 s87, s68, 3\n"
  "s_lshl2_add_u32 s64, s92, s87\n"
  "s_lshr_b32 s59, s58, 1\n"
  "s_and_b32 s60, s58, 1\n"
  "v_and_b32 v2, 15, v0\n"
  "v_lshrrev_b32 v3, 4, v0\n"
  "v_lshlrev_b32 v1, 4, v0\n"
  "s_lshl_b32 s87, s61, 5\n"
  "s_add_u32 s87, s87, s58\n"
  "s_lshl_b32 s87, s87, 12\n"
  "s_lshl_b32 s88, s58, 2\n"
  "v_add_u32 v45, s88, v3\n"
  "v_xor_b32 v45, v45, v2\n"
  "v_lshl_add_u32 v144, v3, 10, s87\n"
  "v_lshl_add_u32 v144, v45, 4, v144\n"
  "s_lshl_b32 s12, s58, 10\n"
  "s_cmp_eq_u32 s61, 0\n"
  "s_cselect_b32 s89, 1073741824, -16384\n"
  "v_add_u32 v145, s89, v144\n"
  "s_mul_i32 s13, s58, 6144\n"
  "s_add_u32 m0, s12, 53248\n"
  "s_mov_b32 s9, 0\n"
  "s_waitcnt lgkmcnt(0)\n"
  "buffer_load_dwordx4 v145, s[24:27], s9 offen lds\n"
  "s_add_u32 m0, s12, 57344\n"
  "s_mov_b32 s9, 0\n"
  "buffer_load_dwordx4 v144, s[24:27], s9 offen lds\n"
  "s_add_u32 m0, s12, 61440\n"
  "s_mov_b32 s9, 16384\n"
  "buffer_load_dwordx4 v144, s[24:27], s9 offen lds\n"
  "s_load_dwordx2 s[56:57], s[20:21], 0\n"
  "s_lshl_b32 s87, s58, 5\n"
  "v_lshrrev_b32 v187, 2, v0\n"
  "v_add_u32 v187, s87, v187\n"
  "v_lshrrev_b32 v46, 3, v0\n"
  "v_and_b32 v46, 2, v46\n"
  "v_xor_b32 v46, v46, v0\n"
  "v_and_b32 v46, 3, v46\n"
  "s_lshl_b32 s87, s62, 6\n"
  "v_lshl_add_u32 v46, v46, 4, s87\n"
  "s_movk_i32 s87, 0xc00\n"
  "v_mad_u32_u24 v187, v187, s87, v46\n"
  "s_lshl2_add_u32 s87, s68, s58\n"
  "s_bfe_u32 s89, s99, 0x20001\n"
  "s_mul_i32 s87, s87, 12288\n"
  "s_and_b32 s89, s99, 1\n"
  "s_lshl_b32 s89, s89, 30\n"
  "s_add_u32 s87, s87, s89\n"
  "s_add_u32 s87, s87, 384\n"
  "v_min_u32 v45, 11, v2\n"
  "v_lshl_add_u32 v45, v45, 10, s87\n"
  "v_lshl_add_u32 v45, v3, 5, v45\n"
  "buffer_load_dword a215, v45, s[28:31], 0 offen\n"
  "s_add_u32 m0, s12, 65536\n"
  "s_mov_b32 s9, 32768\n"
  "buffer_load_dwordx4 v144, s[24:27], s9 offen lds\n"
  "s_add_u32 m0, s12, 69632\n"
  "s_mov_b32 s9, 49152\n"
  "buffer_load_dwordx4 v144, s[24:27], s9 offen lds\n"
  "s_add_u32 m0, s12, 73728\n"
  "s_mov_b32 s9, 65536\n"
  "buffer_load_dwordx4 v144, s[24:27], s9 offen lds\n"
  "s_add_u32 m0, s12, 77824\n"
  "s_mov_b32 s9, 81920\n"
  "buffer_load_dwordx4 v144, s[24:27], s9 offen lds\n"
  "s_add_u32 m0, s12, 81920\n"
  "s_mov_b32 s9, 98304\n"
  "buffer_load_dwordx4 v144, s[24:27], s9 offen lds\n"
  "s_add_u32 m0, s12, 86016\n"
  "s_mov_b32 s9, 114688\n"
  "buffer_load_dwordx4 v144, s[24:27], s9 offen lds\n"
  "s_add_u32 m0, s13, 0\n"
  "s_mov_b32 s9, 0\n"
  "buffer_load_dwordx4 v187, s[28:31], s9 offen lds\n"
  "s_add_u32 m0, s13, 0\n"
  "s_mov_b32 s9, 0\n"
  "buffer_load_dwordx4 v187, s[28:31], s9 offen offset:1024 lds\n"
  "s_add_u32 m0, s13, 0\n"
  "s_mov_b32 s9, 0\n"
  "buffer_load_dwordx4 v187, s[28:31], s9 offen offset:2048 lds\n"
  "s_add_u32 m0, s13, 3072\n"
  "s_mov_b32 s9, 49152\n"
  "buffer_load_dwordx4 v187, s[28:31], s9 offen lds\n"
  "s_add_u32 m0, s13, 3072\n"
  "s_mov_b32 s9, 49152\n"
  "buffer_load_dwordx4 v187, s[28:31], s9 offen offset:1024 lds\n"
  "s_add_u32 m0, s13, 3072\n"
  "s_mov_b32 s9, 49152\n"
  "buffer_load_dwordx4 v187, s[28:31], s9 offen offset:2048 lds\n"
  "s_lshl2_add_u32 s87, s68, s58\n"
  "s_and_b32 s90, s99, 1\n"
  "s_mul_i32 s87, s87, 12288\n"
  "s_lshl_b32 s90, s90, 9\n"
  "s_add_u32 s87, s87, s90\n"
  "v_mul_u32_u24 v45, 3072, v3\n"
  "v_and_b32 v46, 3, v0\n"
  "v_lshl_add_u32 v45, v46, 7, v45\n"
  "v_bfe_u32 v46, v0, 2, 2\n"
  "v_min_u32 v46, 2, v46\n"
  "v_lshl_add_u32 v45, v46, 10, v45\n"
  "v_add_u32 v45, s87, v45\n"
  "buffer_load_dword a214, v45, s[28:31], 0 offen\n"
  "s_mov_b32 s72, 4096\n"
  "s_mov_b32 s73, 8192\n"
  "s_mov_b32 s74, 12288\n"
  "s_mov_b32 s75, 16384\n"
  "s_mov_b32 s76, 20480\n"
  "s_mov_b32 s77, 24576\n"
  "s_mov_b32 s78, 28672\n"
  "s_mov_b32 s79, 32768\n"
  "s_mov_b32 s80, 36864\n"
  "s_mov_b32 s81, 40960\n"
  "s_mov_b32 s82, 45056\n"
  "s_mov_b32 s83, 49152\n"
  "s_mov_b32 s84, 53248\n"
  "s_add_u32 m0, s12, 90112\n"
  "s_mov_b32 s9, 256\n"
  "buffer_load_dwordx4 v145, s[24:27], s9 offen lds\n"
  "s_mov_b32 s85, 57344\n"
  "s_mov_b32 s86, 61440\n"
  "s_lshr_b32 s87, s68, 2\n"
  "s_add_u32 s87, s87, 4\n"
  "s_and_b32 s87, s87, 7\n"
  "s_mul_i32 s65, s69, 7\n"
  "s_add_u32 s65, s65, s87\n"
  "s_lshl3_add_u32 s69, s69, s87\n"
  "s_mov_b32 s71, s64\n"
  "s_cmp_lt_u32 s87, 7\n"
  "s_cselect_b32 s70, 0, 1073741824\n"
  "s_lshl2_add_u32 s87, s61, s58\n"
  "s_lshl_b32 s66, s87, 1\n"
  "s_lshl2_add_u32 s67, s63, s58\n"
  "s_lshl4_add_u32 s88, s67, s64\n"
  "s_lshl_b32 s88, s88, 10\n"
  "s_add_u32 s18, s88, 14680064\n"
  "s_add_u32 s19, s18, 8192\n"
  "s_mul_i32 s8, s58, 24576\n"
  "v_lshrrev_b32 v45, 5, v0\n"
  "v_xor_b32 v45, v45, v2\n"
  "v_lshlrev_b32 v45, 4, v45\n"
  "v_lshl_add_u32 v45, v2, 8, v45\n"
  "v_bfe_u32 v46, v0, 4, 1\n"
  "v_lshl_add_u32 v45, v46, 3, v45\n"
  "s_lshl_b32 s87, s58, 13\n"
  "s_add_u32 m0, s12, 94208\n"
  "s_mov_b32 s9, 256\n"
  "buffer_load_dwordx4 v144, s[24:27], s9 offen lds\n"
  "s_add_u32 s87, s87, 90112\n"
  "v_add_u32 v136, s87, v45\n"
  "v_xor_b32 v137, 32, v136\n"
  "v_xor_b32 v138, 64, v136\n"
  "v_xor_b32 v139, 96, v136\n"
  "v_xor_b32 v140, 128, v136\n"
  "v_xor_b32 v141, 160, v136\n"
  "v_xor_b32 v142, 192, v136\n"
  "v_xor_b32 v143, 224, v136\n"
  "v_subrev_u32 v188, 36864, v136\n"
  "v_subrev_u32 v189, 36864, v137\n"
  "v_subrev_u32 v190, 36864, v138\n"
  "v_subrev_u32 v191, 36864, v139\n"
  "v_subrev_u32 v192, 36864, v140\n"
  "v_subrev_u32 v193, 36864, v141\n"
  "v_subrev_u32 v194, 36864, v142\n"
  "v_subrev_u32 v195, 36864, v143\n"
  "v_mov_b32 v35, 1.0\n"
  "v_cmp_gt_u32 s[96:97], 16, v0\n"
  "v_mov_b32 v38, 0\n"
  "v_mov_b32 v39, 0\n"
  "v_cndmask_b32 v34, 0, v35, s[96:97]\n"
  "v_mov_b32 v42, 0\n"
  "v_mov_b32 v43, 0\n"
  "v_lshlrev_b32 v46, 2, v3\n"
  "v_sub_u32 v46, v2, v46\n"
  "s_add_u32 m0, s12, 98304\n"
  "s_mov_b32 s9, 16640\n"
  "buffer_load_dwordx4 v144, s[24:27], s9 offen lds\n"
  "v_cmp_eq_u32 s[96:97], 0, v46\n"
  "v_cmp_eq_u32 s[88:89], 1, v46\n"
  "v_mov_b32 v45, 0x3f80\n"
  "v_cndmask_b32 v40, 0, v45, s[96:97]\n"
  "v_cndmask_b32 v40, v40, v35, s[88:89]\n"
  "v_lshlrev_b32 v46, 2, v3\n"
  "v_sub_u32 v46, v2, v46\n"
  "v_cmp_eq_u32 s[96:97], 2, v46\n"
  "v_cmp_eq_u32 s[88:89], 3, v46\n"
  "v_mov_b32 v45, 0x3f80\n"
  "v_cndmask_b32 v41, 0, v45, s[96:97]\n"
  "v_cndmask_b32 v41, v41, v35, s[88:89]\n"
  "v_lshrrev_b32 v222, 2, v0\n"
  "v_lshlrev_b32 v222, 6, v222\n"
  "v_and_b32 v45, 3, v0\n"
  "v_lshl_add_u32 v222, v45, 3, v222\n"
  "v_and_b32 v45, 16, v0\n"
  "v_lshl_add_u32 v222, v45, 1, v222\n"
  "v_mov_b32 v30, 0x3f803f80\n"
  "v_mov_b32 v31, 0x3f803f80\n"
  "v_mov_b32 v32, 0x3f803f80\n"
  "v_mov_b32 v33, 0x3f803f80\n"
  "v_xor_b32 v223, 32, v222\n"
  "v_add_u32 v224, 49152, v222\n"
  "v_add_u32 v225, 49152, v223\n"
  "s_add_u32 m0, s12, 102400\n"
  "s_mov_b32 s9, 33024\n"
  "buffer_load_dwordx4 v144, s[24:27], s9 offen lds\n"
  "v_lshlrev_b32 v46, 2, v3\n"
  "v_sub_u32 v46, v2, v46\n"
  "v_subrev_u32 v46, 0, v46\n"
  "v_and_b32 v46, 0x80000001, v46\n"
  "v_cmp_eq_u32 s[96:97], 0, v46\n"
  "s_mov_b32 s87, 0x80000000\n"
  "v_cmp_eq_u32 s[90:91], s87, v46\n"
  "v_mov_b32 v45, 0xf149f2ca\n"
  "s_cmp_eq_u32 s66, 0\n"
  "s_cselect_b32 s88, 0xf149f2ca, 0\n"
  "v_cndmask_b32 v208, v45, 0, s[96:97]\n"
  "v_cndmask_b32 v212, v45, 0, s[90:91]\n"
  "v_min_f32 v216, s88, v212\n"
  "v_ashrrev_i32 v46, 31, v46\n"
  "v_and_b32 v220, 0xffff, v46\n"
  "v_lshlrev_b32 v46, 2, v3\n"
  "v_sub_u32 v46, v2, v46\n"
  "v_subrev_u32 v46, 1, v46\n"
  "v_and_b32 v46, 0x80000001, v46\n"
  "v_cmp_eq_u32 s[96:97], 0, v46\n"
  "s_mov_b32 s87, 0x80000000\n"
  "v_cmp_eq_u32 s[90:91], s87, v46\n"
  "v_mov_b32 v45, 0xf149f2ca\n"
  "s_cmp_eq_u32 s66, 0\n"
  "s_cselect_b32 s88, 0xf149f2ca, 0\n"
  "v_cndmask_b32 v209, v45, 0, s[96:97]\n"
  "s_add_u32 m0, s12, 106496\n"
  "s_mov_b32 s9, 49408\n"
  "buffer_load_dwordx4 v144, s[24:27], s9 offen lds\n"
  "v_cndmask_b32 v213, v45, 0, s[90:91]\n"
  "v_min_f32 v217, s88, v213\n"
  "v_ashrrev_i32 v46, 31, v46\n"
  "v_lshl_or_b32 v220, v46, 16, v220\n"
  "v_lshlrev_b32 v46, 2, v3\n"
  "v_sub_u32 v46, v2, v46\n"
  "v_subrev_u32 v46, 2, v46\n"
  "v_and_b32 v46, 0x80000001, v46\n"
  "v_cmp_eq_u32 s[96:97], 0, v46\n"
  "s_mov_b32 s87, 0x80000000\n"
  "v_cmp_eq_u32 s[90:91], s87, v46\n"
  "v_mov_b32 v45, 0xf149f2ca\n"
  "s_cmp_eq_u32 s66, 0\n"
  "s_cselect_b32 s88, 0xf149f2ca, 0\n"
  "v_cndmask_b32 v210, v45, 0, s[96:97]\n"
  "v_cndmask_b32 v214, v45, 0, s[90:91]\n"
  "v_min_f32 v218, s88, v214\n"
  "v_ashrrev_i32 v46, 31, v46\n"
  "v_and_b32 v221, 0xffff, v46\n"
  "v_lshlrev_b32 v46, 2, v3\n"
  "v_sub_u32 v46, v2, v46\n"
  "v_subrev_u32 v46, 3, v46\n"
  "v_and_b32 v46, 0x80000001, v46\n"
  "v_cmp_eq_u32 s[96:97], 0, v46\n"
  "s_mov_b32 s87, 0x80000000\n"
  "v_cmp_eq_u32 s[90:91], s87, v46\n"
  "s_add_u32 m0, s12, 110592\n"
  "s_mov_b32 s9, 65792\n"
  "buffer_load_dwordx4 v144, s[24:27], s9 offen lds\n"
  "v_mov_b32 v45, 0xf149f2ca\n"
  "s_cmp_eq_u32 s66, 0\n"
  "s_cselect_b32 s88, 0xf149f2ca, 0\n"
  "v_cndmask_b32 v211, v45, 0, s[96:97]\n"
  "v_cndmask_b32 v215, v45, 0, s[90:91]\n"
  "v_min_f32 v219, s88, v215\n"
  "v_ashrrev_i32 v46, 31, v46\n"
  "v_lshl_or_b32 v221, v46, 16, v221\n"
  "v_mul_u32_u24 v226, 80, v3\n"
  "s_cmp_eq_u32 s58, 0\n"
  "s_cselect_b32 s87, 0, 1073741824\n"
  "v_lshl_add_u32 v248, v0, 25, s87\n"
  "v_mov_b32 v205, 0xc0135761\n"
  "s_mov_b32 s4, 0x3b000000\n"
  "s_mov_b32 s5, 0x3727c5ac\n"
  "s_mov_b32 s6, 0x3eb8aa3b\n"
  "s_mov_b32 s7, 0xbdd2d3e8\n"
  "s_lshl_b32 s10, s58, 14\n"
  "s_waitcnt lgkmcnt(0)\n"
  "s_add_u32 s56, s56, 1\n"
  "s_addc_u32 s57, s57, 0\n"
  "v_mov_b32 v36, s56\n"
  "v_mov_b32 v37, s57\n"
  "s_lshr_b32 s94, s64, 1\n"
  "s_lshl2_add_u32 s94, s67, s94\n"
  "s_and_b32 s95, s64, 1\n"
  "s_add_u32 m0, s12, 114688\n"
  "s_mov_b32 s9, 82176\n"
  "buffer_load_dwordx4 v144, s[24:27], s9 offen lds\n"
  "s_lshl2_add_u32 s94, s94, s95\n"
  "s_lshl_b32 s94, s94, 7\n"
  "s_add_u32 s87, s94, 1048576\n"
  "v_lshl_add_u32 v26, v3, 18, s87\n"
  "v_lshl_add_u32 v26, v2, 3, v26\n"
  "v_lshrrev_b32 v27, 5, v0\n"
  "v_lshl_add_u32 v26, v27, 30, v26\n"
  "v_mov_b32 v28, -1\n"
  "v_mov_b32 v29, -1\n"
  "s_lshl_b32 s87, s67, 5\n"
  "v_lshl_add_u32 v27, v2, 1, s87\n"
  "v_accvgpr_write_b32 a224, v30\n"
  "v_accvgpr_write_b32 a225, v30\n"
  "v_accvgpr_write_b32 a226, v30\n"
  "v_accvgpr_write_b32 a227, v30\n"
  "v_accvgpr_write_b32 a236, v30\n"
  "v_accvgpr_write_b32 a237, v30\n"
  "v_accvgpr_write_b32 a238, v30\n"
  "v_accvgpr_write_b32 a239, v30\n"
  "v_accvgpr_write_b32 a248, v30\n"
  "v_accvgpr_write_b32 a249, v30\n"
  "v_accvgpr_write_b32 a250, v30\n"
  "v_accvgpr_write_b32 a251, v30\n"
  "v_accvgpr_write_b32 a4, v30\n"
  "v_accvgpr_write_b32 a5, v30\n"
  "v_accvgpr_write_b32 a6, v30\n"
  "s_add_u32 m0, s12, 118784\n"
  "s_mov_b32 s9, 98560\n"
  "buffer_load_dwordx4 v144, s[24:27], s9 offen lds\n"
  "v_accvgpr_write_b32 a7, v30\n"
  "v_accvgpr_write_b32 a16, v30\n"
  "v_accvgpr_write_b32 a17, v30\n"
  "v_accvgpr_write_b32 a18, v30\n"
  "v_accvgpr_write_b32 a19, v30\n"
  "v_accvgpr_write_b32 a28, v30\n"
  "v_accvgpr_write_b32 a29, v30\n"
  "v_accvgpr_write_b32 a30, v30\n"
  "v_accvgpr_write_b32 a31, v30\n"
  "v_accvgpr_write_b32 a40, v30\n"
  "v_accvgpr_write_b32 a41, v30\n"
  "v_accvgpr_write_b32 a42, v30\n"
  "v_accvgpr_write_b32 a43, v30\n"
  "v_accvgpr_write_b32 a52, v30\n"
  "v_accvgpr_write_b32 a53, v30\n"
  "v_accvgpr_write_b32 a54, v30\n"
  "v_accvgpr_write_b32 a55, v30\n"
  "v_accvgpr_write_b32 a64, v30\n"
  "v_accvgpr_write_b32 a65, v30\n"
  "v_accvgpr_write_b32 a66, v30\n"
  "v_accvgpr_write_b32 a67, v30\n"
  "v_accvgpr_write_b32 a76, v30\n"
  "v_accvgpr_write_b32 a77, v30\n"
  "v_accvgpr_write_b32 a78, v30\n"
  "v_accvgpr_write_b32 a79, v30\n"
  "v_accvgpr_write_b32 a88, v30\n"
  "s_add_u32 m0, s12, 122880\n"
  "s_mov_b32 s9, 114944\n"
  "buffer_load_dwordx4 v144, s[24:27], s9 offen lds\n"
  "v_accvgpr_write_b32 a89, v30\n"
  "v_accvgpr_write_b32 a90, v30\n"
  "v_accvgpr_write_b32 a91, v30\n"
  "v_accvgpr_write_b32 a100, v30\n"
  "v_accvgpr_write_b32 a101, v30\n"
  "v_accvgpr_write_b32 a102, v30\n"
  "v_accvgpr_write_b32 a103, v30\n"
  "v_accvgpr_write_b32 a112, v30\n"
  "v_accvgpr_write_b32 a113, v30\n"
  "v_accvgpr_write_b32 a114, v30\n"
  "v_accvgpr_write_b32 a115, v30\n"
  "v_accvgpr_write_b32 a124, v30\n"
  "v_accvgpr_write_b32 a125, v30\n"
  "v_accvgpr_write_b32 a126, v30\n"
  "v_accvgpr_write_b32 a127, v30\n"
  "v_accvgpr_write_b32 a136, v30\n"
  "v_accvgpr_write_b32 a137, v30\n"
  "v_accvgpr_write_b32 a138, v30\n"
  "v_accvgpr_write_b32 a139, v30\n"
  "v_accvgpr_write_b32 a148, v30\n"
  "v_accvgpr_write_b32 a149, v30\n"
  "v_accvgpr_write_b32 a150, v30\n"
  "v_accvgpr_write_b32 a151, v30\n"
  "s_mov_b32 s16, 0xfffefffe\n"
  "s_mov_b32 s17, 0xfffefffe\n"
  "s_cmp_eq_u32 s70, 0\n"
  "s_cbranch_scc1 .Lact_%=\n"
  "s_waitcnt vmcnt(10)\n"
  "s_barrier\n"
  "s_mov_b64 exec, s[16:17]\n"
  "ds_read_b64 a[224:225], v188 offset:0\n"
  "ds_read_b64 a[226:227], v189 offset:0\n"
  "s_mov_b64 exec, -1\n"
  "ds_read_b64 a[228:229], v188 offset:4096\n"
  "ds_read_b64 a[230:231], v189 offset:4096\n"
  "ds_read_b64 a[232:233], v188 offset:8192\n"
  "ds_read_b64 a[234:235], v189 offset:8192\n"
  "s_mov_b64 exec, s[16:17]\n"
  "ds_read_b64 a[236:237], v190 offset:0\n"
  "ds_read_b64 a[238:239], v191 offset:0\n"
  "s_mov_b64 exec, -1\n"
  "ds_read_b64 a[240:241], v190 offset:4096\n"
  "ds_read_b64 a[242:243], v191 offset:4096\n"
  "ds_read_b64 a[244:245], v190 offset:8192\n"
  "s_add_u32 m0, s13, 24576\n"
  "s_mov_b32 s9, 393216\n"
  "buffer_load_dwordx4 v187, s[28:31], s9 offen lds\n"
  "ds_read_b64 a[246:247], v191 offset:8192\n"
  "s_mov_b64 exec, s[16:17]\n"
  "ds_read_b64 a[248:249], v192 offset:0\n"
  "ds_read_b64 a[250:251], v193 offset:0\n"
  "s_mov_b64 exec, -1\n"
  "ds_read_b64 a[252:253], v192 offset:4096\n"
  "s_waitcnt lgkmcnt(13)\n"
  "v_mfma_f32_16x16x32_bf16 v[160:163], a[224:227], a[224:227], 0\n"
  "s_add_u32 m0, s13, 24576\n"
  "s_mov_b32 s9, 393216\n"
  "buffer_load_dwordx4 v187, s[28:31], s9 offen offset:1024 lds\n"
  "s_waitcnt lgkmcnt(11)\n"
  "v_mfma_f32_16x16x32_bf16 v[164:167], a[228:231], a[228:231], 0\n"
  "ds_read_b64 a[254:255], v193 offset:4096\n"
  "s_waitcnt lgkmcnt(10)\n"
  "v_mfma_f32_16x16x32_bf16 v[168:171], a[232:235], a[232:235], 0\n"
  "ds_read_b64 a[0:1], v192 offset:8192\n"
  "s_waitcnt lgkmcnt(9)\n"
  "v_mfma_f32_16x16x32_bf16 v[160:163], a[236:239], a[236:239], v[160:163]\n"
  "s_add_u32 m0, s13, 24576\n"
  "s_mov_b32 s9, 393216\n"
  "buffer_load_dwordx4 v187, s[28:31], s9 offen offset:2048 lds\n"
  "v_mfma_f32_16x16x32_bf16 v[152:155], v[30:33], a[228:231], 0\n"
  "ds_read_b64 a[2:3], v193 offset:8192\n"
  "s_waitcnt lgkmcnt(7)\n"
  "v_mfma_f32_16x16x32_bf16 v[164:167], a[240:243], a[240:243], v[164:167]\n"
  "s_mov_b64 exec, s[16:17]\n"
  "ds_read_b64 a[4:5], v194 offset:0\n"
  "ds_read_b64 a[6:7], v195 offset:0\n"
  "s_mov_b64 exec, -1\n"
  "v_mfma_f32_16x16x32_bf16 v[156:159], v[30:33], a[232:235], 0\n"
  "ds_read_b64 a[8:9], v194 offset:4096\n"
  "s_waitcnt lgkmcnt(6)\n"
  "v_mfma_f32_16x16x32_bf16 v[168:171], a[244:247], a[244:247], v[168:171]\n"
  "ds_read_b64 a[10:11], v195 offset:4096\n"
  "s_waitcnt lgkmcnt(6)\n"
  "v_mfma_f32_16x16x32_bf16 v[164:167], a[252:255], a[252:255], v[164:167]\n"
  "ds_read_b64 a[12:13], v194 offset:8192\n"
  "s_add_u32 m0, s13, 27648\n"
  "s_mov_b32 s9, 442368\n"
  "buffer_load_dwordx4 v187, s[28:31], s9 offen lds\n"
  "v_mfma_f32_16x16x32_bf16 v[152:155], v[30:33], a[240:243], v[152:155]\n"
  "ds_read_b64 a[14:15], v195 offset:8192\n"
  "ds_read_b64_tr_b16 v[112:113], v222 offset:1024\n"
  "v_mfma_f32_16x16x32_bf16 v[156:159], v[30:33], a[244:247], v[156:159]\n"
  "ds_read_b64_tr_b16 v[114:115], v222 offset:4096\n"
  "s_waitcnt lgkmcnt(8)\n"
  "v_mfma_f32_16x16x32_bf16 v[168:171], a[0:3], a[0:3], v[168:171]\n"
  "ds_read_b64_tr_b16 v[116:117], v222 offset:2048\n"
  "ds_read_b64_tr_b16 v[118:119], v222 offset:5120\n"
  "v_mfma_f32_16x16x32_bf16 v[160:163], a[248:251], a[248:251], v[160:163]\n"
  "ds_read_b64_tr_b16 v[120:121], v222 offset:0\n"
  "ds_read_b64_tr_b16 v[122:123], v222 offset:3072\n"
  "v_mfma_f32_16x16x32_bf16 v[152:155], v[30:33], a[252:255], v[152:155]\n"
  "ds_read_b64_tr_b16 v[124:125], v223 offset:1024\n"
  "s_waitcnt lgkmcnt(11)\n"
  "v_mfma_f32_16x16x32_bf16 v[160:163], a[4:7], a[4:7], v[160:163]\n"
  "ds_read_b64_tr_b16 v[126:127], v223 offset:4096\n"
  "s_waitcnt lgkmcnt(10)\n"
  "v_mfma_f32_16x16x32_bf16 v[164:167], a[8:11], a[8:11], v[164:167]\n"
  "s_add_u32 m0, s13, 27648\n"
  "s_mov_b32 s9, 442368\n"
  "buffer_load_dwordx4 v187, s[28:31], s9 offen offset:1024 lds\n"
  "v_mfma_f32_16x16x32_bf16 v[156:159], v[30:33], a[0:3], v[156:159]\n"
  "ds_read_b64_tr_b16 v[128:129], v223 offset:2048\n"
  "s_waitcnt lgkmcnt(7)\n"
  "v_mfma_f32_16x16x32_bf16 v[48:51], v[112:115], a[224:227], 0\n"
  "ds_read_b64_tr_b16 v[130:131], v223 offset:5120\n"
  "v_lshlrev_b32 v206, 2, v2\n"
  "v_mfma_f32_16x16x32_bf16 v[56:59], v[112:115], a[228:231], 0\n"
  "ds_read_b64_tr_b16 v[132:133], v223 offset:0\n"
  "s_lshl2_add_u32 s87, s68, s58\n"
  "v_mfma_f32_16x16x32_bf16 v[64:67], v[112:115], a[232:235], 0\n"
  "ds_read_b64_tr_b16 v[134:135], v223 offset:3072\n"
  "s_bfe_u32 s89, s99, 0x20001\n"
  "s_waitcnt lgkmcnt(8)\n"
  "v_mfma_f32_16x16x32_bf16 v[72:75], a[224:227], v[116:119], 0\n"
  "ds_read_b64_tr_b16 v[112:113], v222 offset:7168\n"
  "s_and_b32 s90, s99, 1\n"
  "v_mfma_f32_16x16x32_bf16 v[80:83], a[228:231], v[116:119], 0\n"
  "s_add_u32 m0, s13, 27648\n"
  "s_mov_b32 s9, 442368\n"
  "buffer_load_dwordx4 v187, s[28:31], s9 offen offset:2048 lds\n"
  "v_mfma_f32_16x16x32_bf16 v[88:91], a[232:235], v[116:119], 0\n"
  "ds_read_b64_tr_b16 v[114:115], v222 offset:10240\n"
  "s_lshr_b32 s88, s87, 5\n"
  "s_waitcnt lgkmcnt(6)\n"
  "v_mfma_f32_16x16x32_bf16 v[96:99], v[120:123], a[228:231], 0\n"
  "ds_read_b64_tr_b16 v[116:117], v222 offset:8192\n"
  "s_mul_i32 s88, s88, 512\n"
  "v_mfma_f32_16x16x32_bf16 v[104:107], v[120:123], a[232:235], 0\n"
  "ds_read_b64_tr_b16 v[118:119], v222 offset:11264\n"
  "s_and_b32 s87, s87, 31\n"
  "v_mfma_f32_16x16x32_bf16 v[4:7], v[120:123], a[224:227], 0\n"
  "s_waitcnt vmcnt(6) lgkmcnt(6)\n"
  "s_barrier\n"
  "v_mfma_f32_16x16x32_bf16 v[52:55], v[124:127], a[224:227], 0\n"
  "s_mov_b64 exec, s[16:17]\n"
  "ds_read_b64 a[16:17], v136 offset:0\n"
  "ds_read_b64 a[18:19], v137 offset:0\n"
  "s_mov_b64 exec, -1\n"
  "v_mfma_f32_16x16x32_bf16 v[60:63], v[124:127], a[228:231], 0\n"
  "s_add_u32 m0, s12, 126976\n"
  "s_mov_b32 s9, 512\n"
  "buffer_load_dwordx4 v145, s[24:27], s9 offen lds\n"
  "v_mfma_f32_16x16x32_bf16 v[68:71], v[124:127], a[232:235], 0\n"
  "ds_read_b64 a[20:21], v136 offset:4096\n"
  "ds_read_b64 a[22:23], v137 offset:4096\n"
  "v_mfma_f32_16x16x32_bf16 v[76:79], a[224:227], v[128:131], 0\n"
  "ds_read_b64 a[24:25], v136 offset:8192\n"
  "ds_read_b64 a[26:27], v137 offset:8192\n"
  "v_mfma_f32_16x16x32_bf16 v[84:87], a[228:231], v[128:131], 0\n"
  "s_add_u32 m0, s12, 131072\n"
  "s_mov_b32 s9, 512\n"
  "buffer_load_dwordx4 v144, s[24:27], s9 offen lds\n"
  "v_mfma_f32_16x16x32_bf16 v[92:95], a[232:235], v[128:131], 0\n"
  "s_mov_b64 exec, s[16:17]\n"
  "ds_read_b64 a[28:29], v138 offset:0\n"
  "ds_read_b64 a[30:31], v139 offset:0\n"
  "s_mov_b64 exec, -1\n"
  "s_waitcnt lgkmcnt(8)\n"
  "v_mfma_f32_16x16x32_bf16 v[100:103], v[132:135], a[228:231], 0\n"
  "ds_read_b64 a[32:33], v138 offset:4096\n"
  "s_waitcnt lgkmcnt(7)\n"
  "v_mfma_f32_16x16x32_bf16 v[160:163], a[16:19], a[16:19], v[160:163]\n"
  "s_add_u32 m0, s12, 135168\n"
  "s_mov_b32 s9, 16896\n"
  "buffer_load_dwordx4 v144, s[24:27], s9 offen lds\n"
  "v_mfma_f32_16x16x32_bf16 v[108:111], v[132:135], a[232:235], 0\n"
  "ds_read_b64 a[34:35], v139 offset:4096\n"
  "s_waitcnt lgkmcnt(6)\n"
  "v_mfma_f32_16x16x32_bf16 v[164:167], a[20:23], a[20:23], v[164:167]\n"
  "ds_read_b64 a[36:37], v138 offset:8192\n"
  "ds_read_b64 a[38:39], v139 offset:8192\n"
  "v_mfma_f32_16x16x32_bf16 v[8:11], v[132:135], a[224:227], 0\n"
  "ds_read_b64 a[44:45], v140 offset:4096\n"
  "s_add_u32 m0, s12, 139264\n"
  "s_mov_b32 s9, 33280\n"
  "buffer_load_dwordx4 v144, s[24:27], s9 offen lds\n"
  "v_mfma_f32_16x16x32_bf16 v[48:51], v[112:115], a[236:239], v[48:51]\n"
  "s_mov_b64 exec, s[16:17]\n"
  "ds_read_b64 a[40:41], v140 offset:0\n"
  "ds_read_b64 a[42:43], v141 offset:0\n"
  "s_mov_b64 exec, -1\n"
  "v_mfma_f32_16x16x32_bf16 v[56:59], v[112:115], a[240:243], v[56:59]\n"
  "ds_read_b64 a[46:47], v141 offset:4096\n"
  "s_waitcnt lgkmcnt(8)\n"
  "v_mfma_f32_16x16x32_bf16 v[160:163], a[28:31], a[28:31], v[160:163]\n"
  "s_add_u32 m0, s12, 143360\n"
  "s_mov_b32 s9, 49664\n"
  "buffer_load_dwordx4 v144, s[24:27], s9 offen lds\n"
  "v_mfma_f32_16x16x32_bf16 v[64:67], v[112:115], a[244:247], v[64:67]\n"
  "ds_read_b64 a[48:49], v140 offset:8192\n"
  "s_waitcnt lgkmcnt(7)\n"
  "v_mfma_f32_16x16x32_bf16 v[164:167], a[32:35], a[32:35], v[164:167]\n"
  "ds_read_b64 a[50:51], v141 offset:8192\n"
  "s_mov_b64 exec, s[16:17]\n"
  "ds_read_b64 a[52:53], v142 offset:0\n"
  "ds_read_b64 a[54:55], v143 offset:0\n"
  "s_mov_b64 exec, -1\n"
  "v_mfma_f32_16x16x32_bf16 v[72:75], a[236:239], v[116:119], v[72:75]\n"
  "s_add_u32 m0, s12, 147456\n"
  "s_mov_b32 s9, 66048\n"
  "buffer_load_dwordx4 v144, s[24:27], s9 offen lds\n"
  "v_mfma_f32_16x16x32_bf16 v[80:83], a[240:243], v[116:119], v[80:83]\n"
  "ds_read_b64 a[56:57], v142 offset:4096\n"
  "s_waitcnt lgkmcnt(6)\n"
  "v_mfma_f32_16x16x32_bf16 v[160:163], a[40:43], a[40:43], v[160:163]\n"
  "ds_read_b64 a[58:59], v143 offset:4096\n"
  "s_waitcnt lgkmcnt(6)\n"
  "v_mfma_f32_16x16x32_bf16 v[164:167], a[44:47], a[44:47], v[164:167]\n"
  "ds_read_b64 a[60:61], v142 offset:8192\n"
  "s_add_u32 m0, s12, 151552\n"
  "s_mov_b32 s9, 82432\n"
  "buffer_load_dwordx4 v144, s[24:27], s9 offen lds\n"
  "v_mfma_f32_16x16x32_bf16 v[88:91], a[244:247], v[116:119], v[88:91]\n"
  "ds_read_b64 a[62:63], v143 offset:8192\n"
  "s_waitcnt lgkmcnt(4)\n"
  "v_mfma_f32_16x16x32_bf16 v[160:163], a[52:55], a[52:55], v[160:163]\n"
  "ds_read_b64_tr_b16 v[120:121], v222 offset:6144\n"
  "ds_read_b64_tr_b16 v[122:123], v222 offset:9216\n"
  "v_mfma_f32_16x16x32_bf16 v[168:171], a[12:15], a[12:15], v[168:171]\n"
  "ds_read_b64_tr_b16 v[124:125], v223 offset:7168\n"
  "ds_read_b64_tr_b16 v[126:127], v223 offset:10240\n"
  "v_mfma_f32_16x16x32_bf16 v[152:155], v[30:33], a[8:11], v[152:155]\n"
  "s_add_u32 m0, s12, 155648\n"
  "s_mov_b32 s9, 98816\n"
  "buffer_load_dwordx4 v144, s[24:27], s9 offen lds\n"
  "v_mfma_f32_16x16x32_bf16 v[156:159], v[30:33], a[12:15], v[156:159]\n"
  "ds_read_b64_tr_b16 v[128:129], v223 offset:8192\n"
  "s_waitcnt lgkmcnt(7)\n"
  "v_mfma_f32_16x16x32_bf16 v[164:167], a[56:59], a[56:59], v[164:167]\n"
  "ds_read_b64_tr_b16 v[130:131], v223 offset:11264\n"
  "ds_read_b64_tr_b16 v[132:133], v223 offset:6144\n"
  "v_mfma_f32_16x16x32_bf16 v[168:171], a[24:27], a[24:27], v[168:171]\n"
  "s_add_u32 m0, s12, 159744\n"
  "s_mov_b32 s9, 115200\n"
  "buffer_load_dwordx4 v144, s[24:27], s9 offen lds\n"
  "v_mfma_f32_16x16x32_bf16 v[152:155], v[30:33], a[20:23], v[152:155]\n"
  "ds_read_b64_tr_b16 v[134:135], v223 offset:9216\n"
  "s_waitcnt lgkmcnt(6)\n"
  "v_mfma_f32_16x16x32_bf16 v[96:99], v[120:123], a[240:243], v[96:99]\n"
  "ds_read_b64_tr_b16 v[112:113], v222 offset:13312\n"
  "s_add_u32 s87, s87, s88\n"
  "v_mfma_f32_16x16x32_bf16 v[104:107], v[120:123], a[244:247], v[104:107]\n"
  "ds_read_b64_tr_b16 v[114:115], v222 offset:16384\n"
  "s_mul_i32 s87, s87, 3584\n"
  "v_mfma_f32_16x16x32_bf16 v[4:7], v[120:123], a[236:239], v[4:7]\n"
  "s_add_u32 m0, s13, 49152\n"
  "s_mov_b32 s9, 786432\n"
  "buffer_load_dwordx4 v187, s[28:31], s9 offen lds\n"
  "s_waitcnt lgkmcnt(6)\n"
  "v_mfma_f32_16x16x32_bf16 v[52:55], v[124:127], a[236:239], v[52:55]\n"
  "ds_read_b64_tr_b16 v[116:117], v222 offset:14336\n"
  "s_mul_i32 s89, s89, 896\n"
  "v_mfma_f32_16x16x32_bf16 v[60:63], v[124:127], a[240:243], v[60:63]\n"
  "ds_read_b64_tr_b16 v[118:119], v222 offset:17408\n"
  "s_add_u32 s87, s87, s89\n"
  "v_mfma_f32_16x16x32_bf16 v[68:71], v[124:127], a[244:247], v[68:71]\n"
  "ds_read_b64_tr_b16 v[120:121], v222 offset:12288\n"
  "v_lshrrev_b32 v45, 3, v0\n"
  "s_waitcnt lgkmcnt(6)\n"
  "v_mfma_f32_16x16x32_bf16 v[76:79], a[236:239], v[128:131], v[76:79]\n"
  "ds_read_b64_tr_b16 v[122:123], v222 offset:15360\n"
  "v_add_u32 v45, 0, v45\n"
  "v_mfma_f32_16x16x32_bf16 v[84:87], a[240:243], v[128:131], v[84:87]\n"
  "ds_read_b64_tr_b16 v[124:125], v223 offset:13312\n"
  "v_mul_u32_u24 v45, 114688, v45\n"
  "v_mfma_f32_16x16x32_bf16 v[92:95], a[244:247], v[128:131], v[92:95]\n"
  "s_add_u32 m0, s13, 49152\n"
  "s_mov_b32 s9, 786432\n"
  "buffer_load_dwordx4 v187, s[28:31], s9 offen offset:1024 lds\n"
  "s_waitcnt vmcnt(16) lgkmcnt(5)\n"
  "v_mfma_f32_16x16x32_bf16 v[100:103], v[132:135], a[240:243], v[100:103]\n"
  "ds_read_b64_tr_b16 v[126:127], v223 offset:16384\n"
  "v_and_b32 v46, 7, v0\n"
  "v_mfma_f32_16x16x32_bf16 v[108:111], v[132:135], a[244:247], v[108:111]\n"
  "ds_read_b64_tr_b16 v[128:129], v223 offset:14336\n"
  "v_min_u32 v46, 6, v46\n"
  "v_mfma_f32_16x16x32_bf16 v[8:11], v[132:135], a[236:239], v[8:11]\n"
  "ds_read_b64_tr_b16 v[130:131], v223 offset:17408\n"
  "v_lshl_add_u32 v45, v46, 7, v45\n"
  "v_mfma_f32_16x16x32_bf16 v[48:51], v[112:115], a[248:251], v[48:51]\n"
  "ds_read_b64_tr_b16 v[132:133], v223 offset:12288\n"
  "v_add_u32 v45, s87, v45\n"
  "v_mfma_f32_16x16x32_bf16 v[56:59], v[112:115], a[252:255], v[56:59]\n"
  "ds_read_b64_tr_b16 v[134:135], v223 offset:15360\n"
  "v_mov_b32 v201, v45\n"
  "v_mfma_f32_16x16x32_bf16 v[64:67], v[112:115], a[0:3], v[64:67]\n"
  "s_add_u32 m0, s13, 49152\n"
  "s_mov_b32 s9, 786432\n"
  "buffer_load_dwordx4 v187, s[28:31], s9 offen offset:2048 lds\n"
  "s_waitcnt vmcnt(16) lgkmcnt(6)\n"
  "v_mfma_f32_16x16x32_bf16 v[72:75], a[248:251], v[116:119], v[72:75]\n"
  "ds_read_b64_tr_b16 v[112:113], v222 offset:19456\n"
  "s_lshl2_add_u32 s87, s68, s58\n"
  "v_mfma_f32_16x16x32_bf16 v[80:83], a[252:255], v[116:119], v[80:83]\n"
  "ds_read_b64_tr_b16 v[114:115], v222 offset:22528\n"
  "s_bfe_u32 s89, s99, 0x20001\n"
  "v_mfma_f32_16x16x32_bf16 v[88:91], a[0:3], v[116:119], v[88:91]\n"
  "ds_read_b64_tr_b16 v[116:117], v222 offset:20480\n"
  "s_and_b32 s90, s99, 1\n"
  "v_mfma_f32_16x16x32_bf16 v[96:99], v[120:123], a[252:255], v[96:99]\n"
  "ds_read_b64_tr_b16 v[118:119], v222 offset:23552\n"
  "s_lshr_b32 s88, s87, 5\n"
  "v_mfma_f32_16x16x32_bf16 v[104:107], v[120:123], a[0:3], v[104:107]\n"
  "s_mul_i32 s88, s88, 512\n"
  "s_and_b32 s87, s87, 31\n"
  "v_mfma_f32_16x16x32_bf16 v[4:7], v[120:123], a[248:251], v[4:7]\n"
  "ds_read_b64_tr_b16 v[120:121], v222 offset:18432\n"
  "s_add_u32 m0, s13, 52224\n"
  "s_mov_b32 s9, 835584\n"
  "buffer_load_dwordx4 v187, s[28:31], s9 offen lds\n"
  "s_waitcnt vmcnt(16) lgkmcnt(5)\n"
  "v_mfma_f32_16x16x32_bf16 v[52:55], v[124:127], a[248:251], v[52:55]\n"
  "ds_read_b64_tr_b16 v[122:123], v222 offset:21504\n"
  "s_add_u32 s87, s87, s88\n"
  "v_mfma_f32_16x16x32_bf16 v[60:63], v[124:127], a[252:255], v[60:63]\n"
  "s_mul_i32 s87, s87, 3584\n"
  "s_mul_i32 s89, s89, 896\n"
  "v_mfma_f32_16x16x32_bf16 v[68:71], v[124:127], a[0:3], v[68:71]\n"
  "ds_read_b64_tr_b16 v[124:125], v223 offset:19456\n"
  "s_add_u32 s87, s87, s89\n"
  "v_mfma_f32_16x16x32_bf16 v[76:79], a[248:251], v[128:131], v[76:79]\n"
  "ds_read_b64_tr_b16 v[126:127], v223 offset:22528\n"
  "v_lshrrev_b32 v45, 3, v0\n"
  "v_mfma_f32_16x16x32_bf16 v[84:87], a[252:255], v[128:131], v[84:87]\n"
  "v_add_u32 v45, 8, v45\n"
  "v_mul_u32_u24 v45, 114688, v45\n"
  "v_mfma_f32_16x16x32_bf16 v[92:95], a[0:3], v[128:131], v[92:95]\n"
  "ds_read_b64_tr_b16 v[128:129], v223 offset:20480\n"
  "s_add_u32 m0, s13, 52224\n"
  "s_mov_b32 s9, 835584\n"
  "buffer_load_dwordx4 v187, s[28:31], s9 offen offset:1024 lds\n"
  "v_mfma_f32_16x16x32_bf16 v[100:103], v[132:135], a[252:255], v[100:103]\n"
  "ds_read_b64_tr_b16 v[130:131], v223 offset:23552\n"
  "v_and_b32 v46, 7, v0\n"
  "v_mfma_f32_16x16x32_bf16 v[108:111], v[132:135], a[0:3], v[108:111]\n"
  "v_min_u32 v46, 6, v46\n"
  "v_lshl_add_u32 v45, v46, 7, v45\n"
  "v_mfma_f32_16x16x32_bf16 v[8:11], v[132:135], a[248:251], v[8:11]\n"
  "ds_read_b64_tr_b16 v[132:133], v223 offset:18432\n"
  "v_add_u32 v45, s87, v45\n"
  "s_waitcnt vmcnt(16) lgkmcnt(5)\n"
  "v_mfma_f32_16x16x32_bf16 v[48:51], v[112:115], a[4:7], v[48:51]\n"
  "ds_read_b64_tr_b16 v[134:135], v223 offset:21504\n"
  "v_mov_b32 v202, v45\n"
  "v_mfma_f32_16x16x32_bf16 v[56:59], v[112:115], a[8:11], v[56:59]\n"
  "s_lshl2_add_u32 s87, s68, s58\n"
  "s_and_b32 s90, s99, 1\n"
  "v_mfma_f32_16x16x32_bf16 v[64:67], v[112:115], a[12:15], v[64:67]\n"
  "s_mul_i32 s87, s87, 4096\n"
  "s_lshl_b32 s90, s90, 9\n"
  "v_mfma_f32_16x16x32_bf16 v[72:75], a[4:7], v[116:119], v[72:75]\n"
  "s_add_u32 m0, s13, 52224\n"
  "s_mov_b32 s9, 835584\n"
  "buffer_load_dwordx4 v187, s[28:31], s9 offen offset:2048 lds\n"
  "v_mfma_f32_16x16x32_bf16 v[80:83], a[8:11], v[116:119], v[80:83]\n"
  "s_add_u32 s87, s87, s90\n"
  "v_mul_u32_u24 v45, 1024, v3\n"
  "v_mfma_f32_16x16x32_bf16 v[88:91], a[12:15], v[116:119], v[88:91]\n"
  "v_and_b32 v46, 3, v0\n"
  "v_lshl_add_u32 v45, v46, 7, v45\n"
  "v_mfma_f32_16x16x32_bf16 v[96:99], v[120:123], a[8:11], v[96:99]\n"
  "v_add_u32 v45, s87, v45\n"
  "v_mov_b32 v200, v45\n"
  "v_mfma_f32_16x16x32_bf16 v[104:107], v[120:123], a[12:15], v[104:107]\n"
  "s_lshl2_add_u32 s87, s99, s58\n"
  "s_mul_i32 s88, s87, 293\n"
  "v_mfma_f32_16x16x32_bf16 v[4:7], v[120:123], a[4:7], v[4:7]\n"
  "s_lshr_b32 s88, s88, 16\n"
  "s_mul_i32 s89, s88, 224\n"
  "s_waitcnt vmcnt(15) lgkmcnt(0)\n"
  "v_mfma_f32_16x16x32_bf16 v[52:55], v[124:127], a[4:7], v[52:55]\n"
  "s_sub_u32 s87, s87, s89\n"
  "s_add_u32 s87, s87, 416\n"
  "v_mfma_f32_16x16x32_bf16 v[60:63], v[124:127], a[8:11], v[60:63]\n"
  "s_sub_u32 s90, s87, 448\n"
  "s_min_u32 s87, s87, s90\n"
  "v_mfma_f32_16x16x32_bf16 v[68:71], v[124:127], a[12:15], v[68:71]\n"
  "s_lshl1_add_u32 s87, s89, s87\n"
  "buffer_store_dwordx2 v[28:29], v26, s[20:23], 0 offen sc1\n"
  "v_mfma_f32_16x16x32_bf16 v[76:79], a[4:7], v[128:131], v[76:79]\n"
  "s_lshl_b32 s87, s87, 12\n"
  "v_min_u32 v45, 31, v0\n"
  "v_mfma_f32_16x16x32_bf16 v[84:87], a[8:11], v[128:131], v[84:87]\n"
  "buffer_load_ushort v47, v27, s[52:55], 0 offen\n"
  "v_lshl_add_u32 v45, v45, 7, s87\n"
  "v_mfma_f32_16x16x32_bf16 v[92:95], a[12:15], v[128:131], v[92:95]\n"
  "v_mov_b32 v203, v45\n"
  "s_lshl2_add_u32 s87, s61, s58\n"
  "v_mfma_f32_16x16x32_bf16 v[100:103], v[132:135], a[8:11], v[100:103]\n"
  "s_lshl_b32 s88, s87, 10\n"
  "v_add_u32 v196, s88, v1\n"
  "v_mfma_f32_16x16x32_bf16 v[108:111], v[132:135], a[12:15], v[108:111]\n"
  "s_mul_i32 s89, s62, 98304\n"
  "s_add_u32 s89, s89, 14680064\n"
  "v_mfma_f32_16x16x32_bf16 v[8:11], v[132:135], a[4:7], v[8:11]\n"
  "v_add_u32 v238, s89, v196\n"
  "v_mfma_f32_16x16x32_bf16 v[156:159], v[30:33], a[24:27], v[156:159]\n"
  "s_waitcnt vmcnt(8)\n"
  "s_barrier\n"
  "v_mfma_f32_16x16x32_bf16 v[168:171], a[36:39], a[36:39], v[168:171]\n"
  "s_add_u32 m0, s13, 73728\n"
  "s_mov_b32 s9, 1179648\n"
  "buffer_load_dwordx4 v187, s[28:31], s9 offen lds\n"
  "v_mfma_f32_16x16x32_bf16 v[152:155], v[30:33], a[32:35], v[152:155]\n"
  "s_mov_b64 exec, s[16:17]\n"
  "ds_read_b64 a[64:65], v136 offset:36864\n"
  "ds_read_b64 a[66:67], v137 offset:36864\n"
  "s_mov_b64 exec, -1\n"
  "v_mfma_f32_16x16x32_bf16 v[156:159], v[30:33], a[36:39], v[156:159]\n"
  "ds_read_b64 a[68:69], v136 offset:40960\n"
  "ds_read_b64 a[70:71], v137 offset:40960\n"
  "v_mfma_f32_16x16x32_bf16 v[168:171], a[48:51], a[48:51], v[168:171]\n"
  "ds_read_b64 a[72:73], v136 offset:45056\n"
  "ds_read_b64 a[74:75], v137 offset:45056\n"
  "v_mfma_f32_16x16x32_bf16 v[152:155], v[30:33], a[44:47], v[152:155]\n"
  "s_mov_b64 exec, s[16:17]\n"
  "ds_read_b64 a[76:77], v138 offset:36864\n"
  "ds_read_b64 a[78:79], v139 offset:36864\n"
  "s_mov_b64 exec, -1\n"
  "v_mfma_f32_16x16x32_bf16 v[156:159], v[30:33], a[48:51], v[156:159]\n"
  "ds_read_b64 a[80:81], v138 offset:40960\n"
  "ds_read_b64 a[82:83], v139 offset:40960\n"
  "v_mfma_f32_16x16x32_bf16 v[168:171], a[60:63], a[60:63], v[168:171]\n"
  "s_add_u32 m0, s13, 73728\n"
  "s_mov_b32 s9, 1179648\n"
  "buffer_load_dwordx4 v187, s[28:31], s9 offen offset:1024 lds\n"
  "v_mfma_f32_16x16x32_bf16 v[152:155], v[30:33], a[56:59], v[152:155]\n"
  "ds_read_b64 a[84:85], v138 offset:45056\n"
  "s_waitcnt lgkmcnt(9)\n"
  "v_mfma_f32_16x16x32_bf16 v[160:163], a[64:67], a[64:67], v[160:163]\n"
  "ds_read_b64 a[86:87], v139 offset:45056\n"
  "s_waitcnt lgkmcnt(8)\n"
  "v_mfma_f32_16x16x32_bf16 v[164:167], a[68:71], a[68:71], v[164:167]\n"
  "s_mov_b64 exec, s[16:17]\n"
  "ds_read_b64 a[88:89], v140 offset:36864\n"
  "ds_read_b64 a[90:91], v141 offset:36864\n"
  "s_mov_b64 exec, -1\n"
  "v_mfma_f32_16x16x32_bf16 v[156:159], v[30:33], a[60:63], v[156:159]\n"
  "ds_read_b64 a[92:93], v140 offset:40960\n"
  "s_waitcnt lgkmcnt(9)\n"
  "v_mfma_f32_16x16x32_bf16 v[168:171], a[72:75], a[72:75], v[168:171]\n"
  "ds_read_b64 a[94:95], v141 offset:40960\n"
  "s_waitcnt lgkmcnt(8)\n"
  "v_mfma_f32_16x16x32_bf16 v[160:163], a[76:79], a[76:79], v[160:163]\n"
  "s_add_u32 m0, s13, 73728\n"
  "s_mov_b32 s9, 1179648\n"
  "buffer_load_dwordx4 v187, s[28:31], s9 offen offset:2048 lds\n"
  "v_mfma_f32_16x16x32_bf16 v[152:155], v[30:33], a[68:71], v[152:155]\n"
  "ds_read_b64 a[96:97], v140 offset:45056\n"
  "s_waitcnt lgkmcnt(7)\n"
  "v_mfma_f32_16x16x32_bf16 v[164:167], a[80:83], a[80:83], v[164:167]\n"
  "ds_read_b64 a[98:99], v141 offset:45056\n"
  "s_waitcnt lgkmcnt(6)\n"
  "v_mfma_f32_16x16x32_bf16 v[168:171], a[84:87], a[84:87], v[168:171]\n"
  "s_mov_b64 exec, s[16:17]\n"
  "ds_read_b64 a[100:101], v142 offset:36864\n"
  "ds_read_b64 a[102:103], v143 offset:36864\n"
  "s_mov_b64 exec, -1\n"
  "v_mfma_f32_16x16x32_bf16 v[156:159], v[30:33], a[72:75], v[156:159]\n"
  "ds_read_b64 a[104:105], v142 offset:40960\n"
  "s_waitcnt lgkmcnt(7)\n"
  "v_mfma_f32_16x16x32_bf16 v[160:163], a[88:91], a[88:91], v[160:163]\n"
  "ds_read_b64 a[106:107], v143 offset:40960\n"
  "s_waitcnt lgkmcnt(6)\n"
  "v_mfma_f32_16x16x32_bf16 v[164:167], a[92:95], a[92:95], v[164:167]\n"
  "s_add_u32 m0, s13, 76800\n"
  "s_mov_b32 s9, 1228800\n"
  "buffer_load_dwordx4 v187, s[28:31], s9 offen lds\n"
  "v_mfma_f32_16x16x32_bf16 v[152:155], v[30:33], a[80:83], v[152:155]\n"
  "ds_read_b64 a[108:109], v142 offset:45056\n"
  "ds_read_b64 a[110:111], v143 offset:45056\n"
  "v_mfma_f32_16x16x32_bf16 v[156:159], v[30:33], a[84:87], v[156:159]\n"
  "s_add_u32 m0, s13, 76800\n"
  "s_mov_b32 s9, 1228800\n"
  "buffer_load_dwordx4 v187, s[28:31], s9 offen offset:1024 lds\n"
  "s_waitcnt lgkmcnt(6)\n"
  "v_mfma_f32_16x16x32_bf16 v[168:171], a[96:99], a[96:99], v[168:171]\n"
  "ds_read_b64_tr_b16 v[112:113], v222 offset:25600\n"
  "s_waitcnt lgkmcnt(5)\n"
  "v_mfma_f32_16x16x32_bf16 v[160:163], a[100:103], a[100:103], v[160:163]\n"
  "ds_read_b64_tr_b16 v[114:115], v222 offset:28672\n"
  "ds_read_b64_tr_b16 v[116:117], v222 offset:26624\n"
  "v_mfma_f32_16x16x32_bf16 v[152:155], v[30:33], a[92:95], v[152:155]\n"
  "ds_read_b64_tr_b16 v[118:119], v222 offset:29696\n"
  "s_waitcnt lgkmcnt(6)\n"
  "v_mfma_f32_16x16x32_bf16 v[164:167], a[104:107], a[104:107], v[164:167]\n"
  "ds_read_b64_tr_b16 v[120:121], v222 offset:24576\n"
  "ds_read_b64_tr_b16 v[122:123], v222 offset:27648\n"
  "v_mfma_f32_16x16x32_bf16 v[156:159], v[30:33], a[96:99], v[156:159]\n"
  "ds_read_b64_tr_b16 v[124:125], v223 offset:25600\n"
  "s_waitcnt lgkmcnt(7)\n"
  "v_mfma_f32_16x16x32_bf16 v[168:171], a[108:111], a[108:111], v[168:171]\n"
  "s_add_u32 m0, s13, 76800\n"
  "s_mov_b32 s9, 1228800\n"
  "buffer_load_dwordx4 v187, s[28:31], s9 offen offset:2048 lds\n"
  "v_mfma_f32_16x16x32_bf16 v[152:155], v[30:33], a[104:107], v[152:155]\n"
  "ds_read_b64_tr_b16 v[126:127], v223 offset:28672\n"
  "ds_read_b64_tr_b16 v[128:129], v223 offset:26624\n"
  "v_mfma_f32_16x16x32_bf16 v[156:159], v[30:33], a[108:111], v[156:159]\n"
  "ds_read_b64_tr_b16 v[130:131], v223 offset:29696\n"
  "s_waitcnt vmcnt(13) lgkmcnt(8)\n"
  "v_mfma_f32_16x16x32_bf16 v[48:51], v[112:115], a[16:19], v[48:51]\n"
  "ds_read_b64_tr_b16 v[132:133], v223 offset:24576\n"
  "v_mfma_f32_16x16x32_bf16 v[56:59], v[112:115], a[20:23], v[56:59]\n"
  "ds_read_b64_tr_b16 v[134:135], v223 offset:27648\n"
  "v_mfma_f32_16x16x32_bf16 v[64:67], v[112:115], a[24:27], v[64:67]\n"
  "ds_read_b64_tr_b16 v[112:113], v222 offset:31744\n"
  "s_waitcnt lgkmcnt(9)\n"
  "v_mfma_f32_16x16x32_bf16 v[72:75], a[16:19], v[116:119], v[72:75]\n"
  "ds_read_b64_tr_b16 v[114:115], v222 offset:34816\n"
  "v_mfma_f32_16x16x32_bf16 v[80:83], a[20:23], v[116:119], v[80:83]\n"
  "s_cmp_lt_u32 s87, 32\n"
  "s_cselect_b32 s88, 65536, 0\n"
  "s_cselect_b32 s89, 65536, 1073741824\n"
  "v_mfma_f32_16x16x32_bf16 v[88:91], a[24:27], v[116:119], v[88:91]\n"
  "ds_read_b64_tr_b16 v[116:117], v222 offset:32768\n"
  "v_add_u32 v198, s88, v196\n"
  "s_waitcnt vmcnt(12) lgkmcnt(8)\n"
  "v_mfma_f32_16x16x32_bf16 v[96:99], v[120:123], a[20:23], v[96:99]\n"
  "s_waitcnt vmcnt(8) lgkmcnt(7)\n"
  "s_barrier\n"
  "v_mfma_f32_16x16x32_bf16 v[104:107], v[120:123], a[24:27], v[104:107]\n"
  "s_add_u32 m0, s12, 126976\n"
  "s_mov_b32 s9, 768\n"
  "buffer_load_dwordx4 v145, s[24:27], s9 offen lds\n"
  "v_mfma_f32_16x16x32_bf16 v[4:7], v[120:123], a[16:19], v[4:7]\n"
  "s_add_u32 m0, s12, 131072\n"
  "s_mov_b32 s9, 768\n"
  "buffer_load_dwordx4 v144, s[24:27], s9 offen lds\n"
  "v_mfma_f32_16x16x32_bf16 v[52:55], v[124:127], a[16:19], v[52:55]\n"
  "ds_read_b64_tr_b16 v[118:119], v222 offset:35840\n"
  "v_add_u32 v239, s89, v238\n"
  "v_mfma_f32_16x16x32_bf16 v[60:63], v[124:127], a[20:23], v[60:63]\n"
  "ds_read_b64_tr_b16 v[120:121], v222 offset:30720\n"
  "s_add_u32 m0, s12, 135168\n"
  "s_mov_b32 s9, 17152\n"
  "buffer_load_dwordx4 v144, s[24:27], s9 offen lds\n"
  "v_mfma_f32_16x16x32_bf16 v[68:71], v[124:127], a[24:27], v[68:71]\n"
  "ds_read_b64_tr_b16 v[122:123], v222 offset:33792\n"
  "s_lshl_b32 s87, s58, 17\n"
  "s_waitcnt lgkmcnt(4)\n"
  "v_mfma_f32_16x16x32_bf16 v[76:79], a[16:19], v[128:131], v[76:79]\n"
  "ds_read_b64_tr_b16 v[124:125], v223 offset:31744\n"
  "s_lshl_b32 s88, s64, 7\n"
  "v_mfma_f32_16x16x32_bf16 v[84:87], a[20:23], v[128:131], v[84:87]\n"
  "ds_read_b64_tr_b16 v[126:127], v223 offset:34816\n"
  "s_add_u32 m0, s12, 139264\n"
  "s_mov_b32 s9, 33536\n"
  "buffer_load_dwordx4 v144, s[24:27], s9 offen lds\n"
  "v_mfma_f32_16x16x32_bf16 v[92:95], a[24:27], v[128:131], v[92:95]\n"
  "ds_read_b64_tr_b16 v[128:129], v223 offset:32768\n"
  "s_add_u32 s87, s87, s88\n"
  "v_mfma_f32_16x16x32_bf16 v[100:103], v[132:135], a[20:23], v[100:103]\n"
  "ds_read_b64_tr_b16 v[130:131], v223 offset:35840\n"
  "v_lshrrev_b32 v184, 3, v0\n"
  "v_mfma_f32_16x16x32_bf16 v[108:111], v[132:135], a[24:27], v[108:111]\n"
  "v_lshl_add_u32 v184, v184, 10, s87\n"
  "v_lshlrev_b32 v45, 1, v3\n"
  "v_mfma_f32_16x16x32_bf16 v[8:11], v[132:135], a[16:19], v[8:11]\n"
  "s_add_u32 m0, s12, 143360\n"
  "s_mov_b32 s9, 49920\n"
  "buffer_load_dwordx4 v144, s[24:27], s9 offen lds\n"
  "v_mfma_f32_16x16x32_bf16 v[48:51], v[112:115], a[28:31], v[48:51]\n"
  "ds_read_b64_tr_b16 v[132:133], v223 offset:30720\n"
  "v_xor_b32 v45, v45, v0\n"
  "v_mfma_f32_16x16x32_bf16 v[56:59], v[112:115], a[32:35], v[56:59]\n"
  "ds_read_b64_tr_b16 v[134:135], v223 offset:33792\n"
  "v_and_b32 v45, 7, v45\n"
  "v_mfma_f32_16x16x32_bf16 v[64:67], v[112:115], a[36:39], v[64:67]\n"
  "ds_read_b64_tr_b16 v[112:113], v222 offset:37888\n"
  "s_add_u32 m0, s12, 147456\n"
  "s_mov_b32 s9, 66304\n"
  "buffer_load_dwordx4 v144, s[24:27], s9 offen lds\n"
  "s_waitcnt lgkmcnt(5)\n"
  "v_mfma_f32_16x16x32_bf16 v[72:75], a[28:31], v[116:119], v[72:75]\n"
  "ds_read_b64_tr_b16 v[114:115], v222 offset:40960\n"
  "v_lshl_add_u32 v184, v45, 4, v184\n"
  "v_mfma_f32_16x16x32_bf16 v[80:83], a[32:35], v[116:119], v[80:83]\n"
  "v_add_u32 v185, 0x10000, v184\n"
  "s_lshl_b32 s88, s61, 9\n"
  "v_mfma_f32_16x16x32_bf16 v[88:91], a[36:39], v[116:119], v[88:91]\n"
  "ds_read_b64_tr_b16 v[116:117], v222 offset:38912\n"
  "s_add_u32 m0, s12, 151552\n"
  "s_mov_b32 s9, 82688\n"
  "buffer_load_dwordx4 v144, s[24:27], s9 offen lds\n"
  "v_mfma_f32_16x16x32_bf16 v[96:99], v[120:123], a[32:35], v[96:99]\n"
  "ds_read_b64_tr_b16 v[118:119], v222 offset:41984\n"
  "s_add_u32 s88, s88, 16384\n"
  "v_mfma_f32_16x16x32_bf16 v[104:107], v[120:123], a[36:39], v[104:107]\n"
  "v_lshl_add_u32 v249, v2, 3, s88\n"
  "s_lshl_b32 s88, s67, 14\n"
  "v_mfma_f32_16x16x32_bf16 v[4:7], v[120:123], a[28:31], v[4:7]\n"
  "ds_read_b64_tr_b16 v[120:121], v222 offset:36864\n"
  "s_add_u32 s88, s88, 2097152\n"
  "v_mfma_f32_16x16x32_bf16 v[52:55], v[124:127], a[28:31], v[52:55]\n"
  "s_add_u32 m0, s12, 155648\n"
  "s_mov_b32 s9, 99072\n"
  "buffer_load_dwordx4 v144, s[24:27], s9 offen lds\n"
  "v_mfma_f32_16x16x32_bf16 v[60:63], v[124:127], a[32:35], v[60:63]\n"
  "ds_read_b64_tr_b16 v[122:123], v222 offset:39936\n"
  "v_add_u32 v250, s88, v1\n"
  "v_mfma_f32_16x16x32_bf16 v[68:71], v[124:127], a[36:39], v[68:71]\n"
  "ds_read_b64_tr_b16 v[124:125], v223 offset:37888\n"
  "s_lshl_b32 s87, s67, 14\n"
  "s_waitcnt lgkmcnt(5)\n"
  "v_mfma_f32_16x16x32_bf16 v[76:79], a[28:31], v[128:131], v[76:79]\n"
  "s_add_u32 m0, s12, 159744\n"
  "s_mov_b32 s9, 115456\n"
  "buffer_load_dwordx4 v144, s[24:27], s9 offen lds\n"
  "v_mfma_f32_16x16x32_bf16 v[84:87], a[32:35], v[128:131], v[84:87]\n"
  "ds_read_b64_tr_b16 v[126:127], v223 offset:40960\n"
  "s_lshl_b32 s88, s64, 7\n"
  "v_mfma_f32_16x16x32_bf16 v[92:95], a[36:39], v[128:131], v[92:95]\n"
  "ds_read_b64_tr_b16 v[128:129], v223 offset:38912\n"
  "s_add_u32 s87, s87, s88\n"
  "v_mfma_f32_16x16x32_bf16 v[100:103], v[132:135], a[32:35], v[100:103]\n"
  "ds_read_b64_tr_b16 v[130:131], v223 offset:41984\n"
  "v_lshl_add_u32 v247, v2, 10, s87\n"
  "v_mfma_f32_16x16x32_bf16 v[108:111], v[132:135], a[36:39], v[108:111]\n"
  "buffer_load_dword v44, v200, s[32:35], 0 offen\n"
  "v_and_b32 v45, 16, v0\n"
  "v_mfma_f32_16x16x32_bf16 v[8:11], v[132:135], a[28:31], v[8:11]\n"
  "ds_read_b64_tr_b16 v[132:133], v223 offset:36864\n"
  "v_lshl_add_u32 v247, v45, 1, v247\n"
  "v_mfma_f32_16x16x32_bf16 v[48:51], v[112:115], a[40:43], v[48:51]\n"
  "ds_read_b64_tr_b16 v[134:135], v223 offset:39936\n"
  "v_lshrrev_b32 v45, 5, v0\n"
  "v_mfma_f32_16x16x32_bf16 v[56:59], v[112:115], a[44:47], v[56:59]\n"
  "v_lshl_add_u32 v247, v45, 4, v247\n"
  "s_lshr_b32 s94, s64, 1\n"
  "v_mfma_f32_16x16x32_bf16 v[64:67], v[112:115], a[48:51], v[64:67]\n"
  "ds_read_b64_tr_b16 v[112:113], v222 offset:44032\n"
  "s_lshl2_add_u32 s94, s67, s94\n"
  "s_waitcnt lgkmcnt(6)\n"
  "v_mfma_f32_16x16x32_bf16 v[72:75], a[40:43], v[116:119], v[72:75]\n"
  "ds_read_b64_tr_b16 v[114:115], v222 offset:47104\n"
  "s_and_b32 s95, s64, 1\n"
  "v_mfma_f32_16x16x32_bf16 v[80:83], a[44:47], v[116:119], v[80:83]\n"
  "s_lshl2_add_u32 s94, s94, s95\n"
  "s_lshl_b32 s94, s94, 7\n"
  "v_mfma_f32_16x16x32_bf16 v[88:91], a[48:51], v[116:119], v[88:91]\n"
  "ds_read_b64_tr_b16 v[116:117], v222 offset:45056\n"
  "s_add_u32 s87, s94, 1048576\n"
  "v_mfma_f32_16x16x32_bf16 v[96:99], v[120:123], a[44:47], v[96:99]\n"
  "ds_read_b64_tr_b16 v[118:119], v222 offset:48128\n"
  "v_lshl_add_u32 v251, v0, 1, s87\n"
  "v_mfma_f32_16x16x32_bf16 v[104:107], v[120:123], a[48:51], v[104:107]\n"
  "v_and_b32 v45, 3, v0\n"
  "v_lshl_add_u32 v251, v45, 28, v251\n"
  "v_mfma_f32_16x16x32_bf16 v[4:7], v[120:123], a[40:43], v[4:7]\n"
  "ds_read_b64_tr_b16 v[120:121], v222 offset:43008\n"
  "s_lshl_b32 s87, s67, 11\n"
  "s_waitcnt lgkmcnt(5)\n"
  "v_mfma_f32_16x16x32_bf16 v[52:55], v[124:127], a[40:43], v[52:55]\n"
  "buffer_load_dword v44, v201, s[36:39], 0 offen\n"
  "v_mfma_f32_16x16x32_bf16 v[60:63], v[124:127], a[44:47], v[60:63]\n"
  "ds_read_b64_tr_b16 v[122:123], v222 offset:46080\n"
  "s_add_u32 s87, s87, 1048576\n"
  "v_mfma_f32_16x16x32_bf16 v[68:71], v[124:127], a[48:51], v[68:71]\n"
  "ds_read_b64_tr_b16 v[124:125], v223 offset:44032\n"
  "v_lshl_add_u32 v252, v3, 9, s87\n"
  "v_mfma_f32_16x16x32_bf16 v[76:79], a[40:43], v[128:131], v[76:79]\n"
  "ds_read_b64_tr_b16 v[126:127], v223 offset:47104\n"
  "v_lshl_add_u32 v252, v2, 3, v252\n"
  "v_mfma_f32_16x16x32_bf16 v[84:87], a[44:47], v[128:131], v[84:87]\n"
  "s_lshl_b32 s87, s64, 5\n"
  "s_add_u32 s89, s87, s63\n"
  "v_mfma_f32_16x16x32_bf16 v[92:95], a[48:51], v[128:131], v[92:95]\n"
  "ds_read_b64_tr_b16 v[128:129], v223 offset:45056\n"
  "s_lshr_b32 s94, s89, 3\n"
  "v_mfma_f32_16x16x32_bf16 v[100:103], v[132:135], a[44:47], v[100:103]\n"
  "ds_read_b64_tr_b16 v[130:131], v223 offset:48128\n"
  "s_and_b32 s95, s89, 7\n"
  "v_mfma_f32_16x16x32_bf16 v[108:111], v[132:135], a[48:51], v[108:111]\n"
  "s_lshl_b32 s94, s94, 9\n"
  "s_lshl3_add_u32 s94, s95, s94\n"
  "v_mfma_f32_16x16x32_bf16 v[8:11], v[132:135], a[40:43], v[8:11]\n"
  "ds_read_b64_tr_b16 v[132:133], v223 offset:43008\n"
  "s_add_u32 s12, s94, 32768\n"
  "s_waitcnt lgkmcnt(6)\n"
  "v_mfma_f32_16x16x32_bf16 v[48:51], v[112:115], a[52:55], v[48:51]\n"
  "ds_read_b64_tr_b16 v[134:135], v223 offset:46080\n"
  "s_add_u32 s87, s87, s89\n"
  "v_mfma_f32_16x16x32_bf16 v[56:59], v[112:115], a[56:59], v[56:59]\n"
  "s_lshl1_add_u32 s87, s87, s59\n"
  "s_lshl2_add_u32 s87, s87, s60\n"
  "v_mfma_f32_16x16x32_bf16 v[64:67], v[112:115], a[60:63], v[64:67]\n"
  "ds_read_b64_tr_b16 v[112:113], v224 offset:1024\n"
  "s_lshl_b32 s87, s87, 9\n"
  "v_mfma_f32_16x16x32_bf16 v[72:75], a[52:55], v[116:119], v[72:75]\n"
  "buffer_load_dword v44, v202, s[36:39], 0 offen\n"
  "ds_read_b64_tr_b16 v[114:115], v224 offset:4096\n"
  "v_mfma_f32_16x16x32_bf16 v[80:83], a[56:59], v[116:119], v[80:83]\n"
  "s_mov_b32 s52, 131072\n"
  "s_add_u32 s87, s87, 6291456\n"
  "v_mfma_f32_16x16x32_bf16 v[88:91], a[60:63], v[116:119], v[88:91]\n"
  "ds_read_b64_tr_b16 v[116:117], v224 offset:2048\n"
  "v_lshl_add_u32 v245, v2, 4, s87\n"
  "s_waitcnt lgkmcnt(5)\n"
  "v_mfma_f32_16x16x32_bf16 v[96:99], v[120:123], a[56:59], v[96:99]\n"
  "ds_read_b64_tr_b16 v[118:119], v224 offset:5120\n"
  "v_and_b32 v45, 16, v0\n"
  "v_mfma_f32_16x16x32_bf16 v[104:107], v[120:123], a[60:63], v[104:107]\n"
  "v_lshl_add_u32 v245, v45, 6, v245\n"
  "v_and_b32 v45, 32, v0\n"
  "v_mfma_f32_16x16x32_bf16 v[4:7], v[120:123], a[52:55], v[4:7]\n"
  "ds_read_b64_tr_b16 v[120:121], v224 offset:0\n"
  "v_lshl_add_u32 v245, v45, 3, v245\n"
  "v_mfma_f32_16x16x32_bf16 v[52:55], v[124:127], a[52:55], v[52:55]\n"
  "ds_read_b64_tr_b16 v[122:123], v224 offset:3072\n"
  "s_lshl2_add_u32 s88, s71, s58\n"
  "v_mfma_f32_16x16x32_bf16 v[60:63], v[124:127], a[56:59], v[60:63]\n"
  "s_lshl_b32 s89, s88, 9\n"
  "s_add_u32 s89, s89, 32768\n"
  "v_mfma_f32_16x16x32_bf16 v[68:71], v[124:127], a[60:63], v[68:71]\n"
  "ds_read_b64_tr_b16 v[124:125], v225 offset:1024\n"
  "v_and_b32 v246, 7, v0\n"
  "v_mfma_f32_16x16x32_bf16 v[76:79], a[52:55], v[128:131], v[76:79]\n"
  "ds_read_b64_tr_b16 v[126:127], v225 offset:4096\n"
  "v_lshl_add_u32 v246, v246, 3, s89\n"
  "v_mfma_f32_16x16x32_bf16 v[84:87], a[56:59], v[128:131], v[84:87]\n"
  "s_mul_i32 s87, s58, 1835008\n"
  "s_lshl_b32 s88, s63, 7\n"
  "v_mfma_f32_16x16x32_bf16 v[92:95], a[60:63], v[128:131], v[92:95]\n"
  "ds_read_b64_tr_b16 v[128:129], v225 offset:2048\n"
  "s_add_u32 s87, s87, s88\n"
  "s_waitcnt lgkmcnt(5)\n"
  "v_mfma_f32_16x16x32_bf16 v[100:103], v[132:135], a[56:59], v[100:103]\n"
  "buffer_load_dword v44, v203, s[40:43], 0 offen\n"
  "ds_read_b64_tr_b16 v[130:131], v225 offset:5120\n"
  "v_mfma_f32_16x16x32_bf16 v[108:111], v[132:135], a[60:63], v[108:111]\n"
  "v_lshrrev_b32 v240, 3, v0\n"
  "v_lshl_add_u32 v240, v240, 12, s87\n"
  "v_mfma_f32_16x16x32_bf16 v[8:11], v[132:135], a[52:55], v[8:11]\n"
  "ds_read_b64_tr_b16 v[132:133], v225 offset:0\n"
  "v_lshlrev_b32 v45, 1, v3\n"
  "v_mfma_f32_16x16x32_bf16 v[48:51], v[112:115], a[64:67], v[48:51]\n"
  "ds_read_b64_tr_b16 v[134:135], v225 offset:3072\n"
  "v_xor_b32 v45, v45, v0\n"
  "v_mfma_f32_16x16x32_bf16 v[56:59], v[112:115], a[68:71], v[56:59]\n"
  "v_and_b32 v45, 7, v45\n"
  "v_lshl_add_u32 v240, v45, 4, v240\n"
  "v_mfma_f32_16x16x32_bf16 v[64:67], v[112:115], a[72:75], v[64:67]\n"
  "ds_read_b64_tr_b16 v[112:113], v224 offset:7168\n"
  "v_lshrrev_b32 v45, 3, v0\n"
  "v_mfma_f32_16x16x32_bf16 v[72:75], a[64:67], v[116:119], v[72:75]\n"
  "ds_read_b64_tr_b16 v[114:115], v224 offset:10240\n"
  "v_xor_b32 v45, s58, v45\n"
  "v_mfma_f32_16x16x32_bf16 v[80:83], a[68:71], v[116:119], v[80:83]\n"
  "v_and_b32 v45, 3, v45\n"
  "v_lshl_add_u32 v45, v45, 5, s8\n"
  "v_mfma_f32_16x16x32_bf16 v[88:91], a[72:75], v[116:119], v[88:91]\n"
  "ds_read_b64_tr_b16 v[116:117], v224 offset:8192\n"
  "v_lshrrev_b32 v46, 2, v0\n"
  "s_waitcnt lgkmcnt(6)\n"
  "v_mfma_f32_16x16x32_bf16 v[96:99], v[120:123], a[68:71], v[96:99]\n"
  "ds_read_b64_tr_b16 v[118:119], v224 offset:11264\n"
  "v_lshl_add_u32 v45, v46, 7, v45\n"
  "v_mfma_f32_16x16x32_bf16 v[104:107], v[120:123], a[72:75], v[104:107]\n"
  "v_and_b32 v46, 3, v0\n"
  "v_lshl_add_u32 v234, v46, 3, v45\n"
  "v_mfma_f32_16x16x32_bf16 v[4:7], v[120:123], a[64:67], v[4:7]\n"
  "ds_read_b64_tr_b16 v[120:121], v224 offset:6144\n"
  "v_xor_b32 v235, 32, v234\n"
  "v_mfma_f32_16x16x32_bf16 v[52:55], v[124:127], a[64:67], v[52:55]\n"
  "ds_read_b64_tr_b16 v[122:123], v224 offset:9216\n"
  "v_xor_b32 v236, 64, v234\n"
  "v_mfma_f32_16x16x32_bf16 v[60:63], v[124:127], a[68:71], v[60:63]\n"
  "s_waitcnt vmcnt(4) lgkmcnt(6)\n"
  "s_barrier\n"
  "v_mfma_f32_16x16x32_bf16 v[68:71], v[124:127], a[72:75], v[68:71]\n"
  "s_mov_b64 exec, s[16:17]\n"
  "ds_read_b64 a[112:113], v136 offset:36864\n"
  "ds_read_b64 a[114:115], v137 offset:36864\n"
  "s_mov_b64 exec, -1\n"
  "v_mfma_f32_16x16x32_bf16 v[76:79], a[64:67], v[128:131], v[76:79]\n"
  "ds_read_b64 a[116:117], v136 offset:40960\n"
  "ds_read_b64 a[118:119], v137 offset:40960\n"
  "v_mfma_f32_16x16x32_bf16 v[84:87], a[68:71], v[128:131], v[84:87]\n"
  "ds_read_b64 a[120:121], v136 offset:45056\n"
  "ds_read_b64 a[122:123], v137 offset:45056\n"
  "v_mfma_f32_16x16x32_bf16 v[92:95], a[72:75], v[128:131], v[92:95]\n"
  "s_mov_b64 exec, s[16:17]\n"
  "ds_read_b64 a[124:125], v138 offset:36864\n"
  "ds_read_b64 a[126:127], v139 offset:36864\n"
  "s_mov_b64 exec, -1\n"
  "v_mfma_f32_16x16x32_bf16 v[100:103], v[132:135], a[68:71], v[100:103]\n"
  "ds_read_b64 a[128:129], v138 offset:40960\n"
  "ds_read_b64 a[130:131], v139 offset:40960\n"
  "v_mfma_f32_16x16x32_bf16 v[108:111], v[132:135], a[72:75], v[108:111]\n"
  "ds_read_b64 a[132:133], v138 offset:45056\n"
  "s_waitcnt lgkmcnt(9)\n"
  "v_mfma_f32_16x16x32_bf16 v[160:163], a[112:115], a[112:115], v[160:163]\n"
  "ds_read_b64 a[134:135], v139 offset:45056\n"
  "s_mov_b64 exec, s[16:17]\n"
  "ds_read_b64 a[136:137], v140 offset:36864\n"
  "ds_read_b64 a[138:139], v141 offset:36864\n"
  "s_mov_b64 exec, -1\n"
  "s_waitcnt lgkmcnt(10)\n"
  "v_mfma_f32_16x16x32_bf16 v[164:167], a[116:119], a[116:119], v[164:167]\n"
  "ds_read_b64 a[140:141], v140 offset:40960\n"
  "s_waitcnt lgkmcnt(9)\n"
  "v_mfma_f32_16x16x32_bf16 v[168:171], a[120:123], a[120:123], v[168:171]\n"
  "ds_read_b64 a[142:143], v141 offset:40960\n"
  "s_waitcnt lgkmcnt(8)\n"
  "v_mfma_f32_16x16x32_bf16 v[160:163], a[124:127], a[124:127], v[160:163]\n"
  "ds_read_b64 a[144:145], v140 offset:45056\n"
  "ds_read_b64 a[146:147], v141 offset:45056\n"
  "v_mfma_f32_16x16x32_bf16 v[152:155], v[30:33], a[116:119], v[152:155]\n"
  "s_mov_b64 exec, s[16:17]\n"
  "ds_read_b64 a[148:149], v142 offset:36864\n"
  "ds_read_b64 a[150:151], v143 offset:36864\n"
  "s_mov_b64 exec, -1\n"
  "v_mfma_f32_16x16x32_bf16 v[156:159], v[30:33], a[120:123], v[156:159]\n"
  "ds_read_b64 a[152:153], v142 offset:40960\n"
  "s_waitcnt lgkmcnt(11)\n"
  "v_mfma_f32_16x16x32_bf16 v[164:167], a[128:131], a[128:131], v[164:167]\n"
  "ds_read_b64 a[154:155], v143 offset:40960\n"
  "s_waitcnt lgkmcnt(10)\n"
  "v_mfma_f32_16x16x32_bf16 v[168:171], a[132:135], a[132:135], v[168:171]\n"
  "ds_read_b64 a[156:157], v142 offset:45056\n"
  "s_waitcnt lgkmcnt(9)\n"
  "v_mfma_f32_16x16x32_bf16 v[160:163], a[136:139], a[136:139], v[160:163]\n"
  "ds_read_b64 a[158:159], v143 offset:45056\n"
  "s_waitcnt lgkmcnt(8)\n"
  "v_mfma_f32_16x16x32_bf16 v[164:167], a[140:143], a[140:143], v[164:167]\n"
  "ds_read_b64_tr_b16 v[124:125], v225 offset:7168\n"
  "s_waitcnt lgkmcnt(7)\n"
  "v_mfma_f32_16x16x32_bf16 v[168:171], a[144:147], a[144:147], v[168:171]\n"
  "ds_read_b64_tr_b16 v[126:127], v225 offset:10240\n"
  "s_waitcnt lgkmcnt(6)\n"
  "v_mfma_f32_16x16x32_bf16 v[160:163], a[148:151], a[148:151], v[160:163]\n"
  "ds_read_b64_tr_b16 v[128:129], v225 offset:8192\n"
  "v_xor_b32 v237, 0x60, v234\n"
  "v_mfma_f32_16x16x32_bf16 v[152:155], v[30:33], a[128:131], v[152:155]\n"
  "ds_read_b64_tr_b16 v[130:131], v225 offset:11264\n"
  "v_lshlrev_b32 v45, 2, v3\n"
  "v_mfma_f32_16x16x32_bf16 v[156:159], v[30:33], a[132:135], v[156:159]\n"
  "v_sub_u32 v45, v2, v45\n"
  "s_waitcnt lgkmcnt(6)\n"
  "v_mfma_f32_16x16x32_bf16 v[164:167], a[152:155], a[152:155], v[164:167]\n"
  "s_waitcnt lgkmcnt(4)\n"
  "s_barrier\n"
  "v_mfma_f32_16x16x32_bf16 v[152:155], v[30:33], a[140:143], v[152:155]\n"
  "v_cmp_eq_u32 s[88:89], 0, v45\n"
  "v_cmp_eq_u32 s[90:91], 1, v45\n"
  "v_mfma_f32_16x16x32_bf16 v[156:159], v[30:33], a[144:147], v[156:159]\n"
  "v_cmp_eq_u32 s[92:93], 2, v45\n"
  "v_cmp_eq_u32 s[94:95], 3, v45\n"
  "v_mfma_f32_16x16x32_bf16 v[168:171], a[156:159], a[156:159], v[168:171]\n"
  "v_cndmask_b32 v137, 0, v160, s[88:89]\n"
  "v_cndmask_b32 v137, v137, v161, s[90:91]\n"
  "v_mfma_f32_16x16x32_bf16 v[152:155], v[30:33], a[152:155], v[152:155]\n"
  "v_cndmask_b32 v137, v137, v162, s[92:93]\n"
  "v_cndmask_b32 v137, v137, v163, s[94:95]\n"
  "v_mfma_f32_16x16x32_bf16 v[156:159], v[30:33], a[156:159], v[156:159]\n"
  "v_mul_f32 v136, v34, v160\n"
  "v_lshlrev_b32 v45, 2, v3\n"
  "v_mfma_f32_16x16x32_bf16 v[8:11], v[132:135], a[64:67], v[8:11]\n"
  "ds_read_b64_tr_b16 v[132:133], v225 offset:6144\n"
  "v_sub_u32 v45, v2, v45\n"
  "v_mfma_f32_16x16x32_bf16 v[48:51], v[112:115], a[76:79], v[48:51]\n"
  "ds_read_b64_tr_b16 v[134:135], v225 offset:9216\n"
  "v_cmp_eq_u32 s[88:89], 0, v45\n"
  "v_mfma_f32_16x16x32_bf16 v[56:59], v[112:115], a[80:83], v[56:59]\n"
  "v_cmp_eq_u32 s[90:91], 1, v45\n"
  "v_cmp_eq_u32 s[92:93], 2, v45\n"
  "v_mfma_f32_16x16x32_bf16 v[64:67], v[112:115], a[84:87], v[64:67]\n"
  "ds_read_b64_tr_b16 v[112:113], v224 offset:13312\n"
  "v_cmp_eq_u32 s[94:95], 3, v45\n"
  "v_mfma_f32_16x16x32_bf16 v[72:75], a[76:79], v[116:119], v[72:75]\n"
  "ds_read_b64_tr_b16 v[114:115], v224 offset:16384\n"
  "v_cndmask_b32 v141, 0, v164, s[88:89]\n"
  "v_mfma_f32_16x16x32_bf16 v[80:83], a[80:83], v[116:119], v[80:83]\n"
  "v_cndmask_b32 v141, v141, v165, s[90:91]\n"
  "v_cndmask_b32 v141, v141, v166, s[92:93]\n"
  "v_mfma_f32_16x16x32_bf16 v[88:91], a[84:87], v[116:119], v[88:91]\n"
  "ds_read_b64_tr_b16 v[116:117], v224 offset:14336\n"
  "v_cndmask_b32 v141, v141, v167, s[94:95]\n"
  "v_mfma_f32_16x16x32_bf16 v[96:99], v[120:123], a[80:83], v[96:99]\n"
  "ds_read_b64_tr_b16 v[118:119], v224 offset:17408\n"
  "v_mul_f32 v140, v34, v152\n"
  "v_mfma_f32_16x16x32_bf16 v[104:107], v[120:123], a[84:87], v[104:107]\n"
  "v_lshlrev_b32 v45, 2, v3\n"
  "v_sub_u32 v45, v2, v45\n"
  "v_mfma_f32_16x16x32_bf16 v[4:7], v[120:123], a[76:79], v[4:7]\n"
  "ds_read_b64_tr_b16 v[120:121], v224 offset:12288\n"
  "v_cmp_eq_u32 s[88:89], 0, v45\n"
  "s_waitcnt lgkmcnt(5)\n"
  "v_mfma_f32_16x16x32_bf16 v[52:55], v[124:127], a[76:79], v[52:55]\n"
  "ds_read_b64_tr_b16 v[122:123], v224 offset:15360\n"
  "v_cmp_eq_u32 s[90:91], 1, v45\n"
  "v_mfma_f32_16x16x32_bf16 v[60:63], v[124:127], a[80:83], v[60:63]\n"
  "v_cmp_eq_u32 s[92:93], 2, v45\n"
  "v_cmp_eq_u32 s[94:95], 3, v45\n"
  "v_mfma_f32_16x16x32_bf16 v[68:71], v[124:127], a[84:87], v[68:71]\n"
  "ds_read_b64_tr_b16 v[124:125], v225 offset:13312\n"
  "v_cndmask_b32 v145, 0, v168, s[88:89]\n"
  "v_mfma_f32_16x16x32_bf16 v[76:79], a[76:79], v[128:131], v[76:79]\n"
  "ds_read_b64_tr_b16 v[126:127], v225 offset:16384\n"
  "v_cndmask_b32 v145, v145, v169, s[90:91]\n"
  "v_mfma_f32_16x16x32_bf16 v[84:87], a[80:83], v[128:131], v[84:87]\n"
  "v_cndmask_b32 v145, v145, v170, s[92:93]\n"
  "v_cndmask_b32 v145, v145, v171, s[94:95]\n"
  "v_mfma_f32_16x16x32_bf16 v[92:95], a[84:87], v[128:131], v[92:95]\n"
  "ds_read_b64_tr_b16 v[128:129], v225 offset:14336\n"
  "v_mul_f32 v144, v34, v156\n"
  "v_mfma_f32_16x16x32_bf16 v[100:103], v[132:135], a[80:83], v[100:103]\n"
  "ds_read_b64_tr_b16 v[130:131], v225 offset:17408\n"
  "s_lshl4_add_u32 s87, s66, s62\n"
  "v_mfma_f32_16x16x32_bf16 v[108:111], v[132:135], a[84:87], v[108:111]\n"
  "v_permlane16_swap_b32 v136, v137\n"
  "v_mfma_f32_16x16x32_bf16 v[8:11], v[132:135], a[76:79], v[8:11]\n"
  "ds_read_b64_tr_b16 v[132:133], v225 offset:12288\n"
  "v_add_f32 v136, v136, v137\n"
  "s_waitcnt lgkmcnt(5)\n"
  "v_mfma_f32_16x16x32_bf16 v[48:51], v[112:115], a[88:91], v[48:51]\n"
  "ds_read_b64_tr_b16 v[134:135], v225 offset:15360\n"
  "v_mov_b32 v137, v136\n"
  "v_mfma_f32_16x16x32_bf16 v[56:59], v[112:115], a[92:95], v[56:59]\n"
  "v_permlane16_swap_b32 v140, v141\n"
  "v_mfma_f32_16x16x32_bf16 v[64:67], v[112:115], a[96:99], v[64:67]\n"
  "ds_read_b64_tr_b16 v[112:113], v224 offset:19456\n"
  "v_add_f32 v140, v140, v141\n"
  "v_mfma_f32_16x16x32_bf16 v[72:75], a[88:91], v[116:119], v[72:75]\n"
  "ds_read_b64_tr_b16 v[114:115], v224 offset:22528\n"
  "v_mov_b32 v141, v140\n"
  "v_mfma_f32_16x16x32_bf16 v[80:83], a[92:95], v[116:119], v[80:83]\n"
  "v_permlane16_swap_b32 v144, v145\n"
  "v_mfma_f32_16x16x32_bf16 v[88:91], a[96:99], v[116:119], v[88:91]\n"
  "ds_read_b64_tr_b16 v[116:117], v224 offset:20480\n"
  "v_add_f32 v144, v144, v145\n"
  "v_mfma_f32_16x16x32_bf16 v[96:99], v[120:123], a[92:95], v[96:99]\n"
  "ds_read_b64_tr_b16 v[118:119], v224 offset:23552\n"
  "v_mov_b32 v145, v144\n"
  "v_mfma_f32_16x16x32_bf16 v[104:107], v[120:123], a[96:99], v[104:107]\n"
  "v_permlane32_swap_b32 v136, v137\n"
  "v_mfma_f32_16x16x32_bf16 v[4:7], v[120:123], a[88:91], v[4:7]\n"
  "ds_read_b64_tr_b16 v[120:121], v224 offset:18432\n"
  "v_add_f32 v136, v136, v137\n"
  "s_waitcnt lgkmcnt(5)\n"
  "v_mfma_f32_16x16x32_bf16 v[52:55], v[124:127], a[88:91], v[52:55]\n"
  "ds_read_b64_tr_b16 v[122:123], v224 offset:21504\n"
  "v_mov_b32 v137, v136\n"
  "v_mfma_f32_16x16x32_bf16 v[60:63], v[124:127], a[92:95], v[60:63]\n"
  "v_permlane32_swap_b32 v140, v141\n"
  "v_mfma_f32_16x16x32_bf16 v[68:71], v[124:127], a[96:99], v[68:71]\n"
  "ds_read_b64_tr_b16 v[124:125], v225 offset:19456\n"
  "v_add_f32 v140, v140, v141\n"
  "v_mfma_f32_16x16x32_bf16 v[76:79], a[88:91], v[128:131], v[76:79]\n"
  "ds_read_b64_tr_b16 v[126:127], v225 offset:22528\n"
  "v_mov_b32 v141, v140\n"
  "v_mfma_f32_16x16x32_bf16 v[84:87], a[92:95], v[128:131], v[84:87]\n"
  "v_permlane32_swap_b32 v144, v145\n"
  "v_mfma_f32_16x16x32_bf16 v[92:95], a[96:99], v[128:131], v[92:95]\n"
  "ds_read_b64_tr_b16 v[128:129], v225 offset:20480\n"
  "v_add_f32 v144, v144, v145\n"
  "v_mfma_f32_16x16x32_bf16 v[100:103], v[132:135], a[92:95], v[100:103]\n"
  "ds_read_b64_tr_b16 v[130:131], v225 offset:23552\n"
  "v_mov_b32 v145, v144\n"
  "v_mfma_f32_16x16x32_bf16 v[108:111], v[132:135], a[96:99], v[108:111]\n"
  "v_permlane16_swap_b32 v136, v137\n"
  "v_mfma_f32_16x16x32_bf16 v[8:11], v[132:135], a[88:91], v[8:11]\n"
  "ds_read_b64_tr_b16 v[132:133], v225 offset:18432\n"
  "v_mul_f32 v138, s4, v136\n"
  "s_waitcnt lgkmcnt(5)\n"
  "v_mfma_f32_16x16x32_bf16 v[48:51], v[112:115], a[100:103], v[48:51]\n"
  "ds_read_b64_tr_b16 v[134:135], v225 offset:21504\n"
  "v_mul_f32 v139, v138, v138\n"
  "v_mfma_f32_16x16x32_bf16 v[56:59], v[112:115], a[104:107], v[56:59]\n"
  "v_permlane16_swap_b32 v140, v141\n"
  "v_mfma_f32_16x16x32_bf16 v[64:67], v[112:115], a[108:111], v[64:67]\n"
  "ds_read_b64_tr_b16 v[112:113], v224 offset:25600\n"
  "v_mul_f32 v142, s4, v140\n"
  "v_mfma_f32_16x16x32_bf16 v[72:75], a[100:103], v[116:119], v[72:75]\n"
  "ds_read_b64_tr_b16 v[114:115], v224 offset:28672\n"
  "v_mul_f32 v143, v142, v142\n"
  "v_mfma_f32_16x16x32_bf16 v[80:83], a[104:107], v[116:119], v[80:83]\n"
  "v_permlane16_swap_b32 v144, v145\n"
  "v_mfma_f32_16x16x32_bf16 v[88:91], a[108:111], v[116:119], v[88:91]\n"
  "ds_read_b64_tr_b16 v[116:117], v224 offset:26624\n"
  "v_mul_f32 v146, s4, v144\n"
  "v_mfma_f32_16x16x32_bf16 v[96:99], v[120:123], a[104:107], v[96:99]\n"
  "ds_read_b64_tr_b16 v[118:119], v224 offset:29696\n"
  "v_mul_f32 v147, v146, v146\n"
  "v_mfma_f32_16x16x32_bf16 v[104:107], v[120:123], a[108:111], v[104:107]\n"
  "v_fma_f32 v137, v137, s4, -v139\n"
  "v_fma_f32 v141, v141, s4, -v143\n"
  "v_mfma_f32_16x16x32_bf16 v[4:7], v[120:123], a[100:103], v[4:7]\n"
  "ds_read_b64_tr_b16 v[120:121], v224 offset:24576\n"
  "v_fma_f32 v145, v145, s4, -v147\n"
  "s_waitcnt lgkmcnt(5)\n"
  "v_mfma_f32_16x16x32_bf16 v[52:55], v[124:127], a[100:103], v[52:55]\n"
  "ds_read_b64_tr_b16 v[122:123], v224 offset:27648\n"
  "v_max_f32 v137, 0, v137\n"
  "v_mfma_f32_16x16x32_bf16 v[60:63], v[124:127], a[104:107], v[60:63]\n"
  "v_max_f32 v141, 0, v141\n"
  "v_max_f32 v145, 0, v145\n"
  "v_mfma_f32_16x16x32_bf16 v[68:71], v[124:127], a[108:111], v[68:71]\n"
  "ds_read_b64_tr_b16 v[124:125], v225 offset:25600\n"
  "v_add_f32 v137, s5, v137\n"
  "v_mfma_f32_16x16x32_bf16 v[76:79], a[100:103], v[128:131], v[76:79]\n"
  "ds_read_b64_tr_b16 v[126:127], v225 offset:28672\n"
  "v_add_f32 v141, s5, v141\n"
  "v_mfma_f32_16x16x32_bf16 v[84:87], a[104:107], v[128:131], v[84:87]\n"
  "v_add_f32 v145, s5, v145\n"
  "ds_bpermute_b32 v148, v226, v138 offset:0\n"
  "v_mfma_f32_16x16x32_bf16 v[92:95], a[108:111], v[128:131], v[92:95]\n"
  "ds_read_b64_tr_b16 v[128:129], v225 offset:26624\n"
  "s_lshl_b32 s87, s87, 10\n"
  "v_mfma_f32_16x16x32_bf16 v[100:103], v[132:135], a[104:107], v[100:103]\n"
  "ds_read_b64_tr_b16 v[130:131], v225 offset:29696\n"
  "s_add_u32 s15, s87, 2097152\n"
  "v_mfma_f32_16x16x32_bf16 v[108:111], v[132:135], a[108:111], v[108:111]\n"
  "v_rsq_f32 v139, v137\n"
  "v_mfma_f32_16x16x32_bf16 v[8:11], v[132:135], a[100:103], v[8:11]\n"
  "ds_read_b64_tr_b16 v[132:133], v225 offset:24576\n"
  "s_add_u32 s16, s15, 16384\n"
  "s_waitcnt lgkmcnt(6)\n"
  "v_mfma_f32_16x16x32_bf16 v[48:51], v[112:115], a[112:115], v[48:51]\n"
  "ds_read_b64_tr_b16 v[134:135], v225 offset:27648\n"
  "s_lshl4_add_u32 s87, s61, s62\n"
  "v_mfma_f32_16x16x32_bf16 v[56:59], v[112:115], a[116:119], v[56:59]\n"
  "v_rsq_f32 v143, v141\n"
  "v_mfma_f32_16x16x32_bf16 v[64:67], v[112:115], a[120:123], v[64:67]\n"
  "ds_read_b64_tr_b16 v[112:113], v224 offset:31744\n"
  "v_mul_f32 v140, s6, v143\n"
  "v_mfma_f32_16x16x32_bf16 v[72:75], a[112:115], v[116:119], v[72:75]\n"
  "ds_read_b64_tr_b16 v[114:115], v224 offset:34816\n"
  "s_lshr_b32 s94, s87, 4\n"
  "v_mfma_f32_16x16x32_bf16 v[80:83], a[116:119], v[116:119], v[80:83]\n"
  "v_rsq_f32 v147, v145\n"
  "v_mfma_f32_16x16x32_bf16 v[88:91], a[120:123], v[116:119], v[88:91]\n"
  "ds_read_b64_tr_b16 v[116:117], v224 offset:32768\n"
  "v_mul_f32 v144, s6, v147\n"
  "v_mfma_f32_16x16x32_bf16 v[96:99], v[120:123], a[116:119], v[96:99]\n"
  "ds_read_b64_tr_b16 v[118:119], v224 offset:35840\n"
  "s_and_b32 s95, s87, 15\n"
  "v_mfma_f32_16x16x32_bf16 v[104:107], v[120:123], a[120:123], v[104:107]\n"
  "ds_bpermute_b32 v149, v226, v138 offset:4\n"
  "s_lshl_b32 s94, s94, 9\n"
  "v_mfma_f32_16x16x32_bf16 v[4:7], v[120:123], a[112:115], v[4:7]\n"
  "ds_read_b64_tr_b16 v[120:121], v224 offset:30720\n"
  "s_lshl3_add_u32 s94, s95, s94\n"
  "s_waitcnt lgkmcnt(6)\n"
  "v_mfma_f32_16x16x32_bf16 v[52:55], v[124:127], a[112:115], v[52:55]\n"
  "ds_read_b64_tr_b16 v[122:123], v224 offset:33792\n"
  "s_add_u32 s11, s94, 16384\n"
  "v_mfma_f32_16x16x32_bf16 v[60:63], v[124:127], a[116:119], v[60:63]\n"
  "ds_bpermute_b32 v150, v226, v138 offset:8\n"
  "v_lshlrev_b32 v47, 16, v47\n"
  "v_mfma_f32_16x16x32_bf16 v[68:71], v[124:127], a[120:123], v[68:71]\n"
  "ds_read_b64_tr_b16 v[124:125], v225 offset:31744\n"
  "s_xor_b32 s87, s58, 1\n"
  "v_mfma_f32_16x16x32_bf16 v[76:79], a[112:115], v[128:131], v[76:79]\n"
  "ds_read_b64_tr_b16 v[126:127], v225 offset:34816\n"
  "s_mul_i32 s87, s87, 6144\n"
  "v_mfma_f32_16x16x32_bf16 v[84:87], a[116:119], v[128:131], v[84:87]\n"
  "ds_bpermute_b32 v151, v226, v138 offset:12\n"
  "s_add_u32 s87, s87, 98304\n"
  "v_mfma_f32_16x16x32_bf16 v[92:95], a[120:123], v[128:131], v[92:95]\n"
  "ds_read_b64_tr_b16 v[128:129], v225 offset:32768\n"
  "v_add_u32 v242, s87, v1\n"
  "v_mfma_f32_16x16x32_bf16 v[100:103], v[132:135], a[116:119], v[100:103]\n"
  "ds_read_b64_tr_b16 v[130:131], v225 offset:35840\n"
  "s_xor_b32 s87, s58, 2\n"
  "v_mfma_f32_16x16x32_bf16 v[108:111], v[132:135], a[120:123], v[108:111]\n"
  "ds_bpermute_b32 v156, v226, v142 offset:0\n"
  "s_mul_i32 s87, s87, 6144\n"
  "v_mfma_f32_16x16x32_bf16 v[8:11], v[132:135], a[112:115], v[8:11]\n"
  "ds_read_b64_tr_b16 v[132:133], v225 offset:30720\n"
  "s_add_u32 s87, s87, 100352\n"
  "s_waitcnt lgkmcnt(8)\n"
  "v_mfma_f32_16x16x32_bf16 v[48:51], v[112:115], a[124:127], v[48:51]\n"
  "ds_read_b64_tr_b16 v[134:135], v225 offset:33792\n"
  "v_add_u32 v243, s87, v1\n"
  "v_mfma_f32_16x16x32_bf16 v[56:59], v[112:115], a[128:131], v[56:59]\n"
  "ds_bpermute_b32 v157, v226, v142 offset:4\n"
  "s_xor_b32 s87, s58, 3\n"
  "v_mfma_f32_16x16x32_bf16 v[64:67], v[112:115], a[132:135], v[64:67]\n"
  "ds_read_b64_tr_b16 v[112:113], v224 offset:37888\n"
  "s_mul_i32 s87, s87, 6144\n"
  "v_mfma_f32_16x16x32_bf16 v[72:75], a[124:127], v[116:119], v[72:75]\n"
  "ds_read_b64_tr_b16 v[114:115], v224 offset:40960\n"
  "s_add_u32 s87, s87, 102400\n"
  "v_mfma_f32_16x16x32_bf16 v[80:83], a[128:131], v[116:119], v[80:83]\n"
  "ds_bpermute_b32 v158, v226, v142 offset:8\n"
  "v_add_u32 v244, s87, v1\n"
  "v_mfma_f32_16x16x32_bf16 v[88:91], a[132:135], v[116:119], v[88:91]\n"
  "ds_read_b64_tr_b16 v[116:117], v224 offset:38912\n"
  "s_xor_b32 s87, s58, 0\n"
  "v_mfma_f32_16x16x32_bf16 v[96:99], v[120:123], a[128:131], v[96:99]\n"
  "ds_read_b64_tr_b16 v[118:119], v224 offset:41984\n"
  "s_mul_i32 s87, s87, 6144\n"
  "v_mfma_f32_16x16x32_bf16 v[104:107], v[120:123], a[132:135], v[104:107]\n"
  "ds_bpermute_b32 v159, v226, v142 offset:12\n"
  "s_add_u32 s87, s87, 98304\n"
  "v_mfma_f32_16x16x32_bf16 v[4:7], v[120:123], a[124:127], v[4:7]\n"
  "ds_read_b64_tr_b16 v[120:121], v224 offset:36864\n"
  "v_add_u32 v241, s87, v1\n"
  "s_waitcnt lgkmcnt(8)\n"
  "v_mfma_f32_16x16x32_bf16 v[52:55], v[124:127], a[124:127], v[52:55]\n"
  "ds_read_b64_tr_b16 v[122:123], v224 offset:39936\n"
  "s_lshl1_add_u32 s87, s61, s59\n"
  "v_mfma_f32_16x16x32_bf16 v[60:63], v[124:127], a[128:131], v[60:63]\n"
  "ds_bpermute_b32 v152, v226, v139 offset:0\n"
  "s_add_u32 s88, s87, s60\n"
  "v_mfma_f32_16x16x32_bf16 v[68:71], v[124:127], a[132:135], v[68:71]\n"
  "ds_read_b64_tr_b16 v[124:125], v225 offset:37888\n"
  "s_sub_u32 s88, s88, 1\n"
  "v_mfma_f32_16x16x32_bf16 v[76:79], a[124:127], v[128:131], v[76:79]\n"
  "ds_read_b64_tr_b16 v[126:127], v225 offset:40960\n"
  "s_max_i32 s88, s88, 0\n"
  "v_mfma_f32_16x16x32_bf16 v[84:87], a[128:131], v[128:131], v[84:87]\n"
  "ds_bpermute_b32 v153, v226, v139 offset:4\n"
  "s_sub_u32 s88, s88, s87\n"
  "v_mfma_f32_16x16x32_bf16 v[92:95], a[132:135], v[128:131], v[92:95]\n"
  "ds_read_b64_tr_b16 v[128:129], v225 offset:38912\n"
  "s_lshl_b32 s88, s88, 6\n"
  "v_mfma_f32_16x16x32_bf16 v[100:103], v[132:135], a[128:131], v[100:103]\n"
  "ds_read_b64_tr_b16 v[130:131], v225 offset:41984\n"
  "s_lshl_b32 s87, s87, 9\n"
  "v_mfma_f32_16x16x32_bf16 v[108:111], v[132:135], a[132:135], v[108:111]\n"
  "ds_bpermute_b32 v154, v226, v139 offset:8\n"
  "s_add_u32 s87, s87, 65536\n"
  "v_mfma_f32_16x16x32_bf16 v[8:11], v[132:135], a[124:127], v[8:11]\n"
  "ds_read_b64_tr_b16 v[132:133], v225 offset:36864\n"
  "v_and_b32 v204, 8, v0\n"
  "s_waitcnt lgkmcnt(8)\n"
  "v_mfma_f32_16x16x32_bf16 v[48:51], v[112:115], a[136:139], v[48:51]\n"
  "ds_read_b64_tr_b16 v[134:135], v225 offset:39936\n"
  "v_mul_lo_u32 v204, v204, s88\n"
  "v_mfma_f32_16x16x32_bf16 v[56:59], v[112:115], a[140:143], v[56:59]\n"
  "ds_bpermute_b32 v155, v226, v139 offset:12\n"
  "v_and_b32 v45, 7, v0\n"
  "v_mfma_f32_16x16x32_bf16 v[64:67], v[112:115], a[144:147], v[64:67]\n"
  "ds_read_b64_tr_b16 v[112:113], v224 offset:44032\n"
  "v_lshl_add_u32 v204, v45, 3, v204\n"
  "v_mfma_f32_16x16x32_bf16 v[72:75], a[136:139], v[116:119], v[72:75]\n"
  "ds_read_b64_tr_b16 v[114:115], v224 offset:47104\n"
  "v_add_u32 v204, s87, v204\n"
  "v_mfma_f32_16x16x32_bf16 v[80:83], a[140:143], v[116:119], v[80:83]\n"
  "ds_bpermute_b32 v160, v226, v143 offset:0\n"
  "s_lshl_b32 s87, s66, 14\n"
  "v_mfma_f32_16x16x32_bf16 v[88:91], a[144:147], v[116:119], v[88:91]\n"
  "ds_read_b64_tr_b16 v[116:117], v224 offset:45056\n"
  "s_add_u32 s87, s87, 16777216\n"
  "v_mfma_f32_16x16x32_bf16 v[96:99], v[120:123], a[140:143], v[96:99]\n"
  "ds_read_b64_tr_b16 v[118:119], v224 offset:48128\n"
  "v_add_u32 v228, s87, v1\n"
  "v_mfma_f32_16x16x32_bf16 v[104:107], v[120:123], a[144:147], v[104:107]\n"
  "ds_bpermute_b32 v161, v226, v143 offset:4\n"
  "v_add_u32 v229, 0x4000, v228\n"
  "v_mfma_f32_16x16x32_bf16 v[4:7], v[120:123], a[136:139], v[4:7]\n"
  "ds_read_b64_tr_b16 v[120:121], v224 offset:43008\n"
  "s_waitcnt lgkmcnt(8)\n"
  "v_mfma_f32_16x16x32_bf16 v[52:55], v[124:127], a[136:139], v[52:55]\n"
  "ds_read_b64_tr_b16 v[122:123], v224 offset:46080\n"
  "v_mfma_f32_16x16x32_bf16 v[60:63], v[124:127], a[140:143], v[60:63]\n"
  "ds_bpermute_b32 v162, v226, v143 offset:8\n"
  "v_mfma_f32_16x16x32_bf16 v[68:71], v[124:127], a[144:147], v[68:71]\n"
  "ds_read_b64_tr_b16 v[124:125], v225 offset:44032\n"
  "v_mfma_f32_16x16x32_bf16 v[76:79], a[136:139], v[128:131], v[76:79]\n"
  "ds_read_b64_tr_b16 v[126:127], v225 offset:47104\n"
  "v_mfma_f32_16x16x32_bf16 v[84:87], a[140:143], v[128:131], v[84:87]\n"
  "ds_bpermute_b32 v163, v226, v143 offset:12\n"
  "v_mfma_f32_16x16x32_bf16 v[92:95], a[144:147], v[128:131], v[92:95]\n"
  "ds_read_b64_tr_b16 v[128:129], v225 offset:45056\n"
  "v_mfma_f32_16x16x32_bf16 v[100:103], v[132:135], a[140:143], v[100:103]\n"
  "ds_read_b64_tr_b16 v[130:131], v225 offset:48128\n"
  "v_mfma_f32_16x16x32_bf16 v[108:111], v[132:135], a[144:147], v[108:111]\n"
  "ds_bpermute_b32 v164, v226, v146 offset:0\n"
  "v_mfma_f32_16x16x32_bf16 v[8:11], v[132:135], a[136:139], v[8:11]\n"
  "ds_read_b64_tr_b16 v[132:133], v225 offset:43008\n"
  "s_waitcnt lgkmcnt(9)\n"
  "v_mfma_f32_16x16x32_bf16 v[48:51], v[112:115], a[148:151], v[48:51]\n"
  "ds_read_b64_tr_b16 v[134:135], v225 offset:46080\n"
  "v_mfma_f32_16x16x32_bf16 v[56:59], v[112:115], a[152:155], v[56:59]\n"
  "ds_bpermute_b32 v165, v226, v146 offset:4\n"
  "v_mfma_f32_16x16x32_bf16 v[64:67], v[112:115], a[156:159], v[64:67]\n"
  "s_nop 3\n"
  "v_mov_b32_dpp v12, v48 row_newbcast:0 row_mask:0xf bank_mask:0xf\n"
  "v_mov_b32_dpp v13, v49 row_newbcast:0 row_mask:0xf bank_mask:0xf\n"
  "v_mfma_f32_16x16x32_bf16 v[72:75], a[148:151], v[116:119], v[72:75]\n"
  "v_mov_b32_dpp v14, v50 row_newbcast:0 row_mask:0xf bank_mask:0xf\n"
  "v_mov_b32_dpp v15, v51 row_newbcast:0 row_mask:0xf bank_mask:0xf\n"
  "v_mfma_f32_16x16x32_bf16 v[80:83], a[152:155], v[116:119], v[80:83]\n"
  "ds_bpermute_b32 v166, v226, v146 offset:8\n"
  "v_mfma_f32_16x16x32_bf16 v[88:91], a[156:159], v[116:119], v[88:91]\n"
  "s_nop 2\n"
  "ds_bpermute_b32 v20, v206, v72\n"
  "s_waitcnt lgkmcnt(6)\n"
  "v_mfma_f32_16x16x32_bf16 v[96:99], v[120:123], a[152:155], v[96:99]\n"
  "ds_bpermute_b32 v167, v226, v146 offset:12\n"
  "v_mfma_f32_16x16x32_bf16 v[104:107], v[120:123], a[156:159], v[104:107]\n"
  "ds_bpermute_b32 v168, v226, v147 offset:0\n"
  "v_mfma_f32_16x16x32_bf16 v[4:7], v[120:123], a[148:151], v[4:7]\n"
  "ds_bpermute_b32 v169, v226, v147 offset:4\n"
  "v_mfma_f32_16x16x32_bf16 v[52:55], v[124:127], a[148:151], v[52:55]\n"
  "ds_bpermute_b32 v170, v226, v147 offset:8\n"
  "v_mfma_f32_16x16x32_bf16 v[60:63], v[124:127], a[152:155], v[60:63]\n"
  "s_nop 3\n"
  "v_mov_b32_dpp v4, v4 row_newbcast:0 row_mask:0xf bank_mask:0xf\n"
  "v_mov_b32_dpp v5, v5 row_newbcast:0 row_mask:0xf bank_mask:0xf\n"
  "v_mfma_f32_16x16x32_bf16 v[68:71], v[124:127], a[156:159], v[68:71]\n"
  "v_mov_b32_dpp v16, v52 row_newbcast:0 row_mask:0xf bank_mask:0xf\n"
  "v_mov_b32_dpp v17, v53 row_newbcast:0 row_mask:0xf bank_mask:0xf\n"
  "v_mfma_f32_16x16x32_bf16 v[76:79], a[148:151], v[128:131], v[76:79]\n"
  "v_mov_b32_dpp v18, v54 row_newbcast:0 row_mask:0xf bank_mask:0xf\n"
  "v_mov_b32_dpp v19, v55 row_newbcast:0 row_mask:0xf bank_mask:0xf\n"
  "v_mfma_f32_16x16x32_bf16 v[84:87], a[152:155], v[128:131], v[84:87]\n"
  "v_mov_b32_dpp v6, v6 row_newbcast:0 row_mask:0xf bank_mask:0xf\n"
  "v_mov_b32_dpp v7, v7 row_newbcast:0 row_mask:0xf bank_mask:0xf\n"
  "v_mfma_f32_16x16x32_bf16 v[92:95], a[156:159], v[128:131], v[92:95]\n"
  "s_nop 1\n"
  "ds_bpermute_b32 v21, v206, v76\n"
  "s_waitcnt lgkmcnt(5)\n"
  "v_mfma_f32_16x16x32_bf16 v[100:103], v[132:135], a[152:155], v[100:103]\n"
  "ds_bpermute_b32 v171, v226, v147 offset:12\n"
  "v_mfma_f32_16x16x32_bf16 v[108:111], v[132:135], a[156:159], v[108:111]\n"
  "ds_read_b128 v[22:25], v196\n"
  "v_mfma_f32_16x16x32_bf16 v[8:11], v[132:135], a[148:151], v[8:11]\n"
  "ds_read_b128 v[26:29], v198\n"
  "v_pk_fma_f32 v[48:49], v[12:13], v[138:139], v[48:49] op_sel_hi:[1,0,1] neg_lo:[0,1,0] neg_hi:[0,1,0]\n"
  "v_pk_fma_f32 v[52:53], v[16:17], v[138:139], v[52:53] op_sel_hi:[1,0,1] neg_lo:[0,1,0] neg_hi:[0,1,0]\n"
  "v_pk_fma_f32 v[56:57], v[12:13], v[142:143], v[56:57] op_sel_hi:[1,0,1] neg_lo:[0,1,0] neg_hi:[0,1,0]\n"
  "v_pk_fma_f32 v[60:61], v[16:17], v[142:143], v[60:61] op_sel_hi:[1,0,1] neg_lo:[0,1,0] neg_hi:[0,1,0]\n"
  "v_pk_fma_f32 v[64:65], v[12:13], v[146:147], v[64:65] op_sel_hi:[1,0,1] neg_lo:[0,1,0] neg_hi:[0,1,0]\n"
  "v_pk_fma_f32 v[68:69], v[16:17], v[146:147], v[68:69] op_sel_hi:[1,0,1] neg_lo:[0,1,0] neg_hi:[0,1,0]\n"
  "s_nop 0\n"
  "v_mov_b32_dpp v8, v8 row_newbcast:0 row_mask:0xf bank_mask:0xf\n"
  "v_mov_b32_dpp v9, v9 row_newbcast:0 row_mask:0xf bank_mask:0xf\n"
  "v_mov_b32_dpp v10, v10 row_newbcast:0 row_mask:0xf bank_mask:0xf\n"
  "v_mov_b32_dpp v11, v11 row_newbcast:0 row_mask:0xf bank_mask:0xf\n"
  "v_pk_mul_f32 v[48:49], v[48:49], v[138:139] op_sel:[0,1]\n"
  "v_pk_fma_f32 v[50:51], v[14:15], v[138:139], v[50:51] op_sel_hi:[1,0,1] neg_lo:[0,1,0] neg_hi:[0,1,0]\n"
  "v_pk_mul_f32 v[52:53], v[52:53], v[138:139] op_sel:[0,1]\n"
  "v_pk_fma_f32 v[54:55], v[18:19], v[138:139], v[54:55] op_sel_hi:[1,0,1] neg_lo:[0,1,0] neg_hi:[0,1,0]\n"
  "v_pk_mul_f32 v[56:57], v[56:57], v[142:143] op_sel:[0,1]\n"
  "v_pk_fma_f32 v[58:59], v[14:15], v[142:143], v[58:59] op_sel_hi:[1,0,1] neg_lo:[0,1,0] neg_hi:[0,1,0]\n"
  "v_pk_fma_f32 v[96:97], v[4:5], v[142:143], v[96:97] op_sel_hi:[1,0,1] neg_lo:[0,1,0] neg_hi:[0,1,0]\n"
  "v_pk_mul_f32 v[60:61], v[60:61], v[142:143] op_sel:[0,1]\n"
  "v_pk_fma_f32 v[62:63], v[18:19], v[142:143], v[62:63] op_sel_hi:[1,0,1] neg_lo:[0,1,0] neg_hi:[0,1,0]\n"
  "v_pk_fma_f32 v[100:101], v[8:9], v[142:143], v[100:101] op_sel_hi:[1,0,1] neg_lo:[0,1,0] neg_hi:[0,1,0]\n"
  "s_waitcnt lgkmcnt(0)\n"
  "s_barrier\n"
  "v_pk_mul_f32 v[64:65], v[64:65], v[146:147] op_sel:[0,1]\n"
  "v_pk_fma_f32 v[66:67], v[14:15], v[146:147], v[66:67] op_sel_hi:[1,0,1] neg_lo:[0,1,0] neg_hi:[0,1,0]\n"
  "v_pk_fma_f32 v[104:105], v[4:5], v[146:147], v[104:105] op_sel_hi:[1,0,1] neg_lo:[0,1,0] neg_hi:[0,1,0]\n"
  "v_pk_mul_f32 v[68:69], v[68:69], v[146:147] op_sel:[0,1]\n"
  "v_pk_fma_f32 v[70:71], v[18:19], v[146:147], v[70:71] op_sel_hi:[1,0,1] neg_lo:[0,1,0] neg_hi:[0,1,0]\n"
  "v_pk_fma_f32 v[108:109], v[8:9], v[146:147], v[108:109] op_sel_hi:[1,0,1] neg_lo:[0,1,0] neg_hi:[0,1,0]\n"
  "v_pk_mul_f32 v[50:51], v[50:51], v[138:139] op_sel:[0,1]\n"
  "v_cvt_pk_bf16_f32 v48, v48, v49\n"
  "v_pk_mul_f32 v[54:55], v[54:55], v[138:139] op_sel:[0,1]\n"
  "v_cvt_pk_bf16_f32 v52, v52, v53\n"
  "v_pk_mul_f32 v[58:59], v[58:59], v[142:143] op_sel:[0,1]\n"
  "v_cvt_pk_bf16_f32 v56, v56, v57\n"
  "v_pk_fma_f32 v[98:99], v[6:7], v[142:143], v[98:99] op_sel_hi:[1,0,1] neg_lo:[0,1,0] neg_hi:[0,1,0]\n"
  "v_cvt_pk_bf16_f32 v96, v96, v97\n"
  "v_pk_mul_f32 v[62:63], v[62:63], v[142:143] op_sel:[0,1]\n"
  "v_cvt_pk_bf16_f32 v60, v60, v61\n"
  "v_pk_fma_f32 v[102:103], v[10:11], v[142:143], v[102:103] op_sel_hi:[1,0,1] neg_lo:[0,1,0] neg_hi:[0,1,0]\n"
  "v_cvt_pk_bf16_f32 v100, v100, v101\n"
  "v_pk_mul_f32 v[66:67], v[66:67], v[146:147] op_sel:[0,1]\n"
  "v_cvt_pk_bf16_f32 v64, v64, v65\n"
  "v_pk_fma_f32 v[106:107], v[6:7], v[146:147], v[106:107] op_sel_hi:[1,0,1] neg_lo:[0,1,0] neg_hi:[0,1,0]\n"
  "v_cvt_pk_bf16_f32 v104, v104, v105\n"
  "v_pk_mul_f32 v[70:71], v[70:71], v[146:147] op_sel:[0,1]\n"
  "v_cvt_pk_bf16_f32 v68, v68, v69\n"
  "v_pk_fma_f32 v[110:111], v[10:11], v[146:147], v[110:111] op_sel_hi:[1,0,1] neg_lo:[0,1,0] neg_hi:[0,1,0]\n"
  "v_cvt_pk_bf16_f32 v108, v108, v109\n"
  "v_cvt_pk_bf16_f32 v49, v50, v51\n"
  "v_cvt_pk_bf16_f32 v53, v54, v55\n"
  "v_cvt_pk_bf16_f32 v57, v58, v59\n"
  "v_cvt_pk_bf16_f32 v97, v98, v99\n"
  "v_cvt_pk_bf16_f32 v61, v62, v63\n"
  "v_cvt_pk_bf16_f32 v101, v102, v103\n"
  "v_mfma_f32_16x16x16_bf16 v[112:115], v[48:49], v[96:97], v[216:219]\n"
  "v_cvt_pk_bf16_f32 v65, v66, v67\n"
  "v_cvt_pk_bf16_f32 v105, v106, v107\n"
  "v_mfma_f32_16x16x16_bf16 v[116:119], v[56:57], v[96:97], v[208:211]\n"
  "v_cvt_pk_bf16_f32 v69, v70, v71\n"
  "v_cvt_pk_bf16_f32 v109, v110, v111\n"
  "v_mfma_f32_16x16x16_bf16 v[120:123], v[52:53], v[100:101], v[216:219]\n"
  "s_cmp_eq_u32 s66, 0\n"
  "s_cselect_b32 s89, 1073741824, 0\n"
  "v_mfma_f32_16x16x16_bf16 v[124:127], v[60:61], v[100:101], v[208:211]\n"
  "s_nop 1\n"
  "v_max_f32 v112, v112, v116\n"
  "v_max_f32 v113, v113, v117\n"
  "v_mfma_f32_16x16x16_bf16 v[128:131], v[56:57], v[104:105], v[212:215]\n"
  "v_max_f32 v114, v114, v118\n"
  "v_max_f32 v115, v115, v119\n"
  "v_mfma_f32_16x16x16_bf16 v[132:135], v[64:65], v[104:105], v[208:211]\n"
  "v_max_f32 v120, v120, v124\n"
  "v_max_f32 v121, v121, v125\n"
  "v_mfma_f32_16x16x16_bf16 v[188:191], v[60:61], v[108:109], v[212:215]\n"
  "v_max_f32 v122, v122, v126\n"
  "v_max3_f32 v50, v112, v113, v114\n"
  "v_mfma_f32_16x16x16_bf16 v[192:195], v[68:69], v[108:109], v[208:211]\n"
  "s_nop 1\n"
  "v_max_f32 v128, v128, v132\n"
  "v_max_f32 v129, v129, v133\n"
  "v_max_f32 v130, v130, v134\n"
  "v_max_f32 v123, v123, v127\n"
  "v_max3_f32 v54, v120, v121, v122\n"
  "v_max_f32 v131, v131, v135\n"
  "v_max3_f32 v66, v128, v129, v130\n"
  "v_max_f32 v50, v50, v115\n"
  "v_max_f32 v188, v188, v192\n"
  "v_max_f32 v189, v189, v193\n"
  "v_max_f32 v190, v190, v194\n"
  "v_max_f32 v191, v191, v195\n"
  "v_max3_f32 v70, v188, v189, v190\n"
  "v_max_f32 v54, v54, v123\n"
  "v_max_f32 v66, v66, v131\n"
  "v_max_f32 v70, v70, v191\n"
  "v_mov_b32 v51, v50\n"
  "v_mov_b32 v55, v54\n"
  "v_mov_b32 v67, v66\n"
  "v_mov_b32 v71, v70\n"
  "v_permlane16_swap_b32 v50, v51\n"
  "v_permlane16_swap_b32 v54, v55\n"
  "v_permlane16_swap_b32 v66, v67\n"
  "v_permlane16_swap_b32 v70, v71\n"
  "v_max_f32 v50, v50, v51\n"
  "v_max_f32 v54, v54, v55\n"
  "v_max_f32 v66, v66, v67\n"
  "v_max_f32 v70, v70, v71\n"
  "v_mov_b32 v51, v50\n"
  "v_mov_b32 v55, v54\n"
  "v_mov_b32 v67, v66\n"
  "v_mov_b32 v71, v70\n"
  "v_permlane32_swap_b32 v50, v51\n"
  "v_permlane32_swap_b32 v54, v55\n"
  "v_permlane32_swap_b32 v66, v67\n"
  "v_permlane32_swap_b32 v70, v71\n"
  "v_max_f32 v50, v50, v51\n"
  "v_max_f32 v54, v54, v55\n"
  "v_max_f32 v66, v66, v67\n"
  "v_max_f32 v70, v70, v71\n"
  "v_mul_f32 v50, v50, v140\n"
  "v_mul_f32 v54, v54, v140\n"
  "v_mul_f32 v66, v66, v144\n"
  "v_mul_f32 v70, v70, v144\n"
  "v_pk_fma_f32 v[112:113], v[112:113], v[140:141], v[50:51] op_sel_hi:[1,0,0] neg_lo:[0,0,1] neg_hi:[0,0,1]\n"
  "v_pk_fma_f32 v[114:115], v[114:115], v[140:141], v[50:51] op_sel_hi:[1,0,0] neg_lo:[0,0,1] neg_hi:[0,0,1]\n"
  "v_pk_fma_f32 v[120:121], v[120:121], v[140:141], v[54:55] op_sel_hi:[1,0,0] neg_lo:[0,0,1] neg_hi:[0,0,1]\n"
  "v_pk_fma_f32 v[122:123], v[122:123], v[140:141], v[54:55] op_sel_hi:[1,0,0] neg_lo:[0,0,1] neg_hi:[0,0,1]\n"
  "v_pk_fma_f32 v[128:129], v[128:129], v[144:145], v[66:67] op_sel_hi:[1,0,0] neg_lo:[0,0,1] neg_hi:[0,0,1]\n"
  "v_pk_fma_f32 v[130:131], v[130:131], v[144:145], v[66:67] op_sel_hi:[1,0,0] neg_lo:[0,0,1] neg_hi:[0,0,1]\n"
  "v_pk_fma_f32 v[188:189], v[188:189], v[144:145], v[70:71] op_sel_hi:[1,0,0] neg_lo:[0,0,1] neg_hi:[0,0,1]\n"
  "v_pk_fma_f32 v[190:191], v[190:191], v[144:145], v[70:71] op_sel_hi:[1,0,0] neg_lo:[0,0,1] neg_hi:[0,0,1]\n"
  "v_exp_f32 v112, v112\n"
  "v_exp_f32 v113, v113\n"
  "v_exp_f32 v114, v114\n"
  "v_exp_f32 v115, v115\n"
  "v_exp_f32 v120, v120\n"
  "v_exp_f32 v121, v121\n"
  "v_exp_f32 v122, v122\n"
  "v_exp_f32 v123, v123\n"
  "v_exp_f32 v128, v128\n"
  "v_exp_f32 v129, v129\n"
  "v_exp_f32 v130, v130\n"
  "v_exp_f32 v131, v131\n"
  "v_exp_f32 v188, v188\n"
  "v_exp_f32 v189, v189\n"
  "v_exp_f32 v190, v190\n"
  "v_exp_f32 v191, v191\n"
  "v_cvt_pk_bf16_f32 v98, v112, v113\n"
  "v_cvt_pk_bf16_f32 v99, v114, v115\n"
  "v_cvt_pk_bf16_f32 v102, v120, v121\n"
  "v_cvt_pk_bf16_f32 v103, v122, v123\n"
  "v_cvt_pk_bf16_f32 v106, v128, v129\n"
  "v_cvt_pk_bf16_f32 v107, v130, v131\n"
  "v_cvt_pk_bf16_f32 v110, v188, v189\n"
  "v_cvt_pk_bf16_f32 v111, v190, v191\n"
  "v_and_b32 v116, v220, v98\n"
  "v_and_b32 v117, v221, v99\n"
  "v_and_b32 v124, v220, v102\n"
  "v_and_b32 v125, v221, v103\n"
  "v_and_b32 v132, v220, v106\n"
  "v_and_b32 v133, v221, v107\n"
  "v_and_b32 v192, v220, v110\n"
  "v_and_b32 v193, v221, v111\n"
  "v_pk_fma_f32 v[72:73], v[148:149], v[20:21], v[72:73] op_sel_hi:[1,0,1] neg_lo:[1,0,0] neg_hi:[1,0,0]\n"
  "v_pk_fma_f32 v[74:75], v[150:151], v[20:21], v[74:75] op_sel_hi:[1,0,1] neg_lo:[1,0,0] neg_hi:[1,0,0]\n"
  "v_pk_fma_f32 v[76:77], v[148:149], v[20:21], v[76:77] op_sel:[0,1,0] neg_lo:[1,0,0] neg_hi:[1,0,0]\n"
  "v_pk_fma_f32 v[78:79], v[150:151], v[20:21], v[78:79] op_sel:[0,1,0] neg_lo:[1,0,0] neg_hi:[1,0,0]\n"
  "v_pk_fma_f32 v[80:81], v[156:157], v[20:21], v[80:81] op_sel_hi:[1,0,1] neg_lo:[1,0,0] neg_hi:[1,0,0]\n"
  "v_pk_fma_f32 v[82:83], v[158:159], v[20:21], v[82:83] op_sel_hi:[1,0,1] neg_lo:[1,0,0] neg_hi:[1,0,0]\n"
  "v_pk_fma_f32 v[84:85], v[156:157], v[20:21], v[84:85] op_sel:[0,1,0] neg_lo:[1,0,0] neg_hi:[1,0,0]\n"
  "v_pk_fma_f32 v[86:87], v[158:159], v[20:21], v[86:87] op_sel:[0,1,0] neg_lo:[1,0,0] neg_hi:[1,0,0]\n"
  "v_xor_b32 v118, v116, v98\n"
  "v_xor_b32 v119, v117, v99\n"
  "v_xor_b32 v126, v124, v102\n"
  "v_xor_b32 v127, v125, v103\n"
  "v_xor_b32 v134, v132, v106\n"
  "v_xor_b32 v135, v133, v107\n"
  "v_xor_b32 v194, v192, v110\n"
  "v_xor_b32 v195, v193, v111\n"
  "v_pk_mul_f32 v[72:73], v[72:73], v[152:153]\n"
  "v_pk_mul_f32 v[74:75], v[74:75], v[154:155]\n"
  "v_pk_mul_f32 v[76:77], v[76:77], v[152:153]\n"
  "v_pk_mul_f32 v[78:79], v[78:79], v[154:155]\n"
  "v_pk_mul_f32 v[80:81], v[80:81], v[160:161]\n"
  "v_pk_mul_f32 v[82:83], v[82:83], v[162:163]\n"
  "v_pk_mul_f32 v[84:85], v[84:85], v[160:161]\n"
  "v_pk_mul_f32 v[86:87], v[86:87], v[162:163]\n"
  "v_pk_fma_f32 v[88:89], v[164:165], v[20:21], v[88:89] op_sel_hi:[1,0,1] neg_lo:[1,0,0] neg_hi:[1,0,0]\n"
  "v_pk_fma_f32 v[90:91], v[166:167], v[20:21], v[90:91] op_sel_hi:[1,0,1] neg_lo:[1,0,0] neg_hi:[1,0,0]\n"
  "v_pk_fma_f32 v[92:93], v[164:165], v[20:21], v[92:93] op_sel:[0,1,0] neg_lo:[1,0,0] neg_hi:[1,0,0]\n"
  "v_pk_fma_f32 v[94:95], v[166:167], v[20:21], v[94:95] op_sel:[0,1,0] neg_lo:[1,0,0] neg_hi:[1,0,0]\n"
  "v_cvt_pk_bf16_f32 v172, v72, v73\n"
  "v_cvt_pk_bf16_f32 v173, v74, v75\n"
  "v_mfma_f32_16x16x32_bf16 v[72:75], v[30:33], v[116:119], 0\n"
  "v_cvt_pk_bf16_f32 v178, v76, v77\n"
  "v_cvt_pk_bf16_f32 v179, v78, v79\n"
  "v_mfma_f32_16x16x32_bf16 v[76:79], v[30:33], v[124:127], 0\n"
  "v_cvt_pk_bf16_f32 v174, v80, v81\n"
  "v_cvt_pk_bf16_f32 v175, v82, v83\n"
  "v_mfma_f32_16x16x32_bf16 v[80:83], v[30:33], v[132:135], 0\n"
  "v_cvt_pk_bf16_f32 v180, v84, v85\n"
  "v_cvt_pk_bf16_f32 v181, v86, v87\n"
  "v_pk_mul_f32 v[88:89], v[88:89], v[168:169]\n"
  "v_pk_mul_f32 v[90:91], v[90:91], v[170:171]\n"
  "v_pk_mul_f32 v[92:93], v[92:93], v[168:169]\n"
  "v_pk_mul_f32 v[94:95], v[94:95], v[170:171]\n"
  "s_nop 0\n"
  "v_mfma_f32_16x16x32_bf16 v[84:87], v[30:33], v[192:195], 0\n"
  "v_cvt_pk_bf16_f32 v176, v88, v89\n"
  "v_cvt_pk_bf16_f32 v177, v90, v91\n"
  "v_mfma_f32_16x16x32_bf16 v[112:115], v[172:175], v[116:119], 0\n"
  "v_cvt_pk_bf16_f32 v182, v92, v93\n"
  "v_cvt_pk_bf16_f32 v183, v94, v95\n"
  "v_mfma_f32_16x16x32_bf16 v[120:123], v[178:181], v[124:127], 0\n"
  "v_rcp_f32 v72, v72\n"
  "v_mfma_f32_16x16x32_bf16 v[128:131], v[174:177], v[132:135], 0\n"
  "v_rcp_f32 v76, v76\n"
  "v_mfma_f32_16x16x32_bf16 v[188:191], v[180:183], v[192:195], 0\n"
  "v_rcp_f32 v80, v80\n"
  "v_rcp_f32 v84, v84\n"
  "v_pk_mul_f32 v[112:113], v[112:113], v[72:73] op_sel_hi:[1,0]\n"
  "v_pk_mul_f32 v[114:115], v[114:115], v[72:73] op_sel_hi:[1,0]\n"
  "v_pk_mul_f32 v[120:121], v[120:121], v[76:77] op_sel_hi:[1,0]\n"
  "v_pk_mul_f32 v[122:123], v[122:123], v[76:77] op_sel_hi:[1,0]\n"
  "v_pk_mul_f32 v[128:129], v[128:129], v[80:81] op_sel_hi:[1,0]\n"
  "v_pk_mul_f32 v[130:131], v[130:131], v[80:81] op_sel_hi:[1,0]\n"
  "v_pk_mul_f32 v[188:189], v[188:189], v[84:85] op_sel_hi:[1,0]\n"
  "v_pk_mul_f32 v[190:191], v[190:191], v[84:85] op_sel_hi:[1,0]\n"
  "v_cvt_pk_bf16_f32 v196, v112, v113\n"
  "v_cvt_pk_bf16_f32 v197, v114, v115\n"
  "v_cvt_pk_bf16_f32 v198, v120, v121\n"
  "v_cvt_pk_bf16_f32 v199, v122, v123\n"
  "v_cvt_pk_bf16_f32 v200, v128, v129\n"
  "v_cvt_pk_bf16_f32 v201, v130, v131\n"
  "v_cvt_pk_bf16_f32 v202, v188, v189\n"
  "v_cvt_pk_bf16_f32 v203, v190, v191\n"
  "buffer_store_dwordx4 v[196:199], v1, s[20:23], s15 offen sc1\n"
  "buffer_store_dwordx4 v[200:203], v1, s[20:23], s16 offen sc1\n"
  "s_add_u32 m0, s10, 98304\n"
  "s_nop 0\n"
  "buffer_load_dwordx4 v184, s[32:35], 0 offen lds\n"
  "s_sub_u32 s88, s89, 0x4000\n"
  "v_add_u32 v227, s88, v228\n"
  "s_lshl_b32 s87, s66, 11\n"
  "s_add_u32 s87, s87, 1835008\n"
  "v_lshl_add_u32 v230, v3, 9, s87\n"
  "v_lshl_add_u32 v230, v2, 3, v230\n"
  "s_sub_u32 s89, s89, 2048\n"
  "v_add_u32 v231, s89, v230\n"
  "v_bfe_u32 v45, v0, 3, 2\n"
  "v_lshlrev_b32 v45, 5, v45\n"
  "v_lshrrev_b32 v46, 2, v0\n"
  "s_add_u32 m0, s10, 99328\n"
  "s_nop 0\n"
  "buffer_load_dwordx4 v184, s[32:35], s73 offen lds\n"
  "v_lshl_add_u32 v45, v46, 7, v45\n"
  "v_and_b32 v46, 3, v0\n"
  "v_lshl_add_u32 v45, v46, 3, v45\n"
  "v_add_u32 v200, 98304, v45\n"
  "v_xor_b32 v201, 32, v200\n"
  "v_xor_b32 v202, 64, v200\n"
  "v_xor_b32 v203, 0x60, v200\n"
  "s_add_u32 m0, s10, 100352\n"
  "s_nop 0\n"
  "buffer_load_dwordx4 v184, s[32:35], s75 offen lds\n"
  "s_add_u32 m0, s10, 101376\n"
  "s_nop 0\n"
  "buffer_load_dwordx4 v184, s[32:35], s77 offen lds\n"
  "s_add_u32 m0, s10, 102400\n"
  "s_nop 0\n"
  "buffer_load_dwordx4 v184, s[32:35], s79 offen lds\n"
  "s_add_u32 m0, s10, 103424\n"
  "s_nop 0\n"
  "buffer_load_dwordx4 v184, s[32:35], s81 offen lds\n"
  "s_add_u32 m0, s10, 104448\n"
  "s_nop 0\n"
  "buffer_load_dwordx4 v184, s[32:35], s83 offen lds\n"
  "buffer_load_dwordx4 v[192:195], v247, s[24:27], 0 offen\n"
  "buffer_load_dwordx4 v[196:199], v247, s[24:27], 0 offen offset:64\n"
  "s_lshl2_add_u32 s88, s64, s58\n"
  "s_lshl_b32 s89, s88, 9\n"
  "s_add_u32 s89, s89, 49152\n"
  "v_min_u32 v247, 6, v0\n"
  "v_lshl_add_u32 v247, v247, 3, s89\n"
  "s_lshl3_add_u32 s88, s64, s58\n"
  "s_waitcnt vmcnt(9)\n"
  "s_barrier\n"
  "buffer_store_dwordx2 v[36:37], v248, s[20:23], s11 offen sc1\n"
  "s_mul_i32 s88, s88, 28672\n"
  "s_add_u32 s88, s88, 10485760\n"
  "v_add_u32 v232, s88, v1\n"
  "v_add_u32 v233, 114688, v232\n"
  "s_lshl4_add_u32 s87, s66, s62\n"
  "s_lshl_b32 s87, s87, 10\n"
  "s_add_u32 s15, s87, 4194304\n"
  "s_add_u32 s16, s15, 16384\n"
  "s_lshl4_add_u32 s87, s61, s62\n"
  "s_lshr_b32 s94, s87, 4\n"
  "s_and_b32 s95, s87, 15\n"
  "s_lshl_b32 s94, s94, 9\n"
  "s_lshl3_add_u32 s94, s95, s94\n"
  "s_add_u32 s11, s94, 81920\n"
  "buffer_store_dwordx4 v[22:25], v238, s[20:23], 0 offen sc1\n"
  "buffer_store_dwordx4 v[26:29], v239, s[20:23], 0 offen sc1\n"
  "s_add_u32 m0, s10, 105472\n"
  "s_nop 0\n"
  "buffer_load_dwordx4 v184, s[32:35], s85 offen lds\n"
  "s_add_u32 m0, s10, 106496\n"
  "s_nop 0\n"
  "buffer_load_dwordx4 v185, s[32:35], 0 offen lds\n"
  "s_add_u32 m0, s10, 107520\n"
  "s_nop 0\n"
  "buffer_load_dwordx4 v185, s[32:35], s73 offen lds\n"
  "s_add_u32 m0, s10, 108544\n"
  "s_nop 0\n"
  "buffer_load_dwordx4 v185, s[32:35], s75 offen lds\n"
  "s_add_u32 m0, s10, 109568\n"
  "s_nop 0\n"
  "buffer_load_dwordx4 v185, s[32:35], s77 offen lds\n"
  "s_add_u32 m0, s10, 110592\n"
  "s_nop 0\n"
  "buffer_load_dwordx4 v185, s[32:35], s79 offen lds\n"
  "s_waitcnt vmcnt(10)\n"
  "v_permlane16_swap_b32 v192, v194\n"
  "v_permlane16_swap_b32 v193, v195\n"
  "s_nop 1\n"
  "v_mfma_f32_16x16x32_bf16 v[176:179], v[192:195], v[40:43], 0\n"
  "v_mfma_f32_16x16x32_bf16 v[180:183], v[192:195], v[38:41], 0\n"
  "s_add_u32 m0, s10, 111616\n"
  "s_nop 0\n"
  "buffer_load_dwordx4 v185, s[32:35], s81 offen lds\n"
  "s_add_u32 m0, s10, 112640\n"
  "s_nop 0\n"
  "buffer_load_dwordx4 v185, s[32:35], s83 offen lds\n"
  "s_add_u32 m0, s10, 113664\n"
  "s_nop 0\n"
  "buffer_load_dwordx4 v185, s[32:35], s85 offen lds\n"
  "s_add_u32 m0, s8, 0\n"
  "s_mov_b32 s9, 1703936\n"
  "buffer_load_dwordx4 v240, s[40:43], s9 offen lds\n"
  "s_waitcnt vmcnt(13)\n"
  "v_permlane16_swap_b32 v196, v198\n"
  "v_permlane16_swap_b32 v197, v199\n"
  "s_nop 1\n"
  "v_mfma_f32_16x16x32_bf16 v[184:187], v[196:199], v[40:43], 0\n"
  "v_mfma_f32_16x16x32_bf16 v[188:191], v[196:199], v[38:41], 0\n"
  "s_add_u32 m0, s8, 1024\n"
  "s_mov_b32 s9, 1736704\n"
  "buffer_load_dwordx4 v240, s[40:43], s9 offen lds\n"
  "s_sleep 8\n"
  "buffer_load_dwordx2 v[206:207], v249, s[20:23], 0 offen sc1\n"
  "s_add_u32 m0, s8, 2048\n"
  "s_mov_b32 s9, 1769472\n"
  "buffer_load_dwordx4 v240, s[40:43], s9 offen lds\n"
  "s_waitcnt vmcnt(4)\n"
  "s_barrier\n"
  "ds_read_b64_tr_b16 v[112:113], v200 offset:0\n"
  "ds_read_b64_tr_b16 v[114:115], v200 offset:2048\n"
  "ds_read_b64_tr_b16 v[116:117], v201 offset:0\n"
  "ds_read_b64_tr_b16 v[118:119], v201 offset:2048\n"
  "ds_read_b64_tr_b16 v[120:121], v202 offset:0\n"
  "s_add_u32 m0, s8, 3072\n"
  "s_mov_b32 s9, 1802240\n"
  "buffer_load_dwordx4 v240, s[40:43], s9 offen lds\n"
  "ds_read_b64_tr_b16 v[122:123], v202 offset:2048\n"
  "ds_read_b64_tr_b16 v[124:125], v203 offset:0\n"
  "ds_read_b64_tr_b16 v[126:127], v203 offset:2048\n"
  "ds_read_b64_tr_b16 v[128:129], v200 offset:4096\n"
  "ds_read_b64_tr_b16 v[130:131], v200 offset:6144\n"
  "ds_read_b64_tr_b16 v[132:133], v201 offset:4096\n"
  "ds_read_b64_tr_b16 v[134:135], v201 offset:6144\n"
  "ds_read_b64_tr_b16 v[136:137], v202 offset:4096\n"
  "ds_read_b64_tr_b16 v[138:139], v202 offset:6144\n"
  "ds_read_b64_tr_b16 v[140:141], v203 offset:4096\n"
  "ds_read_b64_tr_b16 v[142:143], v203 offset:6144\n"
  "ds_read_b64_tr_b16 v[144:145], v200 offset:8192\n"
  "ds_read_b64_tr_b16 v[146:147], v200 offset:10240\n"
  "ds_read_b64_tr_b16 v[148:149], v201 offset:8192\n"
  "s_add_u32 m0, s8, 4096\n"
  "s_mov_b32 s9, 0\n"
  "buffer_load_dwordx4 v240, s[40:43], s9 offen lds\n"
  "ds_read_b64_tr_b16 v[150:151], v201 offset:10240\n"
  "ds_read_b64_tr_b16 v[152:153], v202 offset:8192\n"
  "ds_read_b64_tr_b16 v[154:155], v202 offset:10240\n"
  "ds_read_b64_tr_b16 v[156:157], v203 offset:8192\n"
  "ds_read_b64_tr_b16 v[158:159], v203 offset:10240\n"
  "ds_read_b64_tr_b16 v[160:161], v200 offset:12288\n"
  "ds_read_b64_tr_b16 v[162:163], v200 offset:14336\n"
  "ds_read_b64_tr_b16 v[164:165], v201 offset:12288\n"
  "ds_read_b64_tr_b16 v[166:167], v201 offset:14336\n"
  "ds_read_b64_tr_b16 a[224:225], v202 offset:12288\n"
  "ds_read_b64_tr_b16 a[226:227], v202 offset:14336\n"
  "ds_read_b64_tr_b16 a[228:229], v203 offset:12288\n"
  "ds_read_b64_tr_b16 a[230:231], v203 offset:14336\n"
  "ds_read_b64_tr_b16 a[232:233], v200 offset:16384\n"
  "ds_read_b64_tr_b16 a[234:235], v200 offset:18432\n"
  "s_add_u32 m0, s8, 5120\n"
  "s_mov_b32 s9, 32768\n"
  "buffer_load_dwordx4 v240, s[40:43], s9 offen lds\n"
  "ds_read_b64_tr_b16 a[236:237], v201 offset:16384\n"
  "ds_read_b64_tr_b16 a[238:239], v201 offset:18432\n"
  "ds_read_b64_tr_b16 a[240:241], v202 offset:16384\n"
  "ds_read_b64_tr_b16 a[242:243], v202 offset:18432\n"
  "ds_read_b64_tr_b16 a[244:245], v203 offset:16384\n"
  "ds_read_b64_tr_b16 a[246:247], v203 offset:18432\n"
  "ds_read_b64_tr_b16 a[248:249], v200 offset:20480\n"
  "ds_read_b64_tr_b16 a[250:251], v200 offset:22528\n"
  "ds_read_b64_tr_b16 a[252:253], v201 offset:20480\n"
  "ds_read_b64_tr_b16 a[254:255], v201 offset:22528\n"
  "s_add_u32 m0, s8, 6144\n"
  "s_mov_b32 s9, 65536\n"
  "buffer_load_dwordx4 v240, s[40:43], s9 offen lds\n"
  "s_add_u32 m0, s8, 7168\n"
  "s_mov_b32 s9, 98304\n"
  "buffer_load_dwordx4 v240, s[40:43], s9 offen lds\n"
  "s_waitcnt vmcnt(6) lgkmcnt(0)\n"
  "v_cmp_ne_u64 vcc, s[56:57], v[206:207]\n"
  "s_cmp_eq_u64 vcc, 0\n"
  "s_cbranch_scc0 .Lfb1_%=\n"
  ".Lbk1_%=:\n"
  "buffer_load_dwordx4 v[48:51], v250, s[20:23], 0 offen sc1\n"
  "buffer_load_dwordx4 v[52:55], v250, s[20:23], 0 offen offset:1024 sc1\n"
  "buffer_load_dwordx4 v[56:59], v250, s[20:23], 0 offen offset:2048 sc1\n"
  "s_waitcnt vmcnt(7)\n"
  "ds_read_b64_tr_b16 v[196:197], v236 offset:0\n"
  "ds_read_b64_tr_b16 v[198:199], v236 offset:2048\n"
  "ds_read_b64_tr_b16 v[192:193], v237 offset:0\n"
  "ds_read_b64_tr_b16 v[194:195], v237 offset:2048\n"
  "buffer_load_dwordx4 v[60:63], v250, s[20:23], 0 offen offset:3072 sc1\n"
  "buffer_load_dwordx4 v[64:67], v250, s[20:23], s72 offen sc1\n"
  "buffer_load_dwordx4 v[68:71], v250, s[20:23], s72 offen offset:1024 sc1\n"
  "buffer_load_dwordx4 v[72:75], v250, s[20:23], s72 offen offset:2048 sc1\n"
  "buffer_load_dwordx4 v[76:79], v250, s[20:23], s72 offen offset:3072 sc1\n"
  "buffer_load_dwordx4 v[80:83], v250, s[20:23], s73 offen sc1\n"
  "buffer_load_dwordx4 v[84:87], v250, s[20:23], s73 offen offset:1024 sc1\n"
  "buffer_load_dwordx4 v[88:91], v250, s[20:23], s73 offen offset:2048 sc1\n"
  "buffer_load_dwordx4 v[92:95], v250, s[20:23], s73 offen offset:3072 sc1\n"
  "s_waitcnt vmcnt(12) lgkmcnt(0)\n"
  "ds_read_b64_tr_b16 a[0:1], v234 offset:4096\n"
  "ds_read_b64_tr_b16 a[2:3], v234 offset:6144\n"
  "ds_read_b64_tr_b16 a[4:5], v235 offset:4096\n"
  "ds_read_b64_tr_b16 a[6:7], v235 offset:6144\n"
  "ds_read_b64_tr_b16 a[8:9], v236 offset:4096\n"
  "ds_read_b64_tr_b16 a[10:11], v236 offset:6144\n"
  "buffer_load_dwordx4 v[96:99], v250, s[20:23], s74 offen sc1\n"
  "ds_read_b64_tr_b16 a[12:13], v237 offset:4096\n"
  "ds_read_b64_tr_b16 a[14:15], v237 offset:6144\n"
  "s_waitcnt vmcnt(12)\n"
  "v_mfma_f32_16x16x32_bf16 v[176:179], v[48:51], v[112:115], v[176:179]\n"
  "ds_read_b64_tr_b16 v[112:113], v202 offset:20480\n"
  "v_mfma_f32_16x16x32_bf16 v[180:183], v[48:51], v[116:119], v[180:183]\n"
  "ds_read_b64_tr_b16 v[114:115], v202 offset:22528\n"
  "v_mfma_f32_16x16x32_bf16 v[184:187], v[48:51], v[120:123], v[184:187]\n"
  "buffer_load_dwordx4 v[100:103], v250, s[20:23], s74 offen offset:1024 sc1\n"
  "ds_read_b64_tr_b16 v[116:117], v203 offset:20480\n"
  "v_mfma_f32_16x16x32_bf16 v[188:191], v[48:51], v[124:127], v[188:191]\n"
  "ds_read_b64_tr_b16 v[118:119], v203 offset:22528\n"
  "v_mov_b32 v48, v47\n"
  "s_waitcnt vmcnt(12)\n"
  "v_mfma_f32_16x16x32_bf16 v[176:179], v[52:55], v[128:131], v[176:179]\n"
  "ds_read_b64_tr_b16 v[120:121], v200 offset:24576\n"
  "v_mov_b32 v49, v47\n"
  "v_mfma_f32_16x16x32_bf16 v[180:183], v[52:55], v[132:135], v[180:183]\n"
  "ds_read_b64_tr_b16 v[122:123], v200 offset:26624\n"
  "buffer_load_dwordx4 v[104:107], v250, s[20:23], s74 offen offset:2048 sc1\n"
  "v_mfma_f32_16x16x32_bf16 v[184:187], v[52:55], v[136:139], v[184:187]\n"
  "ds_read_b64_tr_b16 v[124:125], v201 offset:24576\n"
  "v_mov_b32 v50, v47\n"
  "v_mfma_f32_16x16x32_bf16 v[188:191], v[52:55], v[140:143], v[188:191]\n"
  "ds_read_b64_tr_b16 v[126:127], v201 offset:26624\n"
  "v_mov_b32 v51, v47\n"
  "s_waitcnt vmcnt(12) lgkmcnt(8)\n"
  "v_mfma_f32_16x16x32_bf16 v[176:179], v[56:59], v[144:147], v[176:179]\n"
  "ds_read_b64_tr_b16 v[128:129], v202 offset:24576\n"
  "v_mov_b32 v52, v47\n"
  "v_mfma_f32_16x16x32_bf16 v[180:183], v[56:59], v[148:151], v[180:183]\n"
  "ds_read_b64_tr_b16 v[130:131], v202 offset:26624\n"
  "buffer_load_dwordx4 v[108:111], v250, s[20:23], s74 offen offset:3072 sc1\n"
  "v_mfma_f32_16x16x32_bf16 v[184:187], v[56:59], v[152:155], v[184:187]\n"
  "ds_read_b64_tr_b16 v[132:133], v203 offset:24576\n"
  "v_mov_b32 v53, v47\n"
  "v_mfma_f32_16x16x32_bf16 v[188:191], v[56:59], v[156:159], v[188:191]\n"
  "ds_read_b64_tr_b16 v[134:135], v203 offset:26624\n"
  "v_mov_b32 v54, v47\n"
  "s_waitcnt vmcnt(12) lgkmcnt(8)\n"
  "v_mfma_f32_16x16x32_bf16 v[176:179], v[60:63], v[160:163], v[176:179]\n"
  "ds_read_b64_tr_b16 v[136:137], v200 offset:28672\n"
  "v_mov_b32 v55, v47\n"
  "v_mfma_f32_16x16x32_bf16 v[180:183], v[60:63], v[164:167], v[180:183]\n"
  "ds_read_b64_tr_b16 v[138:139], v200 offset:30720\n"
  "s_add_u32 m0, s8, 8192\n"
  "s_mov_b32 s9, 131072\n"
  "buffer_load_dwordx4 v240, s[40:43], s9 offen lds\n"
  "v_mfma_f32_16x16x32_bf16 v[184:187], v[60:63], a[224:227], v[184:187]\n"
  "ds_read_b64_tr_b16 v[140:141], v201 offset:28672\n"
  "s_and_b32 s87, s68, 3\n"
  "v_mfma_f32_16x16x32_bf16 v[188:191], v[60:63], a[228:231], v[188:191]\n"
  "ds_read_b64_tr_b16 v[142:143], v201 offset:30720\n"
  "s_lshl_b32 s87, s87, 10\n"
  "s_waitcnt vmcnt(12) lgkmcnt(7)\n"
  "v_mfma_f32_16x16x32_bf16 v[176:179], v[64:67], a[232:235], v[176:179]\n"
  "ds_read_b64_tr_b16 v[144:145], v202 offset:28672\n"
  "s_bfe_u32 s88, s68, 0x10002\n"
  "v_mfma_f32_16x16x32_bf16 v[180:183], v[64:67], a[236:239], v[180:183]\n"
  "ds_read_b64_tr_b16 v[146:147], v202 offset:30720\n"
  "s_lshl_b32 s88, s88, 14\n"
  "v_mfma_f32_16x16x32_bf16 v[184:187], v[64:67], a[240:243], v[184:187]\n"
  "ds_read_b64_tr_b16 v[148:149], v203 offset:28672\n"
  "s_add_u32 s87, s87, s88\n"
  "v_mfma_f32_16x16x32_bf16 v[188:191], v[64:67], a[244:247], v[188:191]\n"
  "ds_read_b64_tr_b16 v[150:151], v203 offset:30720\n"
  "s_add_u32 s87, s87, 4096\n"
  "s_waitcnt vmcnt(11) lgkmcnt(7)\n"
  "v_mfma_f32_16x16x32_bf16 v[176:179], v[68:71], a[248:251], v[176:179]\n"
  "ds_read_b64_tr_b16 v[152:153], v200 offset:32768\n"
  "s_add_u32 m0, s8, 9216\n"
  "s_mov_b32 s9, 163840\n"
  "buffer_load_dwordx4 v240, s[40:43], s9 offen lds\n"
  "v_mfma_f32_16x16x32_bf16 v[180:183], v[68:71], a[252:255], v[180:183]\n"
  "ds_read_b64_tr_b16 v[154:155], v200 offset:34816\n"
  "v_and_b32 v45, 7, v0\n"
  "v_mfma_f32_16x16x32_bf16 v[184:187], v[68:71], v[112:115], v[184:187]\n"
  "ds_read_b64_tr_b16 v[156:157], v201 offset:32768\n"
  "v_lshl_add_u32 v45, v45, 7, s87\n"
  "v_mfma_f32_16x16x32_bf16 v[188:191], v[68:71], v[116:119], v[188:191]\n"
  "ds_read_b64_tr_b16 v[158:159], v201 offset:34816\n"
  "v_and_b32 v46, 0xfffffc00, v228\n"
  "s_waitcnt vmcnt(11) lgkmcnt(7)\n"
  "v_mfma_f32_16x16x32_bf16 v[176:179], v[72:75], v[120:123], v[176:179]\n"
  "ds_read_b64_tr_b16 v[160:161], v202 offset:32768\n"
  "v_add_u32 v45, v45, v46\n"
  "v_mfma_f32_16x16x32_bf16 v[180:183], v[72:75], v[124:127], v[180:183]\n"
  "ds_read_b64_tr_b16 v[162:163], v202 offset:34816\n"
  "v_mov_b32 v250, v45\n"
  "v_mfma_f32_16x16x32_bf16 v[184:187], v[72:75], v[128:131], v[184:187]\n"
  "ds_read_b64_tr_b16 v[164:165], v203 offset:32768\n"
  "v_mfma_f32_16x16x32_bf16 v[188:191], v[72:75], v[132:135], v[188:191]\n"
  "ds_read_b64_tr_b16 v[166:167], v203 offset:34816\n"
  "s_add_u32 m0, s8, 10240\n"
  "s_mov_b32 s9, 196608\n"
  "buffer_load_dwordx4 v240, s[40:43], s9 offen lds\n"
  "s_waitcnt vmcnt(11) lgkmcnt(7)\n"
  "v_mfma_f32_16x16x32_bf16 v[176:179], v[76:79], v[136:139], v[176:179]\n"
  "ds_read_b64_tr_b16 a[224:225], v200 offset:36864\n"
  "v_mfma_f32_16x16x32_bf16 v[180:183], v[76:79], v[140:143], v[180:183]\n"
  "ds_read_b64_tr_b16 a[226:227], v200 offset:38912\n"
  "v_mfma_f32_16x16x32_bf16 v[184:187], v[76:79], v[144:147], v[184:187]\n"
  "ds_read_b64_tr_b16 a[228:229], v201 offset:36864\n"
  "ds_read_b64_tr_b16 a[230:231], v201 offset:38912\n"
  "v_mfma_f32_16x16x32_bf16 v[188:191], v[76:79], v[148:151], v[188:191]\n"
  "ds_read_b64_tr_b16 a[232:233], v202 offset:36864\n"
  "ds_read_b64_tr_b16 a[234:235], v202 offset:38912\n"
  "s_waitcnt vmcnt(10) lgkmcnt(10)\n"
  "v_mfma_f32_16x16x32_bf16 v[176:179], v[80:83], v[152:155], v[176:179]\n"
  "ds_read_b64_tr_b16 a[236:237], v203 offset:36864\n"
  "ds_read_b64_tr_b16 a[238:239], v203 offset:38912\n"
  "v_mfma_f32_16x16x32_bf16 v[180:183], v[80:83], v[156:159], v[180:183]\n"
  "ds_read_b64_tr_b16 a[240:241], v200 offset:40960\n"
  "ds_read_b64_tr_b16 a[242:243], v200 offset:43008\n"
  "s_waitcnt lgkmcnt(11)\n"
  "v_mfma_f32_16x16x32_bf16 v[184:187], v[80:83], v[160:163], v[184:187]\n"
  "ds_read_b64_tr_b16 a[244:245], v201 offset:40960\n"
  "ds_read_b64_tr_b16 a[246:247], v201 offset:43008\n"
  "s_waitcnt lgkmcnt(12)\n"
  "v_mfma_f32_16x16x32_bf16 v[188:191], v[80:83], v[164:167], v[188:191]\n"
  "ds_read_b64_tr_b16 a[248:249], v202 offset:40960\n"
  "ds_read_b64_tr_b16 a[250:251], v202 offset:43008\n"
  "s_waitcnt vmcnt(9) lgkmcnt(12)\n"
  "v_mfma_f32_16x16x32_bf16 v[176:179], v[84:87], a[224:227], v[176:179]\n"
  "ds_read_b64_tr_b16 a[252:253], v203 offset:40960\n"
  "ds_read_b64_tr_b16 a[254:255], v203 offset:43008\n"
  "s_waitcnt lgkmcnt(12)\n"
  "v_mfma_f32_16x16x32_bf16 v[180:183], v[84:87], a[228:231], v[180:183]\n"
  "ds_read_b64_tr_b16 v[112:113], v200 offset:45056\n"
  "s_add_u32 m0, s8, 11264\n"
  "s_mov_b32 s9, 229376\n"
  "buffer_load_dwordx4 v240, s[40:43], s9 offen lds\n"
  "s_waitcnt lgkmcnt(11)\n"
  "v_mfma_f32_16x16x32_bf16 v[184:187], v[84:87], a[232:235], v[184:187]\n"
  "ds_read_b64_tr_b16 v[114:115], v200 offset:47104\n"
  "ds_read_b64_tr_b16 v[116:117], v201 offset:45056\n"
  "s_waitcnt lgkmcnt(11)\n"
  "v_mfma_f32_16x16x32_bf16 v[188:191], v[84:87], a[236:239], v[188:191]\n"
  "ds_read_b64_tr_b16 v[118:119], v201 offset:47104\n"
  "ds_read_b64_tr_b16 v[120:121], v202 offset:45056\n"
  "s_waitcnt vmcnt(9) lgkmcnt(11)\n"
  "v_mfma_f32_16x16x32_bf16 v[176:179], v[88:91], a[240:243], v[176:179]\n"
  "ds_read_b64_tr_b16 v[122:123], v202 offset:47104\n"
  "ds_read_b64_tr_b16 v[124:125], v203 offset:45056\n"
  "s_waitcnt lgkmcnt(11)\n"
  "v_mfma_f32_16x16x32_bf16 v[180:183], v[88:91], a[244:247], v[180:183]\n"
  "ds_read_b64_tr_b16 v[126:127], v203 offset:47104\n"
  "ds_read_b64_tr_b16 v[128:129], v200 offset:49152\n"
  "s_waitcnt lgkmcnt(11)\n"
  "v_mfma_f32_16x16x32_bf16 v[184:187], v[88:91], a[248:251], v[184:187]\n"
  "ds_read_b64_tr_b16 v[130:131], v200 offset:51200\n"
  "ds_read_b64_tr_b16 v[132:133], v201 offset:49152\n"
  "s_waitcnt lgkmcnt(11)\n"
  "v_mfma_f32_16x16x32_bf16 v[188:191], v[88:91], a[252:255], v[188:191]\n"
  "ds_read_b64_tr_b16 v[134:135], v201 offset:51200\n"
  "s_add_u32 m0, s8, 12288\n"
  "s_mov_b32 s9, 262144\n"
  "buffer_load_dwordx4 v240, s[40:43], s9 offen lds\n"
  "s_waitcnt vmcnt(9) lgkmcnt(10)\n"
  "v_mfma_f32_16x16x32_bf16 v[176:179], v[92:95], v[112:115], v[176:179]\n"
  "ds_read_b64_tr_b16 v[136:137], v202 offset:49152\n"
  "ds_read_b64_tr_b16 v[138:139], v202 offset:51200\n"
  "s_waitcnt lgkmcnt(10)\n"
  "v_mfma_f32_16x16x32_bf16 v[180:183], v[92:95], v[116:119], v[180:183]\n"
  "ds_read_b64_tr_b16 v[140:141], v203 offset:49152\n"
  "ds_read_b64_tr_b16 v[142:143], v203 offset:51200\n"
  "s_waitcnt lgkmcnt(10)\n"
  "v_mfma_f32_16x16x32_bf16 v[184:187], v[92:95], v[120:123], v[184:187]\n"
  "ds_read_b64_tr_b16 v[144:145], v200 offset:53248\n"
  "ds_read_b64_tr_b16 v[146:147], v200 offset:55296\n"
  "s_waitcnt lgkmcnt(10)\n"
  "v_mfma_f32_16x16x32_bf16 v[188:191], v[92:95], v[124:127], v[188:191]\n"
  "ds_read_b64_tr_b16 v[148:149], v201 offset:53248\n"
  "ds_read_b64_tr_b16 v[150:151], v201 offset:55296\n"
  "s_waitcnt vmcnt(8) lgkmcnt(10)\n"
  "v_mfma_f32_16x16x32_bf16 v[176:179], v[96:99], v[128:131], v[176:179]\n"
  "ds_read_b64_tr_b16 v[152:153], v202 offset:53248\n"
  "ds_read_b64_tr_b16 v[154:155], v202 offset:55296\n"
  "s_waitcnt lgkmcnt(10)\n"
  "v_mfma_f32_16x16x32_bf16 v[180:183], v[96:99], v[132:135], v[180:183]\n"
  "ds_read_b64_tr_b16 v[156:157], v203 offset:53248\n"
  "s_add_u32 m0, s8, 13312\n"
  "s_mov_b32 s9, 294912\n"
  "buffer_load_dwordx4 v240, s[40:43], s9 offen lds\n"
  "s_waitcnt lgkmcnt(9)\n"
  "v_mfma_f32_16x16x32_bf16 v[184:187], v[96:99], v[136:139], v[184:187]\n"
  "ds_read_b64_tr_b16 v[158:159], v203 offset:55296\n"
  "ds_read_b64_tr_b16 v[160:161], v200 offset:57344\n"
  "s_waitcnt lgkmcnt(9)\n"
  "v_mfma_f32_16x16x32_bf16 v[188:191], v[96:99], v[140:143], v[188:191]\n"
  "ds_read_b64_tr_b16 v[162:163], v200 offset:59392\n"
  "ds_read_b64_tr_b16 v[164:165], v201 offset:57344\n"
  "s_waitcnt vmcnt(8) lgkmcnt(9)\n"
  "v_mfma_f32_16x16x32_bf16 v[176:179], v[100:103], v[144:147], v[176:179]\n"
  "ds_read_b64_tr_b16 v[166:167], v201 offset:59392\n"
  "ds_read_b64_tr_b16 a[224:225], v202 offset:57344\n"
  "s_waitcnt lgkmcnt(9)\n"
  "v_mfma_f32_16x16x32_bf16 v[180:183], v[100:103], v[148:151], v[180:183]\n"
  "ds_read_b64_tr_b16 a[226:227], v202 offset:59392\n"
  "ds_read_b64_tr_b16 a[228:229], v203 offset:57344\n"
  "s_waitcnt lgkmcnt(9)\n"
  "v_mfma_f32_16x16x32_bf16 v[184:187], v[100:103], v[152:155], v[184:187]\n"
  "ds_read_b64_tr_b16 a[230:231], v203 offset:59392\n"
  "ds_read_b64_tr_b16 a[232:233], v200 offset:61440\n"
  "ds_read_b64_tr_b16 a[234:235], v200 offset:63488\n"
  "ds_read_b64_tr_b16 a[236:237], v201 offset:61440\n"
  "ds_read_b64_tr_b16 a[238:239], v201 offset:63488\n"
  "s_waitcnt lgkmcnt(12)\n"
  "v_mfma_f32_16x16x32_bf16 v[188:191], v[100:103], v[156:159], v[188:191]\n"
  "s_add_u32 m0, s8, 14336\n"
  "s_mov_b32 s9, 327680\n"
  "buffer_load_dwordx4 v240, s[40:43], s9 offen lds\n"
  "s_waitcnt vmcnt(8) lgkmcnt(10)\n"
  "v_mfma_f32_16x16x32_bf16 v[176:179], v[104:107], v[160:163], v[176:179]\n"
  "ds_read_b64_tr_b16 a[240:241], v202 offset:61440\n"
  "ds_read_b64_tr_b16 a[242:243], v202 offset:63488\n"
  "s_waitcnt lgkmcnt(10)\n"
  "v_mfma_f32_16x16x32_bf16 v[180:183], v[104:107], v[164:167], v[180:183]\n"
  "ds_read_b64_tr_b16 a[244:245], v203 offset:61440\n"
  "ds_read_b64_tr_b16 a[246:247], v203 offset:63488\n"
  "s_waitcnt lgkmcnt(10)\n"
  "v_mfma_f32_16x16x32_bf16 v[184:187], v[104:107], a[224:227], v[184:187]\n"
  "s_add_u32 m0, s8, 15360\n"
  "s_mov_b32 s9, 360448\n"
  "buffer_load_dwordx4 v240, s[40:43], s9 offen lds\n"
  "s_waitcnt lgkmcnt(8)\n"
  "v_mfma_f32_16x16x32_bf16 v[188:191], v[104:107], a[228:231], v[188:191]\n"
  "ds_read_b64_tr_b16 v[200:201], v235 offset:0\n"
  "v_mov_b32 v80, v47\n"
  "s_waitcnt vmcnt(8) lgkmcnt(7)\n"
  "v_mfma_f32_16x16x32_bf16 v[176:179], v[108:111], a[232:235], v[176:179]\n"
  "ds_read_b64_tr_b16 v[202:203], v235 offset:2048\n"
  "v_mov_b32 v81, v47\n"
  "s_waitcnt lgkmcnt(6)\n"
  "v_mfma_f32_16x16x32_bf16 v[180:183], v[108:111], a[236:239], v[180:183]\n"
  "v_mov_b32 v82, v47\n"
  "v_mov_b32 v83, v47\n"
  "s_waitcnt lgkmcnt(4)\n"
  "v_mfma_f32_16x16x32_bf16 v[184:187], v[108:111], a[240:243], v[184:187]\n"
  "v_cvt_pk_bf16_f32 v22, v176, v177\n"
  "v_lshlrev_b32 v56, 16, v22\n"
  "s_waitcnt lgkmcnt(2)\n"
  "v_mfma_f32_16x16x32_bf16 v[188:191], v[108:111], a[244:247], v[188:191]\n"
  "v_and_b32 v57, 0xffff0000, v22\n"
  "v_cvt_pk_bf16_f32 v24, v180, v181\n"
  "v_lshlrev_b32 v60, 16, v24\n"
  "v_and_b32 v61, 0xffff0000, v24\n"
  "v_cvt_pk_bf16_f32 v26, v184, v185\n"
  "v_pk_mul_f32 v[76:77], v[56:57], v[56:57]\n"
  "v_cvt_pk_bf16_f32 v23, v178, v179\n"
  "v_cvt_pk_bf16_f32 v27, v186, v187\n"
  "v_cvt_pk_bf16_f32 v28, v188, v189\n"
  "v_mfma_f32_16x16x16_bf16 v[48:51], v[22:23], v[40:41], v[48:51]\n"
  "v_lshlrev_b32 v64, 16, v26\n"
  "v_and_b32 v65, 0xffff0000, v26\n"
  "v_mfma_f32_16x16x16_bf16 v[80:83], v[26:27], v[40:41], v[80:83]\n"
  "v_cvt_pk_bf16_f32 v29, v190, v191\n"
  "v_lshlrev_b32 v68, 16, v28\n"
  "v_and_b32 v69, 0xffff0000, v28\n"
  "v_pk_fma_f32 v[76:77], v[60:61], v[60:61], v[76:77]\n"
  "v_lshlrev_b32 v58, 16, v23\n"
  "v_and_b32 v59, 0xffff0000, v23\n"
  "v_cvt_pk_bf16_f32 v25, v182, v183\n"
  "v_lshlrev_b32 v66, 16, v27\n"
  "v_and_b32 v67, 0xffff0000, v27\n"
  "v_mfma_f32_16x16x16_bf16 v[52:55], v[24:25], v[40:41], v[52:55]\n"
  "v_lshlrev_b32 v70, 16, v29\n"
  "v_and_b32 v71, 0xffff0000, v29\n"
  "v_pk_add_f32 v[72:73], v[56:57], v[60:61]\n"
  "v_pk_add_f32 v[88:89], v[64:65], v[68:69]\n"
  "v_pk_fma_f32 v[76:77], v[64:65], v[64:65], v[76:77]\n"
  "v_lshlrev_b32 v62, 16, v25\n"
  "v_and_b32 v63, 0xffff0000, v25\n"
  "v_pk_add_f32 v[72:73], v[72:73], v[88:89]\n"
  "v_pk_fma_f32 v[76:77], v[68:69], v[68:69], v[76:77]\n"
  "v_pk_add_f32 v[90:91], v[66:67], v[70:71]\n"
  "v_pk_mul_f32 v[78:79], v[58:59], v[58:59]\n"
  "v_pk_add_f32 v[74:75], v[58:59], v[62:63]\n"
  "v_pk_fma_f32 v[78:79], v[62:63], v[62:63], v[78:79]\n"
  "v_pk_add_f32 v[74:75], v[74:75], v[90:91]\n"
  "v_pk_fma_f32 v[78:79], v[66:67], v[66:67], v[78:79]\n"
  "v_pk_fma_f32 v[78:79], v[70:71], v[70:71], v[78:79]\n"
  "v_add_f32_dpp v88, v72, v72 row_ror:8 row_mask:0xf bank_mask:0x3\n"
  "v_add_f32_dpp v90, v76, v76 row_ror:8 row_mask:0xf bank_mask:0x3\n"
  "v_add_f32_dpp v89, v73, v73 row_ror:8 row_mask:0xf bank_mask:0x3\n"
  "v_add_f32_dpp v88, v74, v74 row_ror:8 row_mask:0xf bank_mask:0xc\n"
  "v_add_f32_dpp v90, v78, v78 row_ror:8 row_mask:0xf bank_mask:0xc\n"
  "v_add_f32_dpp v91, v77, v77 row_ror:8 row_mask:0xf bank_mask:0x3\n"
  "v_add_f32_dpp v89, v75, v75 row_ror:8 row_mask:0xf bank_mask:0xc\n"
  "v_add_f32_dpp v92, v88, v88 row_shl:4 row_mask:0xf bank_mask:0x5\n"
  "v_add_f32_dpp v91, v79, v79 row_ror:8 row_mask:0xf bank_mask:0xc\n"
  "v_add_f32_dpp v93, v90, v90 row_shl:4 row_mask:0xf bank_mask:0x5\n"
  "v_add_f32_dpp v92, v89, v89 row_shr:4 row_mask:0xf bank_mask:0xa\n"
  "ds_read_b64_tr_b16 v[176:177], v234 offset:0\n"
  "v_add_f32_dpp v93, v91, v91 row_shr:4 row_mask:0xf bank_mask:0xa\n"
  "v_add_f32_dpp v92, v92, v92 quad_perm:[1,0,3,2] row_mask:0xf bank_mask:0xf\n"
  "ds_read_b64_tr_b16 v[178:179], v234 offset:2048\n"
  "v_add_f32_dpp v93, v93, v93 quad_perm:[1,0,3,2] row_mask:0xf bank_mask:0xf\n"
  "v_add_f32_dpp v92, v92, v92 quad_perm:[2,3,0,1] row_mask:0xf bank_mask:0xf\n"
  "v_min_u32 v92, -2, v92\n"
  "v_add_f32_dpp v93, v93, v93 quad_perm:[2,3,0,1] row_mask:0xf bank_mask:0xf\n"
  "v_min_u32 v93, -2, v93\n"
  "buffer_store_dwordx2 v[92:93], v251, s[20:23], 0 offen sc1\n"
  "s_waitcnt vmcnt(5) lgkmcnt(2)\n"
  "ds_read_b64_tr_b16 a[16:17], v234 offset:8192\n"
  "ds_read_b64_tr_b16 a[18:19], v234 offset:10240\n"
  "ds_read_b64_tr_b16 a[20:21], v235 offset:8192\n"
  "ds_read_b64_tr_b16 a[22:23], v235 offset:10240\n"
  "ds_read_b64_tr_b16 a[24:25], v236 offset:8192\n"
  "v_mov_b32 v84, v47\n"
  "ds_read_b64_tr_b16 a[26:27], v236 offset:10240\n"
  "v_mov_b32 v85, v47\n"
  "ds_read_b64_tr_b16 a[28:29], v237 offset:8192\n"
  "v_mov_b32 v86, v47\n"
  "ds_read_b64_tr_b16 a[30:31], v237 offset:10240\n"
  "v_mov_b32 v87, v47\n"
  "s_mul_i32 s87, s62, 98304\n"
  "s_add_u32 s87, s87, s8\n"
  "v_mfma_f32_16x16x16_bf16 v[84:87], v[28:29], v[40:41], v[84:87]\n"
  "s_add_u32 s87, s87, 14680064\n"
  "v_add_u32 v187, s87, v1\n"
  "s_lshl_b32 s87, s58, 17\n"
  "s_lshl_b32 s88, s64, 7\n"
  "s_add_u32 s87, s87, s88\n"
  "v_lshrrev_b32 v184, 3, v0\n"
  "v_lshl_add_u32 v184, v184, 10, s87\n"
  "v_lshlrev_b32 v45, 1, v3\n"
  "v_xor_b32 v45, v45, v0\n"
  "v_and_b32 v45, 7, v45\n"
  "v_lshl_add_u32 v184, v45, 4, v184\n"
  "v_add_u32 v185, 0x10000, v184\n"
  "s_waitcnt vmcnt(1) lgkmcnt(0)\n"
  "ds_read_b64_tr_b16 a[32:33], v234 offset:12288\n"
  "ds_read_b64_tr_b16 a[34:35], v234 offset:14336\n"
  "ds_read_b64_tr_b16 a[36:37], v235 offset:12288\n"
  "ds_read_b64_tr_b16 a[38:39], v235 offset:14336\n"
  "ds_read_b64_tr_b16 a[40:41], v236 offset:12288\n"
  "ds_read_b64_tr_b16 a[42:43], v236 offset:14336\n"
  "ds_read_b64_tr_b16 a[44:45], v237 offset:12288\n"
  "ds_read_b64_tr_b16 a[46:47], v237 offset:14336\n"
  "s_sleep 9\n"
  "buffer_load_dwordx2 v[94:95], v252, s[20:23], 0 offen sc1\n"
  "buffer_load_dwordx2 v[96:97], v252, s[20:23], 0 offen offset:128 sc1\n"
  "s_waitcnt vmcnt(0) lgkmcnt(0)\n"
  "v_max3_u32 v98, v94, v95, v96\n"
  "v_max_u32 v98, v98, v97\n"
  "v_cmp_eq_u32 vcc, -1, v98\n"
  "s_cmp_eq_u64 vcc, 0\n"
  "s_cbranch_scc0 .Lfb2_%=\n"
  ".Lbk2_%=:\n"
  "v_pk_add_f32 v[94:95], v[94:95], v[96:97]\n"
  "s_lshr_b32 s94, s64, 1\n"
  "s_lshl2_add_u32 s94, s67, s94\n"
  "v_mfma_f32_16x16x4_f32 v[100:103], v35, v94, 0\n"
  "s_and_b32 s95, s64, 1\n"
  "s_lshl2_add_u32 s94, s94, s95\n"
  "s_lshl_b32 s94, s94, 7\n"
  "s_add_u32 s87, s94, 1310720\n"
  "v_lshl_add_u32 v251, v0, 1, s87\n"
  "v_and_b32 v45, 3, v0\n"
  "v_mfma_f32_16x16x4_f32 v[104:107], v35, v95, 0\n"
  "v_lshl_add_u32 v251, v45, 28, v251\n"
  "s_lshl_b32 s87, s67, 11\n"
  "s_nop 0\n"
  "v_mul_f32 v101, s4, v100\n"
  "v_mul_f32 v103, v101, v101\n"
  "s_add_u32 s87, s87, 1310720\n"
  "v_lshl_add_u32 v252, v3, 9, s87\n"
  "v_mfma_f32_16x16x4_f32 v[108:111], v101, v34, 0\n"
  "v_lshl_add_u32 v252, v2, 3, v252\n"
  "s_nop 0\n"
  "v_fma_f32 v105, v104, s4, -v103\n"
  "v_max_f32 v105, 0, v105\n"
  "v_add_f32 v102, s5, v105\n"
  "v_rsq_f32 v102, v102\n"
  "s_nop 0\n"
  "v_mul_f32 v105, v105, v102\n"
  "v_fma_f32 v103, v105, v102, s5\n"
  "v_rsq_f32 v103, v103\n"
  "v_pk_add_f32 v[56:57], v[56:57], v[108:109] neg_lo:[0,1] neg_hi:[0,1]\n"
  "v_mul_f32 v102, v102, v103\n"
  "v_pk_add_f32 v[58:59], v[58:59], v[110:111] neg_lo:[0,1] neg_hi:[0,1]\n"
  "v_pk_add_f32 v[60:61], v[60:61], v[108:109] neg_lo:[0,1] neg_hi:[0,1]\n"
  "s_nop 0\n"
  "v_mfma_f32_16x16x4_f32 v[72:75], v102, v34, 0\n"
  "v_pk_add_f32 v[62:63], v[62:63], v[110:111] neg_lo:[0,1] neg_hi:[0,1]\n"
  "v_pk_add_f32 v[64:65], v[64:65], v[108:109] neg_lo:[0,1] neg_hi:[0,1]\n"
  "v_pk_add_f32 v[66:67], v[66:67], v[110:111] neg_lo:[0,1] neg_hi:[0,1]\n"
  "v_pk_add_f32 v[68:69], v[68:69], v[108:109] neg_lo:[0,1] neg_hi:[0,1]\n"
  "v_pk_add_f32 v[70:71], v[70:71], v[110:111] neg_lo:[0,1] neg_hi:[0,1]\n"
  "s_nop 4\n"
  "v_pk_mul_f32 v[56:57], v[56:57], v[72:73]\n"
  "v_pk_mul_f32 v[58:59], v[58:59], v[74:75]\n"
  "v_pk_mul_f32 v[60:61], v[60:61], v[72:73]\n"
  "v_pk_mul_f32 v[62:63], v[62:63], v[74:75]\n"
  "v_pk_mul_f32 v[64:65], v[64:65], v[72:73]\n"
  "v_pk_mul_f32 v[66:67], v[66:67], v[74:75]\n"
  "v_pk_mul_f32 v[68:69], v[68:69], v[72:73]\n"
  "v_pk_mul_f32 v[70:71], v[70:71], v[74:75]\n"
  "v_cvt_pk_bf16_f32 v88, v56, v57\n"
  "v_cvt_pk_bf16_f32 v89, v58, v59\n"
  "v_cvt_pk_bf16_f32 v90, v60, v61\n"
  "v_cvt_pk_bf16_f32 v91, v62, v63\n"
  "v_cvt_pk_bf16_f32 v92, v64, v65\n"
  "v_cvt_pk_bf16_f32 v93, v66, v67\n"
  "v_cvt_pk_bf16_f32 v94, v68, v69\n"
  "v_cvt_pk_bf16_f32 v95, v70, v71\n"
  "v_permlane16_swap_b32 v88, v90\n"
  "v_permlane16_swap_b32 v89, v91\n"
  "v_permlane16_swap_b32 v92, v94\n"
  "v_permlane16_swap_b32 v93, v95\n"
  "buffer_store_dwordx4 v[88:91], v245, s[20:23], 0 offen sc1\n"
  "buffer_store_dwordx4 v[92:95], v245, s[20:23], s52 offen sc1\n"
  "s_lshl3_add_u32 s87, s67, s64\n"
  "s_add_u32 s18, s18, 2097152\n"
  "s_add_u32 s19, s19, 2097152\n"
  "s_lshl3_add_u32 s88, s63, s64\n"
  "s_lshr_b32 s94, s88, 3\n"
  "s_and_b32 s95, s88, 7\n"
  "s_lshl_b32 s94, s94, 9\n"
  "s_lshl3_add_u32 s94, s95, s94\n"
  "s_add_u32 s14, s94, 65536\n"
  "s_lshr_b32 s94, s64, 1\n"
  "s_lshl2_add_u32 s94, s67, s94\n"
  "s_and_b32 s95, s64, 1\n"
  "s_lshl2_add_u32 s94, s94, s95\n"
  "s_lshl_b32 s94, s94, 7\n"
  "s_add_u32 s87, s94, 1835008\n"
  "v_lshl_add_u32 v245, v2, 3, s87\n"
  "v_bfe_u32 v45, v0, 4, 1\n"
  "v_lshl_add_u32 v245, v45, 2, v245\n"
  "v_lshrrev_b32 v45, 5, v0\n"
  "v_lshl_add_u32 v245, v45, 30, v245\n"
  "s_waitcnt vmcnt(0)\n"
  "s_barrier\n"
  "buffer_store_dwordx2 v[36:37], v248, s[20:23], s12 offen sc1\n"
  "s_and_b32 s87, s68, 3\n"
  "s_lshl2_add_u32 s87, s87, s58\n"
  "s_lshl3_add_u32 s87, s87, 0\n"
  "s_bfe_u32 s89, s99, 0x20001\n"
  "s_and_b32 s90, s99, 1\n"
  "s_lshr_b32 s88, s87, 5\n"
  "s_mul_i32 s88, s88, 448\n"
  "s_and_b32 s87, s87, 31\n"
  "s_add_u32 s87, s87, s88\n"
  "s_mul_i32 s87, s87, 4096\n"
  "s_mul_i32 s89, s89, 1024\n"
  "s_add_u32 s87, s87, s89\n"
  "v_lshrrev_b32 v45, 3, v0\n"
  "v_add_u32 v45, 21, v45\n"
  "v_min_u32 v45, 26, v45\n"
  "v_subrev_u32 v46, 14, v45\n"
  "v_min_u32 v45, v45, v46\n"
  "v_mul_u32_u24 v45, 131072, v45\n"
  "v_and_b32 v46, 7, v0\n"
  "v_min_u32 v46, 7, v46\n"
  "v_lshl_add_u32 v45, v46, 7, v45\n"
  "v_add_u32 v45, s87, v45\n"
  "buffer_load_dword v44, v45, s[40:43], 0 offen\n"
  "s_and_b32 s87, s68, 3\n"
  "s_lshl2_add_u32 s87, s87, s58\n"
  "s_lshl3_add_u32 s87, s87, 1\n"
  "s_bfe_u32 s89, s99, 0x20001\n"
  "s_and_b32 s90, s99, 1\n"
  "s_lshr_b32 s88, s87, 5\n"
  "s_mul_i32 s88, s88, 448\n"
  "s_and_b32 s87, s87, 31\n"
  "s_add_u32 s87, s87, s88\n"
  "s_mul_i32 s87, s87, 4096\n"
  "s_mul_i32 s89, s89, 1024\n"
  "s_add_u32 s87, s87, s89\n"
  "v_lshrrev_b32 v45, 3, v0\n"
  "v_add_u32 v45, 21, v45\n"
  "v_min_u32 v45, 26, v45\n"
  "v_subrev_u32 v46, 14, v45\n"
  "v_min_u32 v45, v45, v46\n"
  "v_mul_u32_u24 v45, 131072, v45\n"
  "v_and_b32 v46, 7, v0\n"
  "v_min_u32 v46, 7, v46\n"
  "v_lshl_add_u32 v45, v46, 7, v45\n"
  "v_add_u32 v45, s87, v45\n"
  "buffer_load_dword v44, v45, s[40:43], 0 offen\n"
  "s_and_b32 s87, s68, 3\n"
  "s_lshl2_add_u32 s87, s87, s58\n"
  "s_lshl3_add_u32 s87, s87, 2\n"
  "s_bfe_u32 s89, s99, 0x20001\n"
  "s_and_b32 s90, s99, 1\n"
  "s_lshr_b32 s88, s87, 5\n"
  "s_mul_i32 s88, s88, 448\n"
  "s_and_b32 s87, s87, 31\n"
  "s_add_u32 s87, s87, s88\n"
  "s_mul_i32 s87, s87, 4096\n"
  "s_mul_i32 s89, s89, 1024\n"
  "s_add_u32 s87, s87, s89\n"
  "v_lshrrev_b32 v45, 3, v0\n"
  "v_add_u32 v45, 21, v45\n"
  "v_min_u32 v45, 26, v45\n"
  "v_subrev_u32 v46, 14, v45\n"
  "v_min_u32 v45, v45, v46\n"
  "v_mul_u32_u24 v45, 131072, v45\n"
  "v_and_b32 v46, 7, v0\n"
  "v_min_u32 v46, 7, v46\n"
  "v_lshl_add_u32 v45, v46, 7, v45\n"
  "v_add_u32 v45, s87, v45\n"
  "buffer_load_dword v44, v45, s[40:43], 0 offen\n"
  "s_and_b32 s87, s68, 3\n"
  "s_lshl2_add_u32 s87, s87, s58\n"
  "s_lshl3_add_u32 s87, s87, 3\n"
  "s_bfe_u32 s89, s99, 0x20001\n"
  "s_and_b32 s90, s99, 1\n"
  "s_lshr_b32 s88, s87, 5\n"
  "s_mul_i32 s88, s88, 448\n"
  "s_and_b32 s87, s87, 31\n"
  "s_add_u32 s87, s87, s88\n"
  "s_mul_i32 s87, s87, 4096\n"
  "s_mul_i32 s89, s89, 1024\n"
  "s_add_u32 s87, s87, s89\n"
  "v_lshrrev_b32 v45, 3, v0\n"
  "v_add_u32 v45, 21, v45\n"
  "v_min_u32 v45, 26, v45\n"
  "v_subrev_u32 v46, 14, v45\n"
  "v_min_u32 v45, v45, v46\n"
  "v_mul_u32_u24 v45, 131072, v45\n"
  "v_and_b32 v46, 7, v0\n"
  "v_min_u32 v46, 7, v46\n"
  "v_lshl_add_u32 v45, v46, 7, v45\n"
  "v_add_u32 v45, s87, v45\n"
  "buffer_load_dword v44, v45, s[40:43], 0 offen\n"
  "s_and_b32 s87, s68, 3\n"
  "s_lshl2_add_u32 s87, s87, s58\n"
  "s_lshl3_add_u32 s87, s87, 4\n"
  "s_bfe_u32 s89, s99, 0x20001\n"
  "s_and_b32 s90, s99, 1\n"
  "s_lshr_b32 s88, s87, 5\n"
  "s_mul_i32 s88, s88, 448\n"
  "s_and_b32 s87, s87, 31\n"
  "s_add_u32 s87, s87, s88\n"
  "s_mul_i32 s87, s87, 4096\n"
  "s_mul_i32 s89, s89, 1024\n"
  "s_add_u32 s87, s87, s89\n"
  "v_lshrrev_b32 v45, 3, v0\n"
  "v_add_u32 v45, 21, v45\n"
  "v_min_u32 v45, 26, v45\n"
  "v_subrev_u32 v46, 14, v45\n"
  "v_min_u32 v45, v45, v46\n"
  "v_mul_u32_u24 v45, 131072, v45\n"
  "v_and_b32 v46, 7, v0\n"
  "v_min_u32 v46, 7, v46\n"
  "v_lshl_add_u32 v45, v46, 7, v45\n"
  "v_add_u32 v45, s87, v45\n"
  "buffer_load_dword v44, v45, s[40:43], 0 offen\n"
  "s_and_b32 s87, s68, 3\n"
  "s_lshl2_add_u32 s87, s87, s58\n"
  "s_lshl3_add_u32 s87, s87, 5\n"
  "s_bfe_u32 s89, s99, 0x20001\n"
  "s_and_b32 s90, s99, 1\n"
  "s_lshr_b32 s88, s87, 5\n"
  "s_mul_i32 s88, s88, 448\n"
  "s_and_b32 s87, s87, 31\n"
  "s_add_u32 s87, s87, s88\n"
  "s_mul_i32 s87, s87, 4096\n"
  "s_mul_i32 s89, s89, 1024\n"
  "s_add_u32 s87, s87, s89\n"
  "v_lshrrev_b32 v45, 3, v0\n"
  "v_add_u32 v45, 21, v45\n"
  "v_min_u32 v45, 26, v45\n"
  "v_subrev_u32 v46, 14, v45\n"
  "v_min_u32 v45, v45, v46\n"
  "v_mul_u32_u24 v45, 131072, v45\n"
  "v_and_b32 v46, 7, v0\n"
  "v_min_u32 v46, 7, v46\n"
  "v_lshl_add_u32 v45, v46, 7, v45\n"
  "v_add_u32 v45, s87, v45\n"
  "buffer_load_dword v44, v45, s[40:43], 0 offen\n"
  "s_and_b32 s87, s68, 3\n"
  "s_lshl2_add_u32 s87, s87, s58\n"
  "s_lshl3_add_u32 s87, s87, 6\n"
  "s_bfe_u32 s89, s99, 0x20001\n"
  "s_and_b32 s90, s99, 1\n"
  "s_lshr_b32 s88, s87, 5\n"
  "s_mul_i32 s88, s88, 448\n"
  "s_and_b32 s87, s87, 31\n"
  "s_add_u32 s87, s87, s88\n"
  "s_mul_i32 s87, s87, 4096\n"
  "s_mul_i32 s89, s89, 1024\n"
  "s_add_u32 s87, s87, s89\n"
  "v_lshrrev_b32 v45, 3, v0\n"
  "v_add_u32 v45, 21, v45\n"
  "v_min_u32 v45, 26, v45\n"
  "v_subrev_u32 v46, 14, v45\n"
  "v_min_u32 v45, v45, v46\n"
  "v_mul_u32_u24 v45, 131072, v45\n"
  "v_and_b32 v46, 7, v0\n"
  "v_min_u32 v46, 7, v46\n"
  "v_lshl_add_u32 v45, v46, 7, v45\n"
  "v_add_u32 v45, s87, v45\n"
  "buffer_load_dword v44, v45, s[40:43], 0 offen\n"
  "s_and_b32 s87, s68, 3\n"
  "s_lshl2_add_u32 s87, s87, s58\n"
  "s_lshl3_add_u32 s87, s87, 7\n"
  "s_bfe_u32 s89, s99, 0x20001\n"
  "s_and_b32 s90, s99, 1\n"
  "s_lshr_b32 s88, s87, 5\n"
  "s_mul_i32 s88, s88, 448\n"
  "s_and_b32 s87, s87, 31\n"
  "s_add_u32 s87, s87, s88\n"
  "s_mul_i32 s87, s87, 4096\n"
  "s_mul_i32 s89, s89, 1024\n"
  "s_add_u32 s87, s87, s89\n"
  "v_lshrrev_b32 v45, 3, v0\n"
  "v_add_u32 v45, 21, v45\n"
  "v_min_u32 v45, 26, v45\n"
  "v_subrev_u32 v46, 14, v45\n"
  "v_min_u32 v45, v45, v46\n"
  "v_mul_u32_u24 v45, 131072, v45\n"
  "v_and_b32 v46, 7, v0\n"
  "v_min_u32 v46, 7, v46\n"
  "v_lshl_add_u32 v45, v46, 7, v45\n"
  "v_add_u32 v45, s87, v45\n"
  "buffer_load_dword v44, v45, s[40:43], 0 offen\n"
  "buffer_load_dwordx2 v[206:207], v246, s[20:23], 0 offen sc1\n"
  "s_add_u32 m0, s8, 0\n"
  "s_mov_b32 s9, 393216\n"
  "buffer_load_dwordx4 v240, s[40:43], s9 offen lds\n"
  "s_add_u32 m0, s8, 1024\n"
  "s_mov_b32 s9, 425984\n"
  "buffer_load_dwordx4 v240, s[40:43], s9 offen lds\n"
  "s_add_u32 m0, s8, 2048\n"
  "s_mov_b32 s9, 458752\n"
  "buffer_load_dwordx4 v240, s[40:43], s9 offen lds\n"
  "s_add_u32 m0, s8, 3072\n"
  "s_mov_b32 s9, 491520\n"
  "buffer_load_dwordx4 v240, s[40:43], s9 offen lds\n"
  "s_add_u32 m0, s8, 4096\n"
  "s_mov_b32 s9, 524288\n"
  "buffer_load_dwordx4 v240, s[40:43], s9 offen lds\n"
  "s_add_u32 m0, s8, 5120\n"
  "s_mov_b32 s9, 557056\n"
  "buffer_load_dwordx4 v240, s[40:43], s9 offen lds\n"
  "s_add_u32 m0, s8, 6144\n"
  "s_mov_b32 s9, 589824\n"
  "buffer_load_dwordx4 v240, s[40:43], s9 offen lds\n"
  "s_add_u32 m0, s8, 7168\n"
  "s_mov_b32 s9, 622592\n"
  "buffer_load_dwordx4 v240, s[40:43], s9 offen lds\n"
  "s_waitcnt vmcnt(8)\n"
  "v_cmp_ne_u64 vcc, s[56:57], v[206:207]\n"
  "s_cmp_eq_u64 vcc, 0\n"
  "s_cbranch_scc0 .Lfb3_%=\n"
  ".Lbk3_%=:\n"
  "s_barrier\n"
  "s_add_u32 m0, s8, 8192\n"
  "s_mov_b32 s9, 655360\n"
  "buffer_load_dwordx4 v240, s[40:43], s9 offen lds\n"
  "s_add_u32 m0, s8, 9216\n"
  "s_mov_b32 s9, 688128\n"
  "buffer_load_dwordx4 v240, s[40:43], s9 offen lds\n"
  "s_waitcnt vmcnt(6)\n"
  "ds_read_b64_tr_b16 a[48:49], v234 offset:0\n"
  "ds_read_b64_tr_b16 a[50:51], v234 offset:2048\n"
  "ds_read_b64_tr_b16 a[52:53], v235 offset:0\n"
  "ds_read_b64_tr_b16 a[54:55], v235 offset:2048\n"
  "ds_read_b64_tr_b16 a[56:57], v236 offset:0\n"
  "ds_read_b64_tr_b16 a[58:59], v236 offset:2048\n"
  "ds_read_b64_tr_b16 a[60:61], v237 offset:0\n"
  "ds_read_b64_tr_b16 a[62:63], v237 offset:2048\n"
  "s_add_u32 m0, s8, 10240\n"
  "s_mov_b32 s9, 720896\n"
  "buffer_load_dwordx4 v240, s[40:43], s9 offen lds\n"
  "s_add_u32 m0, s8, 11264\n"
  "s_mov_b32 s9, 753664\n"
  "buffer_load_dwordx4 v240, s[40:43], s9 offen lds\n"
  "s_add_u32 m0, s8, 12288\n"
  "s_mov_b32 s9, 786432\n"
  "buffer_load_dwordx4 v240, s[40:43], s9 offen lds\n"
  "s_add_u32 m0, s8, 13312\n"
  "s_mov_b32 s9, 819200\n"
  "buffer_load_dwordx4 v240, s[40:43], s9 offen lds\n"
  "s_waitcnt vmcnt(6) lgkmcnt(0)\n"
  "ds_read_b64_tr_b16 a[64:65], v234 offset:4096\n"
  "ds_read_b64_tr_b16 a[66:67], v234 offset:6144\n"
  "ds_read_b64_tr_b16 a[68:69], v235 offset:4096\n"
  "ds_read_b64_tr_b16 a[70:71], v235 offset:6144\n"
  "ds_read_b64_tr_b16 a[72:73], v236 offset:4096\n"
  "ds_read_b64_tr_b16 a[74:75], v236 offset:6144\n"
  "ds_read_b64_tr_b16 a[76:77], v237 offset:4096\n"
  "ds_read_b64_tr_b16 a[78:79], v237 offset:6144\n"
  "s_add_u32 m0, s8, 14336\n"
  "s_mov_b32 s9, 851968\n"
  "buffer_load_dwordx4 v240, s[40:43], s9 offen lds\n"
  "s_add_u32 m0, s8, 15360\n"
  "s_mov_b32 s9, 884736\n"
  "buffer_load_dwordx4 v240, s[40:43], s9 offen lds\n"
  "s_add_u32 m0, s8, 0\n"
  "s_mov_b32 s9, 917504\n"
  "buffer_load_dwordx4 v240, s[40:43], s9 offen lds\n"
  "s_add_u32 m0, s8, 1024\n"
  "s_mov_b32 s9, 950272\n"
  "buffer_load_dwordx4 v240, s[40:43], s9 offen lds\n"
  "s_waitcnt vmcnt(6) lgkmcnt(0)\n"
  "ds_read_b64_tr_b16 a[80:81], v234 offset:8192\n"
  "ds_read_b64_tr_b16 a[82:83], v234 offset:10240\n"
  "ds_read_b64_tr_b16 a[84:85], v235 offset:8192\n"
  "ds_read_b64_tr_b16 a[86:87], v235 offset:10240\n"
  "ds_read_b64_tr_b16 a[88:89], v236 offset:8192\n"
  "ds_read_b64_tr_b16 a[90:91], v236 offset:10240\n"
  "ds_read_b64_tr_b16 a[92:93], v237 offset:8192\n"
  "ds_read_b64_tr_b16 a[94:95], v237 offset:10240\n"
  "s_add_u32 m0, s8, 2048\n"
  "s_mov_b32 s9, 983040\n"
  "buffer_load_dwordx4 v240, s[40:43], s9 offen lds\n"
  "s_add_u32 m0, s8, 3072\n"
  "s_mov_b32 s9, 1015808\n"
  "buffer_load_dwordx4 v240, s[40:43], s9 offen lds\n"
  "s_add_u32 m0, s8, 4096\n"
  "s_mov_b32 s9, 1048576\n"
  "buffer_load_dwordx4 v240, s[40:43], s9 offen lds\n"
  "s_add_u32 m0, s8, 5120\n"
  "s_mov_b32 s9, 1081344\n"
  "buffer_load_dwordx4 v240, s[40:43], s9 offen lds\n"
  "s_waitcnt vmcnt(6) lgkmcnt(0)\n"
  "ds_read_b64_tr_b16 a[96:97], v234 offset:12288\n"
  "ds_read_b64_tr_b16 a[98:99], v234 offset:14336\n"
  "ds_read_b64_tr_b16 a[100:101], v235 offset:12288\n"
  "ds_read_b64_tr_b16 a[102:103], v235 offset:14336\n"
  "ds_read_b64_tr_b16 a[104:105], v236 offset:12288\n"
  "ds_read_b64_tr_b16 a[106:107], v236 offset:14336\n"
  "ds_read_b64_tr_b16 a[108:109], v237 offset:12288\n"
  "ds_read_b64_tr_b16 a[110:111], v237 offset:14336\n"
  "s_add_u32 m0, s8, 6144\n"
  "s_mov_b32 s9, 1114112\n"
  "buffer_load_dwordx4 v240, s[40:43], s9 offen lds\n"
  "s_waitcnt vmcnt(3) lgkmcnt(0)\n"
  "ds_read_b64_tr_b16 a[112:113], v234 offset:0\n"
  "ds_read_b64_tr_b16 a[114:115], v234 offset:2048\n"
  "ds_read_b64_tr_b16 a[116:117], v235 offset:0\n"
  "ds_read_b64_tr_b16 a[118:119], v235 offset:2048\n"
  "ds_read_b64_tr_b16 a[120:121], v236 offset:0\n"
  "ds_read_b64_tr_b16 a[122:123], v236 offset:2048\n"
  "ds_read_b64_tr_b16 a[124:125], v237 offset:0\n"
  "buffer_load_dwordx2 v[206:207], v247, s[20:23], 0 offen sc1\n"
  "s_add_u32 m0, s8, 7168\n"
  "s_mov_b32 s9, 1146880\n"
  "buffer_load_dwordx4 v240, s[40:43], s9 offen lds\n"
  "ds_read_b64_tr_b16 a[126:127], v237 offset:2048\n"
  "s_add_u32 m0, s8, 8192\n"
  "s_mov_b32 s9, 1179648\n"
  "buffer_load_dwordx4 v240, s[40:43], s9 offen lds\n"
  "s_add_u32 m0, s8, 9216\n"
  "s_mov_b32 s9, 1212416\n"
  "buffer_load_dwordx4 v240, s[40:43], s9 offen lds\n"
  "s_add_u32 m0, s8, 10240\n"
  "s_mov_b32 s9, 1245184\n"
  "buffer_load_dwordx4 v240, s[40:43], s9 offen lds\n"
  "s_add_u32 m0, s8, 11264\n"
  "s_mov_b32 s9, 1277952\n"
  "buffer_load_dwordx4 v240, s[40:43], s9 offen lds\n"
  "s_add_u32 m0, s8, 12288\n"
  "s_mov_b32 s9, 1310720\n"
  "buffer_load_dwordx4 v240, s[40:43], s9 offen lds\n"
  "s_waitcnt vmcnt(6) lgkmcnt(0)\n"
  "v_cmp_ne_u64 vcc, s[56:57], v[206:207]\n"
  "s_cmp_eq_u64 vcc, 0\n"
  "s_cbranch_scc0 .Lfb4_%=\n"
  ".Lbk4_%=:\n"
  "buffer_load_dwordx4 v[112:115], v232, s[20:23], 0 offen sc1\n"
  "s_waitcnt vmcnt(6)\n"
  "ds_read_b64_tr_b16 a[128:129], v234 offset:4096\n"
  "ds_read_b64_tr_b16 a[130:131], v234 offset:6144\n"
  "ds_read_b64_tr_b16 a[132:133], v235 offset:4096\n"
  "ds_read_b64_tr_b16 a[134:135], v235 offset:6144\n"
  "ds_read_b64_tr_b16 a[136:137], v236 offset:4096\n"
  "ds_read_b64_tr_b16 a[138:139], v236 offset:6144\n"
  "buffer_load_dwordx4 v[116:119], v232, s[20:23], 0 offen offset:1024 sc1\n"
  "ds_read_b64_tr_b16 a[140:141], v237 offset:4096\n"
  "ds_read_b64_tr_b16 a[142:143], v237 offset:6144\n"
  "buffer_load_dwordx4 v[120:123], v232, s[20:23], 0 offen offset:2048 sc1\n"
  "buffer_load_dwordx4 v[124:127], v232, s[20:23], 0 offen offset:3072 sc1\n"
  "buffer_load_dwordx4 v[128:131], v232, s[20:23], s72 offen sc1\n"
  "buffer_load_dwordx4 v[132:135], v232, s[20:23], s72 offen offset:1024 sc1\n"
  "buffer_load_dwordx4 v[136:139], v232, s[20:23], s72 offen offset:2048 sc1\n"
  "buffer_load_dwordx4 v[140:143], v232, s[20:23], s72 offen offset:3072 sc1\n"
  "buffer_load_dwordx4 v[144:147], v232, s[20:23], s73 offen sc1\n"
  "buffer_load_dwordx4 v[148:151], v232, s[20:23], s73 offen offset:1024 sc1\n"
  "s_waitcnt vmcnt(11) lgkmcnt(0)\n"
  "ds_read_b64_tr_b16 a[144:145], v234 offset:8192\n"
  "ds_read_b64_tr_b16 a[146:147], v234 offset:10240\n"
  "ds_read_b64_tr_b16 a[148:149], v235 offset:8192\n"
  "ds_read_b64_tr_b16 a[150:151], v235 offset:10240\n"
  "ds_read_b64_tr_b16 a[152:153], v236 offset:8192\n"
  "ds_read_b64_tr_b16 a[154:155], v236 offset:10240\n"
  "ds_read_b64_tr_b16 a[156:157], v237 offset:8192\n"
  "ds_read_b64_tr_b16 a[158:159], v237 offset:10240\n"
  "buffer_load_dwordx4 v[152:155], v232, s[20:23], s73 offen offset:2048 sc1\n"
  "buffer_load_dwordx4 v[156:159], v232, s[20:23], s73 offen offset:3072 sc1\n"
  "buffer_load_dwordx4 v[160:163], v232, s[20:23], s74 offen sc1\n"
  "s_waitcnt vmcnt(12) lgkmcnt(0)\n"
  "v_mfma_f32_16x16x32_bf16 v[48:51], v[112:115], a[0:3], v[48:51]\n"
  "v_mfma_f32_16x16x32_bf16 v[56:59], v[112:115], a[4:7], 0\n"
  "v_mfma_f32_16x16x32_bf16 v[64:67], v[112:115], a[8:11], 0\n"
  "buffer_load_dwordx4 v[164:167], v232, s[20:23], s74 offen offset:1024 sc1\n"
  "v_mfma_f32_16x16x32_bf16 v[72:75], v[112:115], a[12:15], 0\n"
  "s_waitcnt vmcnt(12)\n"
  "v_mfma_f32_16x16x32_bf16 v[52:55], v[116:119], a[0:3], v[52:55]\n"
  "v_mfma_f32_16x16x32_bf16 v[60:63], v[116:119], a[4:7], 0\n"
  "v_mfma_f32_16x16x32_bf16 v[68:71], v[116:119], a[8:11], 0\n"
  "buffer_load_dwordx4 v[168:171], v232, s[20:23], s74 offen offset:2048 sc1\n"
  "v_mfma_f32_16x16x32_bf16 v[76:79], v[116:119], a[12:15], 0\n"
  "s_waitcnt vmcnt(12)\n"
  "v_mfma_f32_16x16x32_bf16 v[48:51], v[120:123], a[16:19], v[48:51]\n"
  "v_mfma_f32_16x16x32_bf16 v[56:59], v[120:123], a[20:23], v[56:59]\n"
  "buffer_load_dwordx4 v[172:175], v232, s[20:23], s74 offen offset:3072 sc1\n"
  "v_mfma_f32_16x16x32_bf16 v[64:67], v[120:123], a[24:27], v[64:67]\n"
  "v_mfma_f32_16x16x32_bf16 v[72:75], v[120:123], a[28:31], v[72:75]\n"
  "s_waitcnt vmcnt(12)\n"
  "v_mfma_f32_16x16x32_bf16 v[52:55], v[124:127], a[16:19], v[52:55]\n"
  "v_mfma_f32_16x16x32_bf16 v[60:63], v[124:127], a[20:23], v[60:63]\n"
  "buffer_load_dwordx4 v[180:183], v232, s[20:23], s75 offen sc1\n"
  "v_mfma_f32_16x16x32_bf16 v[68:71], v[124:127], a[24:27], v[68:71]\n"
  "v_mfma_f32_16x16x32_bf16 v[76:79], v[124:127], a[28:31], v[76:79]\n"
  "s_waitcnt vmcnt(12)\n"
  "v_mfma_f32_16x16x32_bf16 v[48:51], v[128:131], a[32:35], v[48:51]\n"
  "v_mfma_f32_16x16x32_bf16 v[56:59], v[128:131], a[36:39], v[56:59]\n"
  "buffer_load_dwordx4 v[188:191], v232, s[20:23], s75 offen offset:1024 sc1\n"
  "v_mfma_f32_16x16x32_bf16 v[64:67], v[128:131], a[40:43], v[64:67]\n"
  "v_mfma_f32_16x16x32_bf16 v[72:75], v[128:131], a[44:47], v[72:75]\n"
  "s_waitcnt vmcnt(12)\n"
  "v_mfma_f32_16x16x32_bf16 v[52:55], v[132:135], a[32:35], v[52:55]\n"
  "v_mfma_f32_16x16x32_bf16 v[60:63], v[132:135], a[36:39], v[60:63]\n"
  "buffer_load_dwordx4 v[112:115], v232, s[20:23], s75 offen offset:2048 sc1\n"
  "v_mfma_f32_16x16x32_bf16 v[68:71], v[132:135], a[40:43], v[68:71]\n"
  "v_mfma_f32_16x16x32_bf16 v[76:79], v[132:135], a[44:47], v[76:79]\n"
  "s_waitcnt vmcnt(12)\n"
  "v_mfma_f32_16x16x32_bf16 v[48:51], v[136:139], a[48:51], v[48:51]\n"
  "buffer_load_dwordx4 v[116:119], v232, s[20:23], s75 offen offset:3072 sc1\n"
  "v_mfma_f32_16x16x32_bf16 v[56:59], v[136:139], a[52:55], v[56:59]\n"
  "buffer_load_dwordx2 v[206:207], v247, s[20:23], 0 offen offset:128 sc1\n"
  "v_mfma_f32_16x16x32_bf16 v[64:67], v[136:139], a[56:59], v[64:67]\n"
  "v_mfma_f32_16x16x32_bf16 v[72:75], v[136:139], a[60:63], v[72:75]\n"
  "s_waitcnt vmcnt(13)\n"
  "v_mfma_f32_16x16x32_bf16 v[52:55], v[140:143], a[48:51], v[52:55]\n"
  "v_mfma_f32_16x16x32_bf16 v[60:63], v[140:143], a[52:55], v[60:63]\n"
  "v_mfma_f32_16x16x32_bf16 v[68:71], v[140:143], a[56:59], v[68:71]\n"
  "v_mfma_f32_16x16x32_bf16 v[76:79], v[140:143], a[60:63], v[76:79]\n"
  "s_waitcnt vmcnt(12)\n"
  "v_mfma_f32_16x16x32_bf16 v[48:51], v[144:147], a[64:67], v[48:51]\n"
  "s_add_u32 m0, s8, 13312\n"
  "s_mov_b32 s9, 1343488\n"
  "buffer_load_dwordx4 v240, s[40:43], s9 offen lds\n"
  "v_mfma_f32_16x16x32_bf16 v[56:59], v[144:147], a[68:71], v[56:59]\n"
  "v_mfma_f32_16x16x32_bf16 v[64:67], v[144:147], a[72:75], v[64:67]\n"
  "v_mfma_f32_16x16x32_bf16 v[72:75], v[144:147], a[76:79], v[72:75]\n"
  "s_waitcnt vmcnt(12)\n"
  "v_mfma_f32_16x16x32_bf16 v[52:55], v[148:151], a[64:67], v[52:55]\n"
  "v_mfma_f32_16x16x32_bf16 v[60:63], v[148:151], a[68:71], v[60:63]\n"
  "v_mfma_f32_16x16x32_bf16 v[68:71], v[148:151], a[72:75], v[68:71]\n"
  "v_mfma_f32_16x16x32_bf16 v[76:79], v[148:151], a[76:79], v[76:79]\n"
  "s_waitcnt vmcnt(11)\n"
  "v_mfma_f32_16x16x32_bf16 v[48:51], v[152:155], a[80:83], v[48:51]\n"
  "s_add_u32 m0, s8, 14336\n"
  "s_mov_b32 s9, 1376256\n"
  "buffer_load_dwordx4 v240, s[40:43], s9 offen lds\n"
  "v_mfma_f32_16x16x32_bf16 v[56:59], v[152:155], a[84:87], v[56:59]\n"
  "v_mfma_f32_16x16x32_bf16 v[64:67], v[152:155], a[88:91], v[64:67]\n"
  "v_mfma_f32_16x16x32_bf16 v[72:75], v[152:155], a[92:95], v[72:75]\n"
  "s_waitcnt vmcnt(11)\n"
  "v_mfma_f32_16x16x32_bf16 v[52:55], v[156:159], a[80:83], v[52:55]\n"
  "v_mfma_f32_16x16x32_bf16 v[60:63], v[156:159], a[84:87], v[60:63]\n"
  "v_mfma_f32_16x16x32_bf16 v[68:71], v[156:159], a[88:91], v[68:71]\n"
  "v_mfma_f32_16x16x32_bf16 v[76:79], v[156:159], a[92:95], v[76:79]\n"
  "s_waitcnt vmcnt(10)\n"
  "v_mfma_f32_16x16x32_bf16 v[48:51], v[160:163], a[96:99], v[48:51]\n"
  "s_add_u32 m0, s8, 15360\n"
  "s_mov_b32 s9, 1409024\n"
  "buffer_load_dwordx4 v240, s[40:43], s9 offen lds\n"
  "v_mfma_f32_16x16x32_bf16 v[56:59], v[160:163], a[100:103], v[56:59]\n"
  "v_mfma_f32_16x16x32_bf16 v[64:67], v[160:163], a[104:107], v[64:67]\n"
  "v_mfma_f32_16x16x32_bf16 v[72:75], v[160:163], a[108:111], v[72:75]\n"
  "s_waitcnt vmcnt(10)\n"
  "v_mfma_f32_16x16x32_bf16 v[52:55], v[164:167], a[96:99], v[52:55]\n"
  "v_mfma_f32_16x16x32_bf16 v[60:63], v[164:167], a[100:103], v[60:63]\n"
  "v_mfma_f32_16x16x32_bf16 v[68:71], v[164:167], a[104:107], v[68:71]\n"
  "v_mfma_f32_16x16x32_bf16 v[76:79], v[164:167], a[108:111], v[76:79]\n"
  "s_waitcnt vmcnt(9)\n"
  "v_mfma_f32_16x16x32_bf16 v[48:51], v[168:171], a[112:115], v[48:51]\n"
  "buffer_load_dwordx4 v[120:123], v232, s[20:23], s76 offen sc1\n"
  "v_mfma_f32_16x16x32_bf16 v[56:59], v[168:171], a[116:119], v[56:59]\n"
  "v_mfma_f32_16x16x32_bf16 v[64:67], v[168:171], a[120:123], v[64:67]\n"
  "v_mfma_f32_16x16x32_bf16 v[72:75], v[168:171], a[124:127], v[72:75]\n"
  "s_waitcnt vmcnt(9)\n"
  "v_mfma_f32_16x16x32_bf16 v[52:55], v[172:175], a[112:115], v[52:55]\n"
  "s_add_u32 m0, s8, 0\n"
  "s_mov_b32 s9, 1441792\n"
  "buffer_load_dwordx4 v240, s[40:43], s9 offen lds\n"
  "v_mfma_f32_16x16x32_bf16 v[60:63], v[172:175], a[116:119], v[60:63]\n"
  "v_mfma_f32_16x16x32_bf16 v[68:71], v[172:175], a[120:123], v[68:71]\n"
  "v_mfma_f32_16x16x32_bf16 v[76:79], v[172:175], a[124:127], v[76:79]\n"
  "s_waitcnt vmcnt(9)\n"
  "v_mfma_f32_16x16x32_bf16 v[48:51], v[180:183], a[128:131], v[48:51]\n"
  "v_mfma_f32_16x16x32_bf16 v[56:59], v[180:183], a[132:135], v[56:59]\n"
  "v_mfma_f32_16x16x32_bf16 v[64:67], v[180:183], a[136:139], v[64:67]\n"
  "v_mfma_f32_16x16x32_bf16 v[72:75], v[180:183], a[140:143], v[72:75]\n"
  "s_waitcnt vmcnt(8)\n"
  "v_mfma_f32_16x16x32_bf16 v[52:55], v[188:191], a[128:131], v[52:55]\n"
  "s_add_u32 m0, s8, 1024\n"
  "s_mov_b32 s9, 1474560\n"
  "buffer_load_dwordx4 v240, s[40:43], s9 offen lds\n"
  "v_mfma_f32_16x16x32_bf16 v[60:63], v[188:191], a[132:135], v[60:63]\n"
  "v_mfma_f32_16x16x32_bf16 v[68:71], v[188:191], a[136:139], v[68:71]\n"
  "v_mfma_f32_16x16x32_bf16 v[76:79], v[188:191], a[140:143], v[76:79]\n"
  "s_waitcnt vmcnt(8)\n"
  "v_mfma_f32_16x16x32_bf16 v[48:51], v[112:115], a[144:147], v[48:51]\n"
  "v_mfma_f32_16x16x32_bf16 v[56:59], v[112:115], a[148:151], v[56:59]\n"
  "s_waitcnt vmcnt(6)\n"
  "v_cmp_ne_u64 vcc, s[56:57], v[206:207]\n"
  "s_cmp_eq_u64 vcc, 0\n"
  "s_cbranch_scc0 .Lfb5_%=\n"
  ".Lbk5_%=:\n"
  "v_mfma_f32_16x16x32_bf16 v[64:67], v[112:115], a[152:155], v[64:67]\n"
  "v_mfma_f32_16x16x32_bf16 v[72:75], v[112:115], a[156:159], v[72:75]\n"
  "s_add_u32 m0, s8, 2048\n"
  "s_mov_b32 s9, 1507328\n"
  "buffer_load_dwordx4 v240, s[40:43], s9 offen lds\n"
  "v_mfma_f32_16x16x32_bf16 v[52:55], v[116:119], a[144:147], v[52:55]\n"
  "v_mfma_f32_16x16x32_bf16 v[60:63], v[116:119], a[148:151], v[60:63]\n"
  "v_mfma_f32_16x16x32_bf16 v[68:71], v[116:119], a[152:155], v[68:71]\n"
  "v_mfma_f32_16x16x32_bf16 v[76:79], v[116:119], a[156:159], v[76:79]\n"
  "s_add_u32 m0, s8, 3072\n"
  "s_mov_b32 s9, 1540096\n"
  "buffer_load_dwordx4 v240, s[40:43], s9 offen lds\n"
  "buffer_load_dwordx4 v[124:127], v232, s[20:23], s76 offen offset:1024 sc1\n"
  "buffer_load_dwordx4 v[128:131], v232, s[20:23], s76 offen offset:2048 sc1\n"
  "s_waitcnt vmcnt(7)\n"
  "ds_read_b64_tr_b16 a[160:161], v234 offset:12288\n"
  "ds_read_b64_tr_b16 a[162:163], v234 offset:14336\n"
  "ds_read_b64_tr_b16 a[164:165], v235 offset:12288\n"
  "ds_read_b64_tr_b16 a[166:167], v235 offset:14336\n"
  "s_add_u32 m0, s8, 4096\n"
  "s_mov_b32 s9, 1572864\n"
  "buffer_load_dwordx4 v240, s[40:43], s9 offen lds\n"
  "ds_read_b64_tr_b16 a[168:169], v236 offset:12288\n"
  "ds_read_b64_tr_b16 a[170:171], v236 offset:14336\n"
  "ds_read_b64_tr_b16 a[172:173], v237 offset:12288\n"
  "ds_read_b64_tr_b16 a[174:175], v237 offset:14336\n"
  "s_waitcnt vmcnt(7) lgkmcnt(6)\n"
  "v_mfma_f32_16x16x32_bf16 v[48:51], v[120:123], a[160:163], v[48:51]\n"
  "s_waitcnt lgkmcnt(4)\n"
  "v_mfma_f32_16x16x32_bf16 v[56:59], v[120:123], a[164:167], v[56:59]\n"
  "s_waitcnt lgkmcnt(2)\n"
  "v_mfma_f32_16x16x32_bf16 v[64:67], v[120:123], a[168:171], v[64:67]\n"
  "s_add_u32 m0, s8, 5120\n"
  "s_mov_b32 s9, 1605632\n"
  "buffer_load_dwordx4 v240, s[40:43], s9 offen lds\n"
  "s_waitcnt lgkmcnt(0)\n"
  "v_mfma_f32_16x16x32_bf16 v[72:75], v[120:123], a[172:175], v[72:75]\n"
  "s_add_u32 m0, s8, 6144\n"
  "s_mov_b32 s9, 1638400\n"
  "buffer_load_dwordx4 v240, s[40:43], s9 offen lds\n"
  "s_add_u32 m0, s8, 7168\n"
  "s_mov_b32 s9, 1671168\n"
  "buffer_load_dwordx4 v240, s[40:43], s9 offen lds\n"
  "buffer_load_dwordx4 v[132:135], v232, s[20:23], s76 offen offset:3072 sc1\n"
  "s_waitcnt vmcnt(7)\n"
  "ds_read_b64_tr_b16 a[176:177], v234 offset:0\n"
  "ds_read_b64_tr_b16 a[178:179], v234 offset:2048\n"
  "ds_read_b64_tr_b16 a[180:181], v235 offset:0\n"
  "ds_read_b64_tr_b16 a[182:183], v235 offset:2048\n"
  "buffer_load_dwordx4 v[136:139], v232, s[20:23], s77 offen sc1\n"
  "ds_read_b64_tr_b16 a[184:185], v236 offset:0\n"
  "ds_read_b64_tr_b16 a[186:187], v236 offset:2048\n"
  "ds_read_b64_tr_b16 a[188:189], v237 offset:0\n"
  "ds_read_b64_tr_b16 a[190:191], v237 offset:2048\n"
  "s_waitcnt vmcnt(7)\n"
  "v_mfma_f32_16x16x32_bf16 v[52:55], v[124:127], a[160:163], v[52:55]\n"
  "v_mfma_f32_16x16x32_bf16 v[60:63], v[124:127], a[164:167], v[60:63]\n"
  "buffer_load_dwordx4 v[140:143], v232, s[20:23], s77 offen offset:1024 sc1\n"
  "v_mfma_f32_16x16x32_bf16 v[68:71], v[124:127], a[168:171], v[68:71]\n"
  "v_mfma_f32_16x16x32_bf16 v[76:79], v[124:127], a[172:175], v[76:79]\n"
  "s_waitcnt vmcnt(7) lgkmcnt(6)\n"
  "v_mfma_f32_16x16x32_bf16 v[48:51], v[128:131], a[176:179], v[48:51]\n"
  "buffer_load_dwordx4 v[144:147], v232, s[20:23], s77 offen offset:2048 sc1\n"
  "s_waitcnt lgkmcnt(4)\n"
  "v_mfma_f32_16x16x32_bf16 v[56:59], v[128:131], a[180:183], v[56:59]\n"
  "s_waitcnt lgkmcnt(2)\n"
  "v_mfma_f32_16x16x32_bf16 v[64:67], v[128:131], a[184:187], v[64:67]\n"
  "s_waitcnt lgkmcnt(0)\n"
  "v_mfma_f32_16x16x32_bf16 v[72:75], v[128:131], a[188:191], v[72:75]\n"
  "buffer_load_dwordx4 v[148:151], v232, s[20:23], s77 offen offset:3072 sc1\n"
  "buffer_load_dwordx4 v[152:155], v233, s[20:23], 0 offen sc1\n"
  "buffer_load_dwordx4 v[156:159], v233, s[20:23], 0 offen offset:1024 sc1\n"
  "buffer_load_dwordx4 v[160:163], v233, s[20:23], 0 offen offset:2048 sc1\n"
  "buffer_load_dwordx4 v[164:167], v233, s[20:23], 0 offen offset:3072 sc1\n"
  "buffer_load_dwordx4 v[168:171], v233, s[20:23], s72 offen sc1\n"
  "buffer_load_dwordx4 v[172:175], v233, s[20:23], s72 offen offset:1024 sc1\n"
  "buffer_load_dwordx4 v[180:183], v233, s[20:23], s72 offen offset:2048 sc1\n"
  "s_waitcnt vmcnt(12)\n"
  "ds_read_b64_tr_b16 a[192:193], v234 offset:4096\n"
  "ds_read_b64_tr_b16 a[194:195], v234 offset:6144\n"
  "ds_read_b64_tr_b16 a[196:197], v235 offset:4096\n"
  "ds_read_b64_tr_b16 a[198:199], v235 offset:6144\n"
  "ds_read_b64_tr_b16 a[200:201], v236 offset:4096\n"
  "ds_read_b64_tr_b16 a[202:203], v236 offset:6144\n"
  "buffer_load_dwordx4 v[188:191], v233, s[20:23], s72 offen offset:3072 sc1\n"
  "ds_read_b64_tr_b16 a[204:205], v237 offset:4096\n"
  "ds_read_b64_tr_b16 a[206:207], v237 offset:6144\n"
  "s_waitcnt vmcnt(12)\n"
  "v_mfma_f32_16x16x32_bf16 v[52:55], v[132:135], a[176:179], v[52:55]\n"
  "v_mfma_f32_16x16x32_bf16 v[60:63], v[132:135], a[180:183], v[60:63]\n"
  "v_mfma_f32_16x16x32_bf16 v[68:71], v[132:135], a[184:187], v[68:71]\n"
  "buffer_load_dwordx4 v[112:115], v233, s[20:23], s73 offen sc1\n"
  "v_mfma_f32_16x16x32_bf16 v[76:79], v[132:135], a[188:191], v[76:79]\n"
  "s_waitcnt vmcnt(12) lgkmcnt(6)\n"
  "v_mfma_f32_16x16x32_bf16 v[48:51], v[136:139], a[192:195], v[48:51]\n"
  "s_waitcnt lgkmcnt(4)\n"
  "v_mfma_f32_16x16x32_bf16 v[56:59], v[136:139], a[196:199], v[56:59]\n"
  "buffer_load_dwordx4 v[116:119], v233, s[20:23], s73 offen offset:1024 sc1\n"
  "s_waitcnt lgkmcnt(2)\n"
  "v_mfma_f32_16x16x32_bf16 v[64:67], v[136:139], a[200:203], v[64:67]\n"
  "s_waitcnt lgkmcnt(0)\n"
  "v_mfma_f32_16x16x32_bf16 v[72:75], v[136:139], a[204:207], v[72:75]\n"
  "s_waitcnt vmcnt(12)\n"
  "v_mfma_f32_16x16x32_bf16 v[52:55], v[140:143], a[192:195], v[52:55]\n"
  "v_mfma_f32_16x16x32_bf16 v[60:63], v[140:143], a[196:199], v[60:63]\n"
  "buffer_load_dwordx4 v[120:123], v233, s[20:23], s73 offen offset:2048 sc1\n"
  "v_mfma_f32_16x16x32_bf16 v[68:71], v[140:143], a[200:203], v[68:71]\n"
  "v_mfma_f32_16x16x32_bf16 v[76:79], v[140:143], a[204:207], v[76:79]\n"
  "s_waitcnt vmcnt(12)\n"
  "v_mfma_f32_16x16x32_bf16 v[48:51], v[144:147], v[176:179], v[48:51]\n"
  "buffer_load_dwordx4 v[124:127], v233, s[20:23], s73 offen offset:3072 sc1\n"
  "v_mfma_f32_16x16x32_bf16 v[56:59], v[144:147], v[200:203], v[56:59]\n"
  "v_mfma_f32_16x16x32_bf16 v[64:67], v[144:147], v[196:199], v[64:67]\n"
  "v_mfma_f32_16x16x32_bf16 v[72:75], v[144:147], v[192:195], v[72:75]\n"
  "s_nop 5\n"
  "ds_write_b64 v242, v[56:57] offset:0\n"
  "s_waitcnt vmcnt(12)\n"
  "v_mfma_f32_16x16x32_bf16 v[52:55], v[148:151], v[176:179], v[52:55]\n"
  "ds_write_b64 v242, v[58:59] offset:8\n"
  "v_mfma_f32_16x16x32_bf16 v[60:63], v[148:151], v[200:203], v[60:63]\n"
  "buffer_load_dwordx4 v[128:131], v233, s[20:23], s74 offen sc1\n"
  "v_mfma_f32_16x16x32_bf16 v[68:71], v[148:151], v[196:199], v[68:71]\n"
  "ds_write_b64 v243, v[64:65] offset:0\n"
  "v_mfma_f32_16x16x32_bf16 v[76:79], v[148:151], v[192:195], v[76:79]\n"
  "s_nop 3\n"
  "ds_write_b64 v242, v[60:61] offset:1024\n"
  "s_waitcnt vmcnt(12)\n"
  "v_mfma_f32_16x16x32_bf16 v[80:83], v[152:155], a[0:3], v[80:83]\n"
  "ds_write_b64 v242, v[62:63] offset:1032\n"
  "v_mfma_f32_16x16x32_bf16 v[88:91], v[152:155], a[4:7], 0\n"
  "buffer_load_dwordx4 v[132:135], v233, s[20:23], s74 offen offset:1024 sc1\n"
  "ds_write_b64 v243, v[66:67] offset:8\n"
  "v_mfma_f32_16x16x32_bf16 v[96:99], v[152:155], a[8:11], 0\n"
  "ds_write_b64 v243, v[68:69] offset:1024\n"
  "v_mfma_f32_16x16x32_bf16 v[104:107], v[152:155], a[12:15], 0\n"
  "buffer_load_dwordx4 v[136:139], v233, s[20:23], s74 offen offset:2048 sc1\n"
  "ds_write_b64 v243, v[70:71] offset:1032\n"
  "s_waitcnt vmcnt(13) lgkmcnt(6)\n"
  "v_mfma_f32_16x16x32_bf16 v[84:87], v[156:159], a[0:3], v[84:87]\n"
  "ds_write_b64 v244, v[72:73] offset:0\n"
  "v_mfma_f32_16x16x32_bf16 v[92:95], v[156:159], a[4:7], 0\n"
  "ds_write_b64 v244, v[74:75] offset:8\n"
  "v_mfma_f32_16x16x32_bf16 v[100:103], v[156:159], a[8:11], 0\n"
  "ds_write_b64 v244, v[76:77] offset:1024\n"
  "v_mfma_f32_16x16x32_bf16 v[108:111], v[156:159], a[12:15], 0\n"
  "buffer_load_dwordx4 v[140:143], v233, s[20:23], s74 offen offset:3072 sc1\n"
  "ds_write_b64 v244, v[78:79] offset:1032\n"
  "s_waitcnt vmcnt(13) lgkmcnt(6)\n"
  "v_mfma_f32_16x16x32_bf16 v[80:83], v[160:163], a[16:19], v[80:83]\n"
  "v_mfma_f32_16x16x32_bf16 v[88:91], v[160:163], a[20:23], v[88:91]\n"
  "v_mfma_f32_16x16x32_bf16 v[96:99], v[160:163], a[24:27], v[96:99]\n"
  "buffer_load_dwordx4 v[144:147], v233, s[20:23], s75 offen sc1\n"
  "v_mfma_f32_16x16x32_bf16 v[104:107], v[160:163], a[28:31], v[104:107]\n"
  "s_waitcnt vmcnt(13) lgkmcnt(3)\n"
  "v_mfma_f32_16x16x32_bf16 v[84:87], v[164:167], a[16:19], v[84:87]\n"
  "s_waitcnt lgkmcnt(0)\n"
  "s_barrier\n"
  "v_mfma_f32_16x16x32_bf16 v[92:95], v[164:167], a[20:23], v[92:95]\n"
  "ds_read_b128 v[56:59], v241 offset:0\n"
  "ds_read_b128 v[64:67], v241 offset:2048\n"
  "v_mfma_f32_16x16x32_bf16 v[100:103], v[164:167], a[24:27], v[100:103]\n"
  "ds_read_b128 v[72:75], v241 offset:4096\n"
  "buffer_load_dwordx4 v[148:151], v233, s[20:23], s75 offen offset:1024 sc1\n"
  "v_mfma_f32_16x16x32_bf16 v[108:111], v[164:167], a[28:31], v[108:111]\n"
  "ds_read_b128 v[60:63], v241 offset:1024\n"
  "s_waitcnt vmcnt(13)\n"
  "v_mfma_f32_16x16x32_bf16 v[80:83], v[168:171], a[32:35], v[80:83]\n"
  "ds_read_b128 v[68:71], v241 offset:3072\n"
  "v_mfma_f32_16x16x32_bf16 v[88:91], v[168:171], a[36:39], v[88:91]\n"
  "ds_read_b128 v[76:79], v241 offset:5120\n"
  "buffer_load_dwordx4 v[152:155], v233, s[20:23], s75 offen offset:2048 sc1\n"
  "v_mfma_f32_16x16x32_bf16 v[96:99], v[168:171], a[40:43], v[96:99]\n"
  "v_mfma_f32_16x16x32_bf16 v[104:107], v[168:171], a[44:47], v[104:107]\n"
  "s_waitcnt lgkmcnt(5)\n"
  "v_add_f32 v48, v48, v56\n"
  "s_waitcnt vmcnt(13)\n"
  "v_mfma_f32_16x16x32_bf16 v[84:87], v[172:175], a[32:35], v[84:87]\n"
  "s_waitcnt lgkmcnt(3)\n"
  "v_add_f32 v64, v64, v72\n"
  "v_mfma_f32_16x16x32_bf16 v[92:95], v[172:175], a[36:39], v[92:95]\n"
  "v_add_f32 v49, v49, v57\n"
  "v_add_f32 v65, v65, v73\n"
  "v_mfma_f32_16x16x32_bf16 v[100:103], v[172:175], a[40:43], v[100:103]\n"
  "v_add_f32 v50, v50, v58\n"
  "v_add_f32 v66, v66, v74\n"
  "v_mfma_f32_16x16x32_bf16 v[108:111], v[172:175], a[44:47], v[108:111]\n"
  "v_add_f32 v51, v51, v59\n"
  "v_add_f32 v67, v67, v75\n"
  "s_waitcnt vmcnt(12) lgkmcnt(2)\n"
  "v_mfma_f32_16x16x32_bf16 v[80:83], v[180:183], a[48:51], v[80:83]\n"
  "buffer_load_dwordx4 v[156:159], v233, s[20:23], s75 offen offset:3072 sc1\n"
  "v_add_f32 v52, v52, v60\n"
  "v_mfma_f32_16x16x32_bf16 v[88:91], v[180:183], a[52:55], v[88:91]\n"
  "s_waitcnt lgkmcnt(0)\n"
  "v_add_f32 v68, v68, v76\n"
  "v_mfma_f32_16x16x32_bf16 v[96:99], v[180:183], a[56:59], v[96:99]\n"
  "v_add_f32 v53, v53, v61\n"
  "v_add_f32 v69, v69, v77\n"
  "v_mfma_f32_16x16x32_bf16 v[104:107], v[180:183], a[60:63], v[104:107]\n"
  "v_add_f32 v54, v54, v62\n"
  "v_add_f32 v70, v70, v78\n"
  "s_waitcnt vmcnt(12)\n"
  "v_mfma_f32_16x16x32_bf16 v[84:87], v[188:191], a[48:51], v[84:87]\n"
  "buffer_load_dwordx4 v[160:163], v233, s[20:23], s76 offen sc1\n"
  "v_add_f32 v55, v55, v63\n"
  "v_mfma_f32_16x16x32_bf16 v[92:95], v[188:191], a[52:55], v[92:95]\n"
  "v_add_f32 v71, v71, v79\n"
  "v_add_f32 v48, v48, v64\n"
  "v_mfma_f32_16x16x32_bf16 v[100:103], v[188:191], a[56:59], v[100:103]\n"
  "v_add_f32 v49, v49, v65\n"
  "v_add_f32 v50, v50, v66\n"
  "v_mfma_f32_16x16x32_bf16 v[108:111], v[188:191], a[60:63], v[108:111]\n"
  "v_add_f32 v51, v51, v67\n"
  "v_add_f32 v52, v52, v68\n"
  "s_waitcnt vmcnt(12)\n"
  "v_mfma_f32_16x16x32_bf16 v[80:83], v[112:115], a[64:67], v[80:83]\n"
  "buffer_load_dwordx4 v[164:167], v233, s[20:23], s76 offen offset:1024 sc1\n"
  "v_add_f32 v53, v53, v69\n"
  "v_mfma_f32_16x16x32_bf16 v[88:91], v[112:115], a[68:71], v[88:91]\n"
  "v_add_f32 v54, v54, v70\n"
  "v_add_f32 v55, v55, v71\n"
  "v_mfma_f32_16x16x32_bf16 v[96:99], v[112:115], a[72:75], v[96:99]\n"
  "v_cvt_pk_bf16_f32 v22, v48, v49\n"
  "v_cvt_pk_bf16_f32 v23, v50, v51\n"
  "v_mfma_f32_16x16x32_bf16 v[104:107], v[112:115], a[76:79], v[104:107]\n"
  "v_cvt_pk_bf16_f32 v24, v52, v53\n"
  "buffer_load_dwordx4 v[168:171], v233, s[20:23], s76 offen offset:2048 sc1\n"
  "s_waitcnt vmcnt(13)\n"
  "v_mfma_f32_16x16x32_bf16 v[84:87], v[116:119], a[64:67], v[84:87]\n"
  "v_cvt_pk_bf16_f32 v25, v54, v55\n"
  "buffer_store_dwordx4 v[22:25], v1, s[20:23], s18 offen sc1\n"
  "v_mfma_f32_16x16x32_bf16 v[92:95], v[116:119], a[68:71], v[92:95]\n"
  "v_lshlrev_b32 v64, 16, v22\n"
  "v_and_b32 v65, 0xffff0000, v22\n"
  "v_mfma_f32_16x16x32_bf16 v[100:103], v[116:119], a[72:75], v[100:103]\n"
  "v_lshlrev_b32 v66, 16, v23\n"
  "v_and_b32 v67, 0xffff0000, v23\n"
  "v_mfma_f32_16x16x32_bf16 v[108:111], v[116:119], a[76:79], v[108:111]\n"
  "buffer_load_dwordx4 v[172:175], v233, s[20:23], s76 offen offset:3072 sc1\n"
  "v_mul_f32 v58, v64, v64\n"
  "s_waitcnt vmcnt(14)\n"
  "v_mfma_f32_16x16x32_bf16 v[80:83], v[120:123], a[80:83], v[80:83]\n"
  "v_mul_f32 v59, v65, v65\n"
  "v_lshlrev_b32 v68, 16, v24\n"
  "v_mfma_f32_16x16x32_bf16 v[88:91], v[120:123], a[84:87], v[88:91]\n"
  "v_and_b32 v69, 0xffff0000, v24\n"
  "v_add_f32 v56, v64, v66\n"
  "v_mfma_f32_16x16x32_bf16 v[96:99], v[120:123], a[88:91], v[96:99]\n"
  "v_add_f32 v57, v65, v67\n"
  "v_fmac_f32 v58, v66, v66\n"
  "v_mfma_f32_16x16x32_bf16 v[104:107], v[120:123], a[92:95], v[104:107]\n"
  "buffer_load_dwordx4 v[180:183], v233, s[20:23], s77 offen sc1\n"
  "v_fmac_f32 v59, v67, v67\n"
  "s_waitcnt vmcnt(14)\n"
  "v_mfma_f32_16x16x32_bf16 v[84:87], v[124:127], a[80:83], v[84:87]\n"
  "v_lshlrev_b32 v70, 16, v25\n"
  "v_and_b32 v71, 0xffff0000, v25\n"
  "v_mfma_f32_16x16x32_bf16 v[92:95], v[124:127], a[84:87], v[92:95]\n"
  "v_add_f32 v56, v56, v68\n"
  "v_fmac_f32 v58, v68, v68\n"
  "v_mfma_f32_16x16x32_bf16 v[100:103], v[124:127], a[88:91], v[100:103]\n"
  "v_add_f32 v57, v57, v69\n"
  "v_fmac_f32 v59, v69, v69\n"
  "v_mfma_f32_16x16x32_bf16 v[108:111], v[124:127], a[92:95], v[108:111]\n"
  "buffer_load_dwordx4 v[188:191], v233, s[20:23], s77 offen offset:1024 sc1\n"
  "v_add_f32 v56, v56, v70\n"
  "s_waitcnt vmcnt(14)\n"
  "v_mfma_f32_16x16x32_bf16 v[80:83], v[128:131], a[96:99], v[80:83]\n"
  "v_fmac_f32 v58, v70, v70\n"
  "v_add_f32 v57, v57, v71\n"
  "v_mfma_f32_16x16x32_bf16 v[88:91], v[128:131], a[100:103], v[88:91]\n"
  "v_fmac_f32 v59, v71, v71\n"
  "v_mfma_f32_16x16x32_bf16 v[96:99], v[128:131], a[104:107], v[96:99]\n"
  "buffer_load_dwordx4 v[112:115], v233, s[20:23], s77 offen offset:2048 sc1\n"
  "v_mfma_f32_16x16x32_bf16 v[104:107], v[128:131], a[108:111], v[104:107]\n"
  "s_waitcnt vmcnt(14)\n"
  "v_mfma_f32_16x16x32_bf16 v[84:87], v[132:135], a[96:99], v[84:87]\n"
  "v_mfma_f32_16x16x32_bf16 v[92:95], v[132:135], a[100:103], v[92:95]\n"
  "v_mfma_f32_16x16x32_bf16 v[100:103], v[132:135], a[104:107], v[100:103]\n"
  "buffer_load_dwordx4 v[116:119], v233, s[20:23], s77 offen offset:3072 sc1\n"
  "s_lshl2_add_u32 s88, s64, s58\n"
  "v_mfma_f32_16x16x32_bf16 v[108:111], v[132:135], a[108:111], v[108:111]\n"
  "s_lshl_b32 s89, s88, 9\n"
  "s_add_u32 s89, s89, 114688\n"
  "s_waitcnt vmcnt(14)\n"
  "v_mfma_f32_16x16x32_bf16 v[80:83], v[136:139], a[112:115], v[80:83]\n"
  "v_min_u32 v247, 6, v0\n"
  "v_lshl_add_u32 v247, v247, 3, s89\n"
  "v_mfma_f32_16x16x32_bf16 v[88:91], v[136:139], a[116:119], v[88:91]\n"
  "s_lshl3_add_u32 s88, s64, s58\n"
  "s_mul_i32 s88, s88, 28672\n"
  "v_mfma_f32_16x16x32_bf16 v[96:99], v[136:139], a[120:123], v[96:99]\n"
  "s_add_u32 m0, s8, 0\n"
  "s_nop 0\n"
  "buffer_load_dwordx4 v187, s[20:23], 0 offen sc1 lds\n"
  "v_mfma_f32_16x16x32_bf16 v[104:107], v[136:139], a[124:127], v[104:107]\n"
  "s_add_u32 s88, s88, 12582912\n"
  "v_add_u32 v232, s88, v1\n"
  "s_waitcnt vmcnt(14)\n"
  "v_mfma_f32_16x16x32_bf16 v[84:87], v[140:143], a[112:115], v[84:87]\n"
  "v_add_u32 v233, 114688, v232\n"
  "v_mfma_f32_16x16x32_bf16 v[92:95], v[140:143], a[116:119], v[92:95]\n"
  "v_mfma_f32_16x16x32_bf16 v[100:103], v[140:143], a[120:123], v[100:103]\n"
  "v_mfma_f32_16x16x32_bf16 v[108:111], v[140:143], a[124:127], v[108:111]\n"
  "s_waitcnt vmcnt(13)\n"
  "v_mfma_f32_16x16x32_bf16 v[80:83], v[144:147], a[128:131], v[80:83]\n"
  "v_mfma_f32_16x16x32_bf16 v[88:91], v[144:147], a[132:135], v[88:91]\n"
  "v_mfma_f32_16x16x32_bf16 v[96:99], v[144:147], a[136:139], v[96:99]\n"
  "v_mfma_f32_16x16x32_bf16 v[104:107], v[144:147], a[140:143], v[104:107]\n"
  "s_waitcnt vmcnt(12)\n"
  "v_mfma_f32_16x16x32_bf16 v[84:87], v[148:151], a[128:131], v[84:87]\n"
  "v_mfma_f32_16x16x32_bf16 v[92:95], v[148:151], a[132:135], v[92:95]\n"
  "v_mfma_f32_16x16x32_bf16 v[100:103], v[148:151], a[136:139], v[100:103]\n"
  "v_mfma_f32_16x16x32_bf16 v[108:111], v[148:151], a[140:143], v[108:111]\n"
  "s_waitcnt vmcnt(11)\n"
  "v_mfma_f32_16x16x32_bf16 v[80:83], v[152:155], a[144:147], v[80:83]\n"
  "v_mfma_f32_16x16x32_bf16 v[88:91], v[152:155], a[148:151], v[88:91]\n"
  "v_mfma_f32_16x16x32_bf16 v[96:99], v[152:155], a[152:155], v[96:99]\n"
  "v_mfma_f32_16x16x32_bf16 v[104:107], v[152:155], a[156:159], v[104:107]\n"
  "s_waitcnt vmcnt(10)\n"
  "v_mfma_f32_16x16x32_bf16 v[84:87], v[156:159], a[144:147], v[84:87]\n"
  "v_mfma_f32_16x16x32_bf16 v[92:95], v[156:159], a[148:151], v[92:95]\n"
  "v_mfma_f32_16x16x32_bf16 v[100:103], v[156:159], a[152:155], v[100:103]\n"
  "v_mfma_f32_16x16x32_bf16 v[108:111], v[156:159], a[156:159], v[108:111]\n"
  "s_waitcnt vmcnt(9)\n"
  "v_mfma_f32_16x16x32_bf16 v[80:83], v[160:163], a[160:163], v[80:83]\n"
  "v_mfma_f32_16x16x32_bf16 v[88:91], v[160:163], a[164:167], v[88:91]\n"
  "s_waitcnt vmcnt(6)\n"
  "s_barrier\n"
  "buffer_store_dwordx2 v[36:37], v248, s[20:23], s14 offen sc1\n"
  "v_mfma_f32_16x16x32_bf16 v[96:99], v[160:163], a[168:171], v[96:99]\n"
  "s_add_u32 m0, s8, 0\n"
  "s_nop 0\n"
  "buffer_load_dwordx4 v187, s[20:23], 0 offen offset:1024 sc1 lds\n"
  "v_mfma_f32_16x16x32_bf16 v[104:107], v[160:163], a[172:175], v[104:107]\n"
  "s_add_u32 m0, s8, 0\n"
  "s_nop 0\n"
  "buffer_load_dwordx4 v187, s[20:23], 0 offen offset:2048 sc1 lds\n"
  "v_mfma_f32_16x16x32_bf16 v[84:87], v[164:167], a[160:163], v[84:87]\n"
  "s_add_u32 m0, s8, 0\n"
  "s_nop 0\n"
  "buffer_load_dwordx4 v187, s[20:23], 0 offen offset:3072 sc1 lds\n"
  "v_mfma_f32_16x16x32_bf16 v[92:95], v[164:167], a[164:167], v[92:95]\n"
  "v_mfma_f32_16x16x32_bf16 v[100:103], v[164:167], a[168:171], v[100:103]\n"
  "v_mfma_f32_16x16x32_bf16 v[108:111], v[164:167], a[172:175], v[108:111]\n"
  "v_mfma_f32_16x16x32_bf16 v[80:83], v[168:171], a[176:179], v[80:83]\n"
  "v_mfma_f32_16x16x32_bf16 v[88:91], v[168:171], a[180:183], v[88:91]\n"
  "s_add_u32 m0, s8, 4096\n"
  "s_nop 0\n"
  "buffer_load_dwordx4 v187, s[20:23], s72 offen sc1 lds\n"
  "v_mfma_f32_16x16x32_bf16 v[96:99], v[168:171], a[184:187], v[96:99]\n"
  "v_mfma_f32_16x16x32_bf16 v[104:107], v[168:171], a[188:191], v[104:107]\n"
  "s_waitcnt vmcnt(10)\n"
  "v_mfma_f32_16x16x32_bf16 v[84:87], v[172:175], a[176:179], v[84:87]\n"
  "v_mfma_f32_16x16x32_bf16 v[92:95], v[172:175], a[180:183], v[92:95]\n"
  "v_mfma_f32_16x16x32_bf16 v[100:103], v[172:175], a[184:187], v[100:103]\n"
  "s_add_u32 m0, s8, 4096\n"
  "s_nop 0\n"
  "buffer_load_dwordx4 v187, s[20:23], s72 offen offset:1024 sc1 lds\n"
  "v_mfma_f32_16x16x32_bf16 v[108:111], v[172:175], a[188:191], v[108:111]\n"
  "s_waitcnt vmcnt(10)\n"
  "v_mfma_f32_16x16x32_bf16 v[80:83], v[180:183], a[192:195], v[80:83]\n"
  "v_mfma_f32_16x16x32_bf16 v[88:91], v[180:183], a[196:199], v[88:91]\n"
  "v_mfma_f32_16x16x32_bf16 v[96:99], v[180:183], a[200:203], v[96:99]\n"
  "v_mfma_f32_16x16x32_bf16 v[104:107], v[180:183], a[204:207], v[104:107]\n"
  "s_add_u32 m0, s8, 4096\n"
  "s_nop 0\n"
  "buffer_load_dwordx4 v187, s[20:23], s72 offen offset:2048 sc1 lds\n"
  "s_waitcnt vmcnt(10)\n"
  "v_mfma_f32_16x16x32_bf16 v[84:87], v[188:191], a[192:195], v[84:87]\n"
  "v_mfma_f32_16x16x32_bf16 v[92:95], v[188:191], a[196:199], v[92:95]\n"
  "v_mfma_f32_16x16x32_bf16 v[100:103], v[188:191], a[200:203], v[100:103]\n"
  "v_mfma_f32_16x16x32_bf16 v[108:111], v[188:191], a[204:207], v[108:111]\n"
  "s_add_u32 m0, s8, 4096\n"
  "s_nop 0\n"
  "buffer_load_dwordx4 v187, s[20:23], s72 offen offset:3072 sc1 lds\n"
  "s_waitcnt vmcnt(10)\n"
  "v_mfma_f32_16x16x32_bf16 v[80:83], v[112:115], v[176:179], v[80:83]\n"
  "v_mfma_f32_16x16x32_bf16 v[88:91], v[112:115], v[200:203], v[88:91]\n"
  "v_mfma_f32_16x16x32_bf16 v[96:99], v[112:115], v[196:199], v[96:99]\n"
  "v_mfma_f32_16x16x32_bf16 v[104:107], v[112:115], v[192:195], v[104:107]\n"
  "s_waitcnt vmcnt(9)\n"
  "v_mfma_f32_16x16x32_bf16 v[84:87], v[116:119], v[176:179], v[84:87]\n"
  "s_add_u32 m0, s8, 8192\n"
  "s_nop 0\n"
  "buffer_load_dwordx4 v187, s[20:23], s73 offen sc1 lds\n"
  "v_mfma_f32_16x16x32_bf16 v[92:95], v[116:119], v[200:203], v[92:95]\n"
  "s_lshl2_add_u32 s87, s68, s58\n"
  "s_bfe_u32 s89, s99, 0x20001\n"
  "v_mfma_f32_16x16x32_bf16 v[100:103], v[116:119], v[196:199], v[100:103]\n"
  "s_and_b32 s90, s99, 1\n"
  "s_lshr_b32 s88, s87, 5\n"
  "v_mfma_f32_16x16x32_bf16 v[108:111], v[116:119], v[192:195], v[108:111]\n"
  "ds_write_b128 v242, v[88:91] offset:24576\n"
  "s_mul_i32 s88, s88, 512\n"
  "s_and_b32 s87, s87, 31\n"
  "s_add_u32 m0, s8, 8192\n"
  "s_nop 0\n"
  "buffer_load_dwordx4 v187, s[20:23], s73 offen offset:1024 sc1 lds\n"
  "s_add_u32 s87, s87, s88\n"
  "s_mul_i32 s87, s87, 3584\n"
  "s_mul_i32 s89, s89, 896\n"
  "ds_write_b128 v242, v[92:95] offset:25600\n"
  "s_add_u32 s87, s87, s89\n"
  "v_lshrrev_b32 v45, 3, v0\n"
  "v_add_u32 v45, 0, v45\n"
  "v_mul_u32_u24 v45, 114688, v45\n"
  "v_and_b32 v46, 7, v0\n"
  "v_min_u32 v46, 6, v46\n"
  "v_lshl_add_u32 v45, v46, 7, v45\n"
  "v_add_u32 v45, s87, v45\n"
  "v_mov_b32 v200, v45\n"
  "ds_write_b128 v243, v[96:99] offset:24576\n"
  "s_add_u32 m0, s8, 8192\n"
  "s_nop 0\n"
  "buffer_load_dwordx4 v187, s[20:23], s73 offen offset:2048 sc1 lds\n"
  "s_lshl2_add_u32 s87, s68, s58\n"
  "s_bfe_u32 s89, s99, 0x20001\n"
  "s_mul_i32 s89, s89, 7\n"
  "s_or_b32 s89, s89, 3\n"
  "s_lshr_b32 s90, s89, 2\n"
  "s_mul_i32 s87, s87, 57344\n"
  "ds_write_b128 v243, v[100:103] offset:25600\n"
  "s_lshl_b32 s89, s89, 7\n"
  "s_add_u32 s87, s87, s89\n"
  "v_xor_b32 v46, s90, v0\n"
  "v_and_b32 v46, 1, v46\n"
  "v_mul_u32_u24 v45, 3584, v2\n"
  "v_lshl_add_u32 v45, v46, 9, v45\n"
  "s_add_u32 m0, s8, 8192\n"
  "s_nop 0\n"
  "buffer_load_dwordx4 v187, s[20:23], s73 offen offset:3072 sc1 lds\n"
  "ds_write_b128 v244, v[104:107] offset:24576\n"
  "v_add_u32 v45, s87, v45\n"
  "v_lshl_add_u32 v45, v3, 5, v45\n"
  "v_mov_b32 v201, v45\n"
  "ds_write_b128 v244, v[108:111] offset:25600\n"
  "s_add_u32 m0, s8, 12288\n"
  "s_nop 0\n"
  "buffer_load_dwordx4 v187, s[20:23], s74 offen sc1 lds\n"
  "s_waitcnt lgkmcnt(0)\n"
  "s_barrier\n"
  "ds_read_b128 v[88:91], v241 offset:24576\n"
  "ds_read_b128 v[96:99], v241 offset:26624\n"
  "s_add_u32 m0, s8, 12288\n"
  "s_nop 0\n"
  "buffer_load_dwordx4 v187, s[20:23], s74 offen offset:1024 sc1 lds\n"
  "ds_read_b128 v[104:107], v241 offset:28672\n"
  "ds_read_b128 v[92:95], v241 offset:25600\n"
  "ds_read_b128 v[100:103], v241 offset:27648\n"
  "ds_read_b128 v[108:111], v241 offset:29696\n"
  "s_add_u32 m0, s8, 12288\n"
  "s_nop 0\n"
  "buffer_load_dwordx4 v187, s[20:23], s74 offen offset:2048 sc1 lds\n"
  "s_waitcnt lgkmcnt(5)\n"
  "v_pk_add_f32 v[80:81], v[80:81], v[88:89]\n"
  "v_pk_add_f32 v[82:83], v[82:83], v[90:91]\n"
  "s_waitcnt lgkmcnt(3)\n"
  "v_pk_add_f32 v[96:97], v[96:97], v[104:105]\n"
  "v_pk_add_f32 v[80:81], v[80:81], v[96:97]\n"
  "v_pk_add_f32 v[98:99], v[98:99], v[106:107]\n"
  "v_pk_add_f32 v[82:83], v[82:83], v[98:99]\n"
  "s_waitcnt lgkmcnt(2)\n"
  "v_pk_add_f32 v[84:85], v[84:85], v[92:93]\n"
  "v_cvt_pk_bf16_f32 v26, v80, v81\n"
  "v_pk_add_f32 v[86:87], v[86:87], v[94:95]\n"
  "v_cvt_pk_bf16_f32 v27, v82, v83\n"
  "s_waitcnt lgkmcnt(0)\n"
  "v_pk_add_f32 v[100:101], v[100:101], v[108:109]\n"
  "v_pk_add_f32 v[84:85], v[84:85], v[100:101]\n"
  "v_pk_add_f32 v[102:103], v[102:103], v[110:111]\n"
  "v_lshlrev_b32 v96, 16, v26\n"
  "v_and_b32 v97, 0xffff0000, v26\n"
  "v_pk_add_f32 v[86:87], v[86:87], v[102:103]\n"
  "v_cvt_pk_bf16_f32 v28, v84, v85\n"
  "v_lshlrev_b32 v98, 16, v27\n"
  "v_and_b32 v99, 0xffff0000, v27\n"
  "v_pk_add_f32 v[56:57], v[56:57], v[96:97]\n"
  "v_pk_fma_f32 v[58:59], v[96:97], v[96:97], v[58:59]\n"
  "v_cvt_pk_bf16_f32 v29, v86, v87\n"
  "v_lshlrev_b32 v100, 16, v28\n"
  "v_and_b32 v101, 0xffff0000, v28\n"
  "v_pk_add_f32 v[56:57], v[56:57], v[98:99]\n"
  "v_pk_fma_f32 v[58:59], v[98:99], v[98:99], v[58:59]\n"
  "s_add_u32 m0, s8, 12288\n"
  "s_nop 0\n"
  "buffer_load_dwordx4 v187, s[20:23], s74 offen offset:3072 sc1 lds\n"
  "v_lshlrev_b32 v102, 16, v29\n"
  "v_and_b32 v103, 0xffff0000, v29\n"
  "v_pk_add_f32 v[56:57], v[56:57], v[100:101]\n"
  "v_pk_fma_f32 v[58:59], v[100:101], v[100:101], v[58:59]\n"
  "v_pk_add_f32 v[56:57], v[56:57], v[102:103]\n"
  "v_pk_fma_f32 v[58:59], v[102:103], v[102:103], v[58:59]\n"
  "v_add_f32 v56, v56, v57\n"
  "v_add_f32 v58, v58, v59\n"
  "buffer_store_dwordx4 v[26:29], v1, s[20:23], s19 offen sc1\n"
  "s_nop 0\n"
  "v_permlane16_swap_b32 v56, v58\n"
  "v_add_f32 v56, v56, v58\n"
  "v_mov_b32 v57, v56\n"
  "s_nop 1\n"
  "v_permlane32_swap_b32 v56, v57\n"
  "v_add_f32 v56, v56, v57\n"
  "buffer_store_dword v56, v245, s[20:23], 0 offen sc1\n"
  "s_lshl_b32 s87, s64, 5\n"
  "s_add_u32 s89, s87, s63\n"
  "s_lshr_b32 s94, s89, 3\n"
  "s_and_b32 s95, s89, 7\n"
  "s_lshl_b32 s94, s94, 9\n"
  "s_lshl3_add_u32 s94, s95, s94\n"
  "s_add_u32 s12, s94, 98304\n"
  "s_add_u32 s87, s87, s89\n"
  "s_lshl1_add_u32 s87, s87, s59\n"
  "s_lshl2_add_u32 s87, s87, s60\n"
  "s_lshl_b32 s87, s87, 9\n"
  "s_mov_b32 s52, 131072\n"
  "s_add_u32 s87, s87, 8388608\n"
  "v_lshl_add_u32 v245, v2, 4, s87\n"
  "v_and_b32 v45, 16, v0\n"
  "v_lshl_add_u32 v245, v45, 6, v245\n"
  "v_and_b32 v45, 32, v0\n"
  "v_lshl_add_u32 v245, v45, 3, v245\n"
  "s_sleep 4\n"
  "buffer_load_dwordx2 v[206:207], v204, s[20:23], 0 offen sc1\n"
  "s_waitcnt vmcnt(3)\n"
  "s_barrier\n"
  "ds_read_b64_tr_b16 v[112:113], v222 offset:1024\n"
  "ds_read_b64_tr_b16 v[114:115], v222 offset:4096\n"
  "ds_read_b64_tr_b16 v[116:117], v222 offset:2048\n"
  "ds_read_b64_tr_b16 v[118:119], v222 offset:5120\n"
  "ds_read_b64_tr_b16 v[120:121], v222 offset:0\n"
  "ds_read_b64_tr_b16 v[122:123], v222 offset:3072\n"
  "s_waitcnt vmcnt(1)\n"
  "s_barrier\n"
  "buffer_store_dwordx2 v[36:37], v248, s[20:23], s14 offen offset:128 sc1\n"
  "s_add_u32 m0, s10, 98304\n"
  "s_nop 0\n"
  "buffer_load_dwordx4 v184, s[32:35], 0 offen lds\n"
  "s_add_u32 m0, s10, 99328\n"
  "s_nop 0\n"
  "buffer_load_dwordx4 v184, s[32:35], s73 offen lds\n"
  "ds_read_b64_tr_b16 v[124:125], v223 offset:1024\n"
  "ds_read_b64_tr_b16 v[126:127], v223 offset:4096\n"
  "ds_read_b64_tr_b16 v[128:129], v223 offset:2048\n"
  "ds_read_b64_tr_b16 v[130:131], v223 offset:5120\n"
  "ds_read_b64_tr_b16 v[132:133], v223 offset:0\n"
  "s_add_u32 m0, s10, 100352\n"
  "s_nop 0\n"
  "buffer_load_dwordx4 v184, s[32:35], s75 offen lds\n"
  "ds_read_b64_tr_b16 v[134:135], v223 offset:3072\n"
  "s_add_u32 m0, s10, 101376\n"
  "s_nop 0\n"
  "buffer_load_dwordx4 v184, s[32:35], s77 offen lds\n"
  "s_add_u32 m0, s10, 102400\n"
  "s_nop 0\n"
  "buffer_load_dwordx4 v184, s[32:35], s79 offen lds\n"
  "s_add_u32 m0, s10, 103424\n"
  "s_nop 0\n"
  "buffer_load_dwordx4 v184, s[32:35], s81 offen lds\n"
  "s_waitcnt vmcnt(7) lgkmcnt(0)\n"
  "v_cmp_ne_u64 vcc, s[56:57], v[206:207]\n"
  "s_cmp_eq_u64 vcc, 0\n"
  "s_cbranch_scc0 .Lfb6_%=\n"
  ".Lbk6_%=:\n"
  "buffer_load_dwordx4 a[0:3], v227, s[20:23], 0 offen sc1\n"
  "buffer_load_dwordx4 a[4:7], v228, s[20:23], 0 offen sc1\n"
  "buffer_load_dwordx4 a[8:11], v229, s[20:23], 0 offen sc1\n"
  "buffer_load_dwordx4 a[12:15], v227, s[20:23], 0 offen offset:1024 sc1\n"
  "buffer_load_dwordx4 a[16:19], v228, s[20:23], 0 offen offset:1024 sc1\n"
  "buffer_load_dwordx4 a[20:23], v229, s[20:23], 0 offen offset:1024 sc1\n"
  "buffer_load_dword v44, v250, s[20:23], 0 offen sc1\n"
  "s_and_b32 s87, s68, 3\n"
  "s_lshl_b32 s87, s87, 10\n"
  "s_bfe_u32 s88, s68, 0x10002\n"
  "s_lshl_b32 s88, s88, 14\n"
  "s_add_u32 s87, s87, s88\n"
  "s_add_u32 s87, s87, 12288\n"
  "v_and_b32 v45, 7, v0\n"
  "v_lshl_add_u32 v45, v45, 7, s87\n"
  "v_and_b32 v46, 0xfffffc00, v228\n"
  "v_add_u32 v45, v45, v46\n"
  "v_mov_b32 v250, v45\n"
  "buffer_load_dwordx4 a[24:27], v227, s[20:23], 0 offen offset:2048 sc1\n"
  "buffer_load_dwordx4 a[28:31], v228, s[20:23], 0 offen offset:2048 sc1\n"
  "buffer_load_dwordx4 a[32:35], v229, s[20:23], 0 offen offset:2048 sc1\n"
  "buffer_load_dwordx4 a[36:39], v227, s[20:23], 0 offen offset:3072 sc1\n"
  "buffer_load_dwordx4 a[40:43], v228, s[20:23], 0 offen offset:3072 sc1\n"
  "buffer_load_dwordx4 a[44:47], v229, s[20:23], 0 offen offset:3072 sc1\n"
  "s_waitcnt vmcnt(12)\n"
  "v_mfma_f32_16x16x32_bf16 v[48:51], v[112:115], a[0:3], 0\n"
  "buffer_load_dwordx4 a[48:51], v227, s[20:23], s72 offen sc1\n"
  "s_waitcnt vmcnt(12)\n"
  "v_mfma_f32_16x16x32_bf16 v[56:59], v[112:115], a[4:7], 0\n"
  "buffer_load_dwordx4 a[52:55], v228, s[20:23], s72 offen sc1\n"
  "s_waitcnt vmcnt(12)\n"
  "v_mfma_f32_16x16x32_bf16 v[64:67], v[112:115], a[8:11], 0\n"
  "ds_read_b64_tr_b16 v[112:113], v222 offset:13312\n"
  "v_mfma_f32_16x16x32_bf16 v[72:75], a[0:3], v[116:119], 0\n"
  "ds_read_b64_tr_b16 v[114:115], v222 offset:16384\n"
  "v_mfma_f32_16x16x32_bf16 v[80:83], a[4:7], v[116:119], 0\n"
  "buffer_load_dwordx4 a[56:59], v229, s[20:23], s72 offen sc1\n"
  "v_mfma_f32_16x16x32_bf16 v[88:91], a[8:11], v[116:119], 0\n"
  "ds_read_b64_tr_b16 v[116:117], v222 offset:14336\n"
  "v_mfma_f32_16x16x32_bf16 v[96:99], v[120:123], a[4:7], 0\n"
  "ds_read_b64_tr_b16 v[118:119], v222 offset:17408\n"
  "v_mfma_f32_16x16x32_bf16 v[104:107], v[120:123], a[8:11], 0\n"
  "ds_read_b64_tr_b16 v[120:121], v222 offset:12288\n"
  "v_mfma_f32_16x16x32_bf16 v[52:55], v[124:127], a[0:3], 0\n"
  "ds_read_b64_tr_b16 v[122:123], v222 offset:15360\n"
  "buffer_load_dwordx4 a[60:63], v227, s[20:23], s72 offen offset:1024 sc1\n"
  "v_mfma_f32_16x16x32_bf16 v[60:63], v[124:127], a[4:7], 0\n"
  "v_mfma_f32_16x16x32_bf16 v[68:71], v[124:127], a[8:11], 0\n"
  "ds_read_b64_tr_b16 v[124:125], v223 offset:13312\n"
  "v_mfma_f32_16x16x32_bf16 v[76:79], a[0:3], v[128:131], 0\n"
  "ds_read_b64_tr_b16 v[126:127], v223 offset:16384\n"
  "v_mfma_f32_16x16x32_bf16 v[84:87], a[4:7], v[128:131], 0\n"
  "buffer_load_dwordx4 a[64:67], v228, s[20:23], s72 offen offset:1024 sc1\n"
  "v_mfma_f32_16x16x32_bf16 v[92:95], a[8:11], v[128:131], 0\n"
  "ds_read_b64_tr_b16 v[128:129], v223 offset:14336\n"
  "v_mfma_f32_16x16x32_bf16 v[100:103], v[132:135], a[4:7], 0\n"
  "ds_read_b64_tr_b16 v[130:131], v223 offset:17408\n"
  "v_mfma_f32_16x16x32_bf16 v[108:111], v[132:135], a[8:11], 0\n"
  "ds_read_b64_tr_b16 v[132:133], v223 offset:12288\n"
  "s_waitcnt vmcnt(14) lgkmcnt(7)\n"
  "v_mfma_f32_16x16x32_bf16 v[48:51], v[112:115], a[12:15], v[48:51]\n"
  "ds_read_b64_tr_b16 v[134:135], v223 offset:15360\n"
  "buffer_load_dwordx4 a[68:71], v229, s[20:23], s72 offen offset:1024 sc1\n"
  "s_waitcnt vmcnt(14) lgkmcnt(6)\n"
  "v_mfma_f32_16x16x32_bf16 v[56:59], v[112:115], a[16:19], v[56:59]\n"
  "s_waitcnt vmcnt(13)\n"
  "v_mfma_f32_16x16x32_bf16 v[64:67], v[112:115], a[20:23], v[64:67]\n"
  "ds_read_b64_tr_b16 v[112:113], v222 offset:25600\n"
  "v_mfma_f32_16x16x32_bf16 v[72:75], a[12:15], v[116:119], v[72:75]\n"
  "ds_read_b64_tr_b16 v[114:115], v222 offset:28672\n"
  "buffer_load_dwordx4 a[72:75], v227, s[20:23], s72 offen offset:2048 sc1\n"
  "v_mfma_f32_16x16x32_bf16 v[80:83], a[16:19], v[116:119], v[80:83]\n"
  "v_mfma_f32_16x16x32_bf16 v[88:91], a[20:23], v[116:119], v[88:91]\n"
  "ds_read_b64_tr_b16 v[116:117], v222 offset:26624\n"
  "v_mfma_f32_16x16x32_bf16 v[96:99], v[120:123], a[16:19], v[96:99]\n"
  "ds_read_b64_tr_b16 v[118:119], v222 offset:29696\n"
  "v_mfma_f32_16x16x32_bf16 v[104:107], v[120:123], a[20:23], v[104:107]\n"
  "ds_read_b64_tr_b16 v[120:121], v222 offset:24576\n"
  "buffer_load_dwordx4 a[76:79], v228, s[20:23], s72 offen offset:2048 sc1\n"
  "s_waitcnt lgkmcnt(6)\n"
  "v_mfma_f32_16x16x32_bf16 v[52:55], v[124:127], a[12:15], v[52:55]\n"
  "ds_read_b64_tr_b16 v[122:123], v222 offset:27648\n"
  "v_mfma_f32_16x16x32_bf16 v[60:63], v[124:127], a[16:19], v[60:63]\n"
  "buffer_load_dwordx2 v[206:207], v204, s[20:23], 0 offen offset:128 sc1\n"
  "v_mfma_f32_16x16x32_bf16 v[68:71], v[124:127], a[20:23], v[68:71]\n"
  "ds_read_b64_tr_b16 v[124:125], v223 offset:25600\n"
  "v_mfma_f32_16x16x32_bf16 v[76:79], a[12:15], v[128:131], v[76:79]\n"
  "ds_read_b64_tr_b16 v[126:127], v223 offset:28672\n"
  "v_mfma_f32_16x16x32_bf16 v[84:87], a[16:19], v[128:131], v[84:87]\n"
  "v_mfma_f32_16x16x32_bf16 v[92:95], a[20:23], v[128:131], v[92:95]\n"
  "ds_read_b64_tr_b16 v[128:129], v223 offset:26624\n"
  "s_waitcnt lgkmcnt(6)\n"
  "v_mfma_f32_16x16x32_bf16 v[100:103], v[132:135], a[16:19], v[100:103]\n"
  "ds_read_b64_tr_b16 v[130:131], v223 offset:29696\n"
  "buffer_load_dwordx4 a[80:83], v229, s[20:23], s72 offen offset:2048 sc1\n"
  "v_mfma_f32_16x16x32_bf16 v[108:111], v[132:135], a[20:23], v[108:111]\n"
  "ds_read_b64_tr_b16 v[132:133], v223 offset:24576\n"
  "s_waitcnt vmcnt(15) lgkmcnt(6)\n"
  "v_mfma_f32_16x16x32_bf16 v[48:51], v[112:115], a[24:27], v[48:51]\n"
  "ds_read_b64_tr_b16 v[134:135], v223 offset:27648\n"
  "s_waitcnt vmcnt(14) lgkmcnt(6)\n"
  "v_mfma_f32_16x16x32_bf16 v[56:59], v[112:115], a[28:31], v[56:59]\n"
  "buffer_load_dwordx4 a[84:87], v227, s[20:23], s72 offen offset:3072 sc1\n"
  "s_waitcnt vmcnt(14)\n"
  "v_mfma_f32_16x16x32_bf16 v[64:67], v[112:115], a[32:35], v[64:67]\n"
  "ds_read_b64_tr_b16 v[112:113], v222 offset:37888\n"
  "v_mfma_f32_16x16x32_bf16 v[72:75], a[24:27], v[116:119], v[72:75]\n"
  "ds_read_b64_tr_b16 v[114:115], v222 offset:40960\n"
  "v_mfma_f32_16x16x32_bf16 v[80:83], a[28:31], v[116:119], v[80:83]\n"
  "v_mfma_f32_16x16x32_bf16 v[88:91], a[32:35], v[116:119], v[88:91]\n"
  "ds_read_b64_tr_b16 v[116:117], v222 offset:38912\n"
  "buffer_load_dwordx4 a[88:91], v228, s[20:23], s72 offen offset:3072 sc1\n"
  "v_mfma_f32_16x16x32_bf16 v[96:99], v[120:123], a[28:31], v[96:99]\n"
  "ds_read_b64_tr_b16 v[118:119], v222 offset:41984\n"
  "v_mfma_f32_16x16x32_bf16 v[104:107], v[120:123], a[32:35], v[104:107]\n"
  "ds_read_b64_tr_b16 v[120:121], v222 offset:36864\n"
  "s_waitcnt lgkmcnt(6)\n"
  "v_mfma_f32_16x16x32_bf16 v[52:55], v[124:127], a[24:27], v[52:55]\n"
  "ds_read_b64_tr_b16 v[122:123], v222 offset:39936\n"
  "v_mfma_f32_16x16x32_bf16 v[60:63], v[124:127], a[28:31], v[60:63]\n"
  "buffer_load_dwordx4 a[92:95], v229, s[20:23], s72 offen offset:3072 sc1\n"
  "v_mfma_f32_16x16x32_bf16 v[68:71], v[124:127], a[32:35], v[68:71]\n"
  "ds_read_b64_tr_b16 v[124:125], v223 offset:37888\n"
  "v_mfma_f32_16x16x32_bf16 v[76:79], a[24:27], v[128:131], v[76:79]\n"
  "ds_read_b64_tr_b16 v[126:127], v223 offset:40960\n"
  "v_mfma_f32_16x16x32_bf16 v[84:87], a[28:31], v[128:131], v[84:87]\n"
  "v_mfma_f32_16x16x32_bf16 v[92:95], a[32:35], v[128:131], v[92:95]\n"
  "ds_read_b64_tr_b16 v[128:129], v223 offset:38912\n"
  "s_add_u32 m0, s10, 104448\n"
  "s_nop 0\n"
  "buffer_load_dwordx4 v184, s[32:35], s83 offen lds\n"
  "s_waitcnt lgkmcnt(6)\n"
  "v_mfma_f32_16x16x32_bf16 v[100:103], v[132:135], a[28:31], v[100:103]\n"
  "ds_read_b64_tr_b16 v[130:131], v223 offset:41984\n"
  "v_mfma_f32_16x16x32_bf16 v[108:111], v[132:135], a[32:35], v[108:111]\n"
  "ds_read_b64_tr_b16 v[132:133], v223 offset:36864\n"
  "s_waitcnt vmcnt(16) lgkmcnt(6)\n"
  "v_mfma_f32_16x16x32_bf16 v[48:51], v[112:115], a[36:39], v[48:51]\n"
  "ds_read_b64_tr_b16 v[134:135], v223 offset:39936\n"
  "s_add_u32 m0, s10, 105472\n"
  "s_nop 0\n"
  "buffer_load_dwordx4 v184, s[32:35], s85 offen lds\n"
  "s_waitcnt vmcnt(16) lgkmcnt(6)\n"
  "v_mfma_f32_16x16x32_bf16 v[56:59], v[112:115], a[40:43], v[56:59]\n"
  "s_waitcnt vmcnt(15) lgkmcnt(5)\n"
  "v_mfma_f32_16x16x32_bf16 v[64:67], v[112:115], a[44:47], v[64:67]\n"
  "ds_read_b64_tr_b16 v[112:113], v224 offset:1024\n"
  "v_mfma_f32_16x16x32_bf16 v[72:75], a[36:39], v[116:119], v[72:75]\n"
  "ds_read_b64_tr_b16 v[114:115], v224 offset:4096\n"
  "s_add_u32 m0, s10, 106496\n"
  "s_nop 0\n"
  "buffer_load_dwordx4 v185, s[32:35], 0 offen lds\n"
  "v_mfma_f32_16x16x32_bf16 v[80:83], a[40:43], v[116:119], v[80:83]\n"
  "v_mfma_f32_16x16x32_bf16 v[88:91], a[44:47], v[116:119], v[88:91]\n"
  "ds_read_b64_tr_b16 v[116:117], v224 offset:2048\n"
  "v_mfma_f32_16x16x32_bf16 v[96:99], v[120:123], a[40:43], v[96:99]\n"
  "ds_read_b64_tr_b16 v[118:119], v224 offset:5120\n"
  "s_add_u32 m0, s10, 107520\n"
  "s_nop 0\n"
  "buffer_load_dwordx4 v185, s[32:35], s73 offen lds\n"
  "v_mfma_f32_16x16x32_bf16 v[104:107], v[120:123], a[44:47], v[104:107]\n"
  "ds_read_b64_tr_b16 v[120:121], v224 offset:0\n"
  "s_waitcnt lgkmcnt(5)\n"
  "v_mfma_f32_16x16x32_bf16 v[52:55], v[124:127], a[36:39], v[52:55]\n"
  "ds_read_b64_tr_b16 v[122:123], v224 offset:3072\n"
  "v_mfma_f32_16x16x32_bf16 v[60:63], v[124:127], a[40:43], v[60:63]\n"
  "s_add_u32 m0, s10, 108544\n"
  "s_nop 0\n"
  "buffer_load_dwordx4 v185, s[32:35], s75 offen lds\n"
  "v_mfma_f32_16x16x32_bf16 v[68:71], v[124:127], a[44:47], v[68:71]\n"
  "ds_read_b64_tr_b16 v[124:125], v225 offset:1024\n"
  "v_mfma_f32_16x16x32_bf16 v[76:79], a[36:39], v[128:131], v[76:79]\n"
  "ds_read_b64_tr_b16 v[126:127], v225 offset:4096\n"
  "v_mfma_f32_16x16x32_bf16 v[84:87], a[40:43], v[128:131], v[84:87]\n"
  "v_mfma_f32_16x16x32_bf16 v[92:95], a[44:47], v[128:131], v[92:95]\n"
  "ds_read_b64_tr_b16 v[128:129], v225 offset:2048\n"
  "s_add_u32 m0, s10, 109568\n"
  "s_nop 0\n"
  "buffer_load_dwordx4 v185, s[32:35], s77 offen lds\n"
  "v_mfma_f32_16x16x32_bf16 v[100:103], v[132:135], a[40:43], v[100:103]\n"
  "ds_read_b64_tr_b16 v[130:131], v225 offset:5120\n"
  "v_mfma_f32_16x16x32_bf16 v[108:111], v[132:135], a[44:47], v[108:111]\n"
  "ds_read_b64_tr_b16 v[132:133], v225 offset:0\n"
  "s_waitcnt vmcnt(18) lgkmcnt(6)\n"
  "v_mfma_f32_16x16x32_bf16 v[48:51], v[112:115], a[48:51], v[48:51]\n"
  "ds_read_b64_tr_b16 v[134:135], v225 offset:3072\n"
  "s_add_u32 m0, s10, 110592\n"
  "s_nop 0\n"
  "buffer_load_dwordx4 v185, s[32:35], s79 offen lds\n"
  "s_waitcnt vmcnt(18) lgkmcnt(6)\n"
  "v_mfma_f32_16x16x32_bf16 v[56:59], v[112:115], a[52:55], v[56:59]\n"
  "s_waitcnt vmcnt(17) lgkmcnt(5)\n"
  "v_mfma_f32_16x16x32_bf16 v[64:67], v[112:115], a[56:59], v[64:67]\n"
  "ds_read_b64_tr_b16 v[112:113], v224 offset:13312\n"
  "v_mfma_f32_16x16x32_bf16 v[72:75], a[48:51], v[116:119], v[72:75]\n"
  "ds_read_b64_tr_b16 v[114:115], v224 offset:16384\n"
  "s_add_u32 m0, s10, 111616\n"
  "s_nop 0\n"
  "buffer_load_dwordx4 v185, s[32:35], s81 offen lds\n"
  "v_mfma_f32_16x16x32_bf16 v[80:83], a[52:55], v[116:119], v[80:83]\n"
  "s_waitcnt vmcnt(12) lgkmcnt(5)\n"
  "v_cmp_ne_u64 vcc, s[56:57], v[206:207]\n"
  "s_cmp_eq_u64 vcc, 0\n"
  "s_cbranch_scc0 .Lfb7_%=\n"
  ".Lbk7_%=:\n"
  "v_mfma_f32_16x16x32_bf16 v[88:91], a[56:59], v[116:119], v[88:91]\n"
  "ds_read_b64_tr_b16 v[116:117], v224 offset:14336\n"
  "v_mfma_f32_16x16x32_bf16 v[96:99], v[120:123], a[52:55], v[96:99]\n"
  "buffer_load_dwordx2 v[136:137], v231, s[20:23], 0 offen sc1\n"
  "ds_read_b64_tr_b16 v[118:119], v224 offset:17408\n"
  "v_mfma_f32_16x16x32_bf16 v[104:107], v[120:123], a[56:59], v[104:107]\n"
  "ds_read_b64_tr_b16 v[120:121], v224 offset:12288\n"
  "v_mfma_f32_16x16x32_bf16 v[52:55], v[124:127], a[48:51], v[52:55]\n"
  "ds_read_b64_tr_b16 v[122:123], v224 offset:15360\n"
  "v_mfma_f32_16x16x32_bf16 v[60:63], v[124:127], a[52:55], v[60:63]\n"
  "v_mfma_f32_16x16x32_bf16 v[68:71], v[124:127], a[56:59], v[68:71]\n"
  "buffer_load_dwordx2 v[138:139], v231, s[20:23], 0 offen offset:128 sc1\n"
  "ds_read_b64_tr_b16 v[124:125], v225 offset:13312\n"
  "s_waitcnt lgkmcnt(6)\n"
  "v_mfma_f32_16x16x32_bf16 v[76:79], a[48:51], v[128:131], v[76:79]\n"
  "ds_read_b64_tr_b16 v[126:127], v225 offset:16384\n"
  "v_mfma_f32_16x16x32_bf16 v[84:87], a[52:55], v[128:131], v[84:87]\n"
  "v_mfma_f32_16x16x32_bf16 v[92:95], a[56:59], v[128:131], v[92:95]\n"
  "ds_read_b64_tr_b16 v[128:129], v225 offset:14336\n"
  "buffer_load_dwordx2 v[140:141], v230, s[20:23], 0 offen sc1\n"
  "v_mfma_f32_16x16x32_bf16 v[100:103], v[132:135], a[52:55], v[100:103]\n"
  "ds_read_b64_tr_b16 v[130:131], v225 offset:17408\n"
  "v_mfma_f32_16x16x32_bf16 v[108:111], v[132:135], a[56:59], v[108:111]\n"
  "ds_read_b64_tr_b16 v[132:133], v225 offset:12288\n"
  "s_waitcnt lgkmcnt(6)\n"
  "v_mfma_f32_16x16x32_bf16 v[48:51], v[112:115], a[60:63], v[48:51]\n"
  "ds_read_b64_tr_b16 v[134:135], v225 offset:15360\n"
  "v_mfma_f32_16x16x32_bf16 v[56:59], v[112:115], a[64:67], v[56:59]\n"
  "buffer_load_dwordx2 v[142:143], v230, s[20:23], 0 offen offset:128 sc1\n"
  "v_mfma_f32_16x16x32_bf16 v[64:67], v[112:115], a[68:71], v[64:67]\n"
  "ds_read_b64_tr_b16 v[112:113], v224 offset:25600\n"
  "v_mfma_f32_16x16x32_bf16 v[72:75], a[60:63], v[116:119], v[72:75]\n"
  "ds_read_b64_tr_b16 v[114:115], v224 offset:28672\n"
  "v_mfma_f32_16x16x32_bf16 v[80:83], a[64:67], v[116:119], v[80:83]\n"
  "v_mfma_f32_16x16x32_bf16 v[88:91], a[68:71], v[116:119], v[88:91]\n"
  "buffer_load_dwordx2 v[144:145], v230, s[20:23], 0 offen offset:2048 sc1\n"
  "ds_read_b64_tr_b16 v[116:117], v224 offset:26624\n"
  "s_waitcnt lgkmcnt(6)\n"
  "v_mfma_f32_16x16x32_bf16 v[96:99], v[120:123], a[64:67], v[96:99]\n"
  "ds_read_b64_tr_b16 v[118:119], v224 offset:29696\n"
  "v_mfma_f32_16x16x32_bf16 v[104:107], v[120:123], a[68:71], v[104:107]\n"
  "ds_read_b64_tr_b16 v[120:121], v224 offset:24576\n"
  "v_mfma_f32_16x16x32_bf16 v[52:55], v[124:127], a[60:63], v[52:55]\n"
  "ds_read_b64_tr_b16 v[122:123], v224 offset:27648\n"
  "v_mfma_f32_16x16x32_bf16 v[60:63], v[124:127], a[64:67], v[60:63]\n"
  "buffer_load_dwordx2 v[146:147], v230, s[20:23], 0 offen offset:2176 sc1\n"
  "v_mfma_f32_16x16x32_bf16 v[68:71], v[124:127], a[68:71], v[68:71]\n"
  "ds_read_b64_tr_b16 v[124:125], v225 offset:25600\n"
  "s_waitcnt lgkmcnt(6)\n"
  "v_mfma_f32_16x16x32_bf16 v[76:79], a[60:63], v[128:131], v[76:79]\n"
  "ds_read_b64_tr_b16 v[126:127], v225 offset:28672\n"
  "v_mfma_f32_16x16x32_bf16 v[84:87], a[64:67], v[128:131], v[84:87]\n"
  "buffer_load_dwordx4 a[96:99], v227, s[20:23], s73 offen sc1\n"
  "v_mfma_f32_16x16x32_bf16 v[92:95], a[68:71], v[128:131], v[92:95]\n"
  "ds_read_b64_tr_b16 v[128:129], v225 offset:26624\n"
  "v_mfma_f32_16x16x32_bf16 v[100:103], v[132:135], a[64:67], v[100:103]\n"
  "ds_read_b64_tr_b16 v[130:131], v225 offset:29696\n"
  "v_mfma_f32_16x16x32_bf16 v[108:111], v[132:135], a[68:71], v[108:111]\n"
  "ds_read_b64_tr_b16 v[132:133], v225 offset:24576\n"
  "s_waitcnt vmcnt(18) lgkmcnt(6)\n"
  "v_mfma_f32_16x16x32_bf16 v[48:51], v[112:115], a[72:75], v[48:51]\n"
  "buffer_load_dwordx4 a[100:103], v228, s[20:23], s73 offen sc1\n"
  "ds_read_b64_tr_b16 v[134:135], v225 offset:27648\n"
  "v_mfma_f32_16x16x32_bf16 v[56:59], v[112:115], a[76:79], v[56:59]\n"
  "v_mfma_f32_16x16x32_bf16 v[64:67], v[112:115], a[80:83], v[64:67]\n"
  "ds_read_b64_tr_b16 v[112:113], v224 offset:37888\n"
  "v_mfma_f32_16x16x32_bf16 v[72:75], a[72:75], v[116:119], v[72:75]\n"
  "ds_read_b64_tr_b16 v[114:115], v224 offset:40960\n"
  "v_mfma_f32_16x16x32_bf16 v[80:83], a[76:79], v[116:119], v[80:83]\n"
  "buffer_load_dwordx4 a[104:107], v229, s[20:23], s73 offen sc1\n"
  "v_mfma_f32_16x16x32_bf16 v[88:91], a[80:83], v[116:119], v[88:91]\n"
  "ds_read_b64_tr_b16 v[116:117], v224 offset:38912\n"
  "s_waitcnt vmcnt(19) lgkmcnt(6)\n"
  "v_mfma_f32_16x16x32_bf16 v[96:99], v[120:123], a[76:79], v[96:99]\n"
  "ds_read_b64_tr_b16 v[118:119], v224 offset:41984\n"
  "v_mfma_f32_16x16x32_bf16 v[104:107], v[120:123], a[80:83], v[104:107]\n"
  "ds_read_b64_tr_b16 v[120:121], v224 offset:36864\n"
  "v_mfma_f32_16x16x32_bf16 v[52:55], v[124:127], a[72:75], v[52:55]\n"
  "buffer_load_dwordx4 a[108:111], v227, s[20:23], s73 offen offset:1024 sc1\n"
  "ds_read_b64_tr_b16 v[122:123], v224 offset:39936\n"
  "v_mfma_f32_16x16x32_bf16 v[60:63], v[124:127], a[76:79], v[60:63]\n"
  "v_mfma_f32_16x16x32_bf16 v[68:71], v[124:127], a[80:83], v[68:71]\n"
  "ds_read_b64_tr_b16 v[124:125], v225 offset:37888\n"
  "s_waitcnt vmcnt(18) lgkmcnt(6)\n"
  "v_mfma_f32_16x16x32_bf16 v[76:79], a[72:75], v[128:131], v[76:79]\n"
  "ds_read_b64_tr_b16 v[126:127], v225 offset:40960\n"
  "buffer_load_dwordx4 a[112:115], v228, s[20:23], s73 offen offset:1024 sc1\n"
  "v_mfma_f32_16x16x32_bf16 v[84:87], a[76:79], v[128:131], v[84:87]\n"
  "v_mfma_f32_16x16x32_bf16 v[92:95], a[80:83], v[128:131], v[92:95]\n"
  "ds_read_b64_tr_b16 v[128:129], v225 offset:38912\n"
  "v_mfma_f32_16x16x32_bf16 v[100:103], v[132:135], a[76:79], v[100:103]\n"
  "ds_read_b64_tr_b16 v[130:131], v225 offset:41984\n"
  "v_mfma_f32_16x16x32_bf16 v[108:111], v[132:135], a[80:83], v[108:111]\n"
  "ds_read_b64_tr_b16 v[132:133], v225 offset:36864\n"
  "buffer_load_dwordx4 a[116:119], v229, s[20:23], s73 offen offset:1024 sc1\n"
  "s_waitcnt lgkmcnt(6)\n"
  "v_mfma_f32_16x16x32_bf16 v[48:51], v[112:115], a[84:87], v[48:51]\n"
  "ds_read_b64_tr_b16 v[134:135], v225 offset:39936\n"
  "v_mfma_f32_16x16x32_bf16 v[56:59], v[112:115], a[88:91], v[56:59]\n"
  "v_mfma_f32_16x16x32_bf16 v[64:67], v[112:115], a[92:95], v[64:67]\n"
  "ds_read_b64_tr_b16 v[112:113], v222 offset:7168\n"
  "v_mfma_f32_16x16x32_bf16 v[72:75], a[84:87], v[116:119], v[72:75]\n"
  "buffer_load_dword v44, v250, s[20:23], 0 offen sc1\n"
  "ds_read_b64_tr_b16 v[114:115], v222 offset:10240\n"
  "v_mfma_f32_16x16x32_bf16 v[80:83], a[88:91], v[116:119], v[80:83]\n"
  "s_lshl_b32 s88, s61, 9\n"
  "s_add_u32 s88, s88, 81920\n"
  "v_mfma_f32_16x16x32_bf16 v[88:91], a[92:95], v[116:119], v[88:91]\n"
  "ds_read_b64_tr_b16 v[116:117], v222 offset:8192\n"
  "v_lshl_add_u32 v249, v2, 3, s88\n"
  "s_waitcnt lgkmcnt(6)\n"
  "v_mfma_f32_16x16x32_bf16 v[96:99], v[120:123], a[88:91], v[96:99]\n"
  "ds_read_b64_tr_b16 v[118:119], v222 offset:11264\n"
  "s_lshl_b32 s88, s67, 14\n"
  "v_mfma_f32_16x16x32_bf16 v[104:107], v[120:123], a[92:95], v[104:107]\n"
  "buffer_load_dwordx4 a[120:123], v227, s[20:23], s73 offen offset:2048 sc1\n"
  "ds_read_b64_tr_b16 v[120:121], v222 offset:6144\n"
  "v_mfma_f32_16x16x32_bf16 v[52:55], v[124:127], a[84:87], v[52:55]\n"
  "ds_read_b64_tr_b16 v[122:123], v222 offset:9216\n"
  "s_add_u32 s88, s88, 4194304\n"
  "v_mfma_f32_16x16x32_bf16 v[60:63], v[124:127], a[88:91], v[60:63]\n"
  "s_waitcnt vmcnt(12) lgkmcnt(6)\n"
  "v_add_f32 v136, v136, v138\n"
  "v_mfma_f32_16x16x32_bf16 v[68:71], v[124:127], a[92:95], v[68:71]\n"
  "ds_read_b64_tr_b16 v[124:125], v223 offset:7168\n"
  "buffer_load_dwordx4 a[124:127], v228, s[20:23], s73 offen offset:2048 sc1\n"
  "v_mfma_f32_16x16x32_bf16 v[76:79], a[84:87], v[128:131], v[76:79]\n"
  "ds_read_b64_tr_b16 v[126:127], v223 offset:10240\n"
  "v_add_f32 v137, v137, v139\n"
  "v_mfma_f32_16x16x32_bf16 v[84:87], a[88:91], v[128:131], v[84:87]\n"
  "s_nop 0\n"
  "v_permlane16_swap_b32 v136, v137\n"
  "v_mfma_f32_16x16x32_bf16 v[92:95], a[92:95], v[128:131], v[92:95]\n"
  "ds_read_b64_tr_b16 v[128:129], v223 offset:8192\n"
  "v_add_f32 v136, v136, v137\n"
  "v_mfma_f32_16x16x32_bf16 v[100:103], v[132:135], a[88:91], v[100:103]\n"
  "buffer_load_dwordx4 a[128:131], v229, s[20:23], s73 offen offset:2048 sc1\n"
  "v_mfma_f32_16x16x32_bf16 v[108:111], v[132:135], a[92:95], v[108:111]\n"
  "ds_read_b64_tr_b16 v[130:131], v223 offset:11264\n"
  "ds_read_b64_tr_b16 v[132:133], v223 offset:6144\n"
  "ds_read_b64_tr_b16 v[134:135], v223 offset:9216\n"
  "s_waitcnt vmcnt(12) lgkmcnt(7)\n"
  "v_add_f32 v140, v140, v142\n"
  "v_add_f32 v141, v141, v143\n"
  "v_mov_b32 v137, v136\n"
  "v_add_u32 v250, s88, v1\n"
  "v_permlane16_swap_b32 v140, v141\n"
  "v_add_f32 v140, v140, v141\n"
  "v_mov_b32 v141, v140\n"
  "buffer_load_dwordx4 a[132:135], v227, s[20:23], s73 offen offset:3072 sc1\n"
  "v_permlane32_swap_b32 v136, v137\n"
  "v_permlane32_swap_b32 v140, v141\n"
  "v_add_f32 v136, v136, v137\n"
  "v_add_f32 v140, v140, v141\n"
  "v_mov_b32 v137, v136\n"
  "v_mov_b32 v141, v140\n"
  "s_nop 0\n"
  "v_permlane16_swap_b32 v136, v137\n"
  "v_permlane16_swap_b32 v140, v141\n"
  "v_mul_f32 v138, s4, v136\n"
  "v_mul_f32 v142, s4, v140\n"
  "buffer_load_dwordx4 a[136:139], v228, s[20:23], s73 offen offset:3072 sc1\n"
  "v_mul_f32 v139, v138, v138\n"
  "v_mul_f32 v143, v142, v142\n"
  "v_fma_f32 v137, v137, s4, -v139\n"
  "v_fma_f32 v141, v141, s4, -v143\n"
  "v_max_f32 v137, 0, v137\n"
  "s_waitcnt vmcnt(12) lgkmcnt(3)\n"
  "v_add_f32 v144, v144, v146\n"
  "v_add_f32 v145, v145, v147\n"
  "v_max_f32 v141, 0, v141\n"
  "v_add_f32 v137, s5, v137\n"
  "v_permlane16_swap_b32 v144, v145\n"
  "v_add_f32 v144, v144, v145\n"
  "v_mov_b32 v145, v144\n"
  "v_add_f32 v141, s5, v141\n"
  "buffer_load_dwordx4 a[140:143], v229, s[20:23], s73 offen offset:3072 sc1\n"
  "v_permlane32_swap_b32 v144, v145\n"
  "v_add_f32 v144, v144, v145\n"
  "v_mov_b32 v145, v144\n"
  "s_waitcnt vmcnt(12) lgkmcnt(0)\n"
  "v_mfma_f32_16x16x32_bf16 v[48:51], v[112:115], a[96:99], v[48:51]\n"
  "v_permlane16_swap_b32 v144, v145\n"
  "v_mul_f32 v146, s4, v144\n"
  "v_mul_f32 v147, v146, v146\n"
  "v_fma_f32 v145, v145, s4, -v147\n"
  "v_max_f32 v145, 0, v145\n"
  "v_add_f32 v145, s5, v145\n"
  "buffer_load_dwordx4 a[144:147], v227, s[20:23], s74 offen sc1\n"
  "v_rsq_f32 v139, v137\n"
  "v_rsq_f32 v143, v141\n"
  "ds_bpermute_b32 v148, v226, v138 offset:0\n"
  "s_waitcnt vmcnt(12)\n"
  "v_mfma_f32_16x16x32_bf16 v[56:59], v[112:115], a[100:103], v[56:59]\n"
  "v_rsq_f32 v147, v145\n"
  "ds_bpermute_b32 v149, v226, v138 offset:4\n"
  "ds_bpermute_b32 v150, v226, v138 offset:8\n"
  "ds_bpermute_b32 v151, v226, v138 offset:12\n"
  "ds_bpermute_b32 v156, v226, v142 offset:0\n"
  "buffer_load_dwordx4 a[148:151], v228, s[20:23], s74 offen sc1\n"
  "ds_bpermute_b32 v157, v226, v142 offset:4\n"
  "ds_bpermute_b32 v158, v226, v142 offset:8\n"
  "v_mul_f32 v140, s6, v143\n"
  "ds_bpermute_b32 v159, v226, v142 offset:12\n"
  "s_waitcnt vmcnt(12)\n"
  "v_mfma_f32_16x16x32_bf16 v[64:67], v[112:115], a[104:107], v[64:67]\n"
  "ds_read_b64_tr_b16 v[112:113], v222 offset:19456\n"
  "v_mul_f32 v144, s6, v147\n"
  "v_mfma_f32_16x16x32_bf16 v[72:75], a[96:99], v[116:119], v[72:75]\n"
  "ds_read_b64_tr_b16 v[114:115], v222 offset:22528\n"
  "v_mfma_f32_16x16x32_bf16 v[80:83], a[100:103], v[116:119], v[80:83]\n"
  "buffer_load_dwordx4 a[152:155], v229, s[20:23], s74 offen sc1\n"
  "ds_bpermute_b32 v152, v226, v139 offset:0\n"
  "v_mfma_f32_16x16x32_bf16 v[88:91], a[104:107], v[116:119], v[88:91]\n"
  "ds_read_b64_tr_b16 v[116:117], v222 offset:20480\n"
  "v_mfma_f32_16x16x32_bf16 v[96:99], v[120:123], a[100:103], v[96:99]\n"
  "ds_read_b64_tr_b16 v[118:119], v222 offset:23552\n"
  "v_mfma_f32_16x16x32_bf16 v[104:107], v[120:123], a[104:107], v[104:107]\n"
  "ds_read_b64_tr_b16 v[120:121], v222 offset:18432\n"
  "v_mfma_f32_16x16x32_bf16 v[52:55], v[124:127], a[96:99], v[52:55]\n"
  "ds_read_b64_tr_b16 v[122:123], v222 offset:21504\n"
  "buffer_load_dwordx4 a[156:159], v227, s[20:23], s74 offen offset:1024 sc1\n"
  "v_mfma_f32_16x16x32_bf16 v[60:63], v[124:127], a[100:103], v[60:63]\n"
  "ds_bpermute_b32 v153, v226, v139 offset:4\n"
  "v_mfma_f32_16x16x32_bf16 v[68:71], v[124:127], a[104:107], v[68:71]\n"
  "ds_read_b64_tr_b16 v[124:125], v223 offset:19456\n"
  "v_mfma_f32_16x16x32_bf16 v[76:79], a[96:99], v[128:131], v[76:79]\n"
  "ds_read_b64_tr_b16 v[126:127], v223 offset:22528\n"
  "v_mfma_f32_16x16x32_bf16 v[84:87], a[100:103], v[128:131], v[84:87]\n"
  "buffer_load_dwordx4 a[160:163], v228, s[20:23], s74 offen offset:1024 sc1\n"
  "ds_bpermute_b32 v154, v226, v139 offset:8\n"
  "v_mfma_f32_16x16x32_bf16 v[92:95], a[104:107], v[128:131], v[92:95]\n"
  "ds_read_b64_tr_b16 v[128:129], v223 offset:20480\n"
  "v_mfma_f32_16x16x32_bf16 v[100:103], v[132:135], a[100:103], v[100:103]\n"
  "ds_read_b64_tr_b16 v[130:131], v223 offset:23552\n"
  "v_mfma_f32_16x16x32_bf16 v[108:111], v[132:135], a[104:107], v[108:111]\n"
  "ds_read_b64_tr_b16 v[132:133], v223 offset:18432\n"
  "s_waitcnt vmcnt(14) lgkmcnt(9)\n"
  "v_mfma_f32_16x16x32_bf16 v[48:51], v[112:115], a[108:111], v[48:51]\n"
  "ds_read_b64_tr_b16 v[134:135], v223 offset:21504\n"
  "buffer_load_dwordx4 a[164:167], v229, s[20:23], s74 offen offset:1024 sc1\n"
  "s_waitcnt vmcnt(14) lgkmcnt(8)\n"
  "v_mfma_f32_16x16x32_bf16 v[56:59], v[112:115], a[112:115], v[56:59]\n"
  "ds_bpermute_b32 v155, v226, v139 offset:12\n"
  "s_waitcnt vmcnt(13) lgkmcnt(8)\n"
  "v_mfma_f32_16x16x32_bf16 v[64:67], v[112:115], a[116:119], v[64:67]\n"
  "ds_read_b64_tr_b16 v[112:113], v222 offset:31744\n"
  "v_mfma_f32_16x16x32_bf16 v[72:75], a[108:111], v[116:119], v[72:75]\n"
  "ds_read_b64_tr_b16 v[114:115], v222 offset:34816\n"
  "buffer_load_dwordx4 a[168:171], v227, s[20:23], s74 offen offset:2048 sc1\n"
  "v_mfma_f32_16x16x32_bf16 v[80:83], a[112:115], v[116:119], v[80:83]\n"
  "ds_bpermute_b32 v160, v226, v143 offset:0\n"
  "v_mfma_f32_16x16x32_bf16 v[88:91], a[116:119], v[116:119], v[88:91]\n"
  "ds_read_b64_tr_b16 v[116:117], v222 offset:32768\n"
  "v_mfma_f32_16x16x32_bf16 v[96:99], v[120:123], a[112:115], v[96:99]\n"
  "ds_read_b64_tr_b16 v[118:119], v222 offset:35840\n"
  "v_mfma_f32_16x16x32_bf16 v[104:107], v[120:123], a[116:119], v[104:107]\n"
  "ds_read_b64_tr_b16 v[120:121], v222 offset:30720\n"
  "buffer_load_dwordx4 a[172:175], v228, s[20:23], s74 offen offset:2048 sc1\n"
  "s_waitcnt lgkmcnt(8)\n"
  "v_mfma_f32_16x16x32_bf16 v[52:55], v[124:127], a[108:111], v[52:55]\n"
  "ds_read_b64_tr_b16 v[122:123], v222 offset:33792\n"
  "v_mfma_f32_16x16x32_bf16 v[60:63], v[124:127], a[112:115], v[60:63]\n"
  "ds_bpermute_b32 v161, v226, v143 offset:4\n"
  "v_mfma_f32_16x16x32_bf16 v[68:71], v[124:127], a[116:119], v[68:71]\n"
  "ds_read_b64_tr_b16 v[124:125], v223 offset:31744\n"
  "buffer_load_dwordx4 a[176:179], v229, s[20:23], s74 offen offset:2048 sc1\n"
  "v_mfma_f32_16x16x32_bf16 v[76:79], a[108:111], v[128:131], v[76:79]\n"
  "ds_read_b64_tr_b16 v[126:127], v223 offset:34816\n"
  "v_mfma_f32_16x16x32_bf16 v[84:87], a[112:115], v[128:131], v[84:87]\n"
  "ds_bpermute_b32 v162, v226, v143 offset:8\n"
  "v_mfma_f32_16x16x32_bf16 v[92:95], a[116:119], v[128:131], v[92:95]\n"
  "ds_read_b64_tr_b16 v[128:129], v223 offset:32768\n"
  "s_waitcnt lgkmcnt(8)\n"
  "v_mfma_f32_16x16x32_bf16 v[100:103], v[132:135], a[112:115], v[100:103]\n"
  "ds_read_b64_tr_b16 v[130:131], v223 offset:35840\n"
  "buffer_load_dwordx4 a[180:183], v227, s[20:23], s74 offen offset:3072 sc1\n"
  "v_mfma_f32_16x16x32_bf16 v[108:111], v[132:135], a[116:119], v[108:111]\n"
  "ds_read_b64_tr_b16 v[132:133], v223 offset:30720\n"
  "s_waitcnt vmcnt(15) lgkmcnt(8)\n"
  "v_mfma_f32_16x16x32_bf16 v[48:51], v[112:115], a[120:123], v[48:51]\n"
  "ds_read_b64_tr_b16 v[134:135], v223 offset:33792\n"
  "s_waitcnt vmcnt(14) lgkmcnt(8)\n"
  "v_mfma_f32_16x16x32_bf16 v[56:59], v[112:115], a[124:127], v[56:59]\n"
  "ds_bpermute_b32 v163, v226, v143 offset:12\n"
  "buffer_load_dwordx4 a[184:187], v228, s[20:23], s74 offen offset:3072 sc1\n"
  "s_waitcnt vmcnt(14) lgkmcnt(8)\n"
  "v_mfma_f32_16x16x32_bf16 v[64:67], v[112:115], a[128:131], v[64:67]\n"
  "ds_read_b64_tr_b16 v[112:113], v222 offset:44032\n"
  "v_mfma_f32_16x16x32_bf16 v[72:75], a[120:123], v[116:119], v[72:75]\n"
  "ds_read_b64_tr_b16 v[114:115], v222 offset:47104\n"
  "v_mfma_f32_16x16x32_bf16 v[80:83], a[124:127], v[116:119], v[80:83]\n"
  "ds_bpermute_b32 v164, v226, v146 offset:0\n"
  "v_mfma_f32_16x16x32_bf16 v[88:91], a[128:131], v[116:119], v[88:91]\n"
  "ds_read_b64_tr_b16 v[116:117], v222 offset:45056\n"
  "buffer_load_dwordx4 a[188:191], v229, s[20:23], s74 offen offset:3072 sc1\n"
  "v_mfma_f32_16x16x32_bf16 v[96:99], v[120:123], a[124:127], v[96:99]\n"
  "ds_read_b64_tr_b16 v[118:119], v222 offset:48128\n"
  "v_mfma_f32_16x16x32_bf16 v[104:107], v[120:123], a[128:131], v[104:107]\n"
  "ds_read_b64_tr_b16 v[120:121], v222 offset:43008\n"
  "s_waitcnt lgkmcnt(8)\n"
  "v_mfma_f32_16x16x32_bf16 v[52:55], v[124:127], a[120:123], v[52:55]\n"
  "ds_read_b64_tr_b16 v[122:123], v222 offset:46080\n"
  "v_mfma_f32_16x16x32_bf16 v[60:63], v[124:127], a[124:127], v[60:63]\n"
  "ds_bpermute_b32 v165, v226, v146 offset:4\n"
  "s_add_u32 m0, s10, 112640\n"
  "s_nop 0\n"
  "buffer_load_dwordx4 v185, s[32:35], s83 offen lds\n"
  "v_mfma_f32_16x16x32_bf16 v[68:71], v[124:127], a[128:131], v[68:71]\n"
  "ds_read_b64_tr_b16 v[124:125], v223 offset:44032\n"
  "v_mfma_f32_16x16x32_bf16 v[76:79], a[120:123], v[128:131], v[76:79]\n"
  "ds_read_b64_tr_b16 v[126:127], v223 offset:47104\n"
  "v_mfma_f32_16x16x32_bf16 v[84:87], a[124:127], v[128:131], v[84:87]\n"
  "ds_bpermute_b32 v166, v226, v146 offset:8\n"
  "s_add_u32 m0, s10, 113664\n"
  "s_nop 0\n"
  "buffer_load_dwordx4 v185, s[32:35], s85 offen lds\n"
  "v_mfma_f32_16x16x32_bf16 v[92:95], a[128:131], v[128:131], v[92:95]\n"
  "ds_read_b64_tr_b16 v[128:129], v223 offset:45056\n"
  "s_waitcnt lgkmcnt(7)\n"
  "v_mfma_f32_16x16x32_bf16 v[100:103], v[132:135], a[124:127], v[100:103]\n"
  "ds_read_b64_tr_b16 v[130:131], v223 offset:48128\n"
  "v_mfma_f32_16x16x32_bf16 v[108:111], v[132:135], a[128:131], v[108:111]\n"
  "ds_read_b64_tr_b16 v[132:133], v223 offset:43008\n"
  "buffer_load_dword v44, v200, s[36:39], 0 offen\n"
  "s_waitcnt vmcnt(17) lgkmcnt(7)\n"
  "v_mfma_f32_16x16x32_bf16 v[48:51], v[112:115], a[132:135], v[48:51]\n"
  "ds_read_b64_tr_b16 v[134:135], v223 offset:46080\n"
  "s_waitcnt vmcnt(16) lgkmcnt(7)\n"
  "v_mfma_f32_16x16x32_bf16 v[56:59], v[112:115], a[136:139], v[56:59]\n"
  "ds_bpermute_b32 v167, v226, v146 offset:12\n"
  "s_waitcnt vmcnt(15) lgkmcnt(7)\n"
  "v_mfma_f32_16x16x32_bf16 v[64:67], v[112:115], a[140:143], v[64:67]\n"
  "ds_read_b64_tr_b16 v[112:113], v224 offset:7168\n"
  "v_mfma_f32_16x16x32_bf16 v[72:75], a[132:135], v[116:119], v[72:75]\n"
  "ds_read_b64_tr_b16 v[114:115], v224 offset:10240\n"
  "v_mfma_f32_16x16x32_bf16 v[80:83], a[136:139], v[116:119], v[80:83]\n"
  "ds_bpermute_b32 v168, v226, v147 offset:0\n"
  "v_mfma_f32_16x16x32_bf16 v[88:91], a[140:143], v[116:119], v[88:91]\n"
  "ds_read_b64_tr_b16 v[116:117], v224 offset:8192\n"
  "v_mfma_f32_16x16x32_bf16 v[96:99], v[120:123], a[136:139], v[96:99]\n"
  "ds_read_b64_tr_b16 v[118:119], v224 offset:11264\n"
  "v_mfma_f32_16x16x32_bf16 v[104:107], v[120:123], a[140:143], v[104:107]\n"
  "ds_read_b64_tr_b16 v[120:121], v224 offset:6144\n"
  "s_waitcnt lgkmcnt(8)\n"
  "v_mfma_f32_16x16x32_bf16 v[52:55], v[124:127], a[132:135], v[52:55]\n"
  "ds_read_b64_tr_b16 v[122:123], v224 offset:9216\n"
  "v_mfma_f32_16x16x32_bf16 v[60:63], v[124:127], a[136:139], v[60:63]\n"
  "ds_bpermute_b32 v169, v226, v147 offset:4\n"
  "v_mfma_f32_16x16x32_bf16 v[68:71], v[124:127], a[140:143], v[68:71]\n"
  "ds_read_b64_tr_b16 v[124:125], v225 offset:7168\n"
  "v_mfma_f32_16x16x32_bf16 v[76:79], a[132:135], v[128:131], v[76:79]\n"
  "buffer_load_dword v44, v201, s[36:39], 0 offen\n"
  "ds_read_b64_tr_b16 v[126:127], v225 offset:10240\n"
  "v_mfma_f32_16x16x32_bf16 v[84:87], a[136:139], v[128:131], v[84:87]\n"
  "ds_bpermute_b32 v170, v226, v147 offset:8\n"
  "v_mfma_f32_16x16x32_bf16 v[92:95], a[140:143], v[128:131], v[92:95]\n"
  "ds_read_b64_tr_b16 v[128:129], v225 offset:8192\n"
  "s_waitcnt lgkmcnt(8)\n"
  "v_mfma_f32_16x16x32_bf16 v[100:103], v[132:135], a[136:139], v[100:103]\n"
  "ds_read_b64_tr_b16 v[130:131], v225 offset:11264\n"
  "v_mfma_f32_16x16x32_bf16 v[108:111], v[132:135], a[140:143], v[108:111]\n"
  "ds_read_b64_tr_b16 v[132:133], v225 offset:6144\n"
  "s_waitcnt vmcnt(15) lgkmcnt(8)\n"
  "v_mfma_f32_16x16x32_bf16 v[48:51], v[112:115], a[144:147], v[48:51]\n"
  "ds_read_b64_tr_b16 v[134:135], v225 offset:9216\n"
  "s_waitcnt vmcnt(14) lgkmcnt(8)\n"
  "v_mfma_f32_16x16x32_bf16 v[56:59], v[112:115], a[148:151], v[56:59]\n"
  "ds_bpermute_b32 v171, v226, v147 offset:12\n"
  "s_waitcnt vmcnt(13) lgkmcnt(8)\n"
  "v_mfma_f32_16x16x32_bf16 v[64:67], v[112:115], a[152:155], v[64:67]\n"
  "ds_read_b64_tr_b16 v[112:113], v224 offset:19456\n"
  "v_mfma_f32_16x16x32_bf16 v[72:75], a[144:147], v[116:119], v[72:75]\n"
  "ds_read_b64_tr_b16 v[114:115], v224 offset:22528\n"
  "v_mfma_f32_16x16x32_bf16 v[80:83], a[148:151], v[116:119], v[80:83]\n"
  "v_mfma_f32_16x16x32_bf16 v[88:91], a[152:155], v[116:119], v[88:91]\n"
  "ds_read_b64_tr_b16 v[116:117], v224 offset:20480\n"
  "v_mfma_f32_16x16x32_bf16 v[96:99], v[120:123], a[148:151], v[96:99]\n"
  "ds_read_b64_tr_b16 v[118:119], v224 offset:23552\n"
  "v_mfma_f32_16x16x32_bf16 v[104:107], v[120:123], a[152:155], v[104:107]\n"
  "ds_read_b64_tr_b16 v[120:121], v224 offset:18432\n"
  "s_waitcnt lgkmcnt(7)\n"
  "v_mfma_f32_16x16x32_bf16 v[52:55], v[124:127], a[144:147], v[52:55]\n"
  "ds_read_b64_tr_b16 v[122:123], v224 offset:21504\n"
  "v_mfma_f32_16x16x32_bf16 v[60:63], v[124:127], a[148:151], v[60:63]\n"
  "v_mfma_f32_16x16x32_bf16 v[68:71], v[124:127], a[152:155], v[68:71]\n"
  "ds_read_b64_tr_b16 v[124:125], v225 offset:19456\n"
  "v_mfma_f32_16x16x32_bf16 v[76:79], a[144:147], v[128:131], v[76:79]\n"
  "ds_read_b64_tr_b16 v[126:127], v225 offset:22528\n"
  "v_mfma_f32_16x16x32_bf16 v[84:87], a[148:151], v[128:131], v[84:87]\n"
  "v_mfma_f32_16x16x32_bf16 v[92:95], a[152:155], v[128:131], v[92:95]\n"
  "ds_read_b64_tr_b16 v[128:129], v225 offset:20480\n"
  "s_waitcnt vmcnt(12) lgkmcnt(6)\n"
  "v_mfma_f32_16x16x32_bf16 v[100:103], v[132:135], a[148:151], v[100:103]\n"
  "ds_read_b64_tr_b16 v[130:131], v225 offset:23552\n"
  "v_mfma_f32_16x16x32_bf16 v[108:111], v[132:135], a[152:155], v[108:111]\n"
  "ds_read_b64_tr_b16 v[132:133], v225 offset:18432\n"
  "v_mfma_f32_16x16x32_bf16 v[48:51], v[112:115], a[156:159], v[48:51]\n"
  "ds_read_b64_tr_b16 v[134:135], v225 offset:21504\n"
  "s_waitcnt vmcnt(11) lgkmcnt(6)\n"
  "v_mfma_f32_16x16x32_bf16 v[56:59], v[112:115], a[160:163], v[56:59]\n"
  "s_waitcnt vmcnt(10)\n"
  "v_mfma_f32_16x16x32_bf16 v[64:67], v[112:115], a[164:167], v[64:67]\n"
  "ds_read_b64_tr_b16 v[112:113], v224 offset:31744\n"
  "v_mfma_f32_16x16x32_bf16 v[72:75], a[156:159], v[116:119], v[72:75]\n"
  "ds_read_b64_tr_b16 v[114:115], v224 offset:34816\n"
  "v_mfma_f32_16x16x32_bf16 v[80:83], a[160:163], v[116:119], v[80:83]\n"
  "v_mfma_f32_16x16x32_bf16 v[88:91], a[164:167], v[116:119], v[88:91]\n"
  "ds_read_b64_tr_b16 v[116:117], v224 offset:32768\n"
  "v_mfma_f32_16x16x32_bf16 v[96:99], v[120:123], a[160:163], v[96:99]\n"
  "ds_read_b64_tr_b16 v[118:119], v224 offset:35840\n"
  "v_mfma_f32_16x16x32_bf16 v[104:107], v[120:123], a[164:167], v[104:107]\n"
  "ds_read_b64_tr_b16 v[120:121], v224 offset:30720\n"
  "s_waitcnt vmcnt(9) lgkmcnt(6)\n"
  "v_mfma_f32_16x16x32_bf16 v[52:55], v[124:127], a[156:159], v[52:55]\n"
  "ds_read_b64_tr_b16 v[122:123], v224 offset:33792\n"
  "v_mfma_f32_16x16x32_bf16 v[60:63], v[124:127], a[160:163], v[60:63]\n"
  "v_mfma_f32_16x16x32_bf16 v[68:71], v[124:127], a[164:167], v[68:71]\n"
  "ds_read_b64_tr_b16 v[124:125], v225 offset:31744\n"
  "v_mfma_f32_16x16x32_bf16 v[76:79], a[156:159], v[128:131], v[76:79]\n"
  "ds_read_b64_tr_b16 v[126:127], v225 offset:34816\n"
  "v_mfma_f32_16x16x32_bf16 v[84:87], a[160:163], v[128:131], v[84:87]\n"
  "v_mfma_f32_16x16x32_bf16 v[92:95], a[164:167], v[128:131], v[92:95]\n"
  "ds_read_b64_tr_b16 v[128:129], v225 offset:32768\n"
  "s_waitcnt vmcnt(8) lgkmcnt(6)\n"
  "v_mfma_f32_16x16x32_bf16 v[100:103], v[132:135], a[160:163], v[100:103]\n"
  "ds_read_b64_tr_b16 v[130:131], v225 offset:35840\n"
  "v_mfma_f32_16x16x32_bf16 v[108:111], v[132:135], a[164:167], v[108:111]\n"
  "ds_read_b64_tr_b16 v[132:133], v225 offset:30720\n"
  "v_mfma_f32_16x16x32_bf16 v[48:51], v[112:115], a[168:171], v[48:51]\n"
  "ds_read_b64_tr_b16 v[134:135], v225 offset:33792\n"
  "v_mfma_f32_16x16x32_bf16 v[56:59], v[112:115], a[172:175], v[56:59]\n"
  "s_waitcnt vmcnt(7) lgkmcnt(6)\n"
  "v_mfma_f32_16x16x32_bf16 v[64:67], v[112:115], a[176:179], v[64:67]\n"
  "ds_read_b64_tr_b16 v[112:113], v224 offset:44032\n"
  "v_mfma_f32_16x16x32_bf16 v[72:75], a[168:171], v[116:119], v[72:75]\n"
  "ds_read_b64_tr_b16 v[114:115], v224 offset:47104\n"
  "v_mfma_f32_16x16x32_bf16 v[80:83], a[172:175], v[116:119], v[80:83]\n"
  "v_mfma_f32_16x16x32_bf16 v[88:91], a[176:179], v[116:119], v[88:91]\n"
  "ds_read_b64_tr_b16 v[116:117], v224 offset:45056\n"
  "v_mfma_f32_16x16x32_bf16 v[96:99], v[120:123], a[172:175], v[96:99]\n"
  "ds_read_b64_tr_b16 v[118:119], v224 offset:48128\n"
  "v_mfma_f32_16x16x32_bf16 v[104:107], v[120:123], a[176:179], v[104:107]\n"
  "ds_read_b64_tr_b16 v[120:121], v224 offset:43008\n"
  "s_waitcnt vmcnt(5) lgkmcnt(6)\n"
  "v_mfma_f32_16x16x32_bf16 v[52:55], v[124:127], a[168:171], v[52:55]\n"
  "ds_read_b64_tr_b16 v[122:123], v224 offset:46080\n"
  "v_mfma_f32_16x16x32_bf16 v[60:63], v[124:127], a[172:175], v[60:63]\n"
  "v_mfma_f32_16x16x32_bf16 v[68:71], v[124:127], a[176:179], v[68:71]\n"
  "ds_read_b64_tr_b16 v[124:125], v225 offset:44032\n"
  "v_mfma_f32_16x16x32_bf16 v[76:79], a[168:171], v[128:131], v[76:79]\n"
  "ds_read_b64_tr_b16 v[126:127], v225 offset:47104\n"
  "v_mfma_f32_16x16x32_bf16 v[84:87], a[172:175], v[128:131], v[84:87]\n"
  "v_mfma_f32_16x16x32_bf16 v[92:95], a[176:179], v[128:131], v[92:95]\n"
  "ds_read_b64_tr_b16 v[128:129], v225 offset:45056\n"
  "s_waitcnt vmcnt(4) lgkmcnt(6)\n"
  "v_mfma_f32_16x16x32_bf16 v[100:103], v[132:135], a[172:175], v[100:103]\n"
  "ds_read_b64_tr_b16 v[130:131], v225 offset:48128\n"
  "v_mfma_f32_16x16x32_bf16 v[108:111], v[132:135], a[176:179], v[108:111]\n"
  "ds_read_b64_tr_b16 v[132:133], v225 offset:43008\n"
  "v_mfma_f32_16x16x32_bf16 v[48:51], v[112:115], a[180:183], v[48:51]\n"
  "ds_read_b64_tr_b16 v[134:135], v225 offset:46080\n"
  "v_mfma_f32_16x16x32_bf16 v[56:59], v[112:115], a[184:187], v[56:59]\n"
  "v_mfma_f32_16x16x32_bf16 v[64:67], v[112:115], a[188:191], v[64:67]\n"
  "s_waitcnt lgkmcnt(5)\n"
  "v_mfma_f32_16x16x32_bf16 v[72:75], a[180:183], v[116:119], v[72:75]\n"
  "v_mfma_f32_16x16x32_bf16 v[80:83], a[184:187], v[116:119], v[80:83]\n"
  "v_mfma_f32_16x16x32_bf16 v[88:91], a[188:191], v[116:119], v[88:91]\n"
  "s_waitcnt lgkmcnt(0)\n"
  "s_barrier\n"
  "v_mfma_f32_16x16x32_bf16 v[96:99], v[120:123], a[184:187], v[96:99]\n"
  "s_add_u32 m0, s8, 0\n"
  "s_mov_b32 s9, 1572864\n"
  "buffer_load_dwordx4 v240, s[40:43], s9 offen lds\n"
  "v_mfma_f32_16x16x32_bf16 v[104:107], v[120:123], a[188:191], v[104:107]\n"
  "s_add_u32 m0, s8, 1024\n"
  "s_mov_b32 s9, 1605632\n"
  "buffer_load_dwordx4 v240, s[40:43], s9 offen lds\n"
  "v_mfma_f32_16x16x32_bf16 v[52:55], v[124:127], a[180:183], v[52:55]\n"
  "v_mfma_f32_16x16x32_bf16 v[60:63], v[124:127], a[184:187], v[60:63]\n"
  "v_mfma_f32_16x16x32_bf16 v[68:71], v[124:127], a[188:191], v[68:71]\n"
  "v_mfma_f32_16x16x32_bf16 v[76:79], a[180:183], v[128:131], v[76:79]\n"
  "v_mfma_f32_16x16x32_bf16 v[84:87], a[184:187], v[128:131], v[84:87]\n"
  "v_mfma_f32_16x16x32_bf16 v[92:95], a[188:191], v[128:131], v[92:95]\n"
  "v_mfma_f32_16x16x32_bf16 v[100:103], v[132:135], a[184:187], v[100:103]\n"
  "v_mfma_f32_16x16x32_bf16 v[108:111], v[132:135], a[188:191], v[108:111]\n"
  "s_add_u32 m0, s8, 2048\n"
  "s_mov_b32 s9, 1638400\n"
  "buffer_load_dwordx4 v240, s[40:43], s9 offen lds\n"
  "v_pk_fma_f32 v[48:49], v[12:13], v[138:139], v[48:49] op_sel_hi:[1,0,1] neg_lo:[0,1,0] neg_hi:[0,1,0]\n"
  "v_pk_fma_f32 v[52:53], v[16:17], v[138:139], v[52:53] op_sel_hi:[1,0,1] neg_lo:[0,1,0] neg_hi:[0,1,0]\n"
  "v_pk_fma_f32 v[56:57], v[12:13], v[142:143], v[56:57] op_sel_hi:[1,0,1] neg_lo:[0,1,0] neg_hi:[0,1,0]\n"
  "v_pk_fma_f32 v[60:61], v[16:17], v[142:143], v[60:61] op_sel_hi:[1,0,1] neg_lo:[0,1,0] neg_hi:[0,1,0]\n"
  "v_pk_fma_f32 v[64:65], v[12:13], v[146:147], v[64:65] op_sel_hi:[1,0,1] neg_lo:[0,1,0] neg_hi:[0,1,0]\n"
  "v_pk_fma_f32 v[68:69], v[16:17], v[146:147], v[68:69] op_sel_hi:[1,0,1] neg_lo:[0,1,0] neg_hi:[0,1,0]\n"
  "v_pk_mul_f32 v[48:49], v[48:49], v[138:139] op_sel:[0,1]\n"
  "v_pk_fma_f32 v[50:51], v[14:15], v[138:139], v[50:51] op_sel_hi:[1,0,1] neg_lo:[0,1,0] neg_hi:[0,1,0]\n"
  "v_pk_mul_f32 v[52:53], v[52:53], v[138:139] op_sel:[0,1]\n"
  "v_pk_fma_f32 v[54:55], v[18:19], v[138:139], v[54:55] op_sel_hi:[1,0,1] neg_lo:[0,1,0] neg_hi:[0,1,0]\n"
  "v_pk_mul_f32 v[56:57], v[56:57], v[142:143] op_sel:[0,1]\n"
  "v_pk_fma_f32 v[58:59], v[14:15], v[142:143], v[58:59] op_sel_hi:[1,0,1] neg_lo:[0,1,0] neg_hi:[0,1,0]\n"
  "v_pk_fma_f32 v[96:97], v[4:5], v[142:143], v[96:97] op_sel_hi:[1,0,1] neg_lo:[0,1,0] neg_hi:[0,1,0]\n"
  "v_pk_mul_f32 v[60:61], v[60:61], v[142:143] op_sel:[0,1]\n"
  "v_pk_fma_f32 v[62:63], v[18:19], v[142:143], v[62:63] op_sel_hi:[1,0,1] neg_lo:[0,1,0] neg_hi:[0,1,0]\n"
  "v_pk_fma_f32 v[100:101], v[8:9], v[142:143], v[100:101] op_sel_hi:[1,0,1] neg_lo:[0,1,0] neg_hi:[0,1,0]\n"
  "v_pk_mul_f32 v[64:65], v[64:65], v[146:147] op_sel:[0,1]\n"
  "v_pk_fma_f32 v[66:67], v[14:15], v[146:147], v[66:67] op_sel_hi:[1,0,1] neg_lo:[0,1,0] neg_hi:[0,1,0]\n"
  "v_pk_fma_f32 v[104:105], v[4:5], v[146:147], v[104:105] op_sel_hi:[1,0,1] neg_lo:[0,1,0] neg_hi:[0,1,0]\n"
  "v_pk_mul_f32 v[68:69], v[68:69], v[146:147] op_sel:[0,1]\n"
  "v_pk_fma_f32 v[70:71], v[18:19], v[146:147], v[70:71] op_sel_hi:[1,0,1] neg_lo:[0,1,0] neg_hi:[0,1,0]\n"
  "v_pk_fma_f32 v[108:109], v[8:9], v[146:147], v[108:109] op_sel_hi:[1,0,1] neg_lo:[0,1,0] neg_hi:[0,1,0]\n"
  "v_pk_mul_f32 v[50:51], v[50:51], v[138:139] op_sel:[0,1]\n"
  "s_nop 1\n"
  "v_mfma_f32_16x16x32_bf16 v[184:187], v[26:29], v[40:43], 0\n"
  "v_cvt_pk_bf16_f32 v48, v48, v49\n"
  "v_cvt_pk_bf16_f32 v52, v52, v53\n"
  "v_pk_mul_f32 v[54:55], v[54:55], v[138:139] op_sel:[0,1]\n"
  "v_pk_mul_f32 v[58:59], v[58:59], v[142:143] op_sel:[0,1]\n"
  "v_cvt_pk_bf16_f32 v56, v56, v57\n"
  "v_pk_fma_f32 v[98:99], v[6:7], v[142:143], v[98:99] op_sel_hi:[1,0,1] neg_lo:[0,1,0] neg_hi:[0,1,0]\n"
  "v_cvt_pk_bf16_f32 v96, v96, v97\n"
  "v_pk_mul_f32 v[62:63], v[62:63], v[142:143] op_sel:[0,1]\n"
  "v_cvt_pk_bf16_f32 v60, v60, v61\n"
  "v_pk_fma_f32 v[102:103], v[10:11], v[142:143], v[102:103] op_sel_hi:[1,0,1] neg_lo:[0,1,0] neg_hi:[0,1,0]\n"
  "v_cvt_pk_bf16_f32 v100, v100, v101\n"
  "v_pk_mul_f32 v[66:67], v[66:67], v[146:147] op_sel:[0,1]\n"
  "v_cvt_pk_bf16_f32 v64, v64, v65\n"
  "v_pk_fma_f32 v[106:107], v[6:7], v[146:147], v[106:107] op_sel_hi:[1,0,1] neg_lo:[0,1,0] neg_hi:[0,1,0]\n"
  "v_cvt_pk_bf16_f32 v104, v104, v105\n"
  "v_pk_mul_f32 v[70:71], v[70:71], v[146:147] op_sel:[0,1]\n"
  "v_cvt_pk_bf16_f32 v68, v68, v69\n"
  "v_pk_fma_f32 v[110:111], v[10:11], v[146:147], v[110:111] op_sel_hi:[1,0,1] neg_lo:[0,1,0] neg_hi:[0,1,0]\n"
  "v_cvt_pk_bf16_f32 v108, v108, v109\n"
  "v_cvt_pk_bf16_f32 v49, v50, v51\n"
  "v_cvt_pk_bf16_f32 v53, v54, v55\n"
  "v_cvt_pk_bf16_f32 v57, v58, v59\n"
  "v_cvt_pk_bf16_f32 v97, v98, v99\n"
  "s_add_u32 m0, s8, 3072\n"
  "s_mov_b32 s9, 1671168\n"
  "buffer_load_dwordx4 v240, s[40:43], s9 offen lds\n"
  "v_mfma_f32_16x16x16_bf16 v[112:115], v[48:49], v[96:97], v[216:219]\n"
  "v_cvt_pk_bf16_f32 v61, v62, v63\n"
  "v_cvt_pk_bf16_f32 v101, v102, v103\n"
  "v_mfma_f32_16x16x16_bf16 v[116:119], v[56:57], v[96:97], v[208:211]\n"
  "v_cvt_pk_bf16_f32 v65, v66, v67\n"
  "v_cvt_pk_bf16_f32 v105, v106, v107\n"
  "v_mfma_f32_16x16x16_bf16 v[120:123], v[52:53], v[100:101], v[216:219]\n"
  "v_cvt_pk_bf16_f32 v69, v70, v71\n"
  "v_cvt_pk_bf16_f32 v109, v110, v111\n"
  "v_mfma_f32_16x16x16_bf16 v[124:127], v[60:61], v[100:101], v[208:211]\n"
  "s_nop 1\n"
  "v_max_f32 v112, v112, v116\n"
  "v_max_f32 v113, v113, v117\n"
  "v_mfma_f32_16x16x16_bf16 v[128:131], v[56:57], v[104:105], v[212:215]\n"
  "v_max_f32 v114, v114, v118\n"
  "v_max_f32 v115, v115, v119\n"
  "v_mfma_f32_16x16x16_bf16 v[132:135], v[64:65], v[104:105], v[208:211]\n"
  "v_max_f32 v120, v120, v124\n"
  "v_max_f32 v121, v121, v125\n"
  "v_mfma_f32_16x16x16_bf16 v[188:191], v[60:61], v[108:109], v[212:215]\n"
  "v_max_f32 v122, v122, v126\n"
  "v_max3_f32 v50, v112, v113, v114\n"
  "v_mfma_f32_16x16x16_bf16 v[192:195], v[68:69], v[108:109], v[208:211]\n"
  "s_nop 1\n"
  "v_max_f32 v128, v128, v132\n"
  "v_max_f32 v129, v129, v133\n"
  "s_add_u32 m0, s8, 4096\n"
  "s_mov_b32 s9, 1703936\n"
  "buffer_load_dwordx4 v240, s[40:43], s9 offen lds\n"
  "v_max_f32 v130, v130, v134\n"
  "v_max_f32 v123, v123, v127\n"
  "v_max3_f32 v54, v120, v121, v122\n"
  "v_max_f32 v188, v188, v192\n"
  "v_max_f32 v189, v189, v193\n"
  "v_max_f32 v190, v190, v194\n"
  "v_max_f32 v131, v131, v135\n"
  "v_max3_f32 v66, v128, v129, v130\n"
  "v_max_f32 v191, v191, v195\n"
  "v_max3_f32 v70, v188, v189, v190\n"
  "v_max_f32 v50, v50, v115\n"
  "v_max_f32 v54, v54, v123\n"
  "v_max_f32 v66, v66, v131\n"
  "v_max_f32 v70, v70, v191\n"
  "v_mov_b32 v51, v50\n"
  "v_mov_b32 v55, v54\n"
  "v_mov_b32 v67, v66\n"
  "v_mov_b32 v71, v70\n"
  "v_permlane16_swap_b32 v50, v51\n"
  "v_permlane16_swap_b32 v54, v55\n"
  "v_permlane16_swap_b32 v66, v67\n"
  "v_permlane16_swap_b32 v70, v71\n"
  "v_max_f32 v50, v50, v51\n"
  "v_max_f32 v54, v54, v55\n"
  "v_max_f32 v66, v66, v67\n"
  "v_max_f32 v70, v70, v71\n"
  "v_mov_b32 v51, v50\n"
  "v_mov_b32 v55, v54\n"
  "s_add_u32 m0, s8, 5120\n"
  "s_mov_b32 s9, 1736704\n"
  "buffer_load_dwordx4 v240, s[40:43], s9 offen lds\n"
  "v_mov_b32 v67, v66\n"
  "v_mov_b32 v71, v70\n"
  "v_permlane32_swap_b32 v50, v51\n"
  "v_permlane32_swap_b32 v54, v55\n"
  "v_permlane32_swap_b32 v66, v67\n"
  "v_permlane32_swap_b32 v70, v71\n"
  "v_max_f32 v50, v50, v51\n"
  "v_max_f32 v54, v54, v55\n"
  "v_max_f32 v66, v66, v67\n"
  "v_max_f32 v70, v70, v71\n"
  "v_mul_f32 v50, v50, v140\n"
  "v_mul_f32 v54, v54, v140\n"
  "v_mul_f32 v66, v66, v144\n"
  "v_mul_f32 v70, v70, v144\n"
  "v_pk_fma_f32 v[112:113], v[112:113], v[140:141], v[50:51] op_sel_hi:[1,0,0] neg_lo:[0,0,1] neg_hi:[0,0,1]\n"
  "v_pk_fma_f32 v[114:115], v[114:115], v[140:141], v[50:51] op_sel_hi:[1,0,0] neg_lo:[0,0,1] neg_hi:[0,0,1]\n"
  "v_pk_fma_f32 v[120:121], v[120:121], v[140:141], v[54:55] op_sel_hi:[1,0,0] neg_lo:[0,0,1] neg_hi:[0,0,1]\n"
  "v_pk_fma_f32 v[122:123], v[122:123], v[140:141], v[54:55] op_sel_hi:[1,0,0] neg_lo:[0,0,1] neg_hi:[0,0,1]\n"
  "v_pk_fma_f32 v[128:129], v[128:129], v[144:145], v[66:67] op_sel_hi:[1,0,0] neg_lo:[0,0,1] neg_hi:[0,0,1]\n"
  "v_pk_fma_f32 v[130:131], v[130:131], v[144:145], v[66:67] op_sel_hi:[1,0,0] neg_lo:[0,0,1] neg_hi:[0,0,1]\n"
  "v_pk_fma_f32 v[188:189], v[188:189], v[144:145], v[70:71] op_sel_hi:[1,0,0] neg_lo:[0,0,1] neg_hi:[0,0,1]\n"
  "v_pk_fma_f32 v[190:191], v[190:191], v[144:145], v[70:71] op_sel_hi:[1,0,0] neg_lo:[0,0,1] neg_hi:[0,0,1]\n"
  "v_exp_f32 v112, v112\n"
  "v_exp_f32 v113, v113\n"
  "v_exp_f32 v114, v114\n"
  "s_add_u32 m0, s8, 6144\n"
  "s_mov_b32 s9, 1769472\n"
  "buffer_load_dwordx4 v240, s[40:43], s9 offen lds\n"
  "v_exp_f32 v115, v115\n"
  "v_exp_f32 v120, v120\n"
  "v_exp_f32 v121, v121\n"
  "v_exp_f32 v122, v122\n"
  "v_exp_f32 v123, v123\n"
  "v_exp_f32 v128, v128\n"
  "v_exp_f32 v129, v129\n"
  "v_exp_f32 v130, v130\n"
  "v_exp_f32 v131, v131\n"
  "v_exp_f32 v188, v188\n"
  "v_exp_f32 v189, v189\n"
  "v_exp_f32 v190, v190\n"
  "v_exp_f32 v191, v191\n"
  "v_cvt_pk_bf16_f32 v98, v112, v113\n"
  "v_cvt_pk_bf16_f32 v99, v114, v115\n"
  "v_cvt_pk_bf16_f32 v102, v120, v121\n"
  "v_cvt_pk_bf16_f32 v103, v122, v123\n"
  "v_cvt_pk_bf16_f32 v106, v128, v129\n"
  "v_cvt_pk_bf16_f32 v107, v130, v131\n"
  "s_add_u32 m0, s8, 7168\n"
  "s_mov_b32 s9, 1802240\n"
  "buffer_load_dwordx4 v240, s[40:43], s9 offen lds\n"
  "v_cvt_pk_bf16_f32 v110, v188, v189\n"
  "v_cvt_pk_bf16_f32 v111, v190, v191\n"
  "v_and_b32 v116, v220, v98\n"
  "v_and_b32 v117, v221, v99\n"
  "v_and_b32 v124, v220, v102\n"
  "v_and_b32 v125, v221, v103\n"
  "v_and_b32 v132, v220, v106\n"
  "v_and_b32 v133, v221, v107\n"
  "v_and_b32 v192, v220, v110\n"
  "v_and_b32 v193, v221, v111\n"
  "v_pk_fma_f32 v[72:73], v[148:149], v[20:21], v[72:73] op_sel_hi:[1,0,1] neg_lo:[1,0,0] neg_hi:[1,0,0]\n"
  "v_pk_fma_f32 v[74:75], v[150:151], v[20:21], v[74:75] op_sel_hi:[1,0,1] neg_lo:[1,0,0] neg_hi:[1,0,0]\n"
  "v_pk_fma_f32 v[76:77], v[148:149], v[20:21], v[76:77] op_sel:[0,1,0] neg_lo:[1,0,0] neg_hi:[1,0,0]\n"
  "v_pk_fma_f32 v[78:79], v[150:151], v[20:21], v[78:79] op_sel:[0,1,0] neg_lo:[1,0,0] neg_hi:[1,0,0]\n"
  "v_pk_fma_f32 v[80:81], v[156:157], v[20:21], v[80:81] op_sel_hi:[1,0,1] neg_lo:[1,0,0] neg_hi:[1,0,0]\n"
  "v_pk_fma_f32 v[82:83], v[158:159], v[20:21], v[82:83] op_sel_hi:[1,0,1] neg_lo:[1,0,0] neg_hi:[1,0,0]\n"
  "v_pk_fma_f32 v[84:85], v[156:157], v[20:21], v[84:85] op_sel:[0,1,0] neg_lo:[1,0,0] neg_hi:[1,0,0]\n"
  "v_pk_fma_f32 v[86:87], v[158:159], v[20:21], v[86:87] op_sel:[0,1,0] neg_lo:[1,0,0] neg_hi:[1,0,0]\n"
  "v_xor_b32 v118, v116, v98\n"
  "v_xor_b32 v119, v117, v99\n"
  "v_xor_b32 v126, v124, v102\n"
  "v_xor_b32 v127, v125, v103\n"
  "v_xor_b32 v134, v132, v106\n"
  "v_xor_b32 v135, v133, v107\n"
  "v_xor_b32 v194, v192, v110\n"
  "v_xor_b32 v195, v193, v111\n"
  "v_pk_mul_f32 v[72:73], v[72:73], v[152:153]\n"
  "v_pk_mul_f32 v[74:75], v[74:75], v[154:155]\n"
  "v_pk_mul_f32 v[76:77], v[76:77], v[152:153]\n"
  "v_pk_mul_f32 v[78:79], v[78:79], v[154:155]\n"
  "v_pk_mul_f32 v[80:81], v[80:81], v[160:161]\n"
  "v_pk_mul_f32 v[82:83], v[82:83], v[162:163]\n"
  "v_pk_mul_f32 v[84:85], v[84:85], v[160:161]\n"
  "v_pk_mul_f32 v[86:87], v[86:87], v[162:163]\n"
  "v_pk_fma_f32 v[88:89], v[164:165], v[20:21], v[88:89] op_sel_hi:[1,0,1] neg_lo:[1,0,0] neg_hi:[1,0,0]\n"
  "v_pk_fma_f32 v[90:91], v[166:167], v[20:21], v[90:91] op_sel_hi:[1,0,1] neg_lo:[1,0,0] neg_hi:[1,0,0]\n"
  "v_pk_fma_f32 v[92:93], v[164:165], v[20:21], v[92:93] op_sel:[0,1,0] neg_lo:[1,0,0] neg_hi:[1,0,0]\n"
  "v_pk_fma_f32 v[94:95], v[166:167], v[20:21], v[94:95] op_sel:[0,1,0] neg_lo:[1,0,0] neg_hi:[1,0,0]\n"
  "s_add_u32 m0, s8, 8192\n"
  "s_mov_b32 s9, 0\n"
  "buffer_load_dwordx4 v240, s[40:43], s9 offen lds\n"
  "v_cvt_pk_bf16_f32 v172, v72, v73\n"
  "v_cvt_pk_bf16_f32 v173, v74, v75\n"
  "v_mfma_f32_16x16x32_bf16 v[72:75], v[30:33], v[116:119], 0\n"
  "v_cvt_pk_bf16_f32 v178, v76, v77\n"
  "v_cvt_pk_bf16_f32 v179, v78, v79\n"
  "v_mfma_f32_16x16x32_bf16 v[76:79], v[30:33], v[124:127], 0\n"
  "v_cvt_pk_bf16_f32 v174, v80, v81\n"
  "v_cvt_pk_bf16_f32 v175, v82, v83\n"
  "v_mfma_f32_16x16x32_bf16 v[80:83], v[30:33], v[132:135], 0\n"
  "v_cvt_pk_bf16_f32 v180, v84, v85\n"
  "v_cvt_pk_bf16_f32 v181, v86, v87\n"
  "v_pk_mul_f32 v[88:89], v[88:89], v[168:169]\n"
  "v_pk_mul_f32 v[90:91], v[90:91], v[170:171]\n"
  "v_pk_mul_f32 v[92:93], v[92:93], v[168:169]\n"
  "v_pk_mul_f32 v[94:95], v[94:95], v[170:171]\n"
  "s_nop 0\n"
  "v_mfma_f32_16x16x32_bf16 v[84:87], v[30:33], v[192:195], 0\n"
  "v_cvt_pk_bf16_f32 v176, v88, v89\n"
  "v_cvt_pk_bf16_f32 v177, v90, v91\n"
  "v_mfma_f32_16x16x32_bf16 v[112:115], v[172:175], v[116:119], 0\n"
  "v_cvt_pk_bf16_f32 v182, v92, v93\n"
  "v_cvt_pk_bf16_f32 v183, v94, v95\n"
  "v_mfma_f32_16x16x32_bf16 v[120:123], v[178:181], v[124:127], 0\n"
  "s_add_u32 m0, s8, 9216\n"
  "s_mov_b32 s9, 32768\n"
  "buffer_load_dwordx4 v240, s[40:43], s9 offen lds\n"
  "v_mfma_f32_16x16x32_bf16 v[128:131], v[174:177], v[132:135], 0\n"
  "v_rcp_f32 v72, v72\n"
  "v_mfma_f32_16x16x32_bf16 v[188:191], v[180:183], v[192:195], 0\n"
  "v_rcp_f32 v76, v76\n"
  "v_mfma_f32_16x16x32_bf16 v[176:179], v[22:25], v[40:43], 0\n"
  "v_rcp_f32 v80, v80\n"
  "v_mfma_f32_16x16x32_bf16 v[180:183], v[22:25], v[38:41], 0\n"
  "v_rcp_f32 v84, v84\n"
  "v_pk_mul_f32 v[112:113], v[112:113], v[72:73] op_sel_hi:[1,0]\n"
  "v_pk_mul_f32 v[114:115], v[114:115], v[72:73] op_sel_hi:[1,0]\n"
  "v_pk_mul_f32 v[120:121], v[120:121], v[76:77] op_sel_hi:[1,0]\n"
  "v_pk_mul_f32 v[122:123], v[122:123], v[76:77] op_sel_hi:[1,0]\n"
  "v_pk_mul_f32 v[128:129], v[128:129], v[80:81] op_sel_hi:[1,0]\n"
  "v_pk_mul_f32 v[130:131], v[130:131], v[80:81] op_sel_hi:[1,0]\n"
  "v_pk_mul_f32 v[188:189], v[188:189], v[84:85] op_sel_hi:[1,0]\n"
  "v_pk_mul_f32 v[190:191], v[190:191], v[84:85] op_sel_hi:[1,0]\n"
  "v_cvt_pk_bf16_f32 v196, v112, v113\n"
  "v_cvt_pk_bf16_f32 v197, v114, v115\n"
  "v_cvt_pk_bf16_f32 v198, v120, v121\n"
  "v_cvt_pk_bf16_f32 v199, v122, v123\n"
  "v_cvt_pk_bf16_f32 v200, v128, v129\n"
  "v_cvt_pk_bf16_f32 v201, v130, v131\n"
  "s_add_u32 m0, s8, 10240\n"
  "s_mov_b32 s9, 65536\n"
  "buffer_load_dwordx4 v240, s[40:43], s9 offen lds\n"
  "v_cvt_pk_bf16_f32 v202, v188, v189\n"
  "v_cvt_pk_bf16_f32 v203, v190, v191\n"
  "v_mfma_f32_16x16x32_bf16 v[188:191], v[26:29], v[38:41], 0\n"
  "s_waitcnt vmcnt(7)\n"
  "ds_read_b64_tr_b16 v[212:213], v234 offset:0\n"
  "ds_read_b64_tr_b16 v[214:215], v234 offset:2048\n"
  "ds_read_b64_tr_b16 v[208:209], v235 offset:0\n"
  "ds_read_b64_tr_b16 v[210:211], v235 offset:2048\n"
  "buffer_store_dwordx4 v[196:199], v1, s[20:23], s15 offen sc1\n"
  "buffer_store_dwordx4 v[200:203], v1, s[20:23], s16 offen sc1\n"
  "v_bfe_u32 v45, v0, 3, 2\n"
  "v_lshlrev_b32 v45, 5, v45\n"
  "v_lshrrev_b32 v46, 2, v0\n"
  "v_lshl_add_u32 v45, v46, 7, v45\n"
  "v_and_b32 v46, 3, v0\n"
  "v_lshl_add_u32 v45, v46, 3, v45\n"
  "v_add_u32 v200, 98304, v45\n"
  "v_xor_b32 v201, 32, v200\n"
  "v_xor_b32 v202, 64, v200\n"
  "v_xor_b32 v203, 0x60, v200\n"
  "s_add_u32 m0, s8, 11264\n"
  "s_mov_b32 s9, 98304\n"
  "buffer_load_dwordx4 v240, s[40:43], s9 offen lds\n"
  "s_add_u32 m0, s8, 12288\n"
  "s_mov_b32 s9, 131072\n"
  "buffer_load_dwordx4 v240, s[40:43], s9 offen lds\n"
  "s_waitcnt vmcnt(7) lgkmcnt(0)\n"
  "ds_read_b64_tr_b16 v[196:197], v236 offset:4096\n"
  "ds_read_b64_tr_b16 v[198:199], v236 offset:6144\n"
  "ds_read_b64_tr_b16 v[192:193], v237 offset:4096\n"
  "ds_read_b64_tr_b16 v[194:195], v237 offset:6144\n"
  "s_add_u32 m0, s8, 13312\n"
  "s_mov_b32 s9, 163840\n"
  "buffer_load_dwordx4 v240, s[40:43], s9 offen lds\n"
  "s_waitcnt vmcnt(3) lgkmcnt(0)\n"
  "s_barrier\n"
  "buffer_store_dwordx2 v[36:37], v248, s[20:23], s11 offen sc1\n"
  "s_add_u32 m0, s8, 14336\n"
  "s_mov_b32 s9, 196608\n"
  "buffer_load_dwordx4 v240, s[40:43], s9 offen lds\n"
  "ds_read_b64_tr_b16 v[112:113], v200 offset:0\n"
  "ds_read_b64_tr_b16 v[114:115], v200 offset:2048\n"
  "ds_read_b64_tr_b16 v[116:117], v201 offset:0\n"
  "ds_read_b64_tr_b16 v[118:119], v201 offset:2048\n"
  "ds_read_b64_tr_b16 v[120:121], v202 offset:0\n"
  "s_add_u32 m0, s8, 15360\n"
  "s_mov_b32 s9, 229376\n"
  "buffer_load_dwordx4 v240, s[40:43], s9 offen lds\n"
  "ds_read_b64_tr_b16 v[122:123], v202 offset:2048\n"
  "ds_read_b64_tr_b16 v[124:125], v203 offset:0\n"
  "ds_read_b64_tr_b16 v[126:127], v203 offset:2048\n"
  "ds_read_b64_tr_b16 v[128:129], v200 offset:4096\n"
  "ds_read_b64_tr_b16 v[130:131], v200 offset:6144\n"
  "ds_read_b64_tr_b16 v[132:133], v201 offset:4096\n"
  "ds_read_b64_tr_b16 v[134:135], v201 offset:6144\n"
  "ds_read_b64_tr_b16 v[136:137], v202 offset:4096\n"
  "ds_read_b64_tr_b16 v[138:139], v202 offset:6144\n"
  "ds_read_b64_tr_b16 v[140:141], v203 offset:4096\n"
  "ds_read_b64_tr_b16 v[142:143], v203 offset:6144\n"
  "ds_read_b64_tr_b16 v[144:145], v200 offset:8192\n"
  "ds_read_b64_tr_b16 v[146:147], v200 offset:10240\n"
  "ds_read_b64_tr_b16 v[148:149], v201 offset:8192\n"
  "ds_read_b64_tr_b16 v[150:151], v201 offset:10240\n"
  "ds_read_b64_tr_b16 v[152:153], v202 offset:8192\n"
  "ds_read_b64_tr_b16 v[154:155], v202 offset:10240\n"
  "ds_read_b64_tr_b16 v[156:157], v203 offset:8192\n"
  "ds_read_b64_tr_b16 v[158:159], v203 offset:10240\n"
  "ds_read_b64_tr_b16 v[160:161], v200 offset:12288\n"
  "ds_read_b64_tr_b16 v[162:163], v200 offset:14336\n"
  "ds_read_b64_tr_b16 v[164:165], v201 offset:12288\n"
  "ds_read_b64_tr_b16 v[166:167], v201 offset:14336\n"
  "ds_read_b64_tr_b16 a[176:177], v202 offset:12288\n"
  "ds_read_b64_tr_b16 a[178:179], v202 offset:14336\n"
  "ds_read_b64_tr_b16 a[180:181], v203 offset:12288\n"
  "ds_read_b64_tr_b16 a[182:183], v203 offset:14336\n"
  "ds_read_b64_tr_b16 a[184:185], v200 offset:16384\n"
  "ds_read_b64_tr_b16 a[186:187], v200 offset:18432\n"
  "ds_read_b64_tr_b16 a[188:189], v201 offset:16384\n"
  "ds_read_b64_tr_b16 a[190:191], v201 offset:18432\n"
  "ds_read_b64_tr_b16 a[192:193], v202 offset:16384\n"
  "ds_read_b64_tr_b16 a[194:195], v202 offset:18432\n"
  "ds_read_b64_tr_b16 a[196:197], v203 offset:16384\n"
  "ds_read_b64_tr_b16 a[198:199], v203 offset:18432\n"
  "ds_read_b64_tr_b16 a[200:201], v200 offset:20480\n"
  "ds_read_b64_tr_b16 a[202:203], v200 offset:22528\n"
  "ds_read_b64_tr_b16 a[204:205], v201 offset:20480\n"
  "ds_read_b64_tr_b16 a[206:207], v201 offset:22528\n"
  "s_waitcnt vmcnt(5) lgkmcnt(15)\n"
  "ds_read_b64_tr_b16 a[0:1], v234 offset:8192\n"
  "ds_read_b64_tr_b16 a[2:3], v234 offset:10240\n"
  "ds_read_b64_tr_b16 a[4:5], v235 offset:8192\n"
  "ds_read_b64_tr_b16 a[6:7], v235 offset:10240\n"
  "ds_read_b64_tr_b16 a[8:9], v236 offset:8192\n"
  "ds_read_b64_tr_b16 a[10:11], v236 offset:10240\n"
  "ds_read_b64_tr_b16 a[12:13], v237 offset:8192\n"
  "ds_read_b64_tr_b16 a[14:15], v237 offset:10240\n"
  "s_waitcnt vmcnt(0) lgkmcnt(0)\n"
  "ds_read_b64_tr_b16 a[16:17], v234 offset:12288\n"
  "ds_read_b64_tr_b16 a[18:19], v234 offset:14336\n"
  "ds_read_b64_tr_b16 a[20:21], v235 offset:12288\n"
  "ds_read_b64_tr_b16 a[22:23], v235 offset:14336\n"
  "ds_read_b64_tr_b16 a[24:25], v236 offset:12288\n"
  "ds_read_b64_tr_b16 a[26:27], v236 offset:14336\n"
  "ds_read_b64_tr_b16 a[28:29], v237 offset:12288\n"
  "ds_read_b64_tr_b16 a[30:31], v237 offset:14336\n"
  "s_sleep 9\n"
  "buffer_load_dwordx2 v[206:207], v249, s[20:23], 0 offen sc1\n"
  "s_waitcnt vmcnt(0) lgkmcnt(0)\n"
  "v_cmp_ne_u64 vcc, s[56:57], v[206:207]\n"
  "s_cmp_eq_u64 vcc, 0\n"
  "s_cbranch_scc0 .Lfb8_%=\n"
  ".Lbk8_%=:\n"
  "buffer_load_dwordx4 v[48:51], v250, s[20:23], 0 offen sc1\n"
  "buffer_load_dwordx4 v[52:55], v250, s[20:23], 0 offen offset:1024 sc1\n"
  "buffer_load_dwordx4 v[56:59], v250, s[20:23], 0 offen offset:2048 sc1\n"
  "buffer_load_dwordx4 v[60:63], v250, s[20:23], 0 offen offset:3072 sc1\n"
  "buffer_load_dwordx4 v[64:67], v250, s[20:23], s72 offen sc1\n"
  "buffer_load_dwordx4 v[68:71], v250, s[20:23], s72 offen offset:1024 sc1\n"
  "buffer_load_dwordx4 v[72:75], v250, s[20:23], s72 offen offset:2048 sc1\n"
  "buffer_load_dwordx4 v[76:79], v250, s[20:23], s72 offen offset:3072 sc1\n"
  "buffer_load_dwordx4 v[80:83], v250, s[20:23], s73 offen sc1\n"
  "buffer_load_dwordx4 v[84:87], v250, s[20:23], s73 offen offset:1024 sc1\n"
  "buffer_load_dwordx4 v[88:91], v250, s[20:23], s73 offen offset:2048 sc1\n"
  "buffer_load_dwordx4 v[92:95], v250, s[20:23], s73 offen offset:3072 sc1\n"
  "buffer_load_dwordx4 v[96:99], v250, s[20:23], s74 offen sc1\n"
  "s_waitcnt vmcnt(12)\n"
  "v_mfma_f32_16x16x32_bf16 v[176:179], v[48:51], v[112:115], v[176:179]\n"
  "ds_read_b64_tr_b16 v[112:113], v202 offset:20480\n"
  "v_mfma_f32_16x16x32_bf16 v[180:183], v[48:51], v[116:119], v[180:183]\n"
  "ds_read_b64_tr_b16 v[114:115], v202 offset:22528\n"
  "v_mfma_f32_16x16x32_bf16 v[184:187], v[48:51], v[120:123], v[184:187]\n"
  "buffer_load_dwordx4 v[100:103], v250, s[20:23], s74 offen offset:1024 sc1\n"
  "ds_read_b64_tr_b16 v[116:117], v203 offset:20480\n"
  "v_mfma_f32_16x16x32_bf16 v[188:191], v[48:51], v[124:127], v[188:191]\n"
  "ds_read_b64_tr_b16 v[118:119], v203 offset:22528\n"
  "v_mov_b32 v48, v47\n"
  "s_waitcnt vmcnt(12)\n"
  "v_mfma_f32_16x16x32_bf16 v[176:179], v[52:55], v[128:131], v[176:179]\n"
  "ds_read_b64_tr_b16 v[120:121], v200 offset:24576\n"
  "v_mov_b32 v49, v47\n"
  "v_mfma_f32_16x16x32_bf16 v[180:183], v[52:55], v[132:135], v[180:183]\n"
  "ds_read_b64_tr_b16 v[122:123], v200 offset:26624\n"
  "buffer_load_dwordx4 v[104:107], v250, s[20:23], s74 offen offset:2048 sc1\n"
  "v_mfma_f32_16x16x32_bf16 v[184:187], v[52:55], v[136:139], v[184:187]\n"
  "ds_read_b64_tr_b16 v[124:125], v201 offset:24576\n"
  "v_mov_b32 v50, v47\n"
  "v_mfma_f32_16x16x32_bf16 v[188:191], v[52:55], v[140:143], v[188:191]\n"
  "ds_read_b64_tr_b16 v[126:127], v201 offset:26624\n"
  "v_mov_b32 v51, v47\n"
  "s_waitcnt vmcnt(12)\n"
  "v_mfma_f32_16x16x32_bf16 v[176:179], v[56:59], v[144:147], v[176:179]\n"
  "ds_read_b64_tr_b16 v[128:129], v202 offset:24576\n"
  "v_mov_b32 v52, v47\n"
  "v_mfma_f32_16x16x32_bf16 v[180:183], v[56:59], v[148:151], v[180:183]\n"
  "ds_read_b64_tr_b16 v[130:131], v202 offset:26624\n"
  "buffer_load_dwordx4 v[108:111], v250, s[20:23], s74 offen offset:3072 sc1\n"
  "v_mfma_f32_16x16x32_bf16 v[184:187], v[56:59], v[152:155], v[184:187]\n"
  "ds_read_b64_tr_b16 v[132:133], v203 offset:24576\n"
  "v_mov_b32 v53, v47\n"
  "v_mfma_f32_16x16x32_bf16 v[188:191], v[56:59], v[156:159], v[188:191]\n"
  "ds_read_b64_tr_b16 v[134:135], v203 offset:26624\n"
  "v_mov_b32 v54, v47\n"
  "s_waitcnt vmcnt(12) lgkmcnt(8)\n"
  "v_mfma_f32_16x16x32_bf16 v[176:179], v[60:63], v[160:163], v[176:179]\n"
  "ds_read_b64_tr_b16 v[136:137], v200 offset:28672\n"
  "v_mov_b32 v55, v47\n"
  "v_mfma_f32_16x16x32_bf16 v[180:183], v[60:63], v[164:167], v[180:183]\n"
  "ds_read_b64_tr_b16 v[138:139], v200 offset:30720\n"
  "v_mfma_f32_16x16x32_bf16 v[184:187], v[60:63], a[176:179], v[184:187]\n"
  "ds_read_b64_tr_b16 v[140:141], v201 offset:28672\n"
  "v_mfma_f32_16x16x32_bf16 v[188:191], v[60:63], a[180:183], v[188:191]\n"
  "ds_read_b64_tr_b16 v[142:143], v201 offset:30720\n"
  "s_waitcnt vmcnt(11) lgkmcnt(8)\n"
  "v_mfma_f32_16x16x32_bf16 v[176:179], v[64:67], a[184:187], v[176:179]\n"
  "ds_read_b64_tr_b16 v[144:145], v202 offset:28672\n"
  "v_mfma_f32_16x16x32_bf16 v[180:183], v[64:67], a[188:191], v[180:183]\n"
  "ds_read_b64_tr_b16 v[146:147], v202 offset:30720\n"
  "v_mfma_f32_16x16x32_bf16 v[184:187], v[64:67], a[192:195], v[184:187]\n"
  "ds_read_b64_tr_b16 v[148:149], v203 offset:28672\n"
  "v_mfma_f32_16x16x32_bf16 v[188:191], v[64:67], a[196:199], v[188:191]\n"
  "ds_read_b64_tr_b16 v[150:151], v203 offset:30720\n"
  "s_waitcnt vmcnt(10) lgkmcnt(8)\n"
  "v_mfma_f32_16x16x32_bf16 v[176:179], v[68:71], a[200:203], v[176:179]\n"
  "ds_read_b64_tr_b16 v[152:153], v200 offset:32768\n"
  "v_mfma_f32_16x16x32_bf16 v[180:183], v[68:71], a[204:207], v[180:183]\n"
  "ds_read_b64_tr_b16 v[154:155], v200 offset:34816\n"
  "v_mfma_f32_16x16x32_bf16 v[184:187], v[68:71], v[112:115], v[184:187]\n"
  "ds_read_b64_tr_b16 v[156:157], v201 offset:32768\n"
  "v_mfma_f32_16x16x32_bf16 v[188:191], v[68:71], v[116:119], v[188:191]\n"
  "ds_read_b64_tr_b16 v[158:159], v201 offset:34816\n"
  "s_waitcnt vmcnt(9) lgkmcnt(8)\n"
  "v_mfma_f32_16x16x32_bf16 v[176:179], v[72:75], v[120:123], v[176:179]\n"
  "ds_read_b64_tr_b16 v[160:161], v202 offset:32768\n"
  "v_mfma_f32_16x16x32_bf16 v[180:183], v[72:75], v[124:127], v[180:183]\n"
  "ds_read_b64_tr_b16 v[162:163], v202 offset:34816\n"
  "v_mfma_f32_16x16x32_bf16 v[184:187], v[72:75], v[128:131], v[184:187]\n"
  "ds_read_b64_tr_b16 v[164:165], v203 offset:32768\n"
  "v_mfma_f32_16x16x32_bf16 v[188:191], v[72:75], v[132:135], v[188:191]\n"
  "ds_read_b64_tr_b16 v[166:167], v203 offset:34816\n"
  "s_waitcnt vmcnt(8) lgkmcnt(8)\n"
  "v_mfma_f32_16x16x32_bf16 v[176:179], v[76:79], v[136:139], v[176:179]\n"
  "ds_read_b64_tr_b16 a[176:177], v200 offset:36864\n"
  "v_mfma_f32_16x16x32_bf16 v[180:183], v[76:79], v[140:143], v[180:183]\n"
  "ds_read_b64_tr_b16 a[178:179], v200 offset:38912\n"
  "v_mfma_f32_16x16x32_bf16 v[184:187], v[76:79], v[144:147], v[184:187]\n"
  "ds_read_b64_tr_b16 a[180:181], v201 offset:36864\n"
  "ds_read_b64_tr_b16 a[182:183], v201 offset:38912\n"
  "v_mfma_f32_16x16x32_bf16 v[188:191], v[76:79], v[148:151], v[188:191]\n"
  "ds_read_b64_tr_b16 a[184:185], v202 offset:36864\n"
  "ds_read_b64_tr_b16 a[186:187], v202 offset:38912\n"
  "s_waitcnt vmcnt(7) lgkmcnt(10)\n"
  "v_mfma_f32_16x16x32_bf16 v[176:179], v[80:83], v[152:155], v[176:179]\n"
  "ds_read_b64_tr_b16 a[188:189], v203 offset:36864\n"
  "ds_read_b64_tr_b16 a[190:191], v203 offset:38912\n"
  "v_mfma_f32_16x16x32_bf16 v[180:183], v[80:83], v[156:159], v[180:183]\n"
  "ds_read_b64_tr_b16 a[192:193], v200 offset:40960\n"
  "ds_read_b64_tr_b16 a[194:195], v200 offset:43008\n"
  "s_waitcnt lgkmcnt(12)\n"
  "v_mfma_f32_16x16x32_bf16 v[184:187], v[80:83], v[160:163], v[184:187]\n"
  "ds_read_b64_tr_b16 a[196:197], v201 offset:40960\n"
  "ds_read_b64_tr_b16 a[198:199], v201 offset:43008\n"
  "s_waitcnt lgkmcnt(12)\n"
  "v_mfma_f32_16x16x32_bf16 v[188:191], v[80:83], v[164:167], v[188:191]\n"
  "ds_read_b64_tr_b16 a[200:201], v202 offset:40960\n"
  "ds_read_b64_tr_b16 a[202:203], v202 offset:43008\n"
  "s_waitcnt vmcnt(6) lgkmcnt(12)\n"
  "v_mfma_f32_16x16x32_bf16 v[176:179], v[84:87], a[176:179], v[176:179]\n"
  "ds_read_b64_tr_b16 a[204:205], v203 offset:40960\n"
  "ds_read_b64_tr_b16 a[206:207], v203 offset:43008\n"
  "s_waitcnt lgkmcnt(12)\n"
  "v_mfma_f32_16x16x32_bf16 v[180:183], v[84:87], a[180:183], v[180:183]\n"
  "ds_read_b64_tr_b16 v[112:113], v200 offset:45056\n"
  "ds_read_b64_tr_b16 v[114:115], v200 offset:47104\n"
  "s_waitcnt lgkmcnt(12)\n"
  "v_mfma_f32_16x16x32_bf16 v[184:187], v[84:87], a[184:187], v[184:187]\n"
  "ds_read_b64_tr_b16 v[116:117], v201 offset:45056\n"
  "ds_read_b64_tr_b16 v[118:119], v201 offset:47104\n"
  "s_waitcnt lgkmcnt(12)\n"
  "v_mfma_f32_16x16x32_bf16 v[188:191], v[84:87], a[188:191], v[188:191]\n"
  "ds_read_b64_tr_b16 v[120:121], v202 offset:45056\n"
  "ds_read_b64_tr_b16 v[122:123], v202 offset:47104\n"
  "s_waitcnt vmcnt(5) lgkmcnt(12)\n"
  "v_mfma_f32_16x16x32_bf16 v[176:179], v[88:91], a[192:195], v[176:179]\n"
  "ds_read_b64_tr_b16 v[124:125], v203 offset:45056\n"
  "ds_read_b64_tr_b16 v[126:127], v203 offset:47104\n"
  "s_waitcnt lgkmcnt(12)\n"
  "v_mfma_f32_16x16x32_bf16 v[180:183], v[88:91], a[196:199], v[180:183]\n"
  "ds_read_b64_tr_b16 v[128:129], v200 offset:49152\n"
  "ds_read_b64_tr_b16 v[130:131], v200 offset:51200\n"
  "s_waitcnt lgkmcnt(12)\n"
  "v_mfma_f32_16x16x32_bf16 v[184:187], v[88:91], a[200:203], v[184:187]\n"
  "ds_read_b64_tr_b16 v[132:133], v201 offset:49152\n"
  "ds_read_b64_tr_b16 v[134:135], v201 offset:51200\n"
  "s_waitcnt lgkmcnt(12)\n"
  "v_mfma_f32_16x16x32_bf16 v[188:191], v[88:91], a[204:207], v[188:191]\n"
  "ds_read_b64_tr_b16 v[136:137], v202 offset:49152\n"
  "ds_read_b64_tr_b16 v[138:139], v202 offset:51200\n"
  "s_waitcnt vmcnt(4) lgkmcnt(12)\n"
  "v_mfma_f32_16x16x32_bf16 v[176:179], v[92:95], v[112:115], v[176:179]\n"
  "ds_read_b64_tr_b16 v[140:141], v203 offset:49152\n"
  "ds_read_b64_tr_b16 v[142:143], v203 offset:51200\n"
  "s_waitcnt lgkmcnt(12)\n"
  "v_mfma_f32_16x16x32_bf16 v[180:183], v[92:95], v[116:119], v[180:183]\n"
  "ds_read_b64_tr_b16 v[144:145], v200 offset:53248\n"
  "ds_read_b64_tr_b16 v[146:147], v200 offset:55296\n"
  "s_waitcnt lgkmcnt(12)\n"
  "v_mfma_f32_16x16x32_bf16 v[184:187], v[92:95], v[120:123], v[184:187]\n"
  "ds_read_b64_tr_b16 v[148:149], v201 offset:53248\n"
  "ds_read_b64_tr_b16 v[150:151], v201 offset:55296\n"
  "s_waitcnt lgkmcnt(12)\n"
  "v_mfma_f32_16x16x32_bf16 v[188:191], v[92:95], v[124:127], v[188:191]\n"
  "ds_read_b64_tr_b16 v[152:153], v202 offset:53248\n"
  "ds_read_b64_tr_b16 v[154:155], v202 offset:55296\n"
  "s_waitcnt vmcnt(3) lgkmcnt(12)\n"
  "v_mfma_f32_16x16x32_bf16 v[176:179], v[96:99], v[128:131], v[176:179]\n"
  "ds_read_b64_tr_b16 v[156:157], v203 offset:53248\n"
  "ds_read_b64_tr_b16 v[158:159], v203 offset:55296\n"
  "s_waitcnt lgkmcnt(12)\n"
  "v_mfma_f32_16x16x32_bf16 v[180:183], v[96:99], v[132:135], v[180:183]\n"
  "ds_read_b64_tr_b16 v[160:161], v200 offset:57344\n"
  "ds_read_b64_tr_b16 v[162:163], v200 offset:59392\n"
  "s_waitcnt lgkmcnt(12)\n"
  "v_mfma_f32_16x16x32_bf16 v[184:187], v[96:99], v[136:139], v[184:187]\n"
  "ds_read_b64_tr_b16 v[164:165], v201 offset:57344\n"
  "ds_read_b64_tr_b16 v[166:167], v201 offset:59392\n"
  "s_waitcnt lgkmcnt(12)\n"
  "v_mfma_f32_16x16x32_bf16 v[188:191], v[96:99], v[140:143], v[188:191]\n"
  "ds_read_b64_tr_b16 a[176:177], v202 offset:57344\n"
  "ds_read_b64_tr_b16 a[178:179], v202 offset:59392\n"
  "s_waitcnt vmcnt(2) lgkmcnt(12)\n"
  "v_mfma_f32_16x16x32_bf16 v[176:179], v[100:103], v[144:147], v[176:179]\n"
  "ds_read_b64_tr_b16 a[180:181], v203 offset:57344\n"
  "ds_read_b64_tr_b16 a[182:183], v203 offset:59392\n"
  "s_waitcnt lgkmcnt(12)\n"
  "v_mfma_f32_16x16x32_bf16 v[180:183], v[100:103], v[148:151], v[180:183]\n"
  "ds_read_b64_tr_b16 a[184:185], v200 offset:61440\n"
  "ds_read_b64_tr_b16 a[186:187], v200 offset:63488\n"
  "s_waitcnt lgkmcnt(12)\n"
  "v_mfma_f32_16x16x32_bf16 v[184:187], v[100:103], v[152:155], v[184:187]\n"
  "ds_read_b64_tr_b16 a[188:189], v201 offset:61440\n"
  "ds_read_b64_tr_b16 a[190:191], v201 offset:63488\n"
  "s_waitcnt lgkmcnt(12)\n"
  "v_mfma_f32_16x16x32_bf16 v[188:191], v[100:103], v[156:159], v[188:191]\n"
  "ds_read_b64_tr_b16 a[192:193], v202 offset:61440\n"
  "ds_read_b64_tr_b16 a[194:195], v202 offset:63488\n"
  "s_waitcnt vmcnt(1) lgkmcnt(12)\n"
  "v_mfma_f32_16x16x32_bf16 v[176:179], v[104:107], v[160:163], v[176:179]\n"
  "ds_read_b64_tr_b16 a[196:197], v203 offset:61440\n"
  "ds_read_b64_tr_b16 a[198:199], v203 offset:63488\n"
  "s_waitcnt lgkmcnt(12)\n"
  "v_mfma_f32_16x16x32_bf16 v[180:183], v[104:107], v[164:167], v[180:183]\n"
  "ds_read_b64_tr_b16 v[200:201], v235 offset:4096\n"
  "v_mov_b32 v80, v47\n"
  "s_waitcnt lgkmcnt(11)\n"
  "v_mfma_f32_16x16x32_bf16 v[184:187], v[104:107], a[176:179], v[184:187]\n"
  "ds_read_b64_tr_b16 v[202:203], v235 offset:6144\n"
  "v_mov_b32 v81, v47\n"
  "s_waitcnt lgkmcnt(10)\n"
  "v_mfma_f32_16x16x32_bf16 v[188:191], v[104:107], a[180:183], v[188:191]\n"
  "v_mov_b32 v82, v47\n"
  "v_mov_b32 v83, v47\n"
  "s_waitcnt vmcnt(0) lgkmcnt(8)\n"
  "v_mfma_f32_16x16x32_bf16 v[176:179], v[108:111], a[184:187], v[176:179]\n"
  "v_mov_b32 v84, v47\n"
  "v_mov_b32 v85, v47\n"
  "s_waitcnt lgkmcnt(6)\n"
  "v_mfma_f32_16x16x32_bf16 v[180:183], v[108:111], a[188:191], v[180:183]\n"
  "v_mov_b32 v86, v47\n"
  "v_mov_b32 v87, v47\n"
  "s_waitcnt lgkmcnt(4)\n"
  "v_mfma_f32_16x16x32_bf16 v[184:187], v[108:111], a[192:195], v[184:187]\n"
  "v_cvt_pk_bf16_f32 v22, v176, v177\n"
  "v_lshlrev_b32 v56, 16, v22\n"
  "s_waitcnt lgkmcnt(2)\n"
  "v_mfma_f32_16x16x32_bf16 v[188:191], v[108:111], a[196:199], v[188:191]\n"
  "v_and_b32 v57, 0xffff0000, v22\n"
  "v_cvt_pk_bf16_f32 v24, v180, v181\n"
  "v_lshlrev_b32 v60, 16, v24\n"
  "v_and_b32 v61, 0xffff0000, v24\n"
  "v_cvt_pk_bf16_f32 v26, v184, v185\n"
  "v_pk_mul_f32 v[76:77], v[56:57], v[56:57]\n"
  "v_cvt_pk_bf16_f32 v23, v178, v179\n"
  "v_cvt_pk_bf16_f32 v27, v186, v187\n"
  "v_cvt_pk_bf16_f32 v28, v188, v189\n"
  "v_mfma_f32_16x16x16_bf16 v[48:51], v[22:23], v[40:41], v[48:51]\n"
  "v_lshlrev_b32 v64, 16, v26\n"
  "v_and_b32 v65, 0xffff0000, v26\n"
  "v_mfma_f32_16x16x16_bf16 v[80:83], v[26:27], v[40:41], v[80:83]\n"
  "v_cvt_pk_bf16_f32 v29, v190, v191\n"
  "v_lshlrev_b32 v68, 16, v28\n"
  "v_and_b32 v69, 0xffff0000, v28\n"
  "v_pk_fma_f32 v[76:77], v[60:61], v[60:61], v[76:77]\n"
  "s_nop 0\n"
  "v_mfma_f32_16x16x16_bf16 v[84:87], v[28:29], v[40:41], v[84:87]\n"
  "v_lshlrev_b32 v58, 16, v23\n"
  "v_and_b32 v59, 0xffff0000, v23\n"
  "v_cvt_pk_bf16_f32 v25, v182, v183\n"
  "v_lshlrev_b32 v66, 16, v27\n"
  "v_and_b32 v67, 0xffff0000, v27\n"
  "v_mfma_f32_16x16x16_bf16 v[52:55], v[24:25], v[40:41], v[52:55]\n"
  "v_lshlrev_b32 v70, 16, v29\n"
  "v_and_b32 v71, 0xffff0000, v29\n"
  "v_pk_add_f32 v[72:73], v[56:57], v[60:61]\n"
  "v_pk_add_f32 v[88:89], v[64:65], v[68:69]\n"
  "v_pk_fma_f32 v[76:77], v[64:65], v[64:65], v[76:77]\n"
  "v_lshlrev_b32 v62, 16, v25\n"
  "v_and_b32 v63, 0xffff0000, v25\n"
  "v_pk_add_f32 v[72:73], v[72:73], v[88:89]\n"
  "v_pk_fma_f32 v[76:77], v[68:69], v[68:69], v[76:77]\n"
  "v_pk_add_f32 v[90:91], v[66:67], v[70:71]\n"
  "v_pk_mul_f32 v[78:79], v[58:59], v[58:59]\n"
  "v_pk_add_f32 v[74:75], v[58:59], v[62:63]\n"
  "v_pk_fma_f32 v[78:79], v[62:63], v[62:63], v[78:79]\n"
  "v_pk_add_f32 v[74:75], v[74:75], v[90:91]\n"
  "v_pk_fma_f32 v[78:79], v[66:67], v[66:67], v[78:79]\n"
  "v_pk_fma_f32 v[78:79], v[70:71], v[70:71], v[78:79]\n"
  "v_add_f32_dpp v88, v72, v72 row_ror:8 row_mask:0xf bank_mask:0x3\n"
  "v_add_f32_dpp v90, v76, v76 row_ror:8 row_mask:0xf bank_mask:0x3\n"
  "v_add_f32_dpp v89, v73, v73 row_ror:8 row_mask:0xf bank_mask:0x3\n"
  "v_add_f32_dpp v88, v74, v74 row_ror:8 row_mask:0xf bank_mask:0xc\n"
  "v_add_f32_dpp v90, v78, v78 row_ror:8 row_mask:0xf bank_mask:0xc\n"
  "v_add_f32_dpp v91, v77, v77 row_ror:8 row_mask:0xf bank_mask:0x3\n"
  "v_add_f32_dpp v89, v75, v75 row_ror:8 row_mask:0xf bank_mask:0xc\n"
  "v_add_f32_dpp v92, v88, v88 row_shl:4 row_mask:0xf bank_mask:0x5\n"
  "v_add_f32_dpp v91, v79, v79 row_ror:8 row_mask:0xf bank_mask:0xc\n"
  "v_add_f32_dpp v93, v90, v90 row_shl:4 row_mask:0xf bank_mask:0x5\n"
  "v_add_f32_dpp v92, v89, v89 row_shr:4 row_mask:0xf bank_mask:0xa\n"
  "ds_read_b64_tr_b16 v[184:185], v236 offset:0\n"
  "v_add_f32_dpp v93, v91, v91 row_shr:4 row_mask:0xf bank_mask:0xa\n"
  "v_add_f32_dpp v92, v92, v92 quad_perm:[1,0,3,2] row_mask:0xf bank_mask:0xf\n"
  "ds_read_b64_tr_b16 v[186:187], v236 offset:2048\n"
  "v_add_f32_dpp v93, v93, v93 quad_perm:[1,0,3,2] row_mask:0xf bank_mask:0xf\n"
  "v_add_f32_dpp v92, v92, v92 quad_perm:[2,3,0,1] row_mask:0xf bank_mask:0xf\n"
  "v_min_u32 v92, -2, v92\n"
  "v_add_f32_dpp v93, v93, v93 quad_perm:[2,3,0,1] row_mask:0xf bank_mask:0xf\n"
  "v_min_u32 v93, -2, v93\n"
  "buffer_store_dwordx2 v[92:93], v251, s[20:23], 0 offen sc1\n"
  "ds_read_b64_tr_b16 v[180:181], v237 offset:0\n"
  "ds_read_b64_tr_b16 v[182:183], v237 offset:2048\n"
  "ds_read_b64_tr_b16 v[176:177], v234 offset:4096\n"
  "ds_read_b64_tr_b16 v[178:179], v234 offset:6144\n"
  "s_sleep 18\n"
  "buffer_load_dwordx2 v[94:95], v252, s[20:23], 0 offen sc1\n"
  "buffer_load_dwordx2 v[96:97], v252, s[20:23], 0 offen offset:128 sc1\n"
  "s_waitcnt vmcnt(0) lgkmcnt(0)\n"
  "v_max3_u32 v98, v94, v95, v96\n"
  "v_max_u32 v98, v98, v97\n"
  "v_cmp_eq_u32 vcc, -1, v98\n"
  "s_cmp_eq_u64 vcc, 0\n"
  "s_cbranch_scc0 .Lfb9_%=\n"
  ".Lbk9_%=:\n"
  "v_pk_add_f32 v[94:95], v[94:95], v[96:97]\n"
  "s_nop 1\n"
  "v_mfma_f32_16x16x4_f32 v[100:103], v35, v94, 0\n"
  "v_mfma_f32_16x16x4_f32 v[104:107], v35, v95, 0\n"
  "s_nop 8\n"
  "v_mul_f32 v101, s4, v100\n"
  "v_mul_f32 v103, v101, v101\n"
  "s_nop 0\n"
  "v_mfma_f32_16x16x4_f32 v[108:111], v101, v34, 0\n"
  "v_fma_f32 v105, v104, s4, -v103\n"
  "v_max_f32 v105, 0, v105\n"
  "v_add_f32 v102, s5, v105\n"
  "v_rsq_f32 v102, v102\n"
  "s_nop 0\n"
  "v_mul_f32 v105, v105, v102\n"
  "v_fma_f32 v103, v105, v102, s5\n"
  "v_rsq_f32 v103, v103\n"
  "s_nop 1\n"
  "v_pk_add_f32 v[56:57], v[56:57], v[108:109] neg_lo:[0,1] neg_hi:[0,1]\n"
  "v_mul_f32 v102, v102, v103\n"
  "v_pk_add_f32 v[58:59], v[58:59], v[110:111] neg_lo:[0,1] neg_hi:[0,1]\n"
  "v_pk_add_f32 v[60:61], v[60:61], v[108:109] neg_lo:[0,1] neg_hi:[0,1]\n"
  "s_nop 0\n"
  "v_mfma_f32_16x16x4_f32 v[72:75], v102, v34, 0\n"
  "v_pk_add_f32 v[62:63], v[62:63], v[110:111] neg_lo:[0,1] neg_hi:[0,1]\n"
  "v_pk_add_f32 v[64:65], v[64:65], v[108:109] neg_lo:[0,1] neg_hi:[0,1]\n"
  "v_pk_add_f32 v[66:67], v[66:67], v[110:111] neg_lo:[0,1] neg_hi:[0,1]\n"
  "v_pk_add_f32 v[68:69], v[68:69], v[108:109] neg_lo:[0,1] neg_hi:[0,1]\n"
  "v_pk_add_f32 v[70:71], v[70:71], v[110:111] neg_lo:[0,1] neg_hi:[0,1]\n"
  "s_nop 4\n"
  "v_pk_mul_f32 v[56:57], v[56:57], v[72:73]\n"
  "v_pk_mul_f32 v[58:59], v[58:59], v[74:75]\n"
  "v_pk_mul_f32 v[60:61], v[60:61], v[72:73]\n"
  "v_pk_mul_f32 v[62:63], v[62:63], v[74:75]\n"
  "v_pk_mul_f32 v[64:65], v[64:65], v[72:73]\n"
  "v_pk_mul_f32 v[66:67], v[66:67], v[74:75]\n"
  "v_pk_mul_f32 v[68:69], v[68:69], v[72:73]\n"
  "v_pk_mul_f32 v[70:71], v[70:71], v[74:75]\n"
  "v_cvt_pk_bf16_f32 v88, v56, v57\n"
  "v_cvt_pk_bf16_f32 v89, v58, v59\n"
  "v_cvt_pk_bf16_f32 v90, v60, v61\n"
  "v_cvt_pk_bf16_f32 v91, v62, v63\n"
  "v_cvt_pk_bf16_f32 v92, v64, v65\n"
  "v_cvt_pk_bf16_f32 v93, v66, v67\n"
  "v_cvt_pk_bf16_f32 v94, v68, v69\n"
  "v_cvt_pk_bf16_f32 v95, v70, v71\n"
  "v_permlane16_swap_b32 v88, v90\n"
  "v_permlane16_swap_b32 v89, v91\n"
  "v_permlane16_swap_b32 v92, v94\n"
  "v_permlane16_swap_b32 v93, v95\n"
  "buffer_store_dwordx4 v[88:91], v245, s[20:23], 0 offen sc1\n"
  "buffer_store_dwordx4 v[92:95], v245, s[20:23], s52 offen sc1\n"
  "s_lshl_b32 s87, s67, 14\n"
  "s_lshl_b32 s88, s64, 7\n"
  "s_add_u32 s87, s87, s88\n"
  "v_lshrrev_b32 v45, 2, v0\n"
  "v_lshl_add_u32 v245, v45, 10, s87\n"
  "v_and_b32 v46, 3, v0\n"
  "v_lshl_add_u32 v245, v46, 4, v245\n"
  "v_lshlrev_b32 v204, 2, v45\n"
  "v_and_b32 v46, 1, v0\n"
  "v_lshl_add_u32 v204, v46, 7, v204\n"
  "v_and_b32 v46, 2, v0\n"
  "v_lshl_add_u32 v204, v46, 5, v204\n"
  "s_waitcnt vmcnt(0)\n"
  "s_barrier\n"
  "buffer_store_dwordx2 v[36:37], v248, s[20:23], s12 offen sc1\n"
  "s_add_u32 m0, s8, 16384\n"
  "s_mov_b32 s9, 262144\n"
  "buffer_load_dwordx4 v240, s[40:43], s9 offen lds\n"
  "s_add_u32 m0, s8, 17408\n"
  "s_mov_b32 s9, 294912\n"
  "buffer_load_dwordx4 v240, s[40:43], s9 offen lds\n"
  "s_add_u32 m0, s8, 18432\n"
  "s_mov_b32 s9, 327680\n"
  "buffer_load_dwordx4 v240, s[40:43], s9 offen lds\n"
  "s_add_u32 m0, s8, 19456\n"
  "s_mov_b32 s9, 360448\n"
  "buffer_load_dwordx4 v240, s[40:43], s9 offen lds\n"
  "s_add_u32 m0, s8, 20480\n"
  "s_mov_b32 s9, 393216\n"
  "buffer_load_dwordx4 v240, s[40:43], s9 offen lds\n"
  "s_add_u32 m0, s8, 21504\n"
  "s_mov_b32 s9, 425984\n"
  "buffer_load_dwordx4 v240, s[40:43], s9 offen lds\n"
  "s_add_u32 m0, s8, 22528\n"
  "s_mov_b32 s9, 458752\n"
  "buffer_load_dwordx4 v240, s[40:43], s9 offen lds\n"
  "s_add_u32 m0, s8, 23552\n"
  "s_mov_b32 s9, 491520\n"
  "buffer_load_dwordx4 v240, s[40:43], s9 offen lds\n"
  "s_add_u32 m0, s8, 0\n"
  "s_mov_b32 s9, 524288\n"
  "buffer_load_dwordx4 v240, s[40:43], s9 offen lds\n"
  "s_add_u32 m0, s8, 1024\n"
  "s_mov_b32 s9, 557056\n"
  "buffer_load_dwordx4 v240, s[40:43], s9 offen lds\n"
  "s_waitcnt vmcnt(6)\n"
  "ds_read_b64_tr_b16 a[32:33], v234 offset:16384\n"
  "ds_read_b64_tr_b16 a[34:35], v234 offset:18432\n"
  "ds_read_b64_tr_b16 a[36:37], v235 offset:16384\n"
  "ds_read_b64_tr_b16 a[38:39], v235 offset:18432\n"
  "ds_read_b64_tr_b16 a[40:41], v236 offset:16384\n"
  "ds_read_b64_tr_b16 a[42:43], v236 offset:18432\n"
  "ds_read_b64_tr_b16 a[44:45], v237 offset:16384\n"
  "ds_read_b64_tr_b16 a[46:47], v237 offset:18432\n"
  "s_add_u32 m0, s8, 2048\n"
  "s_mov_b32 s9, 589824\n"
  "buffer_load_dwordx4 v240, s[40:43], s9 offen lds\n"
  "s_add_u32 m0, s8, 3072\n"
  "s_mov_b32 s9, 622592\n"
  "buffer_load_dwordx4 v240, s[40:43], s9 offen lds\n"
  "s_add_u32 m0, s8, 4096\n"
  "s_mov_b32 s9, 655360\n"
  "buffer_load_dwordx4 v240, s[40:43], s9 offen lds\n"
  "s_add_u32 m0, s8, 5120\n"
  "s_mov_b32 s9, 688128\n"
  "buffer_load_dwordx4 v240, s[40:43], s9 offen lds\n"
  "s_waitcnt vmcnt(6) lgkmcnt(0)\n"
  "ds_read_b64_tr_b16 a[48:49], v234 offset:20480\n"
  "ds_read_b64_tr_b16 a[50:51], v234 offset:22528\n"
  "ds_read_b64_tr_b16 a[52:53], v235 offset:20480\n"
  "ds_read_b64_tr_b16 a[54:55], v235 offset:22528\n"
  "ds_read_b64_tr_b16 a[56:57], v236 offset:20480\n"
  "ds_read_b64_tr_b16 a[58:59], v236 offset:22528\n"
  "ds_read_b64_tr_b16 a[60:61], v237 offset:20480\n"
  "ds_read_b64_tr_b16 a[62:63], v237 offset:22528\n"
  "s_add_u32 m0, s8, 6144\n"
  "s_mov_b32 s9, 720896\n"
  "buffer_load_dwordx4 v240, s[40:43], s9 offen lds\n"
  "s_add_u32 m0, s8, 7168\n"
  "s_mov_b32 s9, 753664\n"
  "buffer_load_dwordx4 v240, s[40:43], s9 offen lds\n"
  "s_add_u32 m0, s8, 8192\n"
  "s_mov_b32 s9, 786432\n"
  "buffer_load_dwordx4 v240, s[40:43], s9 offen lds\n"
  "s_add_u32 m0, s8, 9216\n"
  "s_mov_b32 s9, 819200\n"
  "buffer_load_dwordx4 v240, s[40:43], s9 offen lds\n"
  "s_waitcnt vmcnt(6) lgkmcnt(0)\n"
  "ds_read_b64_tr_b16 a[64:65], v234 offset:0\n"
  "ds_read_b64_tr_b16 a[66:67], v234 offset:2048\n"
  "ds_read_b64_tr_b16 a[68:69], v235 offset:0\n"
  "ds_read_b64_tr_b16 a[70:71], v235 offset:2048\n"
  "ds_read_b64_tr_b16 a[72:73], v236 offset:0\n"
  "ds_read_b64_tr_b16 a[74:75], v236 offset:2048\n"
  "ds_read_b64_tr_b16 a[76:77], v237 offset:0\n"
  "ds_read_b64_tr_b16 a[78:79], v237 offset:2048\n"
  "s_add_u32 m0, s8, 10240\n"
  "s_mov_b32 s9, 851968\n"
  "buffer_load_dwordx4 v240, s[40:43], s9 offen lds\n"
  "s_add_u32 m0, s8, 11264\n"
  "s_mov_b32 s9, 884736\n"
  "buffer_load_dwordx4 v240, s[40:43], s9 offen lds\n"
  "s_add_u32 m0, s8, 12288\n"
  "s_mov_b32 s9, 917504\n"
  "buffer_load_dwordx4 v240, s[40:43], s9 offen lds\n"
  "s_add_u32 m0, s8, 13312\n"
  "s_mov_b32 s9, 950272\n"
  "buffer_load_dwordx4 v240, s[40:43], s9 offen lds\n"
  "s_waitcnt vmcnt(6) lgkmcnt(0)\n"
  "ds_read_b64_tr_b16 a[80:81], v234 offset:4096\n"
  "ds_read_b64_tr_b16 a[82:83], v234 offset:6144\n"
  "ds_read_b64_tr_b16 a[84:85], v235 offset:4096\n"
  "ds_read_b64_tr_b16 a[86:87], v235 offset:6144\n"
  "ds_read_b64_tr_b16 a[88:89], v236 offset:4096\n"
  "ds_read_b64_tr_b16 a[90:91], v236 offset:6144\n"
  "ds_read_b64_tr_b16 a[92:93], v237 offset:4096\n"
  "ds_read_b64_tr_b16 a[94:95], v237 offset:6144\n"
  "s_add_u32 m0, s8, 14336\n"
  "s_mov_b32 s9, 983040\n"
  "buffer_load_dwordx4 v240, s[40:43], s9 offen lds\n"
  "s_add_u32 m0, s8, 15360\n"
  "s_mov_b32 s9, 1015808\n"
  "buffer_load_dwordx4 v240, s[40:43], s9 offen lds\n"
  "s_add_u32 m0, s8, 16384\n"
  "s_mov_b32 s9, 1048576\n"
  "buffer_load_dwordx4 v240, s[40:43], s9 offen lds\n"
  "s_add_u32 m0, s8, 17408\n"
  "s_mov_b32 s9, 1081344\n"
  "buffer_load_dwordx4 v240, s[40:43], s9 offen lds\n"
  "s_waitcnt vmcnt(6) lgkmcnt(0)\n"
  "ds_read_b64_tr_b16 a[96:97], v234 offset:8192\n"
  "ds_read_b64_tr_b16 a[98:99], v234 offset:10240\n"
  "ds_read_b64_tr_b16 a[100:101], v235 offset:8192\n"
  "ds_read_b64_tr_b16 a[102:103], v235 offset:10240\n"
  "ds_read_b64_tr_b16 a[104:105], v236 offset:8192\n"
  "ds_read_b64_tr_b16 a[106:107], v236 offset:10240\n"
  "ds_read_b64_tr_b16 a[108:109], v237 offset:8192\n"
  "ds_read_b64_tr_b16 a[110:111], v237 offset:10240\n"
  "s_add_u32 m0, s8, 18432\n"
  "s_mov_b32 s9, 1114112\n"
  "buffer_load_dwordx4 v240, s[40:43], s9 offen lds\n"
  "s_add_u32 m0, s8, 19456\n"
  "s_mov_b32 s9, 1146880\n"
  "buffer_load_dwordx4 v240, s[40:43], s9 offen lds\n"
  "s_add_u32 m0, s8, 20480\n"
  "s_mov_b32 s9, 1179648\n"
  "buffer_load_dwordx4 v240, s[40:43], s9 offen lds\n"
  "s_add_u32 m0, s8, 21504\n"
  "s_mov_b32 s9, 1212416\n"
  "buffer_load_dwordx4 v240, s[40:43], s9 offen lds\n"
  "s_waitcnt vmcnt(6) lgkmcnt(0)\n"
  "ds_read_b64_tr_b16 a[112:113], v234 offset:12288\n"
  "ds_read_b64_tr_b16 a[114:115], v234 offset:14336\n"
  "ds_read_b64_tr_b16 a[116:117], v235 offset:12288\n"
  "ds_read_b64_tr_b16 a[118:119], v235 offset:14336\n"
  "ds_read_b64_tr_b16 a[120:121], v236 offset:12288\n"
  "ds_read_b64_tr_b16 a[122:123], v236 offset:14336\n"
  "ds_read_b64_tr_b16 a[124:125], v237 offset:12288\n"
  "ds_read_b64_tr_b16 a[126:127], v237 offset:14336\n"
  "s_add_u32 m0, s8, 22528\n"
  "s_mov_b32 s9, 1245184\n"
  "buffer_load_dwordx4 v240, s[40:43], s9 offen lds\n"
  "s_add_u32 m0, s8, 23552\n"
  "s_mov_b32 s9, 1277952\n"
  "buffer_load_dwordx4 v240, s[40:43], s9 offen lds\n"
  "s_add_u32 m0, s8, 0\n"
  "s_mov_b32 s9, 1310720\n"
  "buffer_load_dwordx4 v240, s[40:43], s9 offen lds\n"
  "s_add_u32 m0, s8, 1024\n"
  "s_mov_b32 s9, 1343488\n"
  "buffer_load_dwordx4 v240, s[40:43], s9 offen lds\n"
  "s_waitcnt vmcnt(6) lgkmcnt(0)\n"
  "ds_read_b64_tr_b16 a[128:129], v234 offset:16384\n"
  "ds_read_b64_tr_b16 a[130:131], v234 offset:18432\n"
  "ds_read_b64_tr_b16 a[132:133], v235 offset:16384\n"
  "ds_read_b64_tr_b16 a[134:135], v235 offset:18432\n"
  "ds_read_b64_tr_b16 a[136:137], v236 offset:16384\n"
  "ds_read_b64_tr_b16 a[138:139], v236 offset:18432\n"
  "ds_read_b64_tr_b16 a[140:141], v237 offset:16384\n"
  "ds_read_b64_tr_b16 a[142:143], v237 offset:18432\n"
  "s_add_u32 m0, s8, 2048\n"
  "s_mov_b32 s9, 1376256\n"
  "buffer_load_dwordx4 v240, s[40:43], s9 offen lds\n"
  "s_add_u32 m0, s8, 3072\n"
  "s_mov_b32 s9, 1409024\n"
  "buffer_load_dwordx4 v240, s[40:43], s9 offen lds\n"
  "s_add_u32 m0, s8, 4096\n"
  "s_mov_b32 s9, 1441792\n"
  "buffer_load_dwordx4 v240, s[40:43], s9 offen lds\n"
  "s_add_u32 m0, s8, 5120\n"
  "s_mov_b32 s9, 1474560\n"
  "buffer_load_dwordx4 v240, s[40:43], s9 offen lds\n"
  "s_waitcnt vmcnt(6) lgkmcnt(0)\n"
  "ds_read_b64_tr_b16 a[144:145], v234 offset:20480\n"
  "ds_read_b64_tr_b16 a[146:147], v234 offset:22528\n"
  "ds_read_b64_tr_b16 a[148:149], v235 offset:20480\n"
  "ds_read_b64_tr_b16 a[150:151], v235 offset:22528\n"
  "ds_read_b64_tr_b16 a[152:153], v236 offset:20480\n"
  "ds_read_b64_tr_b16 a[154:155], v236 offset:22528\n"
  "ds_read_b64_tr_b16 a[156:157], v237 offset:20480\n"
  "ds_read_b64_tr_b16 a[158:159], v237 offset:22528\n"
  "s_add_u32 m0, s8, 6144\n"
  "s_mov_b32 s9, 1507328\n"
  "buffer_load_dwordx4 v240, s[40:43], s9 offen lds\n"
  "s_add_u32 m0, s8, 7168\n"
  "s_mov_b32 s9, 1540096\n"
  "buffer_load_dwordx4 v240, s[40:43], s9 offen lds\n"
  "s_waitcnt vmcnt(4) lgkmcnt(0)\n"
  "ds_read_b64_tr_b16 a[160:161], v234 offset:0\n"
  "ds_read_b64_tr_b16 a[162:163], v234 offset:2048\n"
  "ds_read_b64_tr_b16 a[164:165], v235 offset:0\n"
  "ds_read_b64_tr_b16 a[166:167], v235 offset:2048\n"
  "ds_read_b64_tr_b16 a[168:169], v236 offset:0\n"
  "ds_read_b64_tr_b16 a[170:171], v236 offset:2048\n"
  "ds_read_b64_tr_b16 a[172:173], v237 offset:0\n"
  "ds_read_b64_tr_b16 a[174:175], v237 offset:2048\n"
  "s_waitcnt vmcnt(0) lgkmcnt(0)\n"
  "ds_read_b64_tr_b16 a[176:177], v234 offset:4096\n"
  "ds_read_b64_tr_b16 a[178:179], v234 offset:6144\n"
  "ds_read_b64_tr_b16 a[180:181], v235 offset:4096\n"
  "ds_read_b64_tr_b16 a[182:183], v235 offset:6144\n"
  "ds_read_b64_tr_b16 a[184:185], v236 offset:4096\n"
  "ds_read_b64_tr_b16 a[186:187], v236 offset:6144\n"
  "ds_read_b64_tr_b16 a[188:189], v237 offset:4096\n"
  "ds_read_b64_tr_b16 a[190:191], v237 offset:6144\n"
  "s_sleep 19\n"
  "buffer_load_dwordx2 v[206:207], v247, s[20:23], 0 offen sc1\n"
  "s_waitcnt vmcnt(0) lgkmcnt(0)\n"
  "v_cmp_ne_u64 vcc, s[56:57], v[206:207]\n"
  "s_cmp_eq_u64 vcc, 0\n"
  "s_cbranch_scc0 .Lfb10_%=\n"
  ".Lbk10_%=:\n"
  "buffer_load_dwordx4 v[112:115], v232, s[20:23], 0 offen sc1\n"
  "buffer_load_dwordx4 v[116:119], v232, s[20:23], 0 offen offset:1024 sc1\n"
  "buffer_load_dwordx4 v[120:123], v232, s[20:23], 0 offen offset:2048 sc1\n"
  "buffer_load_dwordx4 v[124:127], v232, s[20:23], 0 offen offset:3072 sc1\n"
  "buffer_load_dwordx4 v[128:131], v232, s[20:23], s72 offen sc1\n"
  "buffer_load_dwordx4 v[132:135], v232, s[20:23], s72 offen offset:1024 sc1\n"
  "buffer_load_dwordx4 v[136:139], v232, s[20:23], s72 offen offset:2048 sc1\n"
  "buffer_load_dwordx4 v[140:143], v232, s[20:23], s72 offen offset:3072 sc1\n"
  "buffer_load_dwordx4 v[144:147], v232, s[20:23], s73 offen sc1\n"
  "buffer_load_dwordx4 v[148:151], v232, s[20:23], s73 offen offset:1024 sc1\n"
  "buffer_load_dwordx4 v[152:155], v232, s[20:23], s73 offen offset:2048 sc1\n"
  "buffer_load_dwordx4 v[156:159], v232, s[20:23], s73 offen offset:3072 sc1\n"
  "buffer_load_dwordx4 v[160:163], v232, s[20:23], s74 offen sc1\n"
  "s_waitcnt vmcnt(12)\n"
  "v_mfma_f32_16x16x32_bf16 v[48:51], v[112:115], a[0:3], v[48:51]\n"
  "v_mfma_f32_16x16x32_bf16 v[56:59], v[112:115], a[4:7], 0\n"
  "v_mfma_f32_16x16x32_bf16 v[64:67], v[112:115], a[8:11], 0\n"
  "buffer_load_dwordx4 v[164:167], v232, s[20:23], s74 offen offset:1024 sc1\n"
  "v_mfma_f32_16x16x32_bf16 v[72:75], v[112:115], a[12:15], 0\n"
  "s_waitcnt vmcnt(12)\n"
  "v_mfma_f32_16x16x32_bf16 v[52:55], v[116:119], a[0:3], v[52:55]\n"
  "v_mfma_f32_16x16x32_bf16 v[60:63], v[116:119], a[4:7], 0\n"
  "v_mfma_f32_16x16x32_bf16 v[68:71], v[116:119], a[8:11], 0\n"
  "buffer_load_dwordx4 v[168:171], v232, s[20:23], s74 offen offset:2048 sc1\n"
  "v_mfma_f32_16x16x32_bf16 v[76:79], v[116:119], a[12:15], 0\n"
  "s_waitcnt vmcnt(12)\n"
  "v_mfma_f32_16x16x32_bf16 v[48:51], v[120:123], a[16:19], v[48:51]\n"
  "v_mfma_f32_16x16x32_bf16 v[56:59], v[120:123], a[20:23], v[56:59]\n"
  "buffer_load_dwordx4 v[172:175], v232, s[20:23], s74 offen offset:3072 sc1\n"
  "v_mfma_f32_16x16x32_bf16 v[64:67], v[120:123], a[24:27], v[64:67]\n"
  "v_mfma_f32_16x16x32_bf16 v[72:75], v[120:123], a[28:31], v[72:75]\n"
  "s_waitcnt vmcnt(12)\n"
  "v_mfma_f32_16x16x32_bf16 v[52:55], v[124:127], a[16:19], v[52:55]\n"
  "v_mfma_f32_16x16x32_bf16 v[60:63], v[124:127], a[20:23], v[60:63]\n"
  "buffer_load_dwordx4 v[216:219], v232, s[20:23], s75 offen sc1\n"
  "v_mfma_f32_16x16x32_bf16 v[68:71], v[124:127], a[24:27], v[68:71]\n"
  "v_mfma_f32_16x16x32_bf16 v[76:79], v[124:127], a[28:31], v[76:79]\n"
  "s_waitcnt vmcnt(12)\n"
  "v_mfma_f32_16x16x32_bf16 v[48:51], v[128:131], a[32:35], v[48:51]\n"
  "v_mfma_f32_16x16x32_bf16 v[56:59], v[128:131], a[36:39], v[56:59]\n"
  "buffer_load_dwordx4 v[220:223], v232, s[20:23], s75 offen offset:1024 sc1\n"
  "v_mfma_f32_16x16x32_bf16 v[64:67], v[128:131], a[40:43], v[64:67]\n"
  "v_mfma_f32_16x16x32_bf16 v[72:75], v[128:131], a[44:47], v[72:75]\n"
  "s_waitcnt vmcnt(12)\n"
  "v_mfma_f32_16x16x32_bf16 v[52:55], v[132:135], a[32:35], v[52:55]\n"
  "v_mfma_f32_16x16x32_bf16 v[60:63], v[132:135], a[36:39], v[60:63]\n"
  "buffer_load_dwordx4 v[224:227], v232, s[20:23], s75 offen offset:2048 sc1\n"
  "v_mfma_f32_16x16x32_bf16 v[68:71], v[132:135], a[40:43], v[68:71]\n"
  "v_mfma_f32_16x16x32_bf16 v[76:79], v[132:135], a[44:47], v[76:79]\n"
  "s_waitcnt vmcnt(12)\n"
  "v_mfma_f32_16x16x32_bf16 v[48:51], v[136:139], a[48:51], v[48:51]\n"
  "buffer_load_dwordx4 v[228:231], v232, s[20:23], s75 offen offset:3072 sc1\n"
  "v_mfma_f32_16x16x32_bf16 v[56:59], v[136:139], a[52:55], v[56:59]\n"
  "v_mfma_f32_16x16x32_bf16 v[64:67], v[136:139], a[56:59], v[64:67]\n"
  "v_mfma_f32_16x16x32_bf16 v[72:75], v[136:139], a[60:63], v[72:75]\n"
  "s_waitcnt vmcnt(12)\n"
  "v_mfma_f32_16x16x32_bf16 v[52:55], v[140:143], a[48:51], v[52:55]\n"
  "buffer_load_dwordx4 v[112:115], v232, s[20:23], s76 offen sc1\n"
  "v_mfma_f32_16x16x32_bf16 v[60:63], v[140:143], a[52:55], v[60:63]\n"
  "v_mfma_f32_16x16x32_bf16 v[68:71], v[140:143], a[56:59], v[68:71]\n"
  "v_mfma_f32_16x16x32_bf16 v[76:79], v[140:143], a[60:63], v[76:79]\n"
  "s_waitcnt vmcnt(12)\n"
  "v_mfma_f32_16x16x32_bf16 v[48:51], v[144:147], a[64:67], v[48:51]\n"
  "buffer_load_dwordx4 v[116:119], v232, s[20:23], s76 offen offset:1024 sc1\n"
  "v_mfma_f32_16x16x32_bf16 v[56:59], v[144:147], a[68:71], v[56:59]\n"
  "v_mfma_f32_16x16x32_bf16 v[64:67], v[144:147], a[72:75], v[64:67]\n"
  "v_mfma_f32_16x16x32_bf16 v[72:75], v[144:147], a[76:79], v[72:75]\n"
  "s_waitcnt vmcnt(12)\n"
  "v_mfma_f32_16x16x32_bf16 v[52:55], v[148:151], a[64:67], v[52:55]\n"
  "buffer_load_dwordx4 v[120:123], v232, s[20:23], s76 offen offset:2048 sc1\n"
  "v_mfma_f32_16x16x32_bf16 v[60:63], v[148:151], a[68:71], v[60:63]\n"
  "v_mfma_f32_16x16x32_bf16 v[68:71], v[148:151], a[72:75], v[68:71]\n"
  "buffer_load_dwordx2 v[206:207], v247, s[20:23], 0 offen offset:128 sc1\n"
  "v_mfma_f32_16x16x32_bf16 v[76:79], v[148:151], a[76:79], v[76:79]\n"
  "s_waitcnt vmcnt(13)\n"
  "v_mfma_f32_16x16x32_bf16 v[48:51], v[152:155], a[80:83], v[48:51]\n"
  "v_mfma_f32_16x16x32_bf16 v[56:59], v[152:155], a[84:87], v[56:59]\n"
  "v_mfma_f32_16x16x32_bf16 v[64:67], v[152:155], a[88:91], v[64:67]\n"
  "v_mfma_f32_16x16x32_bf16 v[72:75], v[152:155], a[92:95], v[72:75]\n"
  "buffer_load_dwordx4 v[124:127], v232, s[20:23], s76 offen offset:3072 sc1\n"
  "s_waitcnt vmcnt(13)\n"
  "v_mfma_f32_16x16x32_bf16 v[52:55], v[156:159], a[80:83], v[52:55]\n"
  "v_mfma_f32_16x16x32_bf16 v[60:63], v[156:159], a[84:87], v[60:63]\n"
  "v_mfma_f32_16x16x32_bf16 v[68:71], v[156:159], a[88:91], v[68:71]\n"
  "v_mfma_f32_16x16x32_bf16 v[76:79], v[156:159], a[92:95], v[76:79]\n"
  "buffer_load_dwordx4 v[128:131], v232, s[20:23], s77 offen sc1\n"
  "s_waitcnt vmcnt(13)\n"
  "v_mfma_f32_16x16x32_bf16 v[48:51], v[160:163], a[96:99], v[48:51]\n"
  "v_mfma_f32_16x16x32_bf16 v[56:59], v[160:163], a[100:103], v[56:59]\n"
  "v_mfma_f32_16x16x32_bf16 v[64:67], v[160:163], a[104:107], v[64:67]\n"
  "v_mfma_f32_16x16x32_bf16 v[72:75], v[160:163], a[108:111], v[72:75]\n"
  "buffer_load_dwordx4 v[132:135], v232, s[20:23], s77 offen offset:1024 sc1\n"
  "s_waitcnt vmcnt(13)\n"
  "v_mfma_f32_16x16x32_bf16 v[52:55], v[164:167], a[96:99], v[52:55]\n"
  "v_mfma_f32_16x16x32_bf16 v[60:63], v[164:167], a[100:103], v[60:63]\n"
  "v_mfma_f32_16x16x32_bf16 v[68:71], v[164:167], a[104:107], v[68:71]\n"
  "v_mfma_f32_16x16x32_bf16 v[76:79], v[164:167], a[108:111], v[76:79]\n"
  "buffer_load_dwordx4 v[136:139], v232, s[20:23], s77 offen offset:2048 sc1\n"
  "s_waitcnt vmcnt(13)\n"
  "v_mfma_f32_16x16x32_bf16 v[48:51], v[168:171], a[112:115], v[48:51]\n"
  "v_mfma_f32_16x16x32_bf16 v[56:59], v[168:171], a[116:119], v[56:59]\n"
  "v_mfma_f32_16x16x32_bf16 v[64:67], v[168:171], a[120:123], v[64:67]\n"
  "buffer_load_dwordx4 v[140:143], v232, s[20:23], s77 offen offset:3072 sc1\n"
  "v_mfma_f32_16x16x32_bf16 v[72:75], v[168:171], a[124:127], v[72:75]\n"
  "s_waitcnt vmcnt(13)\n"
  "v_mfma_f32_16x16x32_bf16 v[52:55], v[172:175], a[112:115], v[52:55]\n"
  "v_mfma_f32_16x16x32_bf16 v[60:63], v[172:175], a[116:119], v[60:63]\n"
  "v_mfma_f32_16x16x32_bf16 v[68:71], v[172:175], a[120:123], v[68:71]\n"
  "v_mfma_f32_16x16x32_bf16 v[76:79], v[172:175], a[124:127], v[76:79]\n"
  "s_waitcnt vmcnt(12)\n"
  "v_mfma_f32_16x16x32_bf16 v[48:51], v[216:219], a[128:131], v[48:51]\n"
  "v_mfma_f32_16x16x32_bf16 v[56:59], v[216:219], a[132:135], v[56:59]\n"
  "v_mfma_f32_16x16x32_bf16 v[64:67], v[216:219], a[136:139], v[64:67]\n"
  "v_mfma_f32_16x16x32_bf16 v[72:75], v[216:219], a[140:143], v[72:75]\n"
  "s_waitcnt vmcnt(11)\n"
  "v_mfma_f32_16x16x32_bf16 v[52:55], v[220:223], a[128:131], v[52:55]\n"
  "v_mfma_f32_16x16x32_bf16 v[60:63], v[220:223], a[132:135], v[60:63]\n"
  "v_mfma_f32_16x16x32_bf16 v[68:71], v[220:223], a[136:139], v[68:71]\n"
  "v_mfma_f32_16x16x32_bf16 v[76:79], v[220:223], a[140:143], v[76:79]\n"
  "s_waitcnt vmcnt(10)\n"
  "v_mfma_f32_16x16x32_bf16 v[48:51], v[224:227], a[144:147], v[48:51]\n"
  "v_mfma_f32_16x16x32_bf16 v[56:59], v[224:227], a[148:151], v[56:59]\n"
  "v_mfma_f32_16x16x32_bf16 v[64:67], v[224:227], a[152:155], v[64:67]\n"
  "v_mfma_f32_16x16x32_bf16 v[72:75], v[224:227], a[156:159], v[72:75]\n"
  "s_waitcnt vmcnt(9)\n"
  "v_mfma_f32_16x16x32_bf16 v[52:55], v[228:231], a[144:147], v[52:55]\n"
  "v_mfma_f32_16x16x32_bf16 v[60:63], v[228:231], a[148:151], v[60:63]\n"
  "v_mfma_f32_16x16x32_bf16 v[68:71], v[228:231], a[152:155], v[68:71]\n"
  "v_mfma_f32_16x16x32_bf16 v[76:79], v[228:231], a[156:159], v[76:79]\n"
  "s_waitcnt vmcnt(8)\n"
  "v_mfma_f32_16x16x32_bf16 v[48:51], v[112:115], a[160:163], v[48:51]\n"
  "v_mfma_f32_16x16x32_bf16 v[56:59], v[112:115], a[164:167], v[56:59]\n"
  "v_mfma_f32_16x16x32_bf16 v[64:67], v[112:115], a[168:171], v[64:67]\n"
  "v_mfma_f32_16x16x32_bf16 v[72:75], v[112:115], a[172:175], v[72:75]\n"
  "s_waitcnt vmcnt(7)\n"
  "v_mfma_f32_16x16x32_bf16 v[52:55], v[116:119], a[160:163], v[52:55]\n"
  "v_mfma_f32_16x16x32_bf16 v[60:63], v[116:119], a[164:167], v[60:63]\n"
  "v_mfma_f32_16x16x32_bf16 v[68:71], v[116:119], a[168:171], v[68:71]\n"
  "s_waitcnt vmcnt(5)\n"
  "v_cmp_ne_u64 vcc, s[56:57], v[206:207]\n"
  "s_cmp_eq_u64 vcc, 0\n"
  "s_cbranch_scc0 .Lfb11_%=\n"
  ".Lbk11_%=:\n"
  "v_mfma_f32_16x16x32_bf16 v[76:79], v[116:119], a[172:175], v[76:79]\n"
  "buffer_load_dwordx4 v[144:147], v233, s[20:23], 0 offen sc1\n"
  "buffer_load_dwordx4 v[148:151], v233, s[20:23], 0 offen offset:1024 sc1\n"
  "v_mfma_f32_16x16x32_bf16 v[48:51], v[120:123], a[176:179], v[48:51]\n"
  "buffer_load_dwordx4 v[152:155], v233, s[20:23], 0 offen offset:2048 sc1\n"
  "v_mfma_f32_16x16x32_bf16 v[56:59], v[120:123], a[180:183], v[56:59]\n"
  "v_mfma_f32_16x16x32_bf16 v[64:67], v[120:123], a[184:187], v[64:67]\n"
  "v_mfma_f32_16x16x32_bf16 v[72:75], v[120:123], a[188:191], v[72:75]\n"
  "buffer_load_dwordx4 v[156:159], v233, s[20:23], 0 offen offset:3072 sc1\n"
  "s_waitcnt vmcnt(8)\n"
  "v_mfma_f32_16x16x32_bf16 v[52:55], v[124:127], a[176:179], v[52:55]\n"
  "v_mfma_f32_16x16x32_bf16 v[60:63], v[124:127], a[180:183], v[60:63]\n"
  "v_mfma_f32_16x16x32_bf16 v[68:71], v[124:127], a[184:187], v[68:71]\n"
  "v_mfma_f32_16x16x32_bf16 v[76:79], v[124:127], a[188:191], v[76:79]\n"
  "buffer_load_dwordx4 v[160:163], v233, s[20:23], s72 offen sc1\n"
  "s_waitcnt vmcnt(8)\n"
  "v_mfma_f32_16x16x32_bf16 v[48:51], v[128:131], v[212:215], v[48:51]\n"
  "v_mfma_f32_16x16x32_bf16 v[56:59], v[128:131], v[208:211], v[56:59]\n"
  "v_mfma_f32_16x16x32_bf16 v[64:67], v[128:131], v[184:187], v[64:67]\n"
  "v_mfma_f32_16x16x32_bf16 v[72:75], v[128:131], v[180:183], v[72:75]\n"
  "buffer_load_dwordx4 v[164:167], v233, s[20:23], s72 offen offset:1024 sc1\n"
  "s_waitcnt vmcnt(8)\n"
  "v_mfma_f32_16x16x32_bf16 v[52:55], v[132:135], v[212:215], v[52:55]\n"
  "v_mfma_f32_16x16x32_bf16 v[60:63], v[132:135], v[208:211], v[60:63]\n"
  "v_mfma_f32_16x16x32_bf16 v[68:71], v[132:135], v[184:187], v[68:71]\n"
  "buffer_load_dwordx4 v[168:171], v233, s[20:23], s72 offen offset:2048 sc1\n"
  "v_mfma_f32_16x16x32_bf16 v[76:79], v[132:135], v[180:183], v[76:79]\n"
  "s_waitcnt vmcnt(8)\n"
  "v_mfma_f32_16x16x32_bf16 v[48:51], v[136:139], v[176:179], v[48:51]\n"
  "v_mfma_f32_16x16x32_bf16 v[56:59], v[136:139], v[200:203], v[56:59]\n"
  "v_mfma_f32_16x16x32_bf16 v[64:67], v[136:139], v[196:199], v[64:67]\n"
  "buffer_load_dwordx4 v[172:175], v233, s[20:23], s72 offen offset:3072 sc1\n"
  "v_mfma_f32_16x16x32_bf16 v[72:75], v[136:139], v[192:195], v[72:75]\n"
  "s_nop 4\n"
  "ds_write_b64 v242, v[56:57] offset:0\n"
  "s_waitcnt vmcnt(8)\n"
  "v_mfma_f32_16x16x32_bf16 v[52:55], v[140:143], v[176:179], v[52:55]\n"
  "ds_write_b64 v242, v[58:59] offset:8\n"
  "v_mfma_f32_16x16x32_bf16 v[60:63], v[140:143], v[200:203], v[60:63]\n"
  "ds_write_b64 v243, v[64:65] offset:0\n"
  "v_mfma_f32_16x16x32_bf16 v[68:71], v[140:143], v[196:199], v[68:71]\n"
  "buffer_load_dwordx4 v[216:219], v233, s[20:23], s73 offen sc1\n"
  "v_mfma_f32_16x16x32_bf16 v[76:79], v[140:143], v[192:195], v[76:79]\n"
  "s_nop 3\n"
  "ds_write_b64 v242, v[60:61] offset:1024\n"
  "ds_write_b64 v242, v[62:63] offset:1032\n"
  "ds_write_b64 v243, v[66:67] offset:8\n"
  "buffer_load_dwordx4 v[220:223], v233, s[20:23], s73 offen offset:1024 sc1\n"
  "ds_write_b64 v243, v[68:69] offset:1024\n"
  "ds_write_b64 v243, v[70:71] offset:1032\n"
  "ds_write_b64 v244, v[72:73] offset:0\n"
  "buffer_load_dwordx4 v[224:227], v233, s[20:23], s73 offen offset:2048 sc1\n"
  "ds_write_b64 v244, v[74:75] offset:8\n"
  "ds_write_b64 v244, v[76:77] offset:1024\n"
  "ds_write_b64 v244, v[78:79] offset:1032\n"
  "buffer_load_dwordx4 v[228:231], v233, s[20:23], s73 offen offset:3072 sc1\n"
  "buffer_load_dwordx4 v[112:115], v233, s[20:23], s74 offen sc1\n"
  "s_waitcnt lgkmcnt(0)\n"
  "s_barrier\n"
  "s_waitcnt vmcnt(12)\n"
  "v_mfma_f32_16x16x32_bf16 v[80:83], v[144:147], a[0:3], v[80:83]\n"
  "ds_read_b128 v[56:59], v241 offset:0\n"
  "ds_read_b128 v[64:67], v241 offset:2048\n"
  "v_mfma_f32_16x16x32_bf16 v[88:91], v[144:147], a[4:7], 0\n"
  "ds_read_b128 v[72:75], v241 offset:4096\n"
  "v_mfma_f32_16x16x32_bf16 v[96:99], v[144:147], a[8:11], 0\n"
  "ds_read_b128 v[60:63], v241 offset:1024\n"
  "buffer_load_dwordx4 v[116:119], v233, s[20:23], s74 offen offset:1024 sc1\n"
  "v_mfma_f32_16x16x32_bf16 v[104:107], v[144:147], a[12:15], 0\n"
  "ds_read_b128 v[68:71], v241 offset:3072\n"
  "s_waitcnt vmcnt(12)\n"
  "v_mfma_f32_16x16x32_bf16 v[84:87], v[148:151], a[0:3], v[84:87]\n"
  "ds_read_b128 v[76:79], v241 offset:5120\n"
  "v_mfma_f32_16x16x32_bf16 v[92:95], v[148:151], a[4:7], 0\n"
  "buffer_load_dwordx4 v[120:123], v233, s[20:23], s74 offen offset:2048 sc1\n"
  "v_mfma_f32_16x16x32_bf16 v[100:103], v[148:151], a[8:11], 0\n"
  "s_waitcnt lgkmcnt(5)\n"
  "v_add_f32 v48, v48, v56\n"
  "v_mfma_f32_16x16x32_bf16 v[108:111], v[148:151], a[12:15], 0\n"
  "v_add_f32 v49, v49, v57\n"
  "s_waitcnt lgkmcnt(3)\n"
  "v_add_f32 v64, v64, v72\n"
  "s_waitcnt vmcnt(12)\n"
  "v_mfma_f32_16x16x32_bf16 v[80:83], v[152:155], a[16:19], v[80:83]\n"
  "v_add_f32 v65, v65, v73\n"
  "v_add_f32 v50, v50, v58\n"
  "v_mfma_f32_16x16x32_bf16 v[88:91], v[152:155], a[20:23], v[88:91]\n"
  "v_add_f32 v66, v66, v74\n"
  "v_add_f32 v51, v51, v59\n"
  "v_mfma_f32_16x16x32_bf16 v[96:99], v[152:155], a[24:27], v[96:99]\n"
  "v_add_f32 v67, v67, v75\n"
  "s_waitcnt lgkmcnt(2)\n"
  "v_add_f32 v52, v52, v60\n"
  "v_mfma_f32_16x16x32_bf16 v[104:107], v[152:155], a[28:31], v[104:107]\n"
  "s_waitcnt lgkmcnt(0)\n"
  "v_add_f32 v68, v68, v76\n"
  "s_waitcnt vmcnt(11)\n"
  "v_mfma_f32_16x16x32_bf16 v[84:87], v[156:159], a[16:19], v[84:87]\n"
  "buffer_load_dwordx4 v[124:127], v233, s[20:23], s74 offen offset:3072 sc1\n"
  "v_add_f32 v53, v53, v61\n"
  "v_mfma_f32_16x16x32_bf16 v[92:95], v[156:159], a[20:23], v[92:95]\n"
  "v_add_f32 v69, v69, v77\n"
  "v_add_f32 v54, v54, v62\n"
  "v_mfma_f32_16x16x32_bf16 v[100:103], v[156:159], a[24:27], v[100:103]\n"
  "v_add_f32 v70, v70, v78\n"
  "v_add_f32 v55, v55, v63\n"
  "v_mfma_f32_16x16x32_bf16 v[108:111], v[156:159], a[28:31], v[108:111]\n"
  "v_add_f32 v71, v71, v79\n"
  "v_add_f32 v48, v48, v64\n"
  "s_waitcnt vmcnt(11)\n"
  "v_mfma_f32_16x16x32_bf16 v[80:83], v[160:163], a[32:35], v[80:83]\n"
  "buffer_load_dwordx4 v[128:131], v233, s[20:23], s75 offen sc1\n"
  "v_add_f32 v49, v49, v65\n"
  "v_mfma_f32_16x16x32_bf16 v[88:91], v[160:163], a[36:39], v[88:91]\n"
  "v_add_f32 v50, v50, v66\n"
  "v_add_f32 v51, v51, v67\n"
  "v_mfma_f32_16x16x32_bf16 v[96:99], v[160:163], a[40:43], v[96:99]\n"
  "v_add_f32 v52, v52, v68\n"
  "v_add_f32 v53, v53, v69\n"
  "v_mfma_f32_16x16x32_bf16 v[104:107], v[160:163], a[44:47], v[104:107]\n"
  "v_add_f32 v54, v54, v70\n"
  "v_add_f32 v55, v55, v71\n"
  "s_waitcnt vmcnt(11)\n"
  "v_mfma_f32_16x16x32_bf16 v[84:87], v[164:167], a[32:35], v[84:87]\n"
  "buffer_load_dwordx4 v[132:135], v233, s[20:23], s75 offen offset:1024 sc1\n"
  "v_cvt_pk_bf16_f32 v56, v48, v49\n"
  "v_mfma_f32_16x16x32_bf16 v[92:95], v[164:167], a[36:39], v[92:95]\n"
  "v_cvt_pk_bf16_f32 v57, v50, v51\n"
  "v_cvt_pk_bf16_f32 v58, v52, v53\n"
  "v_mfma_f32_16x16x32_bf16 v[100:103], v[164:167], a[40:43], v[100:103]\n"
  "v_cvt_pk_bf16_f32 v59, v54, v55\n"
  "v_permlane16_swap_b32 v56, v58\n"
  "v_mfma_f32_16x16x32_bf16 v[108:111], v[164:167], a[44:47], v[108:111]\n"
  "buffer_load_dwordx4 v[136:139], v233, s[20:23], s75 offen offset:2048 sc1\n"
  "v_permlane16_swap_b32 v57, v59\n"
  "s_waitcnt vmcnt(12)\n"
  "v_mfma_f32_16x16x32_bf16 v[80:83], v[168:171], a[48:51], v[80:83]\n"
  "ds_bpermute_b32 v60, v204, v56\n"
  "ds_bpermute_b32 v61, v204, v57\n"
  "v_mfma_f32_16x16x32_bf16 v[88:91], v[168:171], a[52:55], v[88:91]\n"
  "ds_bpermute_b32 v62, v204, v58\n"
  "ds_bpermute_b32 v63, v204, v59\n"
  "v_mfma_f32_16x16x32_bf16 v[96:99], v[168:171], a[56:59], v[96:99]\n"
  "buffer_load_dwordx4 v[140:143], v233, s[20:23], s75 offen offset:3072 sc1\n"
  "buffer_load_dwordx4 v[144:147], v233, s[20:23], s76 offen sc1\n"
  "v_mfma_f32_16x16x32_bf16 v[104:107], v[168:171], a[60:63], v[104:107]\n"
  "s_waitcnt vmcnt(13)\n"
  "v_mfma_f32_16x16x32_bf16 v[84:87], v[172:175], a[48:51], v[84:87]\n"
  "v_mfma_f32_16x16x32_bf16 v[92:95], v[172:175], a[52:55], v[92:95]\n"
  "v_mfma_f32_16x16x32_bf16 v[100:103], v[172:175], a[56:59], v[100:103]\n"
  "buffer_load_dwordx4 v[148:151], v233, s[20:23], s76 offen offset:1024 sc1\n"
  "v_mfma_f32_16x16x32_bf16 v[108:111], v[172:175], a[60:63], v[108:111]\n"
  "s_waitcnt vmcnt(13)\n"
  "v_mfma_f32_16x16x32_bf16 v[80:83], v[216:219], a[64:67], v[80:83]\n"
  "v_mfma_f32_16x16x32_bf16 v[88:91], v[216:219], a[68:71], v[88:91]\n"
  "v_mfma_f32_16x16x32_bf16 v[96:99], v[216:219], a[72:75], v[96:99]\n"
  "s_waitcnt lgkmcnt(0)\n"
  "buffer_store_dwordx4 v[60:63], v245, s[48:51], 0 offen\n"
  "v_mfma_f32_16x16x32_bf16 v[104:107], v[216:219], a[76:79], v[104:107]\n"
  "s_waitcnt vmcnt(13)\n"
  "v_mfma_f32_16x16x32_bf16 v[84:87], v[220:223], a[64:67], v[84:87]\n"
  "v_mfma_f32_16x16x32_bf16 v[92:95], v[220:223], a[68:71], v[92:95]\n"
  "v_mfma_f32_16x16x32_bf16 v[100:103], v[220:223], a[72:75], v[100:103]\n"
  "buffer_load_dwordx4 v[152:155], v233, s[20:23], s76 offen offset:2048 sc1\n"
  "v_mfma_f32_16x16x32_bf16 v[108:111], v[220:223], a[76:79], v[108:111]\n"
  "s_waitcnt vmcnt(13)\n"
  "v_mfma_f32_16x16x32_bf16 v[80:83], v[224:227], a[80:83], v[80:83]\n"
  "v_mfma_f32_16x16x32_bf16 v[88:91], v[224:227], a[84:87], v[88:91]\n"
  "buffer_load_dwordx4 v[156:159], v233, s[20:23], s76 offen offset:3072 sc1\n"
  "v_mfma_f32_16x16x32_bf16 v[96:99], v[224:227], a[88:91], v[96:99]\n"
  "v_mfma_f32_16x16x32_bf16 v[104:107], v[224:227], a[92:95], v[104:107]\n"
  "s_waitcnt vmcnt(13)\n"
  "v_mfma_f32_16x16x32_bf16 v[84:87], v[228:231], a[80:83], v[84:87]\n"
  "v_mfma_f32_16x16x32_bf16 v[92:95], v[228:231], a[84:87], v[92:95]\n"
  "buffer_load_dwordx4 v[160:163], v233, s[20:23], s77 offen sc1\n"
  "v_mfma_f32_16x16x32_bf16 v[100:103], v[228:231], a[88:91], v[100:103]\n"
  "v_mfma_f32_16x16x32_bf16 v[108:111], v[228:231], a[92:95], v[108:111]\n"
  "s_waitcnt vmcnt(13)\n"
  "v_mfma_f32_16x16x32_bf16 v[80:83], v[112:115], a[96:99], v[80:83]\n"
  "v_mfma_f32_16x16x32_bf16 v[88:91], v[112:115], a[100:103], v[88:91]\n"
  "buffer_load_dwordx4 v[164:167], v233, s[20:23], s77 offen offset:1024 sc1\n"
  "v_mfma_f32_16x16x32_bf16 v[96:99], v[112:115], a[104:107], v[96:99]\n"
  "v_mfma_f32_16x16x32_bf16 v[104:107], v[112:115], a[108:111], v[104:107]\n"
  "s_waitcnt vmcnt(13)\n"
  "v_mfma_f32_16x16x32_bf16 v[84:87], v[116:119], a[96:99], v[84:87]\n"
  "v_mfma_f32_16x16x32_bf16 v[92:95], v[116:119], a[100:103], v[92:95]\n"
  "buffer_load_dwordx4 v[168:171], v233, s[20:23], s77 offen offset:2048 sc1\n"
  "v_mfma_f32_16x16x32_bf16 v[100:103], v[116:119], a[104:107], v[100:103]\n"
  "v_mfma_f32_16x16x32_bf16 v[108:111], v[116:119], a[108:111], v[108:111]\n"
  "s_waitcnt vmcnt(13)\n"
  "v_mfma_f32_16x16x32_bf16 v[80:83], v[120:123], a[112:115], v[80:83]\n"
  "buffer_load_dwordx4 v[172:175], v233, s[20:23], s77 offen offset:3072 sc1\n"
  "v_mfma_f32_16x16x32_bf16 v[88:91], v[120:123], a[116:119], v[88:91]\n"
  "v_mfma_f32_16x16x32_bf16 v[96:99], v[120:123], a[120:123], v[96:99]\n"
  "v_mfma_f32_16x16x32_bf16 v[104:107], v[120:123], a[124:127], v[104:107]\n"
  "s_waitcnt vmcnt(13)\n"
  "v_mfma_f32_16x16x32_bf16 v[84:87], v[124:127], a[112:115], v[84:87]\n"
  "v_mfma_f32_16x16x32_bf16 v[92:95], v[124:127], a[116:119], v[92:95]\n"
  "v_mfma_f32_16x16x32_bf16 v[100:103], v[124:127], a[120:123], v[100:103]\n"
  "v_mfma_f32_16x16x32_bf16 v[108:111], v[124:127], a[124:127], v[108:111]\n"
  "s_waitcnt vmcnt(12)\n"
  "v_mfma_f32_16x16x32_bf16 v[80:83], v[128:131], a[128:131], v[80:83]\n"
  "v_mfma_f32_16x16x32_bf16 v[88:91], v[128:131], a[132:135], v[88:91]\n"
  "v_mfma_f32_16x16x32_bf16 v[96:99], v[128:131], a[136:139], v[96:99]\n"
  "v_mfma_f32_16x16x32_bf16 v[104:107], v[128:131], a[140:143], v[104:107]\n"
  "s_waitcnt vmcnt(11)\n"
  "v_mfma_f32_16x16x32_bf16 v[84:87], v[132:135], a[128:131], v[84:87]\n"
  "v_mfma_f32_16x16x32_bf16 v[92:95], v[132:135], a[132:135], v[92:95]\n"
  "v_mfma_f32_16x16x32_bf16 v[100:103], v[132:135], a[136:139], v[100:103]\n"
  "v_mfma_f32_16x16x32_bf16 v[108:111], v[132:135], a[140:143], v[108:111]\n"
  "s_waitcnt vmcnt(10)\n"
  "v_mfma_f32_16x16x32_bf16 v[80:83], v[136:139], a[144:147], v[80:83]\n"
  "v_mfma_f32_16x16x32_bf16 v[88:91], v[136:139], a[148:151], v[88:91]\n"
  "v_mfma_f32_16x16x32_bf16 v[96:99], v[136:139], a[152:155], v[96:99]\n"
  "v_mfma_f32_16x16x32_bf16 v[104:107], v[136:139], a[156:159], v[104:107]\n"
  "s_waitcnt vmcnt(9)\n"
  "v_mfma_f32_16x16x32_bf16 v[84:87], v[140:143], a[144:147], v[84:87]\n"
  "v_mfma_f32_16x16x32_bf16 v[92:95], v[140:143], a[148:151], v[92:95]\n"
  "v_mfma_f32_16x16x32_bf16 v[100:103], v[140:143], a[152:155], v[100:103]\n"
  "v_mfma_f32_16x16x32_bf16 v[108:111], v[140:143], a[156:159], v[108:111]\n"
  "s_waitcnt vmcnt(8)\n"
  "v_mfma_f32_16x16x32_bf16 v[80:83], v[144:147], a[160:163], v[80:83]\n"
  "v_mfma_f32_16x16x32_bf16 v[88:91], v[144:147], a[164:167], v[88:91]\n"
  "v_mfma_f32_16x16x32_bf16 v[96:99], v[144:147], a[168:171], v[96:99]\n"
  "v_mfma_f32_16x16x32_bf16 v[104:107], v[144:147], a[172:175], v[104:107]\n"
  "s_waitcnt vmcnt(7)\n"
  "v_mfma_f32_16x16x32_bf16 v[84:87], v[148:151], a[160:163], v[84:87]\n"
  "v_mfma_f32_16x16x32_bf16 v[92:95], v[148:151], a[164:167], v[92:95]\n"
  "v_mfma_f32_16x16x32_bf16 v[100:103], v[148:151], a[168:171], v[100:103]\n"
  "v_mfma_f32_16x16x32_bf16 v[108:111], v[148:151], a[172:175], v[108:111]\n"
  "s_waitcnt vmcnt(5)\n"
  "v_mfma_f32_16x16x32_bf16 v[80:83], v[152:155], a[176:179], v[80:83]\n"
  "v_mfma_f32_16x16x32_bf16 v[88:91], v[152:155], a[180:183], v[88:91]\n"
  "v_mfma_f32_16x16x32_bf16 v[96:99], v[152:155], a[184:187], v[96:99]\n"
  "v_mfma_f32_16x16x32_bf16 v[104:107], v[152:155], a[188:191], v[104:107]\n"
  "s_waitcnt vmcnt(4)\n"
  "v_mfma_f32_16x16x32_bf16 v[84:87], v[156:159], a[176:179], v[84:87]\n"
  "v_mfma_f32_16x16x32_bf16 v[92:95], v[156:159], a[180:183], v[92:95]\n"
  "v_mfma_f32_16x16x32_bf16 v[100:103], v[156:159], a[184:187], v[100:103]\n"
  "v_mfma_f32_16x16x32_bf16 v[108:111], v[156:159], a[188:191], v[108:111]\n"
  "s_waitcnt vmcnt(3)\n"
  "v_mfma_f32_16x16x32_bf16 v[80:83], v[160:163], v[212:215], v[80:83]\n"
  "v_mfma_f32_16x16x32_bf16 v[88:91], v[160:163], v[208:211], v[88:91]\n"
  "v_mfma_f32_16x16x32_bf16 v[96:99], v[160:163], v[184:187], v[96:99]\n"
  "v_mfma_f32_16x16x32_bf16 v[104:107], v[160:163], v[180:183], v[104:107]\n"
  "s_waitcnt vmcnt(2)\n"
  "v_mfma_f32_16x16x32_bf16 v[84:87], v[164:167], v[212:215], v[84:87]\n"
  "v_mfma_f32_16x16x32_bf16 v[92:95], v[164:167], v[208:211], v[92:95]\n"
  "v_mfma_f32_16x16x32_bf16 v[100:103], v[164:167], v[184:187], v[100:103]\n"
  "v_mfma_f32_16x16x32_bf16 v[108:111], v[164:167], v[180:183], v[108:111]\n"
  "s_waitcnt vmcnt(1)\n"
  "v_mfma_f32_16x16x32_bf16 v[80:83], v[168:171], v[176:179], v[80:83]\n"
  "v_mfma_f32_16x16x32_bf16 v[88:91], v[168:171], v[200:203], v[88:91]\n"
  "v_mfma_f32_16x16x32_bf16 v[96:99], v[168:171], v[196:199], v[96:99]\n"
  "v_mfma_f32_16x16x32_bf16 v[104:107], v[168:171], v[192:195], v[104:107]\n"
  "s_waitcnt vmcnt(0)\n"
  "v_mfma_f32_16x16x32_bf16 v[84:87], v[172:175], v[176:179], v[84:87]\n"
  "v_mfma_f32_16x16x32_bf16 v[92:95], v[172:175], v[200:203], v[92:95]\n"
  "v_mfma_f32_16x16x32_bf16 v[100:103], v[172:175], v[196:199], v[100:103]\n"
  "v_mfma_f32_16x16x32_bf16 v[108:111], v[172:175], v[192:195], v[108:111]\n"
  "s_nop 0\n"
  "ds_write_b128 v242, v[88:91] offset:24576\n"
  "s_nop 3\n"
  "ds_write_b128 v242, v[92:95] offset:25600\n"
  "ds_write_b128 v243, v[96:99] offset:24576\n"
  "ds_write_b128 v243, v[100:103] offset:25600\n"
  "ds_write_b128 v244, v[104:107] offset:24576\n"
  "ds_write_b128 v244, v[108:111] offset:25600\n"
  "s_waitcnt lgkmcnt(0)\n"
  "s_barrier\n"
  "ds_read_b128 v[88:91], v241 offset:24576\n"
  "ds_read_b128 v[96:99], v241 offset:26624\n"
  "ds_read_b128 v[104:107], v241 offset:28672\n"
  "ds_read_b128 v[92:95], v241 offset:25600\n"
  "ds_read_b128 v[100:103], v241 offset:27648\n"
  "ds_read_b128 v[108:111], v241 offset:29696\n"
  "s_waitcnt lgkmcnt(5)\n"
  "v_pk_add_f32 v[80:81], v[80:81], v[88:89]\n"
  "v_pk_add_f32 v[82:83], v[82:83], v[90:91]\n"
  "s_waitcnt lgkmcnt(3)\n"
  "v_pk_add_f32 v[96:97], v[96:97], v[104:105]\n"
  "v_pk_add_f32 v[98:99], v[98:99], v[106:107]\n"
  "v_pk_add_f32 v[80:81], v[80:81], v[96:97]\n"
  "s_waitcnt lgkmcnt(2)\n"
  "v_pk_add_f32 v[84:85], v[84:85], v[92:93]\n"
  "v_pk_add_f32 v[86:87], v[86:87], v[94:95]\n"
  "v_pk_add_f32 v[82:83], v[82:83], v[98:99]\n"
  "v_cvt_pk_bf16_f32 v88, v80, v81\n"
  "v_cvt_pk_bf16_f32 v89, v82, v83\n"
  "s_waitcnt lgkmcnt(0)\n"
  "v_pk_add_f32 v[100:101], v[100:101], v[108:109]\n"
  "v_pk_add_f32 v[102:103], v[102:103], v[110:111]\n"
  "v_pk_add_f32 v[84:85], v[84:85], v[100:101]\n"
  "v_pk_add_f32 v[86:87], v[86:87], v[102:103]\n"
  "v_cvt_pk_bf16_f32 v90, v84, v85\n"
  "v_cvt_pk_bf16_f32 v91, v86, v87\n"
  "s_nop 0\n"
  "v_permlane16_swap_b32 v88, v90\n"
  "v_permlane16_swap_b32 v89, v91\n"
  "ds_bpermute_b32 v92, v204, v88\n"
  "ds_bpermute_b32 v93, v204, v89\n"
  "ds_bpermute_b32 v94, v204, v90\n"
  "ds_bpermute_b32 v95, v204, v91\n"
  "s_waitcnt lgkmcnt(0)\n"
  "buffer_store_dwordx4 v[92:95], v245, s[48:51], 0 offen offset:64\n"
  "s_branch .Lfin_%=\n"
  ".Lact_%=:\n"
  "s_waitcnt vmcnt(10)\n"
  "s_barrier\n"
  "s_mov_b64 exec, s[16:17]\n"
  "ds_read_b64 a[224:225], v188 offset:0\n"
  "ds_read_b64 a[226:227], v189 offset:0\n"
  "s_mov_b64 exec, -1\n"
  "ds_read_b64 a[228:229], v188 offset:4096\n"
  "ds_read_b64 a[230:231], v189 offset:4096\n"
  "ds_read_b64 a[232:233], v188 offset:8192\n"
  "ds_read_b64 a[234:235], v189 offset:8192\n"
  "s_mov_b64 exec, s[16:17]\n"
  "ds_read_b64 a[236:237], v190 offset:0\n"
  "ds_read_b64 a[238:239], v191 offset:0\n"
  "s_mov_b64 exec, -1\n"
  "ds_read_b64 a[240:241], v190 offset:4096\n"
  "ds_read_b64 a[242:243], v191 offset:4096\n"
  "ds_read_b64 a[244:245], v190 offset:8192\n"
  "s_add_u32 m0, s13, 24576\n"
  "s_mov_b32 s9, 393216\n"
  "buffer_load_dwordx4 v187, s[28:31], s9 offen lds\n"
  "ds_read_b64 a[246:247], v191 offset:8192\n"
  "s_mov_b64 exec, s[16:17]\n"
  "ds_read_b64 a[248:249], v192 offset:0\n"
  "ds_read_b64 a[250:251], v193 offset:0\n"
  "s_mov_b64 exec, -1\n"
  "ds_read_b64 a[252:253], v192 offset:4096\n"
  "s_waitcnt lgkmcnt(13)\n"
  "v_mfma_f32_16x16x32_bf16 v[160:163], a[224:227], a[224:227], 0\n"
  "s_add_u32 m0, s13, 24576\n"
  "s_mov_b32 s9, 393216\n"
  "buffer_load_dwordx4 v187, s[28:31], s9 offen offset:1024 lds\n"
  "s_waitcnt lgkmcnt(11)\n"
  "v_mfma_f32_16x16x32_bf16 v[164:167], a[228:231], a[228:231], 0\n"
  "ds_read_b64 a[254:255], v193 offset:4096\n"
  "s_waitcnt lgkmcnt(10)\n"
  "v_mfma_f32_16x16x32_bf16 v[168:171], a[232:235], a[232:235], 0\n"
  "ds_read_b64 a[0:1], v192 offset:8192\n"
  "s_waitcnt lgkmcnt(9)\n"
  "v_mfma_f32_16x16x32_bf16 v[160:163], a[236:239], a[236:239], v[160:163]\n"
  "s_add_u32 m0, s13, 24576\n"
  "s_mov_b32 s9, 393216\n"
  "buffer_load_dwordx4 v187, s[28:31], s9 offen offset:2048 lds\n"
  "v_mfma_f32_16x16x32_bf16 v[152:155], v[30:33], a[228:231], 0\n"
  "ds_read_b64 a[2:3], v193 offset:8192\n"
  "s_waitcnt lgkmcnt(7)\n"
  "v_mfma_f32_16x16x32_bf16 v[164:167], a[240:243], a[240:243], v[164:167]\n"
  "s_mov_b64 exec, s[16:17]\n"
  "ds_read_b64 a[4:5], v194 offset:0\n"
  "ds_read_b64 a[6:7], v195 offset:0\n"
  "s_mov_b64 exec, -1\n"
  "v_mfma_f32_16x16x32_bf16 v[156:159], v[30:33], a[232:235], 0\n"
  "ds_read_b64 a[8:9], v194 offset:4096\n"
  "s_waitcnt lgkmcnt(6)\n"
  "v_mfma_f32_16x16x32_bf16 v[168:171], a[244:247], a[244:247], v[168:171]\n"
  "ds_read_b64 a[10:11], v195 offset:4096\n"
  "s_waitcnt lgkmcnt(6)\n"
  "v_mfma_f32_16x16x32_bf16 v[164:167], a[252:255], a[252:255], v[164:167]\n"
  "ds_read_b64 a[12:13], v194 offset:8192\n"
  "s_add_u32 m0, s13, 27648\n"
  "s_mov_b32 s9, 442368\n"
  "buffer_load_dwordx4 v187, s[28:31], s9 offen lds\n"
  "v_mfma_f32_16x16x32_bf16 v[152:155], v[30:33], a[240:243], v[152:155]\n"
  "ds_read_b64 a[14:15], v195 offset:8192\n"
  "ds_read_b64_tr_b16 v[112:113], v222 offset:1024\n"
  "v_mfma_f32_16x16x32_bf16 v[156:159], v[30:33], a[244:247], v[156:159]\n"
  "ds_read_b64_tr_b16 v[114:115], v222 offset:4096\n"
  "s_waitcnt lgkmcnt(8)\n"
  "v_mfma_f32_16x16x32_bf16 v[168:171], a[0:3], a[0:3], v[168:171]\n"
  "ds_read_b64_tr_b16 v[116:117], v222 offset:2048\n"
  "ds_read_b64_tr_b16 v[118:119], v222 offset:5120\n"
  "v_mfma_f32_16x16x32_bf16 v[160:163], a[248:251], a[248:251], v[160:163]\n"
  "ds_read_b64_tr_b16 v[120:121], v222 offset:0\n"
  "ds_read_b64_tr_b16 v[122:123], v222 offset:3072\n"
  "v_mfma_f32_16x16x32_bf16 v[152:155], v[30:33], a[252:255], v[152:155]\n"
  "ds_read_b64_tr_b16 v[124:125], v223 offset:1024\n"
  "s_waitcnt lgkmcnt(11)\n"
  "v_mfma_f32_16x16x32_bf16 v[160:163], a[4:7], a[4:7], v[160:163]\n"
  "ds_read_b64_tr_b16 v[126:127], v223 offset:4096\n"
  "s_waitcnt lgkmcnt(10)\n"
  "v_mfma_f32_16x16x32_bf16 v[164:167], a[8:11], a[8:11], v[164:167]\n"
  "s_add_u32 m0, s13, 27648\n"
  "s_mov_b32 s9, 442368\n"
  "buffer_load_dwordx4 v187, s[28:31], s9 offen offset:1024 lds\n"
  "v_mfma_f32_16x16x32_bf16 v[156:159], v[30:33], a[0:3], v[156:159]\n"
  "ds_read_b64_tr_b16 v[128:129], v223 offset:2048\n"
  "s_waitcnt lgkmcnt(7)\n"
  "v_mfma_f32_16x16x32_bf16 v[48:51], v[112:115], a[224:227], 0\n"
  "ds_read_b64_tr_b16 v[130:131], v223 offset:5120\n"
  "v_lshlrev_b32 v206, 2, v2\n"
  "v_mfma_f32_16x16x32_bf16 v[56:59], v[112:115], a[228:231], 0\n"
  "ds_read_b64_tr_b16 v[132:133], v223 offset:0\n"
  "s_lshl2_add_u32 s87, s68, s58\n"
  "v_mfma_f32_16x16x32_bf16 v[64:67], v[112:115], a[232:235], 0\n"
  "ds_read_b64_tr_b16 v[134:135], v223 offset:3072\n"
  "s_bfe_u32 s89, s99, 0x20001\n"
  "s_waitcnt lgkmcnt(8)\n"
  "v_mfma_f32_16x16x32_bf16 v[72:75], a[224:227], v[116:119], 0\n"
  "ds_read_b64_tr_b16 v[112:113], v222 offset:7168\n"
  "s_and_b32 s90, s99, 1\n"
  "v_mfma_f32_16x16x32_bf16 v[80:83], a[228:231], v[116:119], 0\n"
  "s_add_u32 m0, s13, 27648\n"
  "s_mov_b32 s9, 442368\n"
  "buffer_load_dwordx4 v187, s[28:31], s9 offen offset:2048 lds\n"
  "v_mfma_f32_16x16x32_bf16 v[88:91], a[232:235], v[116:119], 0\n"
  "ds_read_b64_tr_b16 v[114:115], v222 offset:10240\n"
  "s_lshr_b32 s88, s87, 5\n"
  "s_waitcnt lgkmcnt(6)\n"
  "v_mfma_f32_16x16x32_bf16 v[96:99], v[120:123], a[228:231], 0\n"
  "ds_read_b64_tr_b16 v[116:117], v222 offset:8192\n"
  "s_mul_i32 s88, s88, 512\n"
  "v_mfma_f32_16x16x32_bf16 v[104:107], v[120:123], a[232:235], 0\n"
  "ds_read_b64_tr_b16 v[118:119], v222 offset:11264\n"
  "s_and_b32 s87, s87, 31\n"
  "v_mfma_f32_16x16x32_bf16 v[4:7], v[120:123], a[224:227], 0\n"
  "s_waitcnt vmcnt(6) lgkmcnt(6)\n"
  "s_barrier\n"
  "v_mfma_f32_16x16x32_bf16 v[52:55], v[124:127], a[224:227], 0\n"
  "s_mov_b64 exec, s[16:17]\n"
  "ds_read_b64 a[16:17], v136 offset:0\n"
  "ds_read_b64 a[18:19], v137 offset:0\n"
  "s_mov_b64 exec, -1\n"
  "v_mfma_f32_16x16x32_bf16 v[60:63], v[124:127], a[228:231], 0\n"
  "s_add_u32 m0, s12, 126976\n"
  "s_mov_b32 s9, 512\n"
  "buffer_load_dwordx4 v145, s[24:27], s9 offen lds\n"
  "v_mfma_f32_16x16x32_bf16 v[68:71], v[124:127], a[232:235], 0\n"
  "ds_read_b64 a[20:21], v136 offset:4096\n"
  "ds_read_b64 a[22:23], v137 offset:4096\n"
  "v_mfma_f32_16x16x32_bf16 v[76:79], a[224:227], v[128:131], 0\n"
  "ds_read_b64 a[24:25], v136 offset:8192\n"
  "ds_read_b64 a[26:27], v137 offset:8192\n"
  "v_mfma_f32_16x16x32_bf16 v[84:87], a[228:231], v[128:131], 0\n"
  "s_add_u32 m0, s12, 131072\n"
  "s_mov_b32 s9, 512\n"
  "buffer_load_dwordx4 v144, s[24:27], s9 offen lds\n"
  "v_mfma_f32_16x16x32_bf16 v[92:95], a[232:235], v[128:131], 0\n"
  "s_mov_b64 exec, s[16:17]\n"
  "ds_read_b64 a[28:29], v138 offset:0\n"
  "ds_read_b64 a[30:31], v139 offset:0\n"
  "s_mov_b64 exec, -1\n"
  "s_waitcnt lgkmcnt(8)\n"
  "v_mfma_f32_16x16x32_bf16 v[100:103], v[132:135], a[228:231], 0\n"
  "ds_read_b64 a[32:33], v138 offset:4096\n"
  "s_waitcnt lgkmcnt(7)\n"
  "v_mfma_f32_16x16x32_bf16 v[160:163], a[16:19], a[16:19], v[160:163]\n"
  "s_add_u32 m0, s12, 135168\n"
  "s_mov_b32 s9, 16896\n"
  "buffer_load_dwordx4 v144, s[24:27], s9 offen lds\n"
  "v_mfma_f32_16x16x32_bf16 v[108:111], v[132:135], a[232:235], 0\n"
  "ds_read_b64 a[34:35], v139 offset:4096\n"
  "s_waitcnt lgkmcnt(6)\n"
  "v_mfma_f32_16x16x32_bf16 v[164:167], a[20:23], a[20:23], v[164:167]\n"
  "ds_read_b64 a[36:37], v138 offset:8192\n"
  "ds_read_b64 a[38:39], v139 offset:8192\n"
  "v_mfma_f32_16x16x32_bf16 v[8:11], v[132:135], a[224:227], 0\n"
  "ds_read_b64 a[44:45], v140 offset:4096\n"
  "s_add_u32 m0, s12, 139264\n"
  "s_mov_b32 s9, 33280\n"
  "buffer_load_dwordx4 v144, s[24:27], s9 offen lds\n"
  "v_mfma_f32_16x16x32_bf16 v[48:51], v[112:115], a[236:239], v[48:51]\n"
  "s_mov_b64 exec, s[16:17]\n"
  "ds_read_b64 a[40:41], v140 offset:0\n"
  "ds_read_b64 a[42:43], v141 offset:0\n"
  "s_mov_b64 exec, -1\n"
  "v_mfma_f32_16x16x32_bf16 v[56:59], v[112:115], a[240:243], v[56:59]\n"
  "ds_read_b64 a[46:47], v141 offset:4096\n"
  "s_waitcnt lgkmcnt(8)\n"
  "v_mfma_f32_16x16x32_bf16 v[160:163], a[28:31], a[28:31], v[160:163]\n"
  "s_add_u32 m0, s12, 143360\n"
  "s_mov_b32 s9, 49664\n"
  "buffer_load_dwordx4 v144, s[24:27], s9 offen lds\n"
  "v_mfma_f32_16x16x32_bf16 v[64:67], v[112:115], a[244:247], v[64:67]\n"
  "ds_read_b64 a[48:49], v140 offset:8192\n"
  "s_waitcnt lgkmcnt(7)\n"
  "v_mfma_f32_16x16x32_bf16 v[164:167], a[32:35], a[32:35], v[164:167]\n"
  "ds_read_b64 a[50:51], v141 offset:8192\n"
  "s_mov_b64 exec, s[16:17]\n"
  "ds_read_b64 a[52:53], v142 offset:0\n"
  "ds_read_b64 a[54:55], v143 offset:0\n"
  "s_mov_b64 exec, -1\n"
  "v_mfma_f32_16x16x32_bf16 v[72:75], a[236:239], v[116:119], v[72:75]\n"
  "s_add_u32 m0, s12, 147456\n"
  "s_mov_b32 s9, 66048\n"
  "buffer_load_dwordx4 v144, s[24:27], s9 offen lds\n"
  "v_mfma_f32_16x16x32_bf16 v[80:83], a[240:243], v[116:119], v[80:83]\n"
  "ds_read_b64 a[56:57], v142 offset:4096\n"
  "s_waitcnt lgkmcnt(6)\n"
  "v_mfma_f32_16x16x32_bf16 v[160:163], a[40:43], a[40:43], v[160:163]\n"
  "ds_read_b64 a[58:59], v143 offset:4096\n"
  "s_waitcnt lgkmcnt(6)\n"
  "v_mfma_f32_16x16x32_bf16 v[164:167], a[44:47], a[44:47], v[164:167]\n"
  "ds_read_b64 a[60:61], v142 offset:8192\n"
  "s_add_u32 m0, s12, 151552\n"
  "s_mov_b32 s9, 82432\n"
  "buffer_load_dwordx4 v144, s[24:27], s9 offen lds\n"
  "v_mfma_f32_16x16x32_bf16 v[88:91], a[244:247], v[116:119], v[88:91]\n"
  "ds_read_b64 a[62:63], v143 offset:8192\n"
  "s_waitcnt lgkmcnt(4)\n"
  "v_mfma_f32_16x16x32_bf16 v[160:163], a[52:55], a[52:55], v[160:163]\n"
  "ds_read_b64_tr_b16 v[120:121], v222 offset:6144\n"
  "ds_read_b64_tr_b16 v[122:123], v222 offset:9216\n"
  "v_mfma_f32_16x16x32_bf16 v[168:171], a[12:15], a[12:15], v[168:171]\n"
  "ds_read_b64_tr_b16 v[124:125], v223 offset:7168\n"
  "ds_read_b64_tr_b16 v[126:127], v223 offset:10240\n"
  "v_mfma_f32_16x16x32_bf16 v[152:155], v[30:33], a[8:11], v[152:155]\n"
  "s_add_u32 m0, s12, 155648\n"
  "s_mov_b32 s9, 98816\n"
  "buffer_load_dwordx4 v144, s[24:27], s9 offen lds\n"
  "v_mfma_f32_16x16x32_bf16 v[156:159], v[30:33], a[12:15], v[156:159]\n"
  "ds_read_b64_tr_b16 v[128:129], v223 offset:8192\n"
  "s_waitcnt lgkmcnt(7)\n"
  "v_mfma_f32_16x16x32_bf16 v[164:167], a[56:59], a[56:59], v[164:167]\n"
  "ds_read_b64_tr_b16 v[130:131], v223 offset:11264\n"
  "ds_read_b64_tr_b16 v[132:133], v223 offset:6144\n"
  "v_mfma_f32_16x16x32_bf16 v[168:171], a[24:27], a[24:27], v[168:171]\n"
  "s_add_u32 m0, s12, 159744\n"
  "s_mov_b32 s9, 115200\n"
  "buffer_load_dwordx4 v144, s[24:27], s9 offen lds\n"
  "v_mfma_f32_16x16x32_bf16 v[152:155], v[30:33], a[20:23], v[152:155]\n"
  "ds_read_b64_tr_b16 v[134:135], v223 offset:9216\n"
  "s_waitcnt lgkmcnt(6)\n"
  "v_mfma_f32_16x16x32_bf16 v[96:99], v[120:123], a[240:243], v[96:99]\n"
  "ds_read_b64_tr_b16 v[112:113], v222 offset:13312\n"
  "s_add_u32 s87, s87, s88\n"
  "v_mfma_f32_16x16x32_bf16 v[104:107], v[120:123], a[244:247], v[104:107]\n"
  "ds_read_b64_tr_b16 v[114:115], v222 offset:16384\n"
  "s_mul_i32 s87, s87, 3584\n"
  "v_mfma_f32_16x16x32_bf16 v[4:7], v[120:123], a[236:239], v[4:7]\n"
  "s_add_u32 m0, s13, 49152\n"
  "s_mov_b32 s9, 786432\n"
  "buffer_load_dwordx4 v187, s[28:31], s9 offen lds\n"
  "s_waitcnt lgkmcnt(6)\n"
  "v_mfma_f32_16x16x32_bf16 v[52:55], v[124:127], a[236:239], v[52:55]\n"
  "ds_read_b64_tr_b16 v[116:117], v222 offset:14336\n"
  "s_mul_i32 s89, s89, 896\n"
  "v_mfma_f32_16x16x32_bf16 v[60:63], v[124:127], a[240:243], v[60:63]\n"
  "ds_read_b64_tr_b16 v[118:119], v222 offset:17408\n"
  "s_add_u32 s87, s87, s89\n"
  "v_mfma_f32_16x16x32_bf16 v[68:71], v[124:127], a[244:247], v[68:71]\n"
  "ds_read_b64_tr_b16 v[120:121], v222 offset:12288\n"
  "v_lshrrev_b32 v45, 3, v0\n"
  "s_waitcnt lgkmcnt(6)\n"
  "v_mfma_f32_16x16x32_bf16 v[76:79], a[236:239], v[128:131], v[76:79]\n"
  "ds_read_b64_tr_b16 v[122:123], v222 offset:15360\n"
  "v_add_u32 v45, 0, v45\n"
  "v_mfma_f32_16x16x32_bf16 v[84:87], a[240:243], v[128:131], v[84:87]\n"
  "ds_read_b64_tr_b16 v[124:125], v223 offset:13312\n"
  "v_mul_u32_u24 v45, 114688, v45\n"
  "v_mfma_f32_16x16x32_bf16 v[92:95], a[244:247], v[128:131], v[92:95]\n"
  "s_add_u32 m0, s13, 49152\n"
  "s_mov_b32 s9, 786432\n"
  "buffer_load_dwordx4 v187, s[28:31], s9 offen offset:1024 lds\n"
  "s_waitcnt vmcnt(16) lgkmcnt(5)\n"
  "v_mfma_f32_16x16x32_bf16 v[100:103], v[132:135], a[240:243], v[100:103]\n"
  "ds_read_b64_tr_b16 v[126:127], v223 offset:16384\n"
  "v_and_b32 v46, 7, v0\n"
  "v_mfma_f32_16x16x32_bf16 v[108:111], v[132:135], a[244:247], v[108:111]\n"
  "ds_read_b64_tr_b16 v[128:129], v223 offset:14336\n"
  "v_min_u32 v46, 6, v46\n"
  "v_mfma_f32_16x16x32_bf16 v[8:11], v[132:135], a[236:239], v[8:11]\n"
  "ds_read_b64_tr_b16 v[130:131], v223 offset:17408\n"
  "v_lshl_add_u32 v45, v46, 7, v45\n"
  "v_mfma_f32_16x16x32_bf16 v[48:51], v[112:115], a[248:251], v[48:51]\n"
  "ds_read_b64_tr_b16 v[132:133], v223 offset:12288\n"
  "v_add_u32 v45, s87, v45\n"
  "v_mfma_f32_16x16x32_bf16 v[56:59], v[112:115], a[252:255], v[56:59]\n"
  "ds_read_b64_tr_b16 v[134:135], v223 offset:15360\n"
  "v_mov_b32 v201, v45\n"
  "v_mfma_f32_16x16x32_bf16 v[64:67], v[112:115], a[0:3], v[64:67]\n"
  "s_add_u32 m0, s13, 49152\n"
  "s_mov_b32 s9, 786432\n"
  "buffer_load_dwordx4 v187, s[28:31], s9 offen offset:2048 lds\n"
  "s_waitcnt vmcnt(16) lgkmcnt(6)\n"
  "v_mfma_f32_16x16x32_bf16 v[72:75], a[248:251], v[116:119], v[72:75]\n"
  "ds_read_b64_tr_b16 v[112:113], v222 offset:19456\n"
  "s_lshl2_add_u32 s87, s68, s58\n"
  "v_mfma_f32_16x16x32_bf16 v[80:83], a[252:255], v[116:119], v[80:83]\n"
  "ds_read_b64_tr_b16 v[114:115], v222 offset:22528\n"
  "s_bfe_u32 s89, s99, 0x20001\n"
  "v_mfma_f32_16x16x32_bf16 v[88:91], a[0:3], v[116:119], v[88:91]\n"
  "ds_read_b64_tr_b16 v[116:117], v222 offset:20480\n"
  "s_and_b32 s90, s99, 1\n"
  "v_mfma_f32_16x16x32_bf16 v[96:99], v[120:123], a[252:255], v[96:99]\n"
  "ds_read_b64_tr_b16 v[118:119], v222 offset:23552\n"
  "s_lshr_b32 s88, s87, 5\n"
  "v_mfma_f32_16x16x32_bf16 v[104:107], v[120:123], a[0:3], v[104:107]\n"
  "s_mul_i32 s88, s88, 512\n"
  "s_and_b32 s87, s87, 31\n"
  "v_mfma_f32_16x16x32_bf16 v[4:7], v[120:123], a[248:251], v[4:7]\n"
  "ds_read_b64_tr_b16 v[120:121], v222 offset:18432\n"
  "s_add_u32 m0, s13, 52224\n"
  "s_mov_b32 s9, 835584\n"
  "buffer_load_dwordx4 v187, s[28:31], s9 offen lds\n"
  "s_waitcnt vmcnt(16) lgkmcnt(5)\n"
  "v_mfma_f32_16x16x32_bf16 v[52:55], v[124:127], a[248:251], v[52:55]\n"
  "ds_read_b64_tr_b16 v[122:123], v222 offset:21504\n"
  "s_add_u32 s87, s87, s88\n"
  "v_mfma_f32_16x16x32_bf16 v[60:63], v[124:127], a[252:255], v[60:63]\n"
  "s_mul_i32 s87, s87, 3584\n"
  "s_mul_i32 s89, s89, 896\n"
  "v_mfma_f32_16x16x32_bf16 v[68:71], v[124:127], a[0:3], v[68:71]\n"
  "ds_read_b64_tr_b16 v[124:125], v223 offset:19456\n"
  "s_add_u32 s87, s87, s89\n"
  "v_mfma_f32_16x16x32_bf16 v[76:79], a[248:251], v[128:131], v[76:79]\n"
  "ds_read_b64_tr_b16 v[126:127], v223 offset:22528\n"
  "v_lshrrev_b32 v45, 3, v0\n"
  "v_mfma_f32_16x16x32_bf16 v[84:87], a[252:255], v[128:131], v[84:87]\n"
  "v_add_u32 v45, 8, v45\n"
  "v_mul_u32_u24 v45, 114688, v45\n"
  "v_mfma_f32_16x16x32_bf16 v[92:95], a[0:3], v[128:131], v[92:95]\n"
  "ds_read_b64_tr_b16 v[128:129], v223 offset:20480\n"
  "s_add_u32 m0, s13, 52224\n"
  "s_mov_b32 s9, 835584\n"
  "buffer_load_dwordx4 v187, s[28:31], s9 offen offset:1024 lds\n"
  "v_mfma_f32_16x16x32_bf16 v[100:103], v[132:135], a[252:255], v[100:103]\n"
  "ds_read_b64_tr_b16 v[130:131], v223 offset:23552\n"
  "v_and_b32 v46, 7, v0\n"
  "v_mfma_f32_16x16x32_bf16 v[108:111], v[132:135], a[0:3], v[108:111]\n"
  "v_min_u32 v46, 6, v46\n"
  "v_lshl_add_u32 v45, v46, 7, v45\n"
  "v_mfma_f32_16x16x32_bf16 v[8:11], v[132:135], a[248:251], v[8:11]\n"
  "ds_read_b64_tr_b16 v[132:133], v223 offset:18432\n"
  "v_add_u32 v45, s87, v45\n"
  "s_waitcnt vmcnt(16) lgkmcnt(5)\n"
  "v_mfma_f32_16x16x32_bf16 v[48:51], v[112:115], a[4:7], v[48:51]\n"
  "ds_read_b64_tr_b16 v[134:135], v223 offset:21504\n"
  "v_mov_b32 v202, v45\n"
  "v_mfma_f32_16x16x32_bf16 v[56:59], v[112:115], a[8:11], v[56:59]\n"
  "s_lshl2_add_u32 s87, s68, s58\n"
  "s_and_b32 s90, s99, 1\n"
  "v_mfma_f32_16x16x32_bf16 v[64:67], v[112:115], a[12:15], v[64:67]\n"
  "s_mul_i32 s87, s87, 4096\n"
  "s_lshl_b32 s90, s90, 9\n"
  "v_mfma_f32_16x16x32_bf16 v[72:75], a[4:7], v[116:119], v[72:75]\n"
  "s_add_u32 m0, s13, 52224\n"
  "s_mov_b32 s9, 835584\n"
  "buffer_load_dwordx4 v187, s[28:31], s9 offen offset:2048 lds\n"
  "v_mfma_f32_16x16x32_bf16 v[80:83], a[8:11], v[116:119], v[80:83]\n"
  "s_add_u32 s87, s87, s90\n"
  "v_mul_u32_u24 v45, 1024, v3\n"
  "v_mfma_f32_16x16x32_bf16 v[88:91], a[12:15], v[116:119], v[88:91]\n"
  "v_and_b32 v46, 3, v0\n"
  "v_lshl_add_u32 v45, v46, 7, v45\n"
  "v_mfma_f32_16x16x32_bf16 v[96:99], v[120:123], a[8:11], v[96:99]\n"
  "v_add_u32 v45, s87, v45\n"
  "v_mov_b32 v200, v45\n"
  "v_mfma_f32_16x16x32_bf16 v[104:107], v[120:123], a[12:15], v[104:107]\n"
  "s_lshl2_add_u32 s87, s99, s58\n"
  "s_mul_i32 s88, s87, 293\n"
  "v_mfma_f32_16x16x32_bf16 v[4:7], v[120:123], a[4:7], v[4:7]\n"
  "s_lshr_b32 s88, s88, 16\n"
  "s_mul_i32 s89, s88, 224\n"
  "s_waitcnt vmcnt(15) lgkmcnt(0)\n"
  "v_mfma_f32_16x16x32_bf16 v[52:55], v[124:127], a[4:7], v[52:55]\n"
  "s_sub_u32 s87, s87, s89\n"
  "s_add_u32 s87, s87, 416\n"
  "v_mfma_f32_16x16x32_bf16 v[60:63], v[124:127], a[8:11], v[60:63]\n"
  "s_sub_u32 s90, s87, 448\n"
  "s_min_u32 s87, s87, s90\n"
  "v_mfma_f32_16x16x32_bf16 v[68:71], v[124:127], a[12:15], v[68:71]\n"
  "s_lshl1_add_u32 s87, s89, s87\n"
  "buffer_store_dwordx2 v[28:29], v26, s[20:23], 0 offen sc1\n"
  "v_mfma_f32_16x16x32_bf16 v[76:79], a[4:7], v[128:131], v[76:79]\n"
  "s_lshl_b32 s87, s87, 12\n"
  "v_min_u32 v45, 31, v0\n"
  "v_mfma_f32_16x16x32_bf16 v[84:87], a[8:11], v[128:131], v[84:87]\n"
  "buffer_load_ushort v47, v27, s[52:55], 0 offen\n"
  "v_lshl_add_u32 v45, v45, 7, s87\n"
  "v_mfma_f32_16x16x32_bf16 v[92:95], a[12:15], v[128:131], v[92:95]\n"
  "v_mov_b32 v203, v45\n"
  "s_lshl2_add_u32 s87, s61, s58\n"
  "v_mfma_f32_16x16x32_bf16 v[100:103], v[132:135], a[8:11], v[100:103]\n"
  "s_lshl_b32 s88, s87, 10\n"
  "v_add_u32 v196, s88, v1\n"
  "v_mfma_f32_16x16x32_bf16 v[108:111], v[132:135], a[12:15], v[108:111]\n"
  "s_mul_i32 s89, s62, 98304\n"
  "s_add_u32 s89, s89, 14680064\n"
  "v_mfma_f32_16x16x32_bf16 v[8:11], v[132:135], a[4:7], v[8:11]\n"
  "v_add_u32 v238, s89, v196\n"
  "v_mfma_f32_16x16x32_bf16 v[156:159], v[30:33], a[24:27], v[156:159]\n"
  "s_waitcnt vmcnt(8)\n"
  "s_barrier\n"
  "v_mfma_f32_16x16x32_bf16 v[168:171], a[36:39], a[36:39], v[168:171]\n"
  "s_add_u32 m0, s13, 73728\n"
  "s_mov_b32 s9, 1179648\n"
  "buffer_load_dwordx4 v187, s[28:31], s9 offen lds\n"
  "v_mfma_f32_16x16x32_bf16 v[152:155], v[30:33], a[32:35], v[152:155]\n"
  "s_mov_b64 exec, s[16:17]\n"
  "ds_read_b64 a[64:65], v136 offset:36864\n"
  "ds_read_b64 a[66:67], v137 offset:36864\n"
  "s_mov_b64 exec, -1\n"
  "v_mfma_f32_16x16x32_bf16 v[156:159], v[30:33], a[36:39], v[156:159]\n"
  "ds_read_b64 a[68:69], v136 offset:40960\n"
  "ds_read_b64 a[70:71], v137 offset:40960\n"
  "v_mfma_f32_16x16x32_bf16 v[168:171], a[48:51], a[48:51], v[168:171]\n"
  "ds_read_b64 a[72:73], v136 offset:45056\n"
  "ds_read_b64 a[74:75], v137 offset:45056\n"
  "v_mfma_f32_16x16x32_bf16 v[152:155], v[30:33], a[44:47], v[152:155]\n"
  "s_mov_b64 exec, s[16:17]\n"
  "ds_read_b64 a[76:77], v138 offset:36864\n"
  "ds_read_b64 a[78:79], v139 offset:36864\n"
  "s_mov_b64 exec, -1\n"
  "v_mfma_f32_16x16x32_bf16 v[156:159], v[30:33], a[48:51], v[156:159]\n"
  "ds_read_b64 a[80:81], v138 offset:40960\n"
  "ds_read_b64 a[82:83], v139 offset:40960\n"
  "v_mfma_f32_16x16x32_bf16 v[168:171], a[60:63], a[60:63], v[168:171]\n"
  "s_add_u32 m0, s13, 73728\n"
  "s_mov_b32 s9, 1179648\n"
  "buffer_load_dwordx4 v187, s[28:31], s9 offen offset:1024 lds\n"
  "v_mfma_f32_16x16x32_bf16 v[152:155], v[30:33], a[56:59], v[152:155]\n"
  "ds_read_b64 a[84:85], v138 offset:45056\n"
  "s_waitcnt lgkmcnt(9)\n"
  "v_mfma_f32_16x16x32_bf16 v[160:163], a[64:67], a[64:67], v[160:163]\n"
  "ds_read_b64 a[86:87], v139 offset:45056\n"
  "s_waitcnt lgkmcnt(8)\n"
  "v_mfma_f32_16x16x32_bf16 v[164:167], a[68:71], a[68:71], v[164:167]\n"
  "s_mov_b64 exec, s[16:17]\n"
  "ds_read_b64 a[88:89], v140 offset:36864\n"
  "ds_read_b64 a[90:91], v141 offset:36864\n"
  "s_mov_b64 exec, -1\n"
  "v_mfma_f32_16x16x32_bf16 v[156:159], v[30:33], a[60:63], v[156:159]\n"
  "ds_read_b64 a[92:93], v140 offset:40960\n"
  "s_waitcnt lgkmcnt(9)\n"
  "v_mfma_f32_16x16x32_bf16 v[168:171], a[72:75], a[72:75], v[168:171]\n"
  "ds_read_b64 a[94:95], v141 offset:40960\n"
  "s_waitcnt lgkmcnt(8)\n"
  "v_mfma_f32_16x16x32_bf16 v[160:163], a[76:79], a[76:79], v[160:163]\n"
  "s_add_u32 m0, s13, 73728\n"
  "s_mov_b32 s9, 1179648\n"
  "buffer_load_dwordx4 v187, s[28:31], s9 offen offset:2048 lds\n"
  "v_mfma_f32_16x16x32_bf16 v[152:155], v[30:33], a[68:71], v[152:155]\n"
  "ds_read_b64 a[96:97], v140 offset:45056\n"
  "s_waitcnt lgkmcnt(7)\n"
  "v_mfma_f32_16x16x32_bf16 v[164:167], a[80:83], a[80:83], v[164:167]\n"
  "ds_read_b64 a[98:99], v141 offset:45056\n"
  "s_waitcnt lgkmcnt(6)\n"
  "v_mfma_f32_16x16x32_bf16 v[168:171], a[84:87], a[84:87], v[168:171]\n"
  "s_mov_b64 exec, s[16:17]\n"
  "ds_read_b64 a[100:101], v142 offset:36864\n"
  "ds_read_b64 a[102:103], v143 offset:36864\n"
  "s_mov_b64 exec, -1\n"
  "v_mfma_f32_16x16x32_bf16 v[156:159], v[30:33], a[72:75], v[156:159]\n"
  "ds_read_b64 a[104:105], v142 offset:40960\n"
  "s_waitcnt lgkmcnt(7)\n"
  "v_mfma_f32_16x16x32_bf16 v[160:163], a[88:91], a[88:91], v[160:163]\n"
  "ds_read_b64 a[106:107], v143 offset:40960\n"
  "s_waitcnt lgkmcnt(6)\n"
  "v_mfma_f32_16x16x32_bf16 v[164:167], a[92:95], a[92:95], v[164:167]\n"
  "s_add_u32 m0, s13, 76800\n"
  "s_mov_b32 s9, 1228800\n"
  "buffer_load_dwordx4 v187, s[28:31], s9 offen lds\n"
  "v_mfma_f32_16x16x32_bf16 v[152:155], v[30:33], a[80:83], v[152:155]\n"
  "ds_read_b64 a[108:109], v142 offset:45056\n"
  "ds_read_b64 a[110:111], v143 offset:45056\n"
  "v_mfma_f32_16x16x32_bf16 v[156:159], v[30:33], a[84:87], v[156:159]\n"
  "s_add_u32 m0, s13, 76800\n"
  "s_mov_b32 s9, 1228800\n"
  "buffer_load_dwordx4 v187, s[28:31], s9 offen offset:1024 lds\n"
  "s_waitcnt lgkmcnt(6)\n"
  "v_mfma_f32_16x16x32_bf16 v[168:171], a[96:99], a[96:99], v[168:171]\n"
  "ds_read_b64_tr_b16 v[112:113], v222 offset:25600\n"
  "s_waitcnt lgkmcnt(5)\n"
  "v_mfma_f32_16x16x32_bf16 v[160:163], a[100:103], a[100:103], v[160:163]\n"
  "ds_read_b64_tr_b16 v[114:115], v222 offset:28672\n"
  "ds_read_b64_tr_b16 v[116:117], v222 offset:26624\n"
  "v_mfma_f32_16x16x32_bf16 v[152:155], v[30:33], a[92:95], v[152:155]\n"
  "ds_read_b64_tr_b16 v[118:119], v222 offset:29696\n"
  "s_waitcnt lgkmcnt(6)\n"
  "v_mfma_f32_16x16x32_bf16 v[164:167], a[104:107], a[104:107], v[164:167]\n"
  "ds_read_b64_tr_b16 v[120:121], v222 offset:24576\n"
  "ds_read_b64_tr_b16 v[122:123], v222 offset:27648\n"
  "v_mfma_f32_16x16x32_bf16 v[156:159], v[30:33], a[96:99], v[156:159]\n"
  "ds_read_b64_tr_b16 v[124:125], v223 offset:25600\n"
  "s_waitcnt lgkmcnt(7)\n"
  "v_mfma_f32_16x16x32_bf16 v[168:171], a[108:111], a[108:111], v[168:171]\n"
  "s_add_u32 m0, s13, 76800\n"
  "s_mov_b32 s9, 1228800\n"
  "buffer_load_dwordx4 v187, s[28:31], s9 offen offset:2048 lds\n"
  "v_mfma_f32_16x16x32_bf16 v[152:155], v[30:33], a[104:107], v[152:155]\n"
  "ds_read_b64_tr_b16 v[126:127], v223 offset:28672\n"
  "ds_read_b64_tr_b16 v[128:129], v223 offset:26624\n"
  "v_mfma_f32_16x16x32_bf16 v[156:159], v[30:33], a[108:111], v[156:159]\n"
  "ds_read_b64_tr_b16 v[130:131], v223 offset:29696\n"
  "s_waitcnt vmcnt(13) lgkmcnt(8)\n"
  "v_mfma_f32_16x16x32_bf16 v[48:51], v[112:115], a[16:19], v[48:51]\n"
  "ds_read_b64_tr_b16 v[132:133], v223 offset:24576\n"
  "v_mfma_f32_16x16x32_bf16 v[56:59], v[112:115], a[20:23], v[56:59]\n"
  "ds_read_b64_tr_b16 v[134:135], v223 offset:27648\n"
  "v_mfma_f32_16x16x32_bf16 v[64:67], v[112:115], a[24:27], v[64:67]\n"
  "ds_read_b64_tr_b16 v[112:113], v222 offset:31744\n"
  "s_waitcnt lgkmcnt(9)\n"
  "v_mfma_f32_16x16x32_bf16 v[72:75], a[16:19], v[116:119], v[72:75]\n"
  "ds_read_b64_tr_b16 v[114:115], v222 offset:34816\n"
  "v_mfma_f32_16x16x32_bf16 v[80:83], a[20:23], v[116:119], v[80:83]\n"
  "s_cmp_lt_u32 s87, 32\n"
  "s_cselect_b32 s88, 65536, 0\n"
  "s_cselect_b32 s89, 65536, 1073741824\n"
  "v_mfma_f32_16x16x32_bf16 v[88:91], a[24:27], v[116:119], v[88:91]\n"
  "ds_read_b64_tr_b16 v[116:117], v222 offset:32768\n"
  "v_add_u32 v198, s88, v196\n"
  "s_waitcnt vmcnt(12) lgkmcnt(8)\n"
  "v_mfma_f32_16x16x32_bf16 v[96:99], v[120:123], a[20:23], v[96:99]\n"
  "s_waitcnt vmcnt(8) lgkmcnt(7)\n"
  "s_barrier\n"
  "v_mfma_f32_16x16x32_bf16 v[104:107], v[120:123], a[24:27], v[104:107]\n"
  "s_add_u32 m0, s12, 126976\n"
  "s_mov_b32 s9, 768\n"
  "buffer_load_dwordx4 v145, s[24:27], s9 offen lds\n"
  "v_mfma_f32_16x16x32_bf16 v[4:7], v[120:123], a[16:19], v[4:7]\n"
  "s_add_u32 m0, s12, 131072\n"
  "s_mov_b32 s9, 768\n"
  "buffer_load_dwordx4 v144, s[24:27], s9 offen lds\n"
  "v_mfma_f32_16x16x32_bf16 v[52:55], v[124:127], a[16:19], v[52:55]\n"
  "ds_read_b64_tr_b16 v[118:119], v222 offset:35840\n"
  "v_add_u32 v239, s89, v238\n"
  "v_mfma_f32_16x16x32_bf16 v[60:63], v[124:127], a[20:23], v[60:63]\n"
  "ds_read_b64_tr_b16 v[120:121], v222 offset:30720\n"
  "s_add_u32 m0, s12, 135168\n"
  "s_mov_b32 s9, 17152\n"
  "buffer_load_dwordx4 v144, s[24:27], s9 offen lds\n"
  "v_mfma_f32_16x16x32_bf16 v[68:71], v[124:127], a[24:27], v[68:71]\n"
  "ds_read_b64_tr_b16 v[122:123], v222 offset:33792\n"
  "s_lshl_b32 s87, s58, 17\n"
  "s_waitcnt lgkmcnt(4)\n"
  "v_mfma_f32_16x16x32_bf16 v[76:79], a[16:19], v[128:131], v[76:79]\n"
  "ds_read_b64_tr_b16 v[124:125], v223 offset:31744\n"
  "s_lshl_b32 s88, s64, 7\n"
  "v_mfma_f32_16x16x32_bf16 v[84:87], a[20:23], v[128:131], v[84:87]\n"
  "ds_read_b64_tr_b16 v[126:127], v223 offset:34816\n"
  "s_add_u32 m0, s12, 139264\n"
  "s_mov_b32 s9, 33536\n"
  "buffer_load_dwordx4 v144, s[24:27], s9 offen lds\n"
  "v_mfma_f32_16x16x32_bf16 v[92:95], a[24:27], v[128:131], v[92:95]\n"
  "ds_read_b64_tr_b16 v[128:129], v223 offset:32768\n"
  "s_add_u32 s87, s87, s88\n"
  "v_mfma_f32_16x16x32_bf16 v[100:103], v[132:135], a[20:23], v[100:103]\n"
  "ds_read_b64_tr_b16 v[130:131], v223 offset:35840\n"
  "v_lshrrev_b32 v184, 3, v0\n"
  "v_mfma_f32_16x16x32_bf16 v[108:111], v[132:135], a[24:27], v[108:111]\n"
  "v_lshl_add_u32 v184, v184, 10, s87\n"
  "v_lshlrev_b32 v45, 1, v3\n"
  "v_mfma_f32_16x16x32_bf16 v[8:11], v[132:135], a[16:19], v[8:11]\n"
  "s_add_u32 m0, s12, 143360\n"
  "s_mov_b32 s9, 49920\n"
  "buffer_load_dwordx4 v144, s[24:27], s9 offen lds\n"
  "v_mfma_f32_16x16x32_bf16 v[48:51], v[112:115], a[28:31], v[48:51]\n"
  "ds_read_b64_tr_b16 v[132:133], v223 offset:30720\n"
  "v_xor_b32 v45, v45, v0\n"
  "v_mfma_f32_16x16x32_bf16 v[56:59], v[112:115], a[32:35], v[56:59]\n"
  "ds_read_b64_tr_b16 v[134:135], v223 offset:33792\n"
  "v_and_b32 v45, 7, v45\n"
  "v_mfma_f32_16x16x32_bf16 v[64:67], v[112:115], a[36:39], v[64:67]\n"
  "ds_read_b64_tr_b16 v[112:113], v222 offset:37888\n"
  "s_add_u32 m0, s12, 147456\n"
  "s_mov_b32 s9, 66304\n"
  "buffer_load_dwordx4 v144, s[24:27], s9 offen lds\n"
  "s_waitcnt lgkmcnt(5)\n"
  "v_mfma_f32_16x16x32_bf16 v[72:75], a[28:31], v[116:119], v[72:75]\n"
  "ds_read_b64_tr_b16 v[114:115], v222 offset:40960\n"
  "v_lshl_add_u32 v184, v45, 4, v184\n"
  "v_mfma_f32_16x16x32_bf16 v[80:83], a[32:35], v[116:119], v[80:83]\n"
  "v_add_u32 v185, 0x10000, v184\n"
  "s_lshl_b32 s88, s61, 9\n"
  "v_mfma_f32_16x16x32_bf16 v[88:91], a[36:39], v[116:119], v[88:91]\n"
  "ds_read_b64_tr_b16 v[116:117], v222 offset:38912\n"
  "s_add_u32 m0, s12, 151552\n"
  "s_mov_b32 s9, 82688\n"
  "buffer_load_dwordx4 v144, s[24:27], s9 offen lds\n"
  "v_mfma_f32_16x16x32_bf16 v[96:99], v[120:123], a[32:35], v[96:99]\n"
  "ds_read_b64_tr_b16 v[118:119], v222 offset:41984\n"
  "s_add_u32 s88, s88, 16384\n"
  "v_mfma_f32_16x16x32_bf16 v[104:107], v[120:123], a[36:39], v[104:107]\n"
  "v_lshl_add_u32 v249, v2, 3, s88\n"
  "s_lshl_b32 s88, s67, 14\n"
  "v_mfma_f32_16x16x32_bf16 v[4:7], v[120:123], a[28:31], v[4:7]\n"
  "ds_read_b64_tr_b16 v[120:121], v222 offset:36864\n"
  "s_add_u32 s88, s88, 2097152\n"
  "v_mfma_f32_16x16x32_bf16 v[52:55], v[124:127], a[28:31], v[52:55]\n"
  "s_add_u32 m0, s12, 155648\n"
  "s_mov_b32 s9, 99072\n"
  "buffer_load_dwordx4 v144, s[24:27], s9 offen lds\n"
  "v_mfma_f32_16x16x32_bf16 v[60:63], v[124:127], a[32:35], v[60:63]\n"
  "ds_read_b64_tr_b16 v[122:123], v222 offset:39936\n"
  "v_add_u32 v250, s88, v1\n"
  "v_mfma_f32_16x16x32_bf16 v[68:71], v[124:127], a[36:39], v[68:71]\n"
  "ds_read_b64_tr_b16 v[124:125], v223 offset:37888\n"
  "s_lshl_b32 s87, s67, 14\n"
  "s_waitcnt lgkmcnt(5)\n"
  "v_mfma_f32_16x16x32_bf16 v[76:79], a[28:31], v[128:131], v[76:79]\n"
  "s_add_u32 m0, s12, 159744\n"
  "s_mov_b32 s9, 115456\n"
  "buffer_load_dwordx4 v144, s[24:27], s9 offen lds\n"
  "v_mfma_f32_16x16x32_bf16 v[84:87], a[32:35], v[128:131], v[84:87]\n"
  "ds_read_b64_tr_b16 v[126:127], v223 offset:40960\n"
  "s_lshl_b32 s88, s64, 7\n"
  "v_mfma_f32_16x16x32_bf16 v[92:95], a[36:39], v[128:131], v[92:95]\n"
  "ds_read_b64_tr_b16 v[128:129], v223 offset:38912\n"
  "s_add_u32 s87, s87, s88\n"
  "v_mfma_f32_16x16x32_bf16 v[100:103], v[132:135], a[32:35], v[100:103]\n"
  "ds_read_b64_tr_b16 v[130:131], v223 offset:41984\n"
  "v_lshl_add_u32 v247, v2, 10, s87\n"
  "v_mfma_f32_16x16x32_bf16 v[108:111], v[132:135], a[36:39], v[108:111]\n"
  "buffer_load_dword v44, v200, s[32:35], 0 offen\n"
  "v_and_b32 v45, 16, v0\n"
  "v_mfma_f32_16x16x32_bf16 v[8:11], v[132:135], a[28:31], v[8:11]\n"
  "ds_read_b64_tr_b16 v[132:133], v223 offset:36864\n"
  "v_lshl_add_u32 v247, v45, 1, v247\n"
  "v_mfma_f32_16x16x32_bf16 v[48:51], v[112:115], a[40:43], v[48:51]\n"
  "ds_read_b64_tr_b16 v[134:135], v223 offset:39936\n"
  "v_lshrrev_b32 v45, 5, v0\n"
  "v_mfma_f32_16x16x32_bf16 v[56:59], v[112:115], a[44:47], v[56:59]\n"
  "v_lshl_add_u32 v247, v45, 4, v247\n"
  "s_mul_i32 s87, s58, 1835008\n"
  "v_mfma_f32_16x16x32_bf16 v[64:67], v[112:115], a[48:51], v[64:67]\n"
  "ds_read_b64_tr_b16 v[112:113], v222 offset:44032\n"
  "s_lshl_b32 s88, s65, 7\n"
  "s_waitcnt lgkmcnt(6)\n"
  "v_mfma_f32_16x16x32_bf16 v[72:75], a[40:43], v[116:119], v[72:75]\n"
  "ds_read_b64_tr_b16 v[114:115], v222 offset:47104\n"
  "s_add_u32 s87, s87, s88\n"
  "v_mfma_f32_16x16x32_bf16 v[80:83], a[44:47], v[116:119], v[80:83]\n"
  "s_or_b32 s87, s87, s70\n"
  "v_lshrrev_b32 v45, 3, v0\n"
  "v_mfma_f32_16x16x32_bf16 v[88:91], a[48:51], v[116:119], v[88:91]\n"
  "ds_read_b64_tr_b16 v[116:117], v222 offset:45056\n"
  "v_mul_u32_u24 v232, 3584, v45\n"
  "v_mfma_f32_16x16x32_bf16 v[96:99], v[120:123], a[44:47], v[96:99]\n"
  "ds_read_b64_tr_b16 v[118:119], v222 offset:48128\n"
  "v_and_b32 v45, 2, v45\n"
  "v_mfma_f32_16x16x32_bf16 v[104:107], v[120:123], a[48:51], v[104:107]\n"
  "v_xor_b32 v45, v45, v0\n"
  "v_and_b32 v45, 7, v45\n"
  "v_mfma_f32_16x16x32_bf16 v[4:7], v[120:123], a[40:43], v[4:7]\n"
  "ds_read_b64_tr_b16 v[120:121], v222 offset:43008\n"
  "v_lshl_add_u32 v232, v45, 4, v232\n"
  "s_waitcnt lgkmcnt(5)\n"
  "v_mfma_f32_16x16x32_bf16 v[52:55], v[124:127], a[40:43], v[52:55]\n"
  "buffer_load_dword v44, v201, s[36:39], 0 offen\n"
  "v_mfma_f32_16x16x32_bf16 v[60:63], v[124:127], a[44:47], v[60:63]\n"
  "ds_read_b64_tr_b16 v[122:123], v222 offset:46080\n"
  "v_add_u32 v232, s87, v232\n"
  "v_mfma_f32_16x16x32_bf16 v[68:71], v[124:127], a[48:51], v[68:71]\n"
  "ds_read_b64_tr_b16 v[124:125], v223 offset:44032\n"
  "v_xor_b32 v233, 64, v232\n"
  "v_mfma_f32_16x16x32_bf16 v[76:79], a[40:43], v[128:131], v[76:79]\n"
  "ds_read_b64_tr_b16 v[126:127], v223 offset:47104\n"
  "s_lshr_b32 s94, s64, 1\n"
  "v_mfma_f32_16x16x32_bf16 v[84:87], a[44:47], v[128:131], v[84:87]\n"
  "s_lshl2_add_u32 s94, s67, s94\n"
  "s_and_b32 s95, s64, 1\n"
  "v_mfma_f32_16x16x32_bf16 v[92:95], a[48:51], v[128:131], v[92:95]\n"
  "ds_read_b64_tr_b16 v[128:129], v223 offset:45056\n"
  "s_lshl2_add_u32 s94, s94, s95\n"
  "v_mfma_f32_16x16x32_bf16 v[100:103], v[132:135], a[44:47], v[100:103]\n"
  "ds_read_b64_tr_b16 v[130:131], v223 offset:48128\n"
  "s_lshl_b32 s94, s94, 7\n"
  "v_mfma_f32_16x16x32_bf16 v[108:111], v[132:135], a[48:51], v[108:111]\n"
  "s_add_u32 s87, s94, 1048576\n"
  "v_lshl_add_u32 v251, v0, 1, s87\n"
  "v_mfma_f32_16x16x32_bf16 v[8:11], v[132:135], a[40:43], v[8:11]\n"
  "ds_read_b64_tr_b16 v[132:133], v223 offset:43008\n"
  "v_and_b32 v45, 3, v0\n"
  "s_waitcnt lgkmcnt(6)\n"
  "v_mfma_f32_16x16x32_bf16 v[48:51], v[112:115], a[52:55], v[48:51]\n"
  "ds_read_b64_tr_b16 v[134:135], v223 offset:46080\n"
  "v_lshl_add_u32 v251, v45, 28, v251\n"
  "v_mfma_f32_16x16x32_bf16 v[56:59], v[112:115], a[56:59], v[56:59]\n"
  "s_lshl_b32 s87, s67, 11\n"
  "s_add_u32 s87, s87, 1048576\n"
  "v_mfma_f32_16x16x32_bf16 v[64:67], v[112:115], a[60:63], v[64:67]\n"
  "ds_read_b64_tr_b16 v[112:113], v224 offset:1024\n"
  "v_lshl_add_u32 v252, v3, 9, s87\n"
  "v_mfma_f32_16x16x32_bf16 v[72:75], a[52:55], v[116:119], v[72:75]\n"
  "buffer_load_dword v44, v202, s[36:39], 0 offen\n"
  "ds_read_b64_tr_b16 v[114:115], v224 offset:4096\n"
  "v_mfma_f32_16x16x32_bf16 v[80:83], a[56:59], v[116:119], v[80:83]\n"
  "v_lshl_add_u32 v252, v2, 3, v252\n"
  "s_lshl_b32 s87, s64, 5\n"
  "v_mfma_f32_16x16x32_bf16 v[88:91], a[60:63], v[116:119], v[88:91]\n"
  "ds_read_b64_tr_b16 v[116:117], v224 offset:2048\n"
  "s_add_u32 s89, s87, s63\n"
  "s_waitcnt lgkmcnt(5)\n"
  "v_mfma_f32_16x16x32_bf16 v[96:99], v[120:123], a[56:59], v[96:99]\n"
  "ds_read_b64_tr_b16 v[118:119], v224 offset:5120\n"
  "s_lshr_b32 s94, s89, 3\n"
  "v_mfma_f32_16x16x32_bf16 v[104:107], v[120:123], a[60:63], v[104:107]\n"
  "s_and_b32 s95, s89, 7\n"
  "s_lshl_b32 s94, s94, 9\n"
  "v_mfma_f32_16x16x32_bf16 v[4:7], v[120:123], a[52:55], v[4:7]\n"
  "ds_read_b64_tr_b16 v[120:121], v224 offset:0\n"
  "s_lshl3_add_u32 s94, s95, s94\n"
  "v_mfma_f32_16x16x32_bf16 v[52:55], v[124:127], a[52:55], v[52:55]\n"
  "ds_read_b64_tr_b16 v[122:123], v224 offset:3072\n"
  "s_add_u32 s12, s94, 32768\n"
  "v_mfma_f32_16x16x32_bf16 v[60:63], v[124:127], a[56:59], v[60:63]\n"
  "s_add_u32 s87, s87, s89\n"
  "s_lshl1_add_u32 s87, s87, s59\n"
  "v_mfma_f32_16x16x32_bf16 v[68:71], v[124:127], a[60:63], v[68:71]\n"
  "ds_read_b64_tr_b16 v[124:125], v225 offset:1024\n"
  "s_lshl2_add_u32 s87, s87, s60\n"
  "v_mfma_f32_16x16x32_bf16 v[76:79], a[52:55], v[128:131], v[76:79]\n"
  "ds_read_b64_tr_b16 v[126:127], v225 offset:4096\n"
  "s_lshl_b32 s87, s87, 9\n"
  "v_mfma_f32_16x16x32_bf16 v[84:87], a[56:59], v[128:131], v[84:87]\n"
  "s_mov_b32 s52, 131072\n"
  "s_add_u32 s87, s87, 6291456\n"
  "v_mfma_f32_16x16x32_bf16 v[92:95], a[60:63], v[128:131], v[92:95]\n"
  "ds_read_b64_tr_b16 v[128:129], v225 offset:2048\n"
  "v_lshl_add_u32 v245, v2, 4, s87\n"
  "s_waitcnt lgkmcnt(5)\n"
  "v_mfma_f32_16x16x32_bf16 v[100:103], v[132:135], a[56:59], v[100:103]\n"
  "buffer_load_dword v44, v203, s[40:43], 0 offen\n"
  "ds_read_b64_tr_b16 v[130:131], v225 offset:5120\n"
  "v_mfma_f32_16x16x32_bf16 v[108:111], v[132:135], a[60:63], v[108:111]\n"
  "v_and_b32 v45, 16, v0\n"
  "v_lshl_add_u32 v245, v45, 6, v245\n"
  "v_mfma_f32_16x16x32_bf16 v[8:11], v[132:135], a[52:55], v[8:11]\n"
  "ds_read_b64_tr_b16 v[132:133], v225 offset:0\n"
  "v_and_b32 v45, 32, v0\n"
  "v_mfma_f32_16x16x32_bf16 v[48:51], v[112:115], a[64:67], v[48:51]\n"
  "ds_read_b64_tr_b16 v[134:135], v225 offset:3072\n"
  "v_lshl_add_u32 v245, v45, 3, v245\n"
  "v_mfma_f32_16x16x32_bf16 v[56:59], v[112:115], a[68:71], v[56:59]\n"
  "s_lshl2_add_u32 s88, s71, s58\n"
  "s_lshl_b32 s89, s88, 9\n"
  "v_mfma_f32_16x16x32_bf16 v[64:67], v[112:115], a[72:75], v[64:67]\n"
  "ds_read_b64_tr_b16 v[112:113], v224 offset:7168\n"
  "s_add_u32 s89, s89, 32768\n"
  "v_mfma_f32_16x16x32_bf16 v[72:75], a[64:67], v[116:119], v[72:75]\n"
  "ds_read_b64_tr_b16 v[114:115], v224 offset:10240\n"
  "v_and_b32 v246, 7, v0\n"
  "v_mfma_f32_16x16x32_bf16 v[80:83], a[68:71], v[116:119], v[80:83]\n"
  "v_lshl_add_u32 v246, v246, 3, s89\n"
  "s_xor_b32 s87, s58, 1\n"
  "v_mfma_f32_16x16x32_bf16 v[88:91], a[72:75], v[116:119], v[88:91]\n"
  "ds_read_b64_tr_b16 v[116:117], v224 offset:8192\n"
  "s_mul_i32 s87, s87, 6144\n"
  "s_waitcnt lgkmcnt(6)\n"
  "v_mfma_f32_16x16x32_bf16 v[96:99], v[120:123], a[68:71], v[96:99]\n"
  "ds_read_b64_tr_b16 v[118:119], v224 offset:11264\n"
  "s_add_u32 s87, s87, 98304\n"
  "v_mfma_f32_16x16x32_bf16 v[104:107], v[120:123], a[72:75], v[104:107]\n"
  "v_add_u32 v242, s87, v1\n"
  "s_xor_b32 s87, s58, 2\n"
  "v_mfma_f32_16x16x32_bf16 v[4:7], v[120:123], a[64:67], v[4:7]\n"
  "ds_read_b64_tr_b16 v[120:121], v224 offset:6144\n"
  "s_mul_i32 s87, s87, 6144\n"
  "v_mfma_f32_16x16x32_bf16 v[52:55], v[124:127], a[64:67], v[52:55]\n"
  "ds_read_b64_tr_b16 v[122:123], v224 offset:9216\n"
  "s_add_u32 s87, s87, 100352\n"
  "v_mfma_f32_16x16x32_bf16 v[60:63], v[124:127], a[68:71], v[60:63]\n"
  "s_waitcnt vmcnt(4) lgkmcnt(6)\n"
  "s_barrier\n"
  "v_mfma_f32_16x16x32_bf16 v[68:71], v[124:127], a[72:75], v[68:71]\n"
  "s_mov_b64 exec, s[16:17]\n"
  "ds_read_b64 a[112:113], v136 offset:36864\n"
  "ds_read_b64 a[114:115], v137 offset:36864\n"
  "s_mov_b64 exec, -1\n"
  "v_mfma_f32_16x16x32_bf16 v[76:79], a[64:67], v[128:131], v[76:79]\n"
  "ds_read_b64 a[116:117], v136 offset:40960\n"
  "ds_read_b64 a[118:119], v137 offset:40960\n"
  "v_mfma_f32_16x16x32_bf16 v[84:87], a[68:71], v[128:131], v[84:87]\n"
  "ds_read_b64 a[120:121], v136 offset:45056\n"
  "ds_read_b64 a[122:123], v137 offset:45056\n"
  "v_mfma_f32_16x16x32_bf16 v[92:95], a[72:75], v[128:131], v[92:95]\n"
  "s_mov_b64 exec, s[16:17]\n"
  "ds_read_b64 a[124:125], v138 offset:36864\n"
  "ds_read_b64 a[126:127], v139 offset:36864\n"
  "s_mov_b64 exec, -1\n"
  "v_mfma_f32_16x16x32_bf16 v[100:103], v[132:135], a[68:71], v[100:103]\n"
  "ds_read_b64 a[128:129], v138 offset:40960\n"
  "ds_read_b64 a[130:131], v139 offset:40960\n"
  "v_mfma_f32_16x16x32_bf16 v[108:111], v[132:135], a[72:75], v[108:111]\n"
  "ds_read_b64 a[132:133], v138 offset:45056\n"
  "s_waitcnt lgkmcnt(9)\n"
  "v_mfma_f32_16x16x32_bf16 v[160:163], a[112:115], a[112:115], v[160:163]\n"
  "ds_read_b64 a[134:135], v139 offset:45056\n"
  "s_mov_b64 exec, s[16:17]\n"
  "ds_read_b64 a[136:137], v140 offset:36864\n"
  "ds_read_b64 a[138:139], v141 offset:36864\n"
  "s_mov_b64 exec, -1\n"
  "s_waitcnt lgkmcnt(10)\n"
  "v_mfma_f32_16x16x32_bf16 v[164:167], a[116:119], a[116:119], v[164:167]\n"
  "ds_read_b64 a[140:141], v140 offset:40960\n"
  "s_waitcnt lgkmcnt(9)\n"
  "v_mfma_f32_16x16x32_bf16 v[168:171], a[120:123], a[120:123], v[168:171]\n"
  "ds_read_b64 a[142:143], v141 offset:40960\n"
  "s_waitcnt lgkmcnt(8)\n"
  "v_mfma_f32_16x16x32_bf16 v[160:163], a[124:127], a[124:127], v[160:163]\n"
  "ds_read_b64 a[144:145], v140 offset:45056\n"
  "ds_read_b64 a[146:147], v141 offset:45056\n"
  "v_mfma_f32_16x16x32_bf16 v[152:155], v[30:33], a[116:119], v[152:155]\n"
  "s_mov_b64 exec, s[16:17]\n"
  "ds_read_b64 a[148:149], v142 offset:36864\n"
  "ds_read_b64 a[150:151], v143 offset:36864\n"
  "s_mov_b64 exec, -1\n"
  "v_mfma_f32_16x16x32_bf16 v[156:159], v[30:33], a[120:123], v[156:159]\n"
  "ds_read_b64 a[152:153], v142 offset:40960\n"
  "s_waitcnt lgkmcnt(11)\n"
  "v_mfma_f32_16x16x32_bf16 v[164:167], a[128:131], a[128:131], v[164:167]\n"
  "ds_read_b64 a[154:155], v143 offset:40960\n"
  "s_waitcnt lgkmcnt(10)\n"
  "v_mfma_f32_16x16x32_bf16 v[168:171], a[132:135], a[132:135], v[168:171]\n"
  "ds_read_b64 a[156:157], v142 offset:45056\n"
  "s_waitcnt lgkmcnt(9)\n"
  "v_mfma_f32_16x16x32_bf16 v[160:163], a[136:139], a[136:139], v[160:163]\n"
  "ds_read_b64 a[158:159], v143 offset:45056\n"
  "s_waitcnt lgkmcnt(8)\n"
  "v_mfma_f32_16x16x32_bf16 v[164:167], a[140:143], a[140:143], v[164:167]\n"
  "ds_read_b64_tr_b16 v[124:125], v225 offset:7168\n"
  "s_waitcnt lgkmcnt(7)\n"
  "v_mfma_f32_16x16x32_bf16 v[168:171], a[144:147], a[144:147], v[168:171]\n"
  "ds_read_b64_tr_b16 v[126:127], v225 offset:10240\n"
  "s_waitcnt lgkmcnt(6)\n"
  "v_mfma_f32_16x16x32_bf16 v[160:163], a[148:151], a[148:151], v[160:163]\n"
  "ds_read_b64_tr_b16 v[128:129], v225 offset:8192\n"
  "v_add_u32 v243, s87, v1\n"
  "v_mfma_f32_16x16x32_bf16 v[152:155], v[30:33], a[128:131], v[152:155]\n"
  "ds_read_b64_tr_b16 v[130:131], v225 offset:11264\n"
  "v_lshlrev_b32 v45, 2, v3\n"
  "v_mfma_f32_16x16x32_bf16 v[156:159], v[30:33], a[132:135], v[156:159]\n"
  "v_sub_u32 v45, v2, v45\n"
  "s_waitcnt lgkmcnt(6)\n"
  "v_mfma_f32_16x16x32_bf16 v[164:167], a[152:155], a[152:155], v[164:167]\n"
  "s_waitcnt lgkmcnt(4)\n"
  "s_barrier\n"
  "v_mfma_f32_16x16x32_bf16 v[152:155], v[30:33], a[140:143], v[152:155]\n"
  "v_cmp_eq_u32 s[88:89], 0, v45\n"
  "v_cmp_eq_u32 s[90:91], 1, v45\n"
  "v_mfma_f32_16x16x32_bf16 v[156:159], v[30:33], a[144:147], v[156:159]\n"
  "v_cmp_eq_u32 s[92:93], 2, v45\n"
  "v_cmp_eq_u32 s[94:95], 3, v45\n"
  "v_mfma_f32_16x16x32_bf16 v[168:171], a[156:159], a[156:159], v[168:171]\n"
  "v_cndmask_b32 v137, 0, v160, s[88:89]\n"
  "v_cndmask_b32 v137, v137, v161, s[90:91]\n"
  "v_mfma_f32_16x16x32_bf16 v[152:155], v[30:33], a[152:155], v[152:155]\n"
  "v_cndmask_b32 v137, v137, v162, s[92:93]\n"
  "v_cndmask_b32 v137, v137, v163, s[94:95]\n"
  "v_mfma_f32_16x16x32_bf16 v[156:159], v[30:33], a[156:159], v[156:159]\n"
  "v_mul_f32 v136, v34, v160\n"
  "v_lshlrev_b32 v45, 2, v3\n"
  "v_mfma_f32_16x16x32_bf16 v[8:11], v[132:135], a[64:67], v[8:11]\n"
  "ds_read_b64_tr_b16 v[132:133], v225 offset:6144\n"
  "v_sub_u32 v45, v2, v45\n"
  "v_mfma_f32_16x16x32_bf16 v[48:51], v[112:115], a[76:79], v[48:51]\n"
  "ds_read_b64_tr_b16 v[134:135], v225 offset:9216\n"
  "v_cmp_eq_u32 s[88:89], 0, v45\n"
  "v_mfma_f32_16x16x32_bf16 v[56:59], v[112:115], a[80:83], v[56:59]\n"
  "v_cmp_eq_u32 s[90:91], 1, v45\n"
  "v_cmp_eq_u32 s[92:93], 2, v45\n"
  "v_mfma_f32_16x16x32_bf16 v[64:67], v[112:115], a[84:87], v[64:67]\n"
  "ds_read_b64_tr_b16 v[112:113], v224 offset:13312\n"
  "v_cmp_eq_u32 s[94:95], 3, v45\n"
  "v_mfma_f32_16x16x32_bf16 v[72:75], a[76:79], v[116:119], v[72:75]\n"
  "ds_read_b64_tr_b16 v[114:115], v224 offset:16384\n"
  "v_cndmask_b32 v141, 0, v164, s[88:89]\n"
  "v_mfma_f32_16x16x32_bf16 v[80:83], a[80:83], v[116:119], v[80:83]\n"
  "v_cndmask_b32 v141, v141, v165, s[90:91]\n"
  "v_cndmask_b32 v141, v141, v166, s[92:93]\n"
  "v_mfma_f32_16x16x32_bf16 v[88:91], a[84:87], v[116:119], v[88:91]\n"
  "ds_read_b64_tr_b16 v[116:117], v224 offset:14336\n"
  "v_cndmask_b32 v141, v141, v167, s[94:95]\n"
  "v_mfma_f32_16x16x32_bf16 v[96:99], v[120:123], a[80:83], v[96:99]\n"
  "ds_read_b64_tr_b16 v[118:119], v224 offset:17408\n"
  "v_mul_f32 v140, v34, v152\n"
  "v_mfma_f32_16x16x32_bf16 v[104:107], v[120:123], a[84:87], v[104:107]\n"
  "v_lshlrev_b32 v45, 2, v3\n"
  "v_sub_u32 v45, v2, v45\n"
  "v_mfma_f32_16x16x32_bf16 v[4:7], v[120:123], a[76:79], v[4:7]\n"
  "ds_read_b64_tr_b16 v[120:121], v224 offset:12288\n"
  "v_cmp_eq_u32 s[88:89], 0, v45\n"
  "s_waitcnt lgkmcnt(5)\n"
  "v_mfma_f32_16x16x32_bf16 v[52:55], v[124:127], a[76:79], v[52:55]\n"
  "ds_read_b64_tr_b16 v[122:123], v224 offset:15360\n"
  "v_cmp_eq_u32 s[90:91], 1, v45\n"
  "v_mfma_f32_16x16x32_bf16 v[60:63], v[124:127], a[80:83], v[60:63]\n"
  "v_cmp_eq_u32 s[92:93], 2, v45\n"
  "v_cmp_eq_u32 s[94:95], 3, v45\n"
  "v_mfma_f32_16x16x32_bf16 v[68:71], v[124:127], a[84:87], v[68:71]\n"
  "ds_read_b64_tr_b16 v[124:125], v225 offset:13312\n"
  "v_cndmask_b32 v145, 0, v168, s[88:89]\n"
  "v_mfma_f32_16x16x32_bf16 v[76:79], a[76:79], v[128:131], v[76:79]\n"
  "ds_read_b64_tr_b16 v[126:127], v225 offset:16384\n"
  "v_cndmask_b32 v145, v145, v169, s[90:91]\n"
  "v_mfma_f32_16x16x32_bf16 v[84:87], a[80:83], v[128:131], v[84:87]\n"
  "v_cndmask_b32 v145, v145, v170, s[92:93]\n"
  "v_cndmask_b32 v145, v145, v171, s[94:95]\n"
  "v_mfma_f32_16x16x32_bf16 v[92:95], a[84:87], v[128:131], v[92:95]\n"
  "ds_read_b64_tr_b16 v[128:129], v225 offset:14336\n"
  "v_mul_f32 v144, v34, v156\n"
  "v_mfma_f32_16x16x32_bf16 v[100:103], v[132:135], a[80:83], v[100:103]\n"
  "ds_read_b64_tr_b16 v[130:131], v225 offset:17408\n"
  "s_lshl4_add_u32 s87, s66, s62\n"
  "v_mfma_f32_16x16x32_bf16 v[108:111], v[132:135], a[84:87], v[108:111]\n"
  "v_permlane16_swap_b32 v136, v137\n"
  "v_mfma_f32_16x16x32_bf16 v[8:11], v[132:135], a[76:79], v[8:11]\n"
  "ds_read_b64_tr_b16 v[132:133], v225 offset:12288\n"
  "v_add_f32 v136, v136, v137\n"
  "s_waitcnt lgkmcnt(5)\n"
  "v_mfma_f32_16x16x32_bf16 v[48:51], v[112:115], a[88:91], v[48:51]\n"
  "ds_read_b64_tr_b16 v[134:135], v225 offset:15360\n"
  "v_mov_b32 v137, v136\n"
  "v_mfma_f32_16x16x32_bf16 v[56:59], v[112:115], a[92:95], v[56:59]\n"
  "v_permlane16_swap_b32 v140, v141\n"
  "v_mfma_f32_16x16x32_bf16 v[64:67], v[112:115], a[96:99], v[64:67]\n"
  "ds_read_b64_tr_b16 v[112:113], v224 offset:19456\n"
  "v_add_f32 v140, v140, v141\n"
  "v_mfma_f32_16x16x32_bf16 v[72:75], a[88:91], v[116:119], v[72:75]\n"
  "ds_read_b64_tr_b16 v[114:115], v224 offset:22528\n"
  "v_mov_b32 v141, v140\n"
  "v_mfma_f32_16x16x32_bf16 v[80:83], a[92:95], v[116:119], v[80:83]\n"
  "v_permlane16_swap_b32 v144, v145\n"
  "v_mfma_f32_16x16x32_bf16 v[88:91], a[96:99], v[116:119], v[88:91]\n"
  "ds_read_b64_tr_b16 v[116:117], v224 offset:20480\n"
  "v_add_f32 v144, v144, v145\n"
  "v_mfma_f32_16x16x32_bf16 v[96:99], v[120:123], a[92:95], v[96:99]\n"
  "ds_read_b64_tr_b16 v[118:119], v224 offset:23552\n"
  "v_mov_b32 v145, v144\n"
  "v_mfma_f32_16x16x32_bf16 v[104:107], v[120:123], a[96:99], v[104:107]\n"
  "v_permlane32_swap_b32 v136, v137\n"
  "v_mfma_f32_16x16x32_bf16 v[4:7], v[120:123], a[88:91], v[4:7]\n"
  "ds_read_b64_tr_b16 v[120:121], v224 offset:18432\n"
  "v_add_f32 v136, v136, v137\n"
  "s_waitcnt lgkmcnt(5)\n"
  "v_mfma_f32_16x16x32_bf16 v[52:55], v[124:127], a[88:91], v[52:55]\n"
  "ds_read_b64_tr_b16 v[122:123], v224 offset:21504\n"
  "v_mov_b32 v137, v136\n"
  "v_mfma_f32_16x16x32_bf16 v[60:63], v[124:127], a[92:95], v[60:63]\n"
  "v_permlane32_swap_b32 v140, v141\n"
  "v_mfma_f32_16x16x32_bf16 v[68:71], v[124:127], a[96:99], v[68:71]\n"
  "ds_read_b64_tr_b16 v[124:125], v225 offset:19456\n"
  "v_add_f32 v140, v140, v141\n"
  "v_mfma_f32_16x16x32_bf16 v[76:79], a[88:91], v[128:131], v[76:79]\n"
  "ds_read_b64_tr_b16 v[126:127], v225 offset:22528\n"
  "v_mov_b32 v141, v140\n"
  "v_mfma_f32_16x16x32_bf16 v[84:87], a[92:95], v[128:131], v[84:87]\n"
  "v_permlane32_swap_b32 v144, v145\n"
  "v_mfma_f32_16x16x32_bf16 v[92:95], a[96:99], v[128:131], v[92:95]\n"
  "ds_read_b64_tr_b16 v[128:129], v225 offset:20480\n"
  "v_add_f32 v144, v144, v145\n"
  "v_mfma_f32_16x16x32_bf16 v[100:103], v[132:135], a[92:95], v[100:103]\n"
  "ds_read_b64_tr_b16 v[130:131], v225 offset:23552\n"
  "v_mov_b32 v145, v144\n"
  "v_mfma_f32_16x16x32_bf16 v[108:111], v[132:135], a[96:99], v[108:111]\n"
  "v_permlane16_swap_b32 v136, v137\n"
  "v_mfma_f32_16x16x32_bf16 v[8:11], v[132:135], a[88:91], v[8:11]\n"
  "ds_read_b64_tr_b16 v[132:133], v225 offset:18432\n"
  "v_mul_f32 v138, s4, v136\n"
  "s_waitcnt lgkmcnt(5)\n"
  "v_mfma_f32_16x16x32_bf16 v[48:51], v[112:115], a[100:103], v[48:51]\n"
  "ds_read_b64_tr_b16 v[134:135], v225 offset:21504\n"
  "v_mul_f32 v139, v138, v138\n"
  "v_mfma_f32_16x16x32_bf16 v[56:59], v[112:115], a[104:107], v[56:59]\n"
  "v_permlane16_swap_b32 v140, v141\n"
  "v_mfma_f32_16x16x32_bf16 v[64:67], v[112:115], a[108:111], v[64:67]\n"
  "ds_read_b64_tr_b16 v[112:113], v224 offset:25600\n"
  "v_mul_f32 v142, s4, v140\n"
  "v_mfma_f32_16x16x32_bf16 v[72:75], a[100:103], v[116:119], v[72:75]\n"
  "ds_read_b64_tr_b16 v[114:115], v224 offset:28672\n"
  "v_mul_f32 v143, v142, v142\n"
  "v_mfma_f32_16x16x32_bf16 v[80:83], a[104:107], v[116:119], v[80:83]\n"
  "v_permlane16_swap_b32 v144, v145\n"
  "v_mfma_f32_16x16x32_bf16 v[88:91], a[108:111], v[116:119], v[88:91]\n"
  "ds_read_b64_tr_b16 v[116:117], v224 offset:26624\n"
  "v_mul_f32 v146, s4, v144\n"
  "v_mfma_f32_16x16x32_bf16 v[96:99], v[120:123], a[104:107], v[96:99]\n"
  "ds_read_b64_tr_b16 v[118:119], v224 offset:29696\n"
  "v_mul_f32 v147, v146, v146\n"
  "v_mfma_f32_16x16x32_bf16 v[104:107], v[120:123], a[108:111], v[104:107]\n"
  "v_fma_f32 v137, v137, s4, -v139\n"
  "v_fma_f32 v141, v141, s4, -v143\n"
  "v_mfma_f32_16x16x32_bf16 v[4:7], v[120:123], a[100:103], v[4:7]\n"
  "ds_read_b64_tr_b16 v[120:121], v224 offset:24576\n"
  "v_fma_f32 v145, v145, s4, -v147\n"
  "s_waitcnt lgkmcnt(5)\n"
  "v_mfma_f32_16x16x32_bf16 v[52:55], v[124:127], a[100:103], v[52:55]\n"
  "ds_read_b64_tr_b16 v[122:123], v224 offset:27648\n"
  "v_max_f32 v137, 0, v137\n"
  "v_mfma_f32_16x16x32_bf16 v[60:63], v[124:127], a[104:107], v[60:63]\n"
  "v_max_f32 v141, 0, v141\n"
  "v_max_f32 v145, 0, v145\n"
  "v_mfma_f32_16x16x32_bf16 v[68:71], v[124:127], a[108:111], v[68:71]\n"
  "ds_read_b64_tr_b16 v[124:125], v225 offset:25600\n"
  "v_add_f32 v137, s5, v137\n"
  "v_mfma_f32_16x16x32_bf16 v[76:79], a[100:103], v[128:131], v[76:79]\n"
  "ds_read_b64_tr_b16 v[126:127], v225 offset:28672\n"
  "v_add_f32 v141, s5, v141\n"
  "v_mfma_f32_16x16x32_bf16 v[84:87], a[104:107], v[128:131], v[84:87]\n"
  "v_add_f32 v145, s5, v145\n"
  "ds_bpermute_b32 v148, v226, v138 offset:0\n"
  "v_mfma_f32_16x16x32_bf16 v[92:95], a[108:111], v[128:131], v[92:95]\n"
  "ds_read_b64_tr_b16 v[128:129], v225 offset:26624\n"
  "s_lshl_b32 s87, s87, 10\n"
  "v_mfma_f32_16x16x32_bf16 v[100:103], v[132:135], a[104:107], v[100:103]\n"
  "ds_read_b64_tr_b16 v[130:131], v225 offset:29696\n"
  "s_add_u32 s15, s87, 2097152\n"
  "v_mfma_f32_16x16x32_bf16 v[108:111], v[132:135], a[108:111], v[108:111]\n"
  "v_rsq_f32 v139, v137\n"
  "v_mfma_f32_16x16x32_bf16 v[8:11], v[132:135], a[100:103], v[8:11]\n"
  "ds_read_b64_tr_b16 v[132:133], v225 offset:24576\n"
  "s_add_u32 s16, s15, 16384\n"
  "s_waitcnt lgkmcnt(6)\n"
  "v_mfma_f32_16x16x32_bf16 v[48:51], v[112:115], a[112:115], v[48:51]\n"
  "ds_read_b64_tr_b16 v[134:135], v225 offset:27648\n"
  "s_lshl4_add_u32 s87, s61, s62\n"
  "v_mfma_f32_16x16x32_bf16 v[56:59], v[112:115], a[116:119], v[56:59]\n"
  "v_rsq_f32 v143, v141\n"
  "v_mfma_f32_16x16x32_bf16 v[64:67], v[112:115], a[120:123], v[64:67]\n"
  "ds_read_b64_tr_b16 v[112:113], v224 offset:31744\n"
  "v_mul_f32 v140, s6, v143\n"
  "v_mfma_f32_16x16x32_bf16 v[72:75], a[112:115], v[116:119], v[72:75]\n"
  "ds_read_b64_tr_b16 v[114:115], v224 offset:34816\n"
  "s_lshr_b32 s94, s87, 4\n"
  "v_mfma_f32_16x16x32_bf16 v[80:83], a[116:119], v[116:119], v[80:83]\n"
  "v_rsq_f32 v147, v145\n"
  "v_mfma_f32_16x16x32_bf16 v[88:91], a[120:123], v[116:119], v[88:91]\n"
  "ds_read_b64_tr_b16 v[116:117], v224 offset:32768\n"
  "v_mul_f32 v144, s6, v147\n"
  "v_mfma_f32_16x16x32_bf16 v[96:99], v[120:123], a[116:119], v[96:99]\n"
  "ds_read_b64_tr_b16 v[118:119], v224 offset:35840\n"
  "s_and_b32 s95, s87, 15\n"
  "v_mfma_f32_16x16x32_bf16 v[104:107], v[120:123], a[120:123], v[104:107]\n"
  "ds_bpermute_b32 v149, v226, v138 offset:4\n"
  "s_lshl_b32 s94, s94, 9\n"
  "v_mfma_f32_16x16x32_bf16 v[4:7], v[120:123], a[112:115], v[4:7]\n"
  "ds_read_b64_tr_b16 v[120:121], v224 offset:30720\n"
  "s_lshl3_add_u32 s94, s95, s94\n"
  "s_waitcnt lgkmcnt(6)\n"
  "v_mfma_f32_16x16x32_bf16 v[52:55], v[124:127], a[112:115], v[52:55]\n"
  "ds_read_b64_tr_b16 v[122:123], v224 offset:33792\n"
  "s_add_u32 s11, s94, 16384\n"
  "v_mfma_f32_16x16x32_bf16 v[60:63], v[124:127], a[116:119], v[60:63]\n"
  "ds_bpermute_b32 v150, v226, v138 offset:8\n"
  "s_xor_b32 s87, s58, 3\n"
  "v_mfma_f32_16x16x32_bf16 v[68:71], v[124:127], a[120:123], v[68:71]\n"
  "ds_read_b64_tr_b16 v[124:125], v225 offset:31744\n"
  "s_mul_i32 s87, s87, 6144\n"
  "v_mfma_f32_16x16x32_bf16 v[76:79], a[112:115], v[128:131], v[76:79]\n"
  "ds_read_b64_tr_b16 v[126:127], v225 offset:34816\n"
  "s_add_u32 s87, s87, 102400\n"
  "v_mfma_f32_16x16x32_bf16 v[84:87], a[116:119], v[128:131], v[84:87]\n"
  "ds_bpermute_b32 v151, v226, v138 offset:12\n"
  "v_add_u32 v244, s87, v1\n"
  "v_mfma_f32_16x16x32_bf16 v[92:95], a[120:123], v[128:131], v[92:95]\n"
  "ds_read_b64_tr_b16 v[128:129], v225 offset:32768\n"
  "s_xor_b32 s87, s58, 0\n"
  "v_mfma_f32_16x16x32_bf16 v[100:103], v[132:135], a[116:119], v[100:103]\n"
  "ds_read_b64_tr_b16 v[130:131], v225 offset:35840\n"
  "s_mul_i32 s87, s87, 6144\n"
  "v_mfma_f32_16x16x32_bf16 v[108:111], v[132:135], a[120:123], v[108:111]\n"
  "ds_bpermute_b32 v156, v226, v142 offset:0\n"
  "s_add_u32 s87, s87, 98304\n"
  "v_mfma_f32_16x16x32_bf16 v[8:11], v[132:135], a[112:115], v[8:11]\n"
  "ds_read_b64_tr_b16 v[132:133], v225 offset:30720\n"
  "v_add_u32 v241, s87, v1\n"
  "s_waitcnt lgkmcnt(8)\n"
  "v_mfma_f32_16x16x32_bf16 v[48:51], v[112:115], a[124:127], v[48:51]\n"
  "ds_read_b64_tr_b16 v[134:135], v225 offset:33792\n"
  "s_mul_i32 s87, s71, 56\n"
  "v_mfma_f32_16x16x32_bf16 v[56:59], v[112:115], a[128:131], v[56:59]\n"
  "ds_bpermute_b32 v157, v226, v142 offset:4\n"
  "s_add_u32 s87, s87, s65\n"
  "v_mfma_f32_16x16x32_bf16 v[64:67], v[112:115], a[132:135], v[64:67]\n"
  "ds_read_b64_tr_b16 v[112:113], v224 offset:37888\n"
  "s_lshl2_add_u32 s87, s87, s58\n"
  "v_mfma_f32_16x16x32_bf16 v[72:75], a[124:127], v[116:119], v[72:75]\n"
  "ds_read_b64_tr_b16 v[114:115], v224 offset:40960\n"
  "s_lshl_b32 s87, s87, 10\n"
  "v_mfma_f32_16x16x32_bf16 v[80:83], a[128:131], v[116:119], v[80:83]\n"
  "ds_bpermute_b32 v158, v226, v142 offset:8\n"
  "s_add_u32 s87, s87, 10485760\n"
  "v_mfma_f32_16x16x32_bf16 v[88:91], a[132:135], v[116:119], v[88:91]\n"
  "ds_read_b64_tr_b16 v[116:117], v224 offset:38912\n"
  "v_lshlrev_b32 v47, 16, v47\n"
  "v_mfma_f32_16x16x32_bf16 v[96:99], v[120:123], a[128:131], v[96:99]\n"
  "ds_read_b64_tr_b16 v[118:119], v224 offset:41984\n"
  "v_mfma_f32_16x16x32_bf16 v[104:107], v[120:123], a[132:135], v[104:107]\n"
  "ds_bpermute_b32 v159, v226, v142 offset:12\n"
  "v_mfma_f32_16x16x32_bf16 v[4:7], v[120:123], a[124:127], v[4:7]\n"
  "ds_read_b64_tr_b16 v[120:121], v224 offset:36864\n"
  "s_waitcnt lgkmcnt(8)\n"
  "v_mfma_f32_16x16x32_bf16 v[52:55], v[124:127], a[124:127], v[52:55]\n"
  "ds_read_b64_tr_b16 v[122:123], v224 offset:39936\n"
  "v_mfma_f32_16x16x32_bf16 v[60:63], v[124:127], a[128:131], v[60:63]\n"
  "ds_bpermute_b32 v152, v226, v139 offset:0\n"
  "v_mfma_f32_16x16x32_bf16 v[68:71], v[124:127], a[132:135], v[68:71]\n"
  "ds_read_b64_tr_b16 v[124:125], v225 offset:37888\n"
  "v_mfma_f32_16x16x32_bf16 v[76:79], a[124:127], v[128:131], v[76:79]\n"
  "ds_read_b64_tr_b16 v[126:127], v225 offset:40960\n"
  "v_mfma_f32_16x16x32_bf16 v[84:87], a[128:131], v[128:131], v[84:87]\n"
  "ds_bpermute_b32 v153, v226, v139 offset:4\n"
  "v_mfma_f32_16x16x32_bf16 v[92:95], a[132:135], v[128:131], v[92:95]\n"
  "ds_read_b64_tr_b16 v[128:129], v225 offset:38912\n"
  "v_mfma_f32_16x16x32_bf16 v[100:103], v[132:135], a[128:131], v[100:103]\n"
  "ds_read_b64_tr_b16 v[130:131], v225 offset:41984\n"
  "v_mfma_f32_16x16x32_bf16 v[108:111], v[132:135], a[132:135], v[108:111]\n"
  "ds_bpermute_b32 v154, v226, v139 offset:8\n"
  "v_mfma_f32_16x16x32_bf16 v[8:11], v[132:135], a[124:127], v[8:11]\n"
  "ds_read_b64_tr_b16 v[132:133], v225 offset:36864\n"
  "s_waitcnt lgkmcnt(9)\n"
  "v_mfma_f32_16x16x32_bf16 v[48:51], v[112:115], a[136:139], v[48:51]\n"
  "ds_read_b64_tr_b16 v[134:135], v225 offset:39936\n"
  "v_mfma_f32_16x16x32_bf16 v[56:59], v[112:115], a[140:143], v[56:59]\n"
  "ds_bpermute_b32 v155, v226, v139 offset:12\n"
  "v_mfma_f32_16x16x32_bf16 v[64:67], v[112:115], a[144:147], v[64:67]\n"
  "ds_read_b64_tr_b16 v[112:113], v224 offset:44032\n"
  "v_mfma_f32_16x16x32_bf16 v[72:75], a[136:139], v[116:119], v[72:75]\n"
  "ds_read_b64_tr_b16 v[114:115], v224 offset:47104\n"
  "v_mfma_f32_16x16x32_bf16 v[80:83], a[140:143], v[116:119], v[80:83]\n"
  "ds_bpermute_b32 v160, v226, v143 offset:0\n"
  "v_mfma_f32_16x16x32_bf16 v[88:91], a[144:147], v[116:119], v[88:91]\n"
  "ds_read_b64_tr_b16 v[116:117], v224 offset:45056\n"
  "s_waitcnt lgkmcnt(8)\n"
  "v_mfma_f32_16x16x32_bf16 v[96:99], v[120:123], a[140:143], v[96:99]\n"
  "ds_read_b64_tr_b16 v[118:119], v224 offset:48128\n"
  "v_mfma_f32_16x16x32_bf16 v[104:107], v[120:123], a[144:147], v[104:107]\n"
  "ds_bpermute_b32 v161, v226, v143 offset:4\n"
  "v_mfma_f32_16x16x32_bf16 v[4:7], v[120:123], a[136:139], v[4:7]\n"
  "ds_read_b64_tr_b16 v[120:121], v224 offset:43008\n"
  "v_mfma_f32_16x16x32_bf16 v[52:55], v[124:127], a[136:139], v[52:55]\n"
  "ds_read_b64_tr_b16 v[122:123], v224 offset:46080\n"
  "v_mfma_f32_16x16x32_bf16 v[60:63], v[124:127], a[140:143], v[60:63]\n"
  "ds_bpermute_b32 v162, v226, v143 offset:8\n"
  "v_mfma_f32_16x16x32_bf16 v[68:71], v[124:127], a[144:147], v[68:71]\n"
  "ds_read_b64_tr_b16 v[124:125], v225 offset:44032\n"
  "v_mfma_f32_16x16x32_bf16 v[76:79], a[136:139], v[128:131], v[76:79]\n"
  "ds_read_b64_tr_b16 v[126:127], v225 offset:47104\n"
  "v_mfma_f32_16x16x32_bf16 v[84:87], a[140:143], v[128:131], v[84:87]\n"
  "ds_bpermute_b32 v163, v226, v143 offset:12\n"
  "v_mfma_f32_16x16x32_bf16 v[92:95], a[144:147], v[128:131], v[92:95]\n"
  "ds_read_b64_tr_b16 v[128:129], v225 offset:45056\n"
  "s_waitcnt lgkmcnt(9)\n"
  "v_mfma_f32_16x16x32_bf16 v[100:103], v[132:135], a[140:143], v[100:103]\n"
  "ds_read_b64_tr_b16 v[130:131], v225 offset:48128\n"
  "v_mfma_f32_16x16x32_bf16 v[108:111], v[132:135], a[144:147], v[108:111]\n"
  "ds_bpermute_b32 v164, v226, v146 offset:0\n"
  "v_mfma_f32_16x16x32_bf16 v[8:11], v[132:135], a[136:139], v[8:11]\n"
  "ds_read_b64_tr_b16 v[132:133], v225 offset:43008\n"
  "v_mfma_f32_16x16x32_bf16 v[48:51], v[112:115], a[148:151], v[48:51]\n"
  "ds_read_b64_tr_b16 v[134:135], v225 offset:46080\n"
  "v_mfma_f32_16x16x32_bf16 v[56:59], v[112:115], a[152:155], v[56:59]\n"
  "ds_bpermute_b32 v165, v226, v146 offset:4\n"
  "v_mfma_f32_16x16x32_bf16 v[64:67], v[112:115], a[156:159], v[64:67]\n"
  "s_nop 3\n"
  "v_mov_b32_dpp v12, v48 row_newbcast:0 row_mask:0xf bank_mask:0xf\n"
  "v_mov_b32_dpp v13, v49 row_newbcast:0 row_mask:0xf bank_mask:0xf\n"
  "s_waitcnt lgkmcnt(7)\n"
  "v_mfma_f32_16x16x32_bf16 v[72:75], a[148:151], v[116:119], v[72:75]\n"
  "v_mov_b32_dpp v14, v50 row_newbcast:0 row_mask:0xf bank_mask:0xf\n"
  "v_mov_b32_dpp v15, v51 row_newbcast:0 row_mask:0xf bank_mask:0xf\n"
  "v_mfma_f32_16x16x32_bf16 v[80:83], a[152:155], v[116:119], v[80:83]\n"
  "ds_bpermute_b32 v166, v226, v146 offset:8\n"
  "v_mfma_f32_16x16x32_bf16 v[88:91], a[156:159], v[116:119], v[88:91]\n"
  "s_nop 2\n"
  "ds_bpermute_b32 v20, v206, v72\n"
  "v_mfma_f32_16x16x32_bf16 v[96:99], v[120:123], a[152:155], v[96:99]\n"
  "ds_bpermute_b32 v167, v226, v146 offset:12\n"
  "v_mfma_f32_16x16x32_bf16 v[104:107], v[120:123], a[156:159], v[104:107]\n"
  "ds_bpermute_b32 v168, v226, v147 offset:0\n"
  "v_mfma_f32_16x16x32_bf16 v[4:7], v[120:123], a[148:151], v[4:7]\n"
  "ds_bpermute_b32 v169, v226, v147 offset:4\n"
  "v_mfma_f32_16x16x32_bf16 v[52:55], v[124:127], a[148:151], v[52:55]\n"
  "ds_bpermute_b32 v170, v226, v147 offset:8\n"
  "v_mfma_f32_16x16x32_bf16 v[60:63], v[124:127], a[152:155], v[60:63]\n"
  "s_nop 3\n"
  "v_mov_b32_dpp v4, v4 row_newbcast:0 row_mask:0xf bank_mask:0xf\n"
  "v_mov_b32_dpp v5, v5 row_newbcast:0 row_mask:0xf bank_mask:0xf\n"
  "v_mfma_f32_16x16x32_bf16 v[68:71], v[124:127], a[156:159], v[68:71]\n"
  "v_mov_b32_dpp v16, v52 row_newbcast:0 row_mask:0xf bank_mask:0xf\n"
  "v_mov_b32_dpp v17, v53 row_newbcast:0 row_mask:0xf bank_mask:0xf\n"
  "s_waitcnt lgkmcnt(6)\n"
  "v_mfma_f32_16x16x32_bf16 v[76:79], a[148:151], v[128:131], v[76:79]\n"
  "v_mov_b32_dpp v18, v54 row_newbcast:0 row_mask:0xf bank_mask:0xf\n"
  "v_mov_b32_dpp v19, v55 row_newbcast:0 row_mask:0xf bank_mask:0xf\n"
  "v_mfma_f32_16x16x32_bf16 v[84:87], a[152:155], v[128:131], v[84:87]\n"
  "v_mov_b32_dpp v6, v6 row_newbcast:0 row_mask:0xf bank_mask:0xf\n"
  "v_mov_b32_dpp v7, v7 row_newbcast:0 row_mask:0xf bank_mask:0xf\n"
  "v_mfma_f32_16x16x32_bf16 v[92:95], a[156:159], v[128:131], v[92:95]\n"
  "s_nop 1\n"
  "ds_bpermute_b32 v21, v206, v76\n"
  "v_mfma_f32_16x16x32_bf16 v[100:103], v[132:135], a[152:155], v[100:103]\n"
  "ds_bpermute_b32 v171, v226, v147 offset:12\n"
  "v_mfma_f32_16x16x32_bf16 v[108:111], v[132:135], a[156:159], v[108:111]\n"
  "ds_read_b128 v[22:25], v196\n"
  "v_mfma_f32_16x16x32_bf16 v[8:11], v[132:135], a[148:151], v[8:11]\n"
  "ds_read_b128 v[26:29], v198\n"
  "v_pk_fma_f32 v[48:49], v[12:13], v[138:139], v[48:49] op_sel_hi:[1,0,1] neg_lo:[0,1,0] neg_hi:[0,1,0]\n"
  "v_pk_fma_f32 v[52:53], v[16:17], v[138:139], v[52:53] op_sel_hi:[1,0,1] neg_lo:[0,1,0] neg_hi:[0,1,0]\n"
  "v_pk_fma_f32 v[56:57], v[12:13], v[142:143], v[56:57] op_sel_hi:[1,0,1] neg_lo:[0,1,0] neg_hi:[0,1,0]\n"
  "v_pk_fma_f32 v[60:61], v[16:17], v[142:143], v[60:61] op_sel_hi:[1,0,1] neg_lo:[0,1,0] neg_hi:[0,1,0]\n"
  "v_pk_fma_f32 v[64:65], v[12:13], v[146:147], v[64:65] op_sel_hi:[1,0,1] neg_lo:[0,1,0] neg_hi:[0,1,0]\n"
  "v_pk_fma_f32 v[68:69], v[16:17], v[146:147], v[68:69] op_sel_hi:[1,0,1] neg_lo:[0,1,0] neg_hi:[0,1,0]\n"
  "s_nop 0\n"
  "v_mov_b32_dpp v8, v8 row_newbcast:0 row_mask:0xf bank_mask:0xf\n"
  "v_mov_b32_dpp v9, v9 row_newbcast:0 row_mask:0xf bank_mask:0xf\n"
  "v_mov_b32_dpp v10, v10 row_newbcast:0 row_mask:0xf bank_mask:0xf\n"
  "v_mov_b32_dpp v11, v11 row_newbcast:0 row_mask:0xf bank_mask:0xf\n"
  "v_pk_mul_f32 v[48:49], v[48:49], v[138:139] op_sel:[0,1]\n"
  "v_pk_fma_f32 v[50:51], v[14:15], v[138:139], v[50:51] op_sel_hi:[1,0,1] neg_lo:[0,1,0] neg_hi:[0,1,0]\n"
  "v_pk_mul_f32 v[52:53], v[52:53], v[138:139] op_sel:[0,1]\n"
  "v_pk_fma_f32 v[54:55], v[18:19], v[138:139], v[54:55] op_sel_hi:[1,0,1] neg_lo:[0,1,0] neg_hi:[0,1,0]\n"
  "v_pk_mul_f32 v[56:57], v[56:57], v[142:143] op_sel:[0,1]\n"
  "v_pk_fma_f32 v[58:59], v[14:15], v[142:143], v[58:59] op_sel_hi:[1,0,1] neg_lo:[0,1,0] neg_hi:[0,1,0]\n"
  "v_pk_fma_f32 v[96:97], v[4:5], v[142:143], v[96:97] op_sel_hi:[1,0,1] neg_lo:[0,1,0] neg_hi:[0,1,0]\n"
  "v_pk_mul_f32 v[60:61], v[60:61], v[142:143] op_sel:[0,1]\n"
  "v_pk_fma_f32 v[62:63], v[18:19], v[142:143], v[62:63] op_sel_hi:[1,0,1] neg_lo:[0,1,0] neg_hi:[0,1,0]\n"
  "v_pk_fma_f32 v[100:101], v[8:9], v[142:143], v[100:101] op_sel_hi:[1,0,1] neg_lo:[0,1,0] neg_hi:[0,1,0]\n"
  "s_waitcnt lgkmcnt(0)\n"
  "s_barrier\n"
  "v_pk_mul_f32 v[64:65], v[64:65], v[146:147] op_sel:[0,1]\n"
  "v_pk_fma_f32 v[66:67], v[14:15], v[146:147], v[66:67] op_sel_hi:[1,0,1] neg_lo:[0,1,0] neg_hi:[0,1,0]\n"
  "v_pk_fma_f32 v[104:105], v[4:5], v[146:147], v[104:105] op_sel_hi:[1,0,1] neg_lo:[0,1,0] neg_hi:[0,1,0]\n"
  "v_pk_mul_f32 v[68:69], v[68:69], v[146:147] op_sel:[0,1]\n"
  "v_pk_fma_f32 v[70:71], v[18:19], v[146:147], v[70:71] op_sel_hi:[1,0,1] neg_lo:[0,1,0] neg_hi:[0,1,0]\n"
  "v_pk_fma_f32 v[108:109], v[8:9], v[146:147], v[108:109] op_sel_hi:[1,0,1] neg_lo:[0,1,0] neg_hi:[0,1,0]\n"
  "v_pk_mul_f32 v[50:51], v[50:51], v[138:139] op_sel:[0,1]\n"
  "v_cvt_pk_bf16_f32 v48, v48, v49\n"
  "v_pk_mul_f32 v[54:55], v[54:55], v[138:139] op_sel:[0,1]\n"
  "v_cvt_pk_bf16_f32 v52, v52, v53\n"
  "v_pk_mul_f32 v[58:59], v[58:59], v[142:143] op_sel:[0,1]\n"
  "v_cvt_pk_bf16_f32 v56, v56, v57\n"
  "v_pk_fma_f32 v[98:99], v[6:7], v[142:143], v[98:99] op_sel_hi:[1,0,1] neg_lo:[0,1,0] neg_hi:[0,1,0]\n"
  "v_cvt_pk_bf16_f32 v96, v96, v97\n"
  "v_pk_mul_f32 v[62:63], v[62:63], v[142:143] op_sel:[0,1]\n"
  "v_cvt_pk_bf16_f32 v60, v60, v61\n"
  "v_pk_fma_f32 v[102:103], v[10:11], v[142:143], v[102:103] op_sel_hi:[1,0,1] neg_lo:[0,1,0] neg_hi:[0,1,0]\n"
  "v_cvt_pk_bf16_f32 v100, v100, v101\n"
  "v_pk_mul_f32 v[66:67], v[66:67], v[146:147] op_sel:[0,1]\n"
  "v_cvt_pk_bf16_f32 v64, v64, v65\n"
  "v_pk_fma_f32 v[106:107], v[6:7], v[146:147], v[106:107] op_sel_hi:[1,0,1] neg_lo:[0,1,0] neg_hi:[0,1,0]\n"
  "v_cvt_pk_bf16_f32 v104, v104, v105\n"
  "v_pk_mul_f32 v[70:71], v[70:71], v[146:147] op_sel:[0,1]\n"
  "v_cvt_pk_bf16_f32 v68, v68, v69\n"
  "v_pk_fma_f32 v[110:111], v[10:11], v[146:147], v[110:111] op_sel_hi:[1,0,1] neg_lo:[0,1,0] neg_hi:[0,1,0]\n"
  "v_cvt_pk_bf16_f32 v108, v108, v109\n"
  "v_cvt_pk_bf16_f32 v49, v50, v51\n"
  "v_cvt_pk_bf16_f32 v53, v54, v55\n"
  "v_cvt_pk_bf16_f32 v57, v58, v59\n"
  "v_cvt_pk_bf16_f32 v97, v98, v99\n"
  "v_cvt_pk_bf16_f32 v61, v62, v63\n"
  "v_cvt_pk_bf16_f32 v101, v102, v103\n"
  "v_mfma_f32_16x16x16_bf16 v[112:115], v[48:49], v[96:97], v[216:219]\n"
  "v_cvt_pk_bf16_f32 v65, v66, v67\n"
  "v_cvt_pk_bf16_f32 v105, v106, v107\n"
  "v_mfma_f32_16x16x16_bf16 v[116:119], v[56:57], v[96:97], v[208:211]\n"
  "v_cvt_pk_bf16_f32 v69, v70, v71\n"
  "v_cvt_pk_bf16_f32 v109, v110, v111\n"
  "v_mfma_f32_16x16x16_bf16 v[120:123], v[52:53], v[100:101], v[216:219]\n"
  "s_cmp_eq_u32 s70, 0\n"
  "s_cselect_b32 s17, s87, 786432\n"
  "v_mfma_f32_16x16x16_bf16 v[124:127], v[60:61], v[100:101], v[208:211]\n"
  "s_nop 1\n"
  "v_max_f32 v112, v112, v116\n"
  "v_max_f32 v113, v113, v117\n"
  "v_mfma_f32_16x16x16_bf16 v[128:131], v[56:57], v[104:105], v[212:215]\n"
  "v_max_f32 v114, v114, v118\n"
  "v_max_f32 v115, v115, v119\n"
  "v_mfma_f32_16x16x16_bf16 v[132:135], v[64:65], v[104:105], v[208:211]\n"
  "v_max_f32 v120, v120, v124\n"
  "v_max_f32 v121, v121, v125\n"
  "v_mfma_f32_16x16x16_bf16 v[188:191], v[60:61], v[108:109], v[212:215]\n"
  "v_max_f32 v122, v122, v126\n"
  "v_max3_f32 v50, v112, v113, v114\n"
  "v_mfma_f32_16x16x16_bf16 v[192:195], v[68:69], v[108:109], v[208:211]\n"
  "s_nop 1\n"
  "v_max_f32 v128, v128, v132\n"
  "v_max_f32 v129, v129, v133\n"
  "v_max_f32 v130, v130, v134\n"
  "v_max_f32 v123, v123, v127\n"
  "v_max3_f32 v54, v120, v121, v122\n"
  "v_max_f32 v131, v131, v135\n"
  "v_max3_f32 v66, v128, v129, v130\n"
  "v_max_f32 v50, v50, v115\n"
  "v_max_f32 v188, v188, v192\n"
  "v_max_f32 v189, v189, v193\n"
  "v_max_f32 v190, v190, v194\n"
  "v_max_f32 v191, v191, v195\n"
  "v_max3_f32 v70, v188, v189, v190\n"
  "v_max_f32 v54, v54, v123\n"
  "v_max_f32 v66, v66, v131\n"
  "v_max_f32 v70, v70, v191\n"
  "v_mov_b32 v51, v50\n"
  "v_mov_b32 v55, v54\n"
  "v_mov_b32 v67, v66\n"
  "v_mov_b32 v71, v70\n"
  "v_permlane16_swap_b32 v50, v51\n"
  "v_permlane16_swap_b32 v54, v55\n"
  "v_permlane16_swap_b32 v66, v67\n"
  "v_permlane16_swap_b32 v70, v71\n"
  "v_max_f32 v50, v50, v51\n"
  "v_max_f32 v54, v54, v55\n"
  "v_max_f32 v66, v66, v67\n"
  "v_max_f32 v70, v70, v71\n"
  "v_mov_b32 v51, v50\n"
  "v_mov_b32 v55, v54\n"
  "v_mov_b32 v67, v66\n"
  "v_mov_b32 v71, v70\n"
  "v_permlane32_swap_b32 v50, v51\n"
  "v_permlane32_swap_b32 v54, v55\n"
  "v_permlane32_swap_b32 v66, v67\n"
  "v_permlane32_swap_b32 v70, v71\n"
  "v_max_f32 v50, v50, v51\n"
  "v_max_f32 v54, v54, v55\n"
  "v_max_f32 v66, v66, v67\n"
  "v_max_f32 v70, v70, v71\n"
  "v_mul_f32 v50, v50, v140\n"
  "v_mul_f32 v54, v54, v140\n"
  "v_mul_f32 v66, v66, v144\n"
  "v_mul_f32 v70, v70, v144\n"
  "v_pk_fma_f32 v[112:113], v[112:113], v[140:141], v[50:51] op_sel_hi:[1,0,0] neg_lo:[0,0,1] neg_hi:[0,0,1]\n"
  "v_pk_fma_f32 v[114:115], v[114:115], v[140:141], v[50:51] op_sel_hi:[1,0,0] neg_lo:[0,0,1] neg_hi:[0,0,1]\n"
  "v_pk_fma_f32 v[120:121], v[120:121], v[140:141], v[54:55] op_sel_hi:[1,0,0] neg_lo:[0,0,1] neg_hi:[0,0,1]\n"
  "v_pk_fma_f32 v[122:123], v[122:123], v[140:141], v[54:55] op_sel_hi:[1,0,0] neg_lo:[0,0,1] neg_hi:[0,0,1]\n"
  "v_pk_fma_f32 v[128:129], v[128:129], v[144:145], v[66:67] op_sel_hi:[1,0,0] neg_lo:[0,0,1] neg_hi:[0,0,1]\n"
  "v_pk_fma_f32 v[130:131], v[130:131], v[144:145], v[66:67] op_sel_hi:[1,0,0] neg_lo:[0,0,1] neg_hi:[0,0,1]\n"
  "v_pk_fma_f32 v[188:189], v[188:189], v[144:145], v[70:71] op_sel_hi:[1,0,0] neg_lo:[0,0,1] neg_hi:[0,0,1]\n"
  "v_pk_fma_f32 v[190:191], v[190:191], v[144:145], v[70:71] op_sel_hi:[1,0,0] neg_lo:[0,0,1] neg_hi:[0,0,1]\n"
  "v_exp_f32 v112, v112\n"
  "v_exp_f32 v113, v113\n"
  "v_exp_f32 v114, v114\n"
  "v_exp_f32 v115, v115\n"
  "v_exp_f32 v120, v120\n"
  "v_exp_f32 v121, v121\n"
  "v_exp_f32 v122, v122\n"
  "v_exp_f32 v123, v123\n"
  "v_exp_f32 v128, v128\n"
  "v_exp_f32 v129, v129\n"
  "v_exp_f32 v130, v130\n"
  "v_exp_f32 v131, v131\n"
  "v_exp_f32 v188, v188\n"
  "v_exp_f32 v189, v189\n"
  "v_exp_f32 v190, v190\n"
  "v_exp_f32 v191, v191\n"
  "v_cvt_pk_bf16_f32 v98, v112, v113\n"
  "v_cvt_pk_bf16_f32 v99, v114, v115\n"
  "v_cvt_pk_bf16_f32 v102, v120, v121\n"
  "v_cvt_pk_bf16_f32 v103, v122, v123\n"
  "v_cvt_pk_bf16_f32 v106, v128, v129\n"
  "v_cvt_pk_bf16_f32 v107, v130, v131\n"
  "v_cvt_pk_bf16_f32 v110, v188, v189\n"
  "v_cvt_pk_bf16_f32 v111, v190, v191\n"
  "v_and_b32 v116, v220, v98\n"
  "v_and_b32 v117, v221, v99\n"
  "v_and_b32 v124, v220, v102\n"
  "v_and_b32 v125, v221, v103\n"
  "v_and_b32 v132, v220, v106\n"
  "v_and_b32 v133, v221, v107\n"
  "v_and_b32 v192, v220, v110\n"
  "v_and_b32 v193, v221, v111\n"
  "v_pk_fma_f32 v[72:73], v[148:149], v[20:21], v[72:73] op_sel_hi:[1,0,1] neg_lo:[1,0,0] neg_hi:[1,0,0]\n"
  "v_pk_fma_f32 v[74:75], v[150:151], v[20:21], v[74:75] op_sel_hi:[1,0,1] neg_lo:[1,0,0] neg_hi:[1,0,0]\n"
  "v_pk_fma_f32 v[76:77], v[148:149], v[20:21], v[76:77] op_sel:[0,1,0] neg_lo:[1,0,0] neg_hi:[1,0,0]\n"
  "v_pk_fma_f32 v[78:79], v[150:151], v[20:21], v[78:79] op_sel:[0,1,0] neg_lo:[1,0,0] neg_hi:[1,0,0]\n"
  "v_pk_fma_f32 v[80:81], v[156:157], v[20:21], v[80:81] op_sel_hi:[1,0,1] neg_lo:[1,0,0] neg_hi:[1,0,0]\n"
  "v_pk_fma_f32 v[82:83], v[158:159], v[20:21], v[82:83] op_sel_hi:[1,0,1] neg_lo:[1,0,0] neg_hi:[1,0,0]\n"
  "v_pk_fma_f32 v[84:85], v[156:157], v[20:21], v[84:85] op_sel:[0,1,0] neg_lo:[1,0,0] neg_hi:[1,0,0]\n"
  "v_pk_fma_f32 v[86:87], v[158:159], v[20:21], v[86:87] op_sel:[0,1,0] neg_lo:[1,0,0] neg_hi:[1,0,0]\n"
  "v_xor_b32 v118, v116, v98\n"
  "v_xor_b32 v119, v117, v99\n"
  "v_xor_b32 v126, v124, v102\n"
  "v_xor_b32 v127, v125, v103\n"
  "v_xor_b32 v134, v132, v106\n"
  "v_xor_b32 v135, v133, v107\n"
  "v_xor_b32 v194, v192, v110\n"
  "v_xor_b32 v195, v193, v111\n"
  "v_pk_mul_f32 v[72:73], v[72:73], v[152:153]\n"
  "v_pk_mul_f32 v[74:75], v[74:75], v[154:155]\n"
  "v_pk_mul_f32 v[76:77], v[76:77], v[152:153]\n"
  "v_pk_mul_f32 v[78:79], v[78:79], v[154:155]\n"
  "v_pk_mul_f32 v[80:81], v[80:81], v[160:161]\n"
  "v_pk_mul_f32 v[82:83], v[82:83], v[162:163]\n"
  "v_pk_mul_f32 v[84:85], v[84:85], v[160:161]\n"
  "v_pk_mul_f32 v[86:87], v[86:87], v[162:163]\n"
  "v_pk_fma_f32 v[88:89], v[164:165], v[20:21], v[88:89] op_sel_hi:[1,0,1] neg_lo:[1,0,0] neg_hi:[1,0,0]\n"
  "v_pk_fma_f32 v[90:91], v[166:167], v[20:21], v[90:91] op_sel_hi:[1,0,1] neg_lo:[1,0,0] neg_hi:[1,0,0]\n"
  "v_pk_fma_f32 v[92:93], v[164:165], v[20:21], v[92:93] op_sel:[0,1,0] neg_lo:[1,0,0] neg_hi:[1,0,0]\n"
  "v_pk_fma_f32 v[94:95], v[166:167], v[20:21], v[94:95] op_sel:[0,1,0] neg_lo:[1,0,0] neg_hi:[1,0,0]\n"
  "v_cvt_pk_bf16_f32 v172, v72, v73\n"
  "v_cvt_pk_bf16_f32 v173, v74, v75\n"
  "v_mfma_f32_16x16x32_bf16 v[72:75], v[30:33], v[116:119], 0\n"
  "v_cvt_pk_bf16_f32 v178, v76, v77\n"
  "v_cvt_pk_bf16_f32 v179, v78, v79\n"
  "v_mfma_f32_16x16x32_bf16 v[76:79], v[30:33], v[124:127], 0\n"
  "v_cvt_pk_bf16_f32 v174, v80, v81\n"
  "v_cvt_pk_bf16_f32 v175, v82, v83\n"
  "v_mfma_f32_16x16x32_bf16 v[80:83], v[30:33], v[132:135], 0\n"
  "v_cvt_pk_bf16_f32 v180, v84, v85\n"
  "v_cvt_pk_bf16_f32 v181, v86, v87\n"
  "v_pk_mul_f32 v[88:89], v[88:89], v[168:169]\n"
  "v_pk_mul_f32 v[90:91], v[90:91], v[170:171]\n"
  "v_pk_mul_f32 v[92:93], v[92:93], v[168:169]\n"
  "v_pk_mul_f32 v[94:95], v[94:95], v[170:171]\n"
  "s_nop 0\n"
  "v_mfma_f32_16x16x32_bf16 v[84:87], v[30:33], v[192:195], 0\n"
  "v_cvt_pk_bf16_f32 v176, v88, v89\n"
  "v_cvt_pk_bf16_f32 v177, v90, v91\n"
  "v_mfma_f32_16x16x32_bf16 v[112:115], v[172:175], v[116:119], 0\n"
  "v_cvt_pk_bf16_f32 v182, v92, v93\n"
  "v_cvt_pk_bf16_f32 v183, v94, v95\n"
  "v_mfma_f32_16x16x32_bf16 v[120:123], v[178:181], v[124:127], 0\n"
  "v_rcp_f32 v72, v72\n"
  "v_mfma_f32_16x16x32_bf16 v[128:131], v[174:177], v[132:135], 0\n"
  "v_rcp_f32 v76, v76\n"
  "v_mfma_f32_16x16x32_bf16 v[188:191], v[180:183], v[192:195], 0\n"
  "v_rcp_f32 v80, v80\n"
  "v_rcp_f32 v84, v84\n"
  "v_pk_mul_f32 v[112:113], v[112:113], v[72:73] op_sel_hi:[1,0]\n"
  "v_pk_mul_f32 v[114:115], v[114:115], v[72:73] op_sel_hi:[1,0]\n"
  "v_pk_mul_f32 v[120:121], v[120:121], v[76:77] op_sel_hi:[1,0]\n"
  "v_pk_mul_f32 v[122:123], v[122:123], v[76:77] op_sel_hi:[1,0]\n"
  "v_pk_mul_f32 v[128:129], v[128:129], v[80:81] op_sel_hi:[1,0]\n"
  "v_pk_mul_f32 v[130:131], v[130:131], v[80:81] op_sel_hi:[1,0]\n"
  "v_pk_mul_f32 v[188:189], v[188:189], v[84:85] op_sel_hi:[1,0]\n"
  "v_pk_mul_f32 v[190:191], v[190:191], v[84:85] op_sel_hi:[1,0]\n"
  "v_cvt_pk_bf16_f32 v196, v112, v113\n"
  "v_cvt_pk_bf16_f32 v197, v114, v115\n"
  "v_cvt_pk_bf16_f32 v198, v120, v121\n"
  "v_cvt_pk_bf16_f32 v199, v122, v123\n"
  "v_cvt_pk_bf16_f32 v200, v128, v129\n"
  "v_cvt_pk_bf16_f32 v201, v130, v131\n"
  "v_cvt_pk_bf16_f32 v202, v188, v189\n"
  "v_cvt_pk_bf16_f32 v203, v190, v191\n"
  "buffer_store_dwordx4 v[196:199], v1, s[20:23], s15 offen sc1\n"
  "buffer_store_dwordx4 v[200:203], v1, s[20:23], s16 offen sc1\n"
  "s_add_u32 m0, s10, 98304\n"
  "s_nop 0\n"
  "buffer_load_dwordx4 v184, s[32:35], 0 offen lds\n"
  "s_add_u32 s53, s17, 114688\n"
  "s_lshl_b32 s88, s71, 5\n"
  "s_add_u32 s88, s88, s69\n"
  "s_lshr_b32 s94, s88, 3\n"
  "s_and_b32 s95, s88, 7\n"
  "s_lshl_b32 s94, s94, 9\n"
  "s_lshl3_add_u32 s94, s95, s94\n"
  "s_add_u32 s13, s94, 49152\n"
  "v_bfe_u32 v45, v0, 3, 2\n"
  "v_lshlrev_b32 v45, 5, v45\n"
  "v_lshrrev_b32 v46, 2, v0\n"
  "s_add_u32 m0, s10, 99328\n"
  "s_nop 0\n"
  "buffer_load_dwordx4 v184, s[32:35], s73 offen lds\n"
  "v_lshl_add_u32 v45, v46, 7, v45\n"
  "v_and_b32 v46, 3, v0\n"
  "v_lshl_add_u32 v45, v46, 3, v45\n"
  "v_add_u32 v200, 98304, v45\n"
  "v_xor_b32 v201, 32, v200\n"
  "v_xor_b32 v202, 64, v200\n"
  "v_xor_b32 v203, 0x60, v200\n"
  "v_bfe_u32 v45, v0, 3, 2\n"
  "s_lshl_b32 s87, s59, 1\n"
  "v_xor_b32 v45, s87, v45\n"
  "v_lshl_add_u32 v45, v45, 5, s8\n"
  "v_lshl_add_u32 v45, v3, 10, v45\n"
  "v_bfe_u32 v46, v0, 2, 2\n"
  "s_add_u32 m0, s10, 100352\n"
  "s_nop 0\n"
  "buffer_load_dwordx4 v184, s[32:35], s75 offen lds\n"
  "v_lshl_add_u32 v45, v46, 7, v45\n"
  "v_and_b32 v46, 3, v0\n"
  "v_lshl_add_u32 v168, v46, 3, v45\n"
  "v_xor_b32 v169, 32, v168\n"
  "v_xor_b32 v170, 64, v168\n"
  "v_xor_b32 v171, 0x60, v168\n"
  "s_mul_i32 s87, s58, 1835008\n"
  "s_lshl_b32 s88, s63, 7\n"
  "s_add_u32 s87, s87, s88\n"
  "v_lshrrev_b32 v240, 3, v0\n"
  "v_lshl_add_u32 v240, v240, 12, s87\n"
  "v_lshlrev_b32 v45, 1, v3\n"
  "v_xor_b32 v45, v45, v0\n"
  "s_add_u32 m0, s10, 101376\n"
  "s_nop 0\n"
  "buffer_load_dwordx4 v184, s[32:35], s77 offen lds\n"
  "v_and_b32 v45, 7, v45\n"
  "v_lshl_add_u32 v240, v45, 4, v240\n"
  "v_lshrrev_b32 v45, 3, v0\n"
  "v_xor_b32 v45, s58, v45\n"
  "v_and_b32 v45, 3, v45\n"
  "v_lshl_add_u32 v45, v45, 5, s8\n"
  "v_lshrrev_b32 v46, 2, v0\n"
  "v_lshl_add_u32 v45, v46, 7, v45\n"
  "v_and_b32 v46, 3, v0\n"
  "v_lshl_add_u32 v234, v46, 3, v45\n"
  "v_xor_b32 v235, 32, v234\n"
  "v_xor_b32 v236, 64, v234\n"
  "v_xor_b32 v237, 0x60, v234\n"
  "s_add_u32 m0, s10, 102400\n"
  "s_nop 0\n"
  "buffer_load_dwordx4 v184, s[32:35], s79 offen lds\n"
  "s_lshl_b32 s87, s66, 14\n"
  "s_add_u32 s87, s87, 16777216\n"
  "v_add_u32 v228, s87, v1\n"
  "v_add_u32 v229, 0x4000, v228\n"
  "s_cmp_eq_u32 s66, 0\n"
  "s_cselect_b32 s89, 1073741824, 0\n"
  "s_sub_u32 s88, s89, 0x4000\n"
  "v_add_u32 v227, s88, v228\n"
  "s_lshl_b32 s87, s66, 11\n"
  "s_add_u32 s87, s87, 1835008\n"
  "v_lshl_add_u32 v230, v3, 9, s87\n"
  "v_lshl_add_u32 v230, v2, 3, v230\n"
  "s_sub_u32 s89, s89, 2048\n"
  "s_add_u32 m0, s10, 103424\n"
  "s_nop 0\n"
  "buffer_load_dwordx4 v184, s[32:35], s81 offen lds\n"
  "v_add_u32 v231, s89, v230\n"
  "s_add_u32 m0, s10, 104448\n"
  "s_nop 0\n"
  "buffer_load_dwordx4 v184, s[32:35], s83 offen lds\n"
  "buffer_load_dwordx4 v[192:195], v247, s[24:27], 0 offen\n"
  "buffer_load_dwordx4 v[196:199], v247, s[24:27], 0 offen offset:64\n"
  "s_waitcnt vmcnt(9)\n"
  "s_barrier\n"
  "buffer_store_dwordx2 v[36:37], v248, s[20:23], s11 offen sc1\n"
  "s_lshl4_add_u32 s87, s66, s62\n"
  "s_lshl_b32 s87, s87, 10\n"
  "s_add_u32 s15, s87, 4194304\n"
  "s_add_u32 s16, s15, 16384\n"
  "s_lshl4_add_u32 s87, s61, s62\n"
  "s_lshr_b32 s94, s87, 4\n"
  "s_and_b32 s95, s87, 15\n"
  "s_lshl_b32 s94, s94, 9\n"
  "s_lshl3_add_u32 s94, s95, s94\n"
  "s_add_u32 s11, s94, 81920\n"
  "buffer_store_dwordx4 v[22:25], v238, s[20:23], 0 offen sc1\n"
  "buffer_store_dwordx4 v[26:29], v239, s[20:23], 0 offen sc1\n"
  "s_lshl3_add_u32 s88, s71, s58\n"
  "s_lshl_b32 s88, s88, 15\n"
  "s_add_u32 s88, s88, 6291456\n"
  "s_or_b32 s88, s88, s70\n"
  "s_lshl_b32 s89, s60, 10\n"
  "s_add_u32 s88, s88, s89\n"
  "v_add_u32 v238, s88, v1\n"
  "v_xor_b32 v239, 0x400, v238\n"
  "s_add_u32 m0, s10, 105472\n"
  "s_nop 0\n"
  "buffer_load_dwordx4 v184, s[32:35], s85 offen lds\n"
  "s_add_u32 m0, s10, 106496\n"
  "s_nop 0\n"
  "buffer_load_dwordx4 v185, s[32:35], 0 offen lds\n"
  "s_add_u32 m0, s10, 107520\n"
  "s_nop 0\n"
  "buffer_load_dwordx4 v185, s[32:35], s73 offen lds\n"
  "s_add_u32 m0, s10, 108544\n"
  "s_nop 0\n"
  "buffer_load_dwordx4 v185, s[32:35], s75 offen lds\n"
  "s_add_u32 m0, s10, 109568\n"
  "s_nop 0\n"
  "buffer_load_dwordx4 v185, s[32:35], s77 offen lds\n"
  "s_add_u32 m0, s10, 110592\n"
  "s_nop 0\n"
  "buffer_load_dwordx4 v185, s[32:35], s79 offen lds\n"
  "s_waitcnt vmcnt(10)\n"
  "v_permlane16_swap_b32 v192, v194\n"
  "v_permlane16_swap_b32 v193, v195\n"
  "s_nop 1\n"
  "v_mfma_f32_16x16x32_bf16 v[176:179], v[192:195], v[40:43], 0\n"
  "v_mfma_f32_16x16x32_bf16 v[180:183], v[192:195], v[38:41], 0\n"
  "s_add_u32 m0, s10, 111616\n"
  "s_nop 0\n"
  "buffer_load_dwordx4 v185, s[32:35], s81 offen lds\n"
  "s_add_u32 m0, s10, 112640\n"
  "s_nop 0\n"
  "buffer_load_dwordx4 v185, s[32:35], s83 offen lds\n"
  "s_add_u32 m0, s10, 113664\n"
  "s_nop 0\n"
  "buffer_load_dwordx4 v185, s[32:35], s85 offen lds\n"
  "s_add_u32 m0, s8, 0\n"
  "s_mov_b32 s9, 0\n"
  "buffer_load_dwordx4 v232, s[36:39], s9 offen lds\n"
  "s_waitcnt vmcnt(13)\n"
  "v_permlane16_swap_b32 v196, v198\n"
  "v_permlane16_swap_b32 v197, v199\n"
  "s_nop 1\n"
  "v_mfma_f32_16x16x32_bf16 v[184:187], v[196:199], v[40:43], 0\n"
  "v_mfma_f32_16x16x32_bf16 v[188:191], v[196:199], v[38:41], 0\n"
  "s_add_u32 m0, s8, 1024\n"
  "s_mov_b32 s9, 28672\n"
  "buffer_load_dwordx4 v233, s[36:39], s9 offen lds\n"
  "s_add_u32 m0, s8, 2048\n"
  "s_mov_b32 s9, 57344\n"
  "buffer_load_dwordx4 v232, s[36:39], s9 offen lds\n"
  "s_add_u32 m0, s8, 3072\n"
  "s_mov_b32 s9, 86016\n"
  "buffer_load_dwordx4 v233, s[36:39], s9 offen lds\n"
  "s_sleep 6\n"
  "buffer_load_dwordx2 v[206:207], v249, s[20:23], 0 offen sc1\n"
  "s_add_u32 m0, s8, 4096\n"
  "s_mov_b32 s9, 114688\n"
  "buffer_load_dwordx4 v232, s[36:39], s9 offen lds\n"
  "s_add_u32 m0, s8, 5120\n"
  "s_mov_b32 s9, 143360\n"
  "buffer_load_dwordx4 v233, s[36:39], s9 offen lds\n"
  "s_waitcnt vmcnt(7)\n"
  "s_barrier\n"
  "ds_read_b64_tr_b16 v[112:113], v200 offset:0\n"
  "ds_read_b64_tr_b16 v[114:115], v200 offset:2048\n"
  "ds_read_b64_tr_b16 v[116:117], v201 offset:0\n"
  "ds_read_b64_tr_b16 v[118:119], v201 offset:2048\n"
  "ds_read_b64_tr_b16 v[120:121], v202 offset:0\n"
  "ds_read_b64_tr_b16 v[122:123], v202 offset:2048\n"
  "ds_read_b64_tr_b16 v[124:125], v203 offset:0\n"
  "s_add_u32 m0, s8, 6144\n"
  "s_mov_b32 s9, 172032\n"
  "buffer_load_dwordx4 v232, s[36:39], s9 offen lds\n"
  "ds_read_b64_tr_b16 v[126:127], v203 offset:2048\n"
  "ds_read_b64_tr_b16 v[128:129], v200 offset:4096\n"
  "ds_read_b64_tr_b16 v[130:131], v200 offset:6144\n"
  "ds_read_b64_tr_b16 v[132:133], v201 offset:4096\n"
  "ds_read_b64_tr_b16 v[134:135], v201 offset:6144\n"
  "ds_read_b64_tr_b16 v[136:137], v202 offset:4096\n"
  "ds_read_b64_tr_b16 v[138:139], v202 offset:6144\n"
  "ds_read_b64_tr_b16 v[140:141], v203 offset:4096\n"
  "ds_read_b64_tr_b16 v[142:143], v203 offset:6144\n"
  "s_add_u32 m0, s8, 7168\n"
  "s_mov_b32 s9, 200704\n"
  "buffer_load_dwordx4 v233, s[36:39], s9 offen lds\n"
  "ds_read_b64_tr_b16 v[144:145], v200 offset:8192\n"
  "ds_read_b64_tr_b16 v[146:147], v200 offset:10240\n"
  "ds_read_b64_tr_b16 v[148:149], v201 offset:8192\n"
  "ds_read_b64_tr_b16 v[150:151], v201 offset:10240\n"
  "ds_read_b64_tr_b16 v[152:153], v202 offset:8192\n"
  "ds_read_b64_tr_b16 v[154:155], v202 offset:10240\n"
  "ds_read_b64_tr_b16 v[156:157], v203 offset:8192\n"
  "ds_read_b64_tr_b16 v[158:159], v203 offset:10240\n"
  "ds_read_b64_tr_b16 v[160:161], v200 offset:12288\n"
  "s_add_u32 m0, s8, 8192\n"
  "s_mov_b32 s9, 229376\n"
  "buffer_load_dwordx4 v232, s[36:39], s9 offen lds\n"
  "ds_read_b64_tr_b16 v[162:163], v200 offset:14336\n"
  "ds_read_b64_tr_b16 v[164:165], v201 offset:12288\n"
  "ds_read_b64_tr_b16 v[166:167], v201 offset:14336\n"
  "ds_read_b64_tr_b16 a[224:225], v202 offset:12288\n"
  "ds_read_b64_tr_b16 a[226:227], v202 offset:14336\n"
  "ds_read_b64_tr_b16 a[228:229], v203 offset:12288\n"
  "ds_read_b64_tr_b16 a[230:231], v203 offset:14336\n"
  "ds_read_b64_tr_b16 a[232:233], v200 offset:16384\n"
  "ds_read_b64_tr_b16 a[234:235], v200 offset:18432\n"
  "ds_read_b64_tr_b16 a[236:237], v201 offset:16384\n"
  "s_add_u32 m0, s8, 9216\n"
  "s_mov_b32 s9, 258048\n"
  "buffer_load_dwordx4 v233, s[36:39], s9 offen lds\n"
  "ds_read_b64_tr_b16 a[238:239], v201 offset:18432\n"
  "ds_read_b64_tr_b16 a[240:241], v202 offset:16384\n"
  "ds_read_b64_tr_b16 a[242:243], v202 offset:18432\n"
  "ds_read_b64_tr_b16 a[244:245], v203 offset:16384\n"
  "ds_read_b64_tr_b16 a[246:247], v203 offset:18432\n"
  "ds_read_b64_tr_b16 a[248:249], v200 offset:20480\n"
  "ds_read_b64_tr_b16 a[250:251], v200 offset:22528\n"
  "ds_read_b64_tr_b16 a[252:253], v201 offset:20480\n"
  "ds_read_b64_tr_b16 a[254:255], v201 offset:22528\n"
  "s_add_u32 m0, s8, 10240\n"
  "s_mov_b32 s9, 286720\n"
  "buffer_load_dwordx4 v232, s[36:39], s9 offen lds\n"
  "s_waitcnt vmcnt(8) lgkmcnt(14)\n"
  "ds_read_b64_tr_b16 a[0:1], v168 offset:0\n"
  "ds_read_b64_tr_b16 a[2:3], v168 offset:512\n"
  "ds_read_b64_tr_b16 a[4:5], v169 offset:0\n"
  "ds_read_b64_tr_b16 a[6:7], v169 offset:512\n"
  "ds_read_b64_tr_b16 a[8:9], v170 offset:0\n"
  "ds_read_b64_tr_b16 a[10:11], v170 offset:512\n"
  "ds_read_b64_tr_b16 a[12:13], v171 offset:0\n"
  "ds_read_b64_tr_b16 a[14:15], v171 offset:512\n"
  "s_add_u32 m0, s8, 11264\n"
  "s_mov_b32 s9, 315392\n"
  "buffer_load_dwordx4 v233, s[36:39], s9 offen lds\n"
  "s_add_u32 m0, s8, 12288\n"
  "s_mov_b32 s9, 344064\n"
  "buffer_load_dwordx4 v232, s[36:39], s9 offen lds\n"
  "s_waitcnt vmcnt(9) lgkmcnt(0)\n"
  "v_cmp_ne_u64 vcc, s[56:57], v[206:207]\n"
  "s_cmp_eq_u64 vcc, 0\n"
  "s_cbranch_scc0 .Lfb12_%=\n"
  ".Lbk12_%=:\n"
  "buffer_load_dwordx4 v[48:51], v250, s[20:23], 0 offen sc1\n"
  "buffer_load_dwordx4 v[52:55], v250, s[20:23], 0 offen offset:1024 sc1\n"
  "buffer_load_dwordx4 v[56:59], v250, s[20:23], 0 offen offset:2048 sc1\n"
  "buffer_load_dwordx4 v[60:63], v250, s[20:23], 0 offen offset:3072 sc1\n"
  "buffer_load_dwordx4 v[64:67], v250, s[20:23], s72 offen sc1\n"
  "s_waitcnt vmcnt(10)\n"
  "ds_read_b64_tr_b16 a[16:17], v168 offset:4096\n"
  "ds_read_b64_tr_b16 a[18:19], v168 offset:4608\n"
  "buffer_load_dwordx4 v[68:71], v250, s[20:23], s72 offen offset:1024 sc1\n"
  "ds_read_b64_tr_b16 a[20:21], v169 offset:4096\n"
  "ds_read_b64_tr_b16 a[22:23], v169 offset:4608\n"
  "ds_read_b64_tr_b16 a[24:25], v170 offset:4096\n"
  "ds_read_b64_tr_b16 a[26:27], v170 offset:4608\n"
  "ds_read_b64_tr_b16 a[28:29], v171 offset:4096\n"
  "ds_read_b64_tr_b16 a[30:31], v171 offset:4608\n"
  "buffer_load_dwordx4 v[72:75], v250, s[20:23], s72 offen offset:2048 sc1\n"
  "buffer_load_dwordx4 v[76:79], v250, s[20:23], s72 offen offset:3072 sc1\n"
  "buffer_load_dwordx4 v[80:83], v250, s[20:23], s73 offen sc1\n"
  "buffer_load_dwordx4 v[84:87], v250, s[20:23], s73 offen offset:1024 sc1\n"
  "buffer_load_dwordx4 v[88:91], v250, s[20:23], s73 offen offset:2048 sc1\n"
  "s_waitcnt vmcnt(12) lgkmcnt(0)\n"
  "ds_read_b64_tr_b16 a[32:33], v168 offset:8192\n"
  "ds_read_b64_tr_b16 a[34:35], v168 offset:8704\n"
  "ds_read_b64_tr_b16 a[36:37], v169 offset:8192\n"
  "ds_read_b64_tr_b16 a[38:39], v169 offset:8704\n"
  "ds_read_b64_tr_b16 a[40:41], v170 offset:8192\n"
  "ds_read_b64_tr_b16 a[42:43], v170 offset:8704\n"
  "ds_read_b64_tr_b16 a[44:45], v171 offset:8192\n"
  "ds_read_b64_tr_b16 a[46:47], v171 offset:8704\n"
  "buffer_load_dwordx4 v[92:95], v250, s[20:23], s73 offen offset:3072 sc1\n"
  "buffer_load_dwordx4 v[96:99], v250, s[20:23], s74 offen sc1\n"
  "s_waitcnt vmcnt(12) lgkmcnt(7)\n"
  "v_mfma_f32_16x16x32_bf16 v[176:179], v[48:51], v[112:115], v[176:179]\n"
  "ds_read_b64_tr_b16 v[112:113], v202 offset:20480\n"
  "v_mfma_f32_16x16x32_bf16 v[180:183], v[48:51], v[116:119], v[180:183]\n"
  "ds_read_b64_tr_b16 v[114:115], v202 offset:22528\n"
  "v_mfma_f32_16x16x32_bf16 v[184:187], v[48:51], v[120:123], v[184:187]\n"
  "buffer_load_dwordx4 v[100:103], v250, s[20:23], s74 offen offset:1024 sc1\n"
  "ds_read_b64_tr_b16 v[116:117], v203 offset:20480\n"
  "v_mfma_f32_16x16x32_bf16 v[188:191], v[48:51], v[124:127], v[188:191]\n"
  "ds_read_b64_tr_b16 v[118:119], v203 offset:22528\n"
  "s_lshl1_add_u32 s87, s65, s59\n"
  "s_waitcnt vmcnt(12) lgkmcnt(4)\n"
  "v_mfma_f32_16x16x32_bf16 v[176:179], v[52:55], v[128:131], v[176:179]\n"
  "ds_read_b64_tr_b16 v[120:121], v200 offset:24576\n"
  "s_lshl_b32 s87, s87, 6\n"
  "v_mfma_f32_16x16x32_bf16 v[180:183], v[52:55], v[132:135], v[180:183]\n"
  "ds_read_b64_tr_b16 v[122:123], v200 offset:26624\n"
  "buffer_load_dwordx4 v[104:107], v250, s[20:23], s74 offen offset:2048 sc1\n"
  "v_mfma_f32_16x16x32_bf16 v[184:187], v[52:55], v[136:139], v[184:187]\n"
  "ds_read_b64_tr_b16 v[124:125], v201 offset:24576\n"
  "s_or_b32 s87, s87, s70\n"
  "v_mfma_f32_16x16x32_bf16 v[188:191], v[52:55], v[140:143], v[188:191]\n"
  "ds_read_b64_tr_b16 v[126:127], v201 offset:26624\n"
  "v_lshl_add_u32 v48, v3, 3, s87\n"
  "s_waitcnt vmcnt(12)\n"
  "v_mfma_f32_16x16x32_bf16 v[176:179], v[56:59], v[144:147], v[176:179]\n"
  "ds_read_b64_tr_b16 v[128:129], v202 offset:24576\n"
  "v_mfma_f32_16x16x32_bf16 v[180:183], v[56:59], v[148:151], v[180:183]\n"
  "ds_read_b64_tr_b16 v[130:131], v202 offset:26624\n"
  "buffer_load_dwordx4 v[108:111], v250, s[20:23], s74 offen offset:3072 sc1\n"
  "v_mfma_f32_16x16x32_bf16 v[184:187], v[56:59], v[152:155], v[184:187]\n"
  "ds_read_b64_tr_b16 v[132:133], v203 offset:24576\n"
  "s_and_b32 s87, s68, 3\n"
  "v_mfma_f32_16x16x32_bf16 v[188:191], v[56:59], v[156:159], v[188:191]\n"
  "ds_read_b64_tr_b16 v[134:135], v203 offset:26624\n"
  "s_lshl_b32 s87, s87, 10\n"
  "s_waitcnt vmcnt(12) lgkmcnt(8)\n"
  "v_mfma_f32_16x16x32_bf16 v[176:179], v[60:63], v[160:163], v[176:179]\n"
  "ds_read_b64_tr_b16 v[136:137], v200 offset:28672\n"
  "s_bfe_u32 s88, s68, 0x10002\n"
  "v_mfma_f32_16x16x32_bf16 v[180:183], v[60:63], v[164:167], v[180:183]\n"
  "ds_read_b64_tr_b16 v[138:139], v200 offset:30720\n"
  "s_add_u32 m0, s8, 13312\n"
  "s_mov_b32 s9, 372736\n"
  "buffer_load_dwordx4 v233, s[36:39], s9 offen lds\n"
  "v_mfma_f32_16x16x32_bf16 v[184:187], v[60:63], a[224:227], v[184:187]\n"
  "ds_read_b64_tr_b16 v[140:141], v201 offset:28672\n"
  "s_lshl_b32 s88, s88, 14\n"
  "v_mfma_f32_16x16x32_bf16 v[188:191], v[60:63], a[228:231], v[188:191]\n"
  "ds_read_b64_tr_b16 v[142:143], v201 offset:30720\n"
  "s_add_u32 s87, s87, s88\n"
  "s_waitcnt vmcnt(12) lgkmcnt(8)\n"
  "v_mfma_f32_16x16x32_bf16 v[176:179], v[64:67], a[232:235], v[176:179]\n"
  "ds_read_b64_tr_b16 v[144:145], v202 offset:28672\n"
  "s_add_u32 s87, s87, 4096\n"
  "v_mfma_f32_16x16x32_bf16 v[180:183], v[64:67], a[236:239], v[180:183]\n"
  "ds_read_b64_tr_b16 v[146:147], v202 offset:30720\n"
  "s_add_u32 m0, s8, 14336\n"
  "s_mov_b32 s9, 401408\n"
  "buffer_load_dwordx4 v232, s[36:39], s9 offen lds\n"
  "v_mfma_f32_16x16x32_bf16 v[184:187], v[64:67], a[240:243], v[184:187]\n"
  "ds_read_b64_tr_b16 v[148:149], v203 offset:28672\n"
  "v_and_b32 v45, 7, v0\n"
  "v_mfma_f32_16x16x32_bf16 v[188:191], v[64:67], a[244:247], v[188:191]\n"
  "ds_read_b64_tr_b16 v[150:151], v203 offset:30720\n"
  "v_lshl_add_u32 v45, v45, 7, s87\n"
  "s_waitcnt vmcnt(12) lgkmcnt(7)\n"
  "v_mfma_f32_16x16x32_bf16 v[176:179], v[68:71], a[248:251], v[176:179]\n"
  "ds_read_b64_tr_b16 v[152:153], v200 offset:32768\n"
  "v_and_b32 v46, 0xfffffc00, v228\n"
  "v_mfma_f32_16x16x32_bf16 v[180:183], v[68:71], a[252:255], v[180:183]\n"
  "ds_read_b64_tr_b16 v[154:155], v200 offset:34816\n"
  "s_add_u32 m0, s8, 15360\n"
  "s_mov_b32 s9, 430080\n"
  "buffer_load_dwordx4 v233, s[36:39], s9 offen lds\n"
  "v_mfma_f32_16x16x32_bf16 v[184:187], v[68:71], v[112:115], v[184:187]\n"
  "ds_read_b64_tr_b16 v[156:157], v201 offset:32768\n"
  "v_add_u32 v45, v45, v46\n"
  "v_mfma_f32_16x16x32_bf16 v[188:191], v[68:71], v[116:119], v[188:191]\n"
  "ds_read_b64_tr_b16 v[158:159], v201 offset:34816\n"
  "v_mov_b32 v250, v45\n"
  "s_waitcnt vmcnt(12) lgkmcnt(7)\n"
  "v_mfma_f32_16x16x32_bf16 v[176:179], v[72:75], v[120:123], v[176:179]\n"
  "ds_read_b64_tr_b16 v[160:161], v202 offset:32768\n"
  "v_mfma_f32_16x16x32_bf16 v[180:183], v[72:75], v[124:127], v[180:183]\n"
  "ds_read_b64_tr_b16 v[162:163], v202 offset:34816\n"
  "v_mfma_f32_16x16x32_bf16 v[184:187], v[72:75], v[128:131], v[184:187]\n"
  "ds_read_b64_tr_b16 v[164:165], v203 offset:32768\n"
  "s_add_u32 m0, s8, 0\n"
  "s_mov_b32 s9, 458752\n"
  "buffer_load_dwordx4 v232, s[36:39], s9 offen lds\n"
  "v_mfma_f32_16x16x32_bf16 v[188:191], v[72:75], v[132:135], v[188:191]\n"
  "ds_read_b64_tr_b16 v[166:167], v203 offset:34816\n"
  "s_waitcnt vmcnt(12) lgkmcnt(7)\n"
  "v_mfma_f32_16x16x32_bf16 v[176:179], v[76:79], v[136:139], v[176:179]\n"
  "ds_read_b64_tr_b16 a[224:225], v200 offset:36864\n"
  "v_mfma_f32_16x16x32_bf16 v[180:183], v[76:79], v[140:143], v[180:183]\n"
  "ds_read_b64_tr_b16 a[226:227], v200 offset:38912\n"
  "v_mfma_f32_16x16x32_bf16 v[184:187], v[76:79], v[144:147], v[184:187]\n"
  "ds_read_b64_tr_b16 a[228:229], v201 offset:36864\n"
  "ds_read_b64_tr_b16 a[230:231], v201 offset:38912\n"
  "v_mfma_f32_16x16x32_bf16 v[188:191], v[76:79], v[148:151], v[188:191]\n"
  "ds_read_b64_tr_b16 a[232:233], v202 offset:36864\n"
  "ds_read_b64_tr_b16 a[234:235], v202 offset:38912\n"
  "s_waitcnt vmcnt(11) lgkmcnt(10)\n"
  "v_mfma_f32_16x16x32_bf16 v[176:179], v[80:83], v[152:155], v[176:179]\n"
  "ds_read_b64_tr_b16 a[236:237], v203 offset:36864\n"
  "ds_read_b64_tr_b16 a[238:239], v203 offset:38912\n"
  "v_mfma_f32_16x16x32_bf16 v[180:183], v[80:83], v[156:159], v[180:183]\n"
  "ds_read_b64_tr_b16 a[240:241], v200 offset:40960\n"
  "s_add_u32 m0, s8, 1024\n"
  "s_mov_b32 s9, 487424\n"
  "buffer_load_dwordx4 v233, s[36:39], s9 offen lds\n"
  "s_waitcnt lgkmcnt(10)\n"
  "v_mfma_f32_16x16x32_bf16 v[184:187], v[80:83], v[160:163], v[184:187]\n"
  "ds_read_b64_tr_b16 a[242:243], v200 offset:43008\n"
  "ds_read_b64_tr_b16 a[244:245], v201 offset:40960\n"
  "s_waitcnt lgkmcnt(11)\n"
  "v_mfma_f32_16x16x32_bf16 v[188:191], v[80:83], v[164:167], v[188:191]\n"
  "ds_read_b64_tr_b16 a[246:247], v201 offset:43008\n"
  "ds_read_b64_tr_b16 a[248:249], v202 offset:40960\n"
  "s_waitcnt vmcnt(11) lgkmcnt(11)\n"
  "v_mfma_f32_16x16x32_bf16 v[176:179], v[84:87], a[224:227], v[176:179]\n"
  "ds_read_b64_tr_b16 a[250:251], v202 offset:43008\n"
  "ds_read_b64_tr_b16 a[252:253], v203 offset:40960\n"
  "s_waitcnt lgkmcnt(11)\n"
  "v_mfma_f32_16x16x32_bf16 v[180:183], v[84:87], a[228:231], v[180:183]\n"
  "s_add_u32 m0, s8, 2048\n"
  "s_mov_b32 s9, 516096\n"
  "buffer_load_dwordx4 v232, s[36:39], s9 offen lds\n"
  "s_waitcnt lgkmcnt(9)\n"
  "v_mfma_f32_16x16x32_bf16 v[184:187], v[84:87], a[232:235], v[184:187]\n"
  "ds_read_b64_tr_b16 a[254:255], v203 offset:43008\n"
  "ds_read_b64_tr_b16 v[112:113], v200 offset:45056\n"
  "s_waitcnt lgkmcnt(9)\n"
  "v_mfma_f32_16x16x32_bf16 v[188:191], v[84:87], a[236:239], v[188:191]\n"
  "ds_read_b64_tr_b16 v[114:115], v200 offset:47104\n"
  "ds_read_b64_tr_b16 v[116:117], v201 offset:45056\n"
  "s_waitcnt vmcnt(11) lgkmcnt(9)\n"
  "v_mfma_f32_16x16x32_bf16 v[176:179], v[88:91], a[240:243], v[176:179]\n"
  "ds_read_b64_tr_b16 v[118:119], v201 offset:47104\n"
  "ds_read_b64_tr_b16 v[120:121], v202 offset:45056\n"
  "s_waitcnt lgkmcnt(9)\n"
  "v_mfma_f32_16x16x32_bf16 v[180:183], v[88:91], a[244:247], v[180:183]\n"
  "s_add_u32 m0, s8, 3072\n"
  "s_mov_b32 s9, 544768\n"
  "buffer_load_dwordx4 v233, s[36:39], s9 offen lds\n"
  "s_waitcnt lgkmcnt(7)\n"
  "v_mfma_f32_16x16x32_bf16 v[184:187], v[88:91], a[248:251], v[184:187]\n"
  "ds_read_b64_tr_b16 v[122:123], v202 offset:47104\n"
  "ds_read_b64_tr_b16 v[124:125], v203 offset:45056\n"
  "ds_read_b64_tr_b16 v[126:127], v203 offset:47104\n"
  "ds_read_b64_tr_b16 v[128:129], v200 offset:49152\n"
  "s_waitcnt lgkmcnt(9)\n"
  "v_mfma_f32_16x16x32_bf16 v[188:191], v[88:91], a[252:255], v[188:191]\n"
  "ds_read_b64_tr_b16 v[130:131], v200 offset:51200\n"
  "ds_read_b64_tr_b16 v[132:133], v201 offset:49152\n"
  "s_waitcnt vmcnt(11) lgkmcnt(9)\n"
  "v_mfma_f32_16x16x32_bf16 v[176:179], v[92:95], v[112:115], v[176:179]\n"
  "s_add_u32 m0, s8, 4096\n"
  "s_mov_b32 s9, 573440\n"
  "buffer_load_dwordx4 v232, s[36:39], s9 offen lds\n"
  "s_waitcnt lgkmcnt(7)\n"
  "v_mfma_f32_16x16x32_bf16 v[180:183], v[92:95], v[116:119], v[180:183]\n"
  "ds_read_b64_tr_b16 v[134:135], v201 offset:51200\n"
  "ds_read_b64_tr_b16 v[136:137], v202 offset:49152\n"
  "ds_read_b64_tr_b16 v[138:139], v202 offset:51200\n"
  "ds_read_b64_tr_b16 v[140:141], v203 offset:49152\n"
  "ds_read_b64_tr_b16 v[142:143], v203 offset:51200\n"
  "s_add_u32 m0, s8, 5120\n"
  "s_mov_b32 s9, 602112\n"
  "buffer_load_dwordx4 v233, s[36:39], s9 offen lds\n"
  "s_waitcnt lgkmcnt(10)\n"
  "v_mfma_f32_16x16x32_bf16 v[184:187], v[92:95], v[120:123], v[184:187]\n"
  "ds_read_b64_tr_b16 v[144:145], v200 offset:53248\n"
  "ds_read_b64_tr_b16 v[146:147], v200 offset:55296\n"
  "s_waitcnt lgkmcnt(10)\n"
  "v_mfma_f32_16x16x32_bf16 v[188:191], v[92:95], v[124:127], v[188:191]\n"
  "ds_read_b64_tr_b16 v[148:149], v201 offset:53248\n"
  "ds_read_b64_tr_b16 v[150:151], v201 offset:55296\n"
  "s_waitcnt vmcnt(12) lgkmcnt(10)\n"
  "v_mfma_f32_16x16x32_bf16 v[176:179], v[96:99], v[128:131], v[176:179]\n"
  "ds_read_b64_tr_b16 v[152:153], v202 offset:53248\n"
  "ds_read_b64_tr_b16 v[154:155], v202 offset:55296\n"
  "ds_read_b64_tr_b16 v[156:157], v203 offset:53248\n"
  "ds_read_b64_tr_b16 v[158:159], v203 offset:55296\n"
  "s_waitcnt lgkmcnt(12)\n"
  "v_mfma_f32_16x16x32_bf16 v[180:183], v[96:99], v[132:135], v[180:183]\n"
  "ds_read_b64_tr_b16 v[160:161], v200 offset:57344\n"
  "ds_read_b64_tr_b16 v[162:163], v200 offset:59392\n"
  "s_waitcnt lgkmcnt(12)\n"
  "v_mfma_f32_16x16x32_bf16 v[184:187], v[96:99], v[136:139], v[184:187]\n"
  "s_add_u32 m0, s8, 6144\n"
  "s_mov_b32 s9, 630784\n"
  "buffer_load_dwordx4 v232, s[36:39], s9 offen lds\n"
  "s_waitcnt lgkmcnt(10)\n"
  "v_mfma_f32_16x16x32_bf16 v[188:191], v[96:99], v[140:143], v[188:191]\n"
  "ds_read_b64_tr_b16 v[164:165], v201 offset:57344\n"
  "ds_read_b64_tr_b16 v[166:167], v201 offset:59392\n"
  "s_waitcnt vmcnt(12) lgkmcnt(10)\n"
  "v_mfma_f32_16x16x32_bf16 v[176:179], v[100:103], v[144:147], v[176:179]\n"
  "ds_read_b64_tr_b16 a[224:225], v202 offset:57344\n"
  "ds_read_b64_tr_b16 a[226:227], v202 offset:59392\n"
  "s_waitcnt lgkmcnt(10)\n"
  "v_mfma_f32_16x16x32_bf16 v[180:183], v[100:103], v[148:151], v[180:183]\n"
  "ds_read_b64_tr_b16 a[228:229], v203 offset:57344\n"
  "ds_read_b64_tr_b16 a[230:231], v203 offset:59392\n"
  "s_waitcnt lgkmcnt(10)\n"
  "v_mfma_f32_16x16x32_bf16 v[184:187], v[100:103], v[152:155], v[184:187]\n"
  "s_add_u32 m0, s8, 7168\n"
  "s_mov_b32 s9, 659456\n"
  "buffer_load_dwordx4 v233, s[36:39], s9 offen lds\n"
  "s_waitcnt lgkmcnt(8)\n"
  "v_mfma_f32_16x16x32_bf16 v[188:191], v[100:103], v[156:159], v[188:191]\n"
  "ds_read_b64_tr_b16 a[232:233], v200 offset:61440\n"
  "ds_read_b64_tr_b16 a[234:235], v200 offset:63488\n"
  "s_waitcnt vmcnt(12) lgkmcnt(8)\n"
  "v_mfma_f32_16x16x32_bf16 v[176:179], v[104:107], v[160:163], v[176:179]\n"
  "ds_read_b64_tr_b16 a[236:237], v201 offset:61440\n"
  "ds_read_b64_tr_b16 a[238:239], v201 offset:63488\n"
  "s_waitcnt lgkmcnt(8)\n"
  "v_mfma_f32_16x16x32_bf16 v[180:183], v[104:107], v[164:167], v[180:183]\n"
  "ds_read_b64_tr_b16 a[240:241], v202 offset:61440\n"
  "ds_read_b64_tr_b16 a[242:243], v202 offset:63488\n"
  "s_waitcnt lgkmcnt(8)\n"
  "v_mfma_f32_16x16x32_bf16 v[184:187], v[104:107], a[224:227], v[184:187]\n"
  "s_add_u32 m0, s8, 8192\n"
  "s_mov_b32 s9, 688128\n"
  "buffer_load_dwordx4 v232, s[36:39], s9 offen lds\n"
  "s_waitcnt lgkmcnt(6)\n"
  "v_mfma_f32_16x16x32_bf16 v[188:191], v[104:107], a[228:231], v[188:191]\n"
  "ds_read_b64_tr_b16 a[244:245], v203 offset:61440\n"
  "ds_read_b64_tr_b16 a[246:247], v203 offset:63488\n"
  "s_add_u32 m0, s8, 9216\n"
  "s_mov_b32 s9, 716800\n"
  "buffer_load_dwordx4 v233, s[36:39], s9 offen lds\n"
  "s_waitcnt vmcnt(13) lgkmcnt(6)\n"
  "v_mfma_f32_16x16x32_bf16 v[176:179], v[108:111], a[232:235], v[176:179]\n"
  "s_waitcnt vmcnt(10)\n"
  "ds_read_b64_tr_b16 a[48:49], v168 offset:12288\n"
  "s_waitcnt lgkmcnt(5)\n"
  "v_mfma_f32_16x16x32_bf16 v[180:183], v[108:111], a[236:239], v[180:183]\n"
  "ds_read_b64_tr_b16 a[50:51], v168 offset:12800\n"
  "s_add_u32 m0, s8, 10240\n"
  "s_mov_b32 s9, 745472\n"
  "buffer_load_dwordx4 v232, s[36:39], s9 offen lds\n"
  "s_waitcnt lgkmcnt(4)\n"
  "v_mfma_f32_16x16x32_bf16 v[184:187], v[108:111], a[240:243], v[184:187]\n"
  "v_cvt_pk_bf16_f32 v22, v176, v177\n"
  "v_lshlrev_b32 v56, 16, v22\n"
  "v_and_b32 v57, 0xffff0000, v22\n"
  "v_cvt_pk_bf16_f32 v24, v180, v181\n"
  "v_lshlrev_b32 v60, 16, v24\n"
  "v_and_b32 v61, 0xffff0000, v24\n"
  "s_waitcnt lgkmcnt(2)\n"
  "v_mfma_f32_16x16x32_bf16 v[188:191], v[108:111], a[244:247], v[188:191]\n"
  "v_cvt_pk_bf16_f32 v26, v184, v185\n"
  "v_cvt_pk_bf16_f32 v23, v178, v179\n"
  "v_pk_mul_f32 v[76:77], v[56:57], v[56:57]\n"
  "v_cvt_pk_bf16_f32 v27, v186, v187\n"
  "v_lshlrev_b32 v64, 16, v26\n"
  "v_and_b32 v65, 0xffff0000, v26\n"
  "v_pk_fma_f32 v[76:77], v[60:61], v[60:61], v[76:77]\n"
  "v_lshlrev_b32 v58, 16, v23\n"
  "v_cvt_pk_bf16_f32 v28, v188, v189\n"
  "v_cvt_pk_bf16_f32 v29, v190, v191\n"
  "v_lshlrev_b32 v68, 16, v28\n"
  "v_and_b32 v69, 0xffff0000, v28\n"
  "v_and_b32 v59, 0xffff0000, v23\n"
  "v_cvt_pk_bf16_f32 v25, v182, v183\n"
  "v_lshlrev_b32 v66, 16, v27\n"
  "v_and_b32 v67, 0xffff0000, v27\n"
  "v_lshlrev_b32 v70, 16, v29\n"
  "v_and_b32 v71, 0xffff0000, v29\n"
  "v_pk_add_f32 v[72:73], v[56:57], v[60:61]\n"
  "v_pk_add_f32 v[88:89], v[64:65], v[68:69]\n"
  "v_pk_fma_f32 v[76:77], v[64:65], v[64:65], v[76:77]\n"
  "v_lshlrev_b32 v62, 16, v25\n"
  "v_and_b32 v63, 0xffff0000, v25\n"
  "v_pk_add_f32 v[72:73], v[72:73], v[88:89]\n"
  "v_pk_fma_f32 v[76:77], v[68:69], v[68:69], v[76:77]\n"
  "v_pk_add_f32 v[90:91], v[66:67], v[70:71]\n"
  "v_pk_mul_f32 v[78:79], v[58:59], v[58:59]\n"
  "v_pk_add_f32 v[74:75], v[58:59], v[62:63]\n"
  "v_pk_fma_f32 v[78:79], v[62:63], v[62:63], v[78:79]\n"
  "v_pk_add_f32 v[74:75], v[74:75], v[90:91]\n"
  "v_pk_fma_f32 v[78:79], v[66:67], v[66:67], v[78:79]\n"
  "v_pk_fma_f32 v[78:79], v[70:71], v[70:71], v[78:79]\n"
  "s_add_u32 m0, s8, 11264\n"
  "s_mov_b32 s9, 774144\n"
  "buffer_load_dwordx4 v233, s[36:39], s9 offen lds\n"
  "v_add_f32_dpp v88, v72, v72 row_ror:8 row_mask:0xf bank_mask:0x3\n"
  "v_add_f32_dpp v90, v76, v76 row_ror:8 row_mask:0xf bank_mask:0x3\n"
  "v_add_f32_dpp v89, v73, v73 row_ror:8 row_mask:0xf bank_mask:0x3\n"
  "v_add_f32_dpp v88, v74, v74 row_ror:8 row_mask:0xf bank_mask:0xc\n"
  "v_add_f32_dpp v90, v78, v78 row_ror:8 row_mask:0xf bank_mask:0xc\n"
  "v_add_f32_dpp v91, v77, v77 row_ror:8 row_mask:0xf bank_mask:0x3\n"
  "v_add_f32_dpp v89, v75, v75 row_ror:8 row_mask:0xf bank_mask:0xc\n"
  "v_add_f32_dpp v92, v88, v88 row_shl:4 row_mask:0xf bank_mask:0x5\n"
  "v_add_f32_dpp v91, v79, v79 row_ror:8 row_mask:0xf bank_mask:0xc\n"
  "buffer_load_dwordx2 v[50:51], v48, s[44:47], 0 offen\n"
  "v_add_f32_dpp v93, v90, v90 row_shl:4 row_mask:0xf bank_mask:0x5\n"
  "v_add_f32_dpp v92, v89, v89 row_shr:4 row_mask:0xf bank_mask:0xa\n"
  "ds_read_b64_tr_b16 a[52:53], v169 offset:12288\n"
  "v_add_f32_dpp v93, v91, v91 row_shr:4 row_mask:0xf bank_mask:0xa\n"
  "v_add_f32_dpp v92, v92, v92 quad_perm:[1,0,3,2] row_mask:0xf bank_mask:0xf\n"
  "ds_read_b64_tr_b16 a[54:55], v169 offset:12800\n"
  "v_add_f32_dpp v93, v93, v93 quad_perm:[1,0,3,2] row_mask:0xf bank_mask:0xf\n"
  "v_add_f32_dpp v92, v92, v92 quad_perm:[2,3,0,1] row_mask:0xf bank_mask:0xf\n"
  "v_min_u32 v92, -2, v92\n"
  "v_add_f32_dpp v93, v93, v93 quad_perm:[2,3,0,1] row_mask:0xf bank_mask:0xf\n"
  "v_min_u32 v93, -2, v93\n"
  "buffer_store_dwordx2 v[92:93], v251, s[20:23], 0 offen sc1\n"
  "ds_read_b64_tr_b16 a[56:57], v170 offset:12288\n"
  "ds_read_b64_tr_b16 a[58:59], v170 offset:12800\n"
  "ds_read_b64_tr_b16 a[60:61], v171 offset:12288\n"
  "ds_read_b64_tr_b16 a[62:63], v171 offset:12800\n"
  "s_waitcnt vmcnt(10) lgkmcnt(6)\n"
  "ds_read_b64_tr_b16 a[64:65], v168 offset:0\n"
  "buffer_load_dwordx2 v[54:55], v48, s[44:47], 0 offen offset:32\n"
  "ds_read_b64_tr_b16 a[66:67], v168 offset:512\n"
  "ds_read_b64_tr_b16 a[68:69], v169 offset:0\n"
  "v_add_u32 v251, 131072, v238\n"
  "ds_read_b64_tr_b16 a[70:71], v169 offset:512\n"
  "s_mul_i32 s87, s62, 98304\n"
  "ds_read_b64_tr_b16 a[72:73], v170 offset:0\n"
  "s_add_u32 s87, s87, s8\n"
  "ds_read_b64_tr_b16 a[74:75], v170 offset:512\n"
  "s_add_u32 s87, s87, 14680064\n"
  "ds_read_b64_tr_b16 a[76:77], v171 offset:0\n"
  "v_add_u32 v187, s87, v1\n"
  "ds_read_b64_tr_b16 a[78:79], v171 offset:512\n"
  "s_lshl_b32 s87, s58, 17\n"
  "s_lshl_b32 s88, s64, 7\n"
  "s_add_u32 s87, s87, s88\n"
  "v_lshrrev_b32 v184, 3, v0\n"
  "s_waitcnt lgkmcnt(8)\n"
  "s_add_u32 m0, s8, 12288\n"
  "s_mov_b32 s9, 802816\n"
  "buffer_load_dwordx4 v232, s[36:39], s9 offen lds\n"
  "s_add_u32 m0, s8, 13312\n"
  "s_mov_b32 s9, 831488\n"
  "buffer_load_dwordx4 v233, s[36:39], s9 offen lds\n"
  "v_lshl_add_u32 v184, v184, 10, s87\n"
  "v_lshlrev_b32 v45, 1, v3\n"
  "v_xor_b32 v45, v45, v0\n"
  "v_and_b32 v45, 7, v45\n"
  "v_lshl_add_u32 v184, v45, 4, v184\n"
  "v_add_u32 v185, 0x10000, v184\n"
  "s_add_u32 m0, s8, 14336\n"
  "s_mov_b32 s9, 860160\n"
  "buffer_load_dwordx4 v232, s[36:39], s9 offen lds\n"
  "s_waitcnt vmcnt(10) lgkmcnt(0)\n"
  "ds_read_b64_tr_b16 a[80:81], v168 offset:4096\n"
  "s_add_u32 m0, s8, 15360\n"
  "s_mov_b32 s9, 888832\n"
  "buffer_load_dwordx4 v233, s[36:39], s9 offen lds\n"
  "ds_read_b64_tr_b16 a[82:83], v168 offset:4608\n"
  "ds_read_b64_tr_b16 a[84:85], v169 offset:4096\n"
  "ds_read_b64_tr_b16 a[86:87], v169 offset:4608\n"
  "ds_read_b64_tr_b16 a[88:89], v170 offset:4096\n"
  "ds_read_b64_tr_b16 a[90:91], v170 offset:4608\n"
  "ds_read_b64_tr_b16 a[92:93], v171 offset:4096\n"
  "ds_read_b64_tr_b16 a[94:95], v171 offset:4608\n"
  "s_add_u32 m0, s8, 0\n"
  "s_mov_b32 s9, 917504\n"
  "buffer_load_dwordx4 v232, s[36:39], s9 offen lds\n"
  "s_add_u32 m0, s8, 1024\n"
  "s_mov_b32 s9, 946176\n"
  "buffer_load_dwordx4 v233, s[36:39], s9 offen lds\n"
  "s_add_u32 m0, s8, 2048\n"
  "s_mov_b32 s9, 974848\n"
  "buffer_load_dwordx4 v232, s[36:39], s9 offen lds\n"
  "s_waitcnt vmcnt(10) lgkmcnt(0)\n"
  "ds_read_b64_tr_b16 a[96:97], v168 offset:8192\n"
  "s_add_u32 m0, s8, 3072\n"
  "s_mov_b32 s9, 1003520\n"
  "buffer_load_dwordx4 v233, s[36:39], s9 offen lds\n"
  "ds_read_b64_tr_b16 a[98:99], v168 offset:8704\n"
  "ds_read_b64_tr_b16 a[100:101], v169 offset:8192\n"
  "ds_read_b64_tr_b16 a[102:103], v169 offset:8704\n"
  "ds_read_b64_tr_b16 a[104:105], v170 offset:8192\n"
  "ds_read_b64_tr_b16 a[106:107], v170 offset:8704\n"
  "ds_read_b64_tr_b16 a[108:109], v171 offset:8192\n"
  "ds_read_b64_tr_b16 a[110:111], v171 offset:8704\n"
  "s_waitcnt vmcnt(10)\n"
  "v_lshlrev_b32 v80, 16, v50\n"
  "v_and_b32 v81, 0xffff0000, v50\n"
  "v_lshlrev_b32 v48, 16, v50\n"
  "v_and_b32 v49, 0xffff0000, v50\n"
  "v_lshlrev_b32 v82, 16, v51\n"
  "v_and_b32 v83, 0xffff0000, v51\n"
  "s_add_u32 m0, s8, 4096\n"
  "s_mov_b32 s9, 1032192\n"
  "buffer_load_dwordx4 v232, s[36:39], s9 offen lds\n"
  "v_lshlrev_b32 v50, 16, v51\n"
  "v_and_b32 v51, 0xffff0000, v51\n"
  "s_waitcnt vmcnt(9) lgkmcnt(7)\n"
  "v_lshlrev_b32 v84, 16, v54\n"
  "v_and_b32 v85, 0xffff0000, v54\n"
  "v_lshlrev_b32 v52, 16, v54\n"
  "v_and_b32 v53, 0xffff0000, v54\n"
  "v_lshlrev_b32 v86, 16, v55\n"
  "v_and_b32 v87, 0xffff0000, v55\n"
  "v_lshlrev_b32 v54, 16, v55\n"
  "v_and_b32 v55, 0xffff0000, v55\n"
  "s_add_u32 m0, s8, 5120\n"
  "s_mov_b32 s9, 1060864\n"
  "buffer_load_dwordx4 v233, s[36:39], s9 offen lds\n"
  "s_waitcnt vmcnt(6) lgkmcnt(0)\n"
  "ds_read_b64_tr_b16 a[112:113], v168 offset:12288\n"
  "ds_read_b64_tr_b16 a[114:115], v168 offset:12800\n"
  "ds_read_b64_tr_b16 a[116:117], v169 offset:12288\n"
  "ds_read_b64_tr_b16 a[118:119], v169 offset:12800\n"
  "ds_read_b64_tr_b16 a[120:121], v170 offset:12288\n"
  "ds_read_b64_tr_b16 a[122:123], v170 offset:12800\n"
  "ds_read_b64_tr_b16 a[124:125], v171 offset:12288\n"
  "ds_read_b64_tr_b16 a[126:127], v171 offset:12800\n"
  "buffer_load_dwordx2 v[94:95], v252, s[20:23], 0 offen sc1\n"
  "buffer_load_dwordx2 v[96:97], v252, s[20:23], 0 offen offset:128 sc1\n"
  "s_add_u32 m0, s8, 6144\n"
  "s_mov_b32 s9, 1089536\n"
  "buffer_load_dwordx4 v232, s[36:39], s9 offen lds\n"
  "s_add_u32 m0, s8, 7168\n"
  "s_mov_b32 s9, 1118208\n"
  "buffer_load_dwordx4 v233, s[36:39], s9 offen lds\n"
  "s_add_u32 m0, s8, 8192\n"
  "s_mov_b32 s9, 1146880\n"
  "buffer_load_dwordx4 v232, s[36:39], s9 offen lds\n"
  "s_waitcnt vmcnt(7) lgkmcnt(0)\n"
  "ds_read_b64_tr_b16 a[128:129], v168 offset:0\n"
  "ds_read_b64_tr_b16 a[130:131], v168 offset:512\n"
  "ds_read_b64_tr_b16 a[132:133], v169 offset:0\n"
  "s_add_u32 m0, s8, 9216\n"
  "s_mov_b32 s9, 1175552\n"
  "buffer_load_dwordx4 v233, s[36:39], s9 offen lds\n"
  "ds_read_b64_tr_b16 a[134:135], v169 offset:512\n"
  "ds_read_b64_tr_b16 a[136:137], v170 offset:0\n"
  "ds_read_b64_tr_b16 a[138:139], v170 offset:512\n"
  "ds_read_b64_tr_b16 a[140:141], v171 offset:0\n"
  "ds_read_b64_tr_b16 a[142:143], v171 offset:512\n"
  "s_add_u32 m0, s8, 10240\n"
  "s_mov_b32 s9, 1204224\n"
  "buffer_load_dwordx4 v232, s[36:39], s9 offen lds\n"
  "s_add_u32 m0, s8, 11264\n"
  "s_mov_b32 s9, 1232896\n"
  "buffer_load_dwordx4 v233, s[36:39], s9 offen lds\n"
  "s_add_u32 m0, s8, 12288\n"
  "s_mov_b32 s9, 1261568\n"
  "buffer_load_dwordx4 v232, s[36:39], s9 offen lds\n"
  "s_add_u32 m0, s8, 13312\n"
  "s_mov_b32 s9, 1290240\n"
  "buffer_load_dwordx4 v233, s[36:39], s9 offen lds\n"
  "s_add_u32 m0, s8, 14336\n"
  "s_mov_b32 s9, 1318912\n"
  "buffer_load_dwordx4 v232, s[36:39], s9 offen lds\n"
  "s_waitcnt vmcnt(9) lgkmcnt(0)\n"
  "v_max3_u32 v98, v94, v95, v96\n"
  "v_max_u32 v98, v98, v97\n"
  "v_cmp_eq_u32 vcc, -1, v98\n"
  "s_cmp_eq_u64 vcc, 0\n"
  "s_cbranch_scc0 .Lfb13_%=\n"
  ".Lbk13_%=:\n"
  "v_pk_add_f32 v[94:95], v[94:95], v[96:97]\n"
  "v_add_u32 v252, 131072, v239\n"
  "s_nop 0\n"
  "v_mfma_f32_16x16x4_f32 v[100:103], v35, v94, 0\n"
  "s_add_u32 m0, s8, 15360\n"
  "s_mov_b32 s9, 1347584\n"
  "buffer_load_dwordx4 v233, s[36:39], s9 offen lds\n"
  "v_mfma_f32_16x16x4_f32 v[104:107], v35, v95, 0\n"
  "s_nop 5\n"
  "v_mul_f32 v101, s4, v100\n"
  "v_mul_f32 v103, v101, v101\n"
  "s_nop 0\n"
  "v_mfma_f32_16x16x4_f32 v[108:111], v101, v34, 0\n"
  "v_fma_f32 v105, v104, s4, -v103\n"
  "v_max_f32 v105, 0, v105\n"
  "v_add_f32 v102, s5, v105\n"
  "v_rsq_f32 v102, v102\n"
  "s_add_u32 m0, s8, 0\n"
  "s_mov_b32 s9, 1376256\n"
  "buffer_load_dwordx4 v232, s[36:39], s9 offen lds\n"
  "v_mul_f32 v105, v105, v102\n"
  "v_fma_f32 v103, v105, v102, s5\n"
  "v_rsq_f32 v103, v103\n"
  "v_pk_add_f32 v[56:57], v[56:57], v[108:109] neg_lo:[0,1] neg_hi:[0,1]\n"
  "v_mul_f32 v102, v102, v103\n"
  "v_pk_add_f32 v[58:59], v[58:59], v[110:111] neg_lo:[0,1] neg_hi:[0,1]\n"
  "v_pk_add_f32 v[60:61], v[60:61], v[108:109] neg_lo:[0,1] neg_hi:[0,1]\n"
  "s_nop 0\n"
  "v_mfma_f32_16x16x4_f32 v[72:75], v102, v34, 0\n"
  "s_waitcnt vmcnt(9)\n"
  "ds_read_b64_tr_b16 a[144:145], v168 offset:4096\n"
  "ds_read_b64_tr_b16 a[146:147], v168 offset:4608\n"
  "ds_read_b64_tr_b16 a[148:149], v169 offset:4096\n"
  "s_add_u32 m0, s8, 1024\n"
  "s_mov_b32 s9, 1404928\n"
  "buffer_load_dwordx4 v233, s[36:39], s9 offen lds\n"
  "v_pk_add_f32 v[62:63], v[62:63], v[110:111] neg_lo:[0,1] neg_hi:[0,1]\n"
  "v_pk_add_f32 v[64:65], v[64:65], v[108:109] neg_lo:[0,1] neg_hi:[0,1]\n"
  "v_pk_add_f32 v[66:67], v[66:67], v[110:111] neg_lo:[0,1] neg_hi:[0,1]\n"
  "v_pk_add_f32 v[68:69], v[68:69], v[108:109] neg_lo:[0,1] neg_hi:[0,1]\n"
  "v_pk_add_f32 v[70:71], v[70:71], v[110:111] neg_lo:[0,1] neg_hi:[0,1]\n"
  "v_pk_mul_f32 v[56:57], v[56:57], v[72:73]\n"
  "v_pk_mul_f32 v[58:59], v[58:59], v[74:75]\n"
  "v_pk_mul_f32 v[60:61], v[60:61], v[72:73]\n"
  "v_pk_mul_f32 v[62:63], v[62:63], v[74:75]\n"
  "v_pk_mul_f32 v[64:65], v[64:65], v[72:73]\n"
  "v_pk_mul_f32 v[66:67], v[66:67], v[74:75]\n"
  "v_pk_mul_f32 v[68:69], v[68:69], v[72:73]\n"
  "v_pk_mul_f32 v[70:71], v[70:71], v[74:75]\n"
  "v_cvt_pk_bf16_f32 v88, v56, v57\n"
  "v_cvt_pk_bf16_f32 v89, v58, v59\n"
  "v_cvt_pk_bf16_f32 v90, v60, v61\n"
  "v_cvt_pk_bf16_f32 v91, v62, v63\n"
  "v_cvt_pk_bf16_f32 v92, v64, v65\n"
  "v_cvt_pk_bf16_f32 v93, v66, v67\n"
  "v_cvt_pk_bf16_f32 v94, v68, v69\n"
  "v_cvt_pk_bf16_f32 v95, v70, v71\n"
  "v_permlane16_swap_b32 v88, v90\n"
  "v_permlane16_swap_b32 v89, v91\n"
  "v_permlane16_swap_b32 v92, v94\n"
  "v_permlane16_swap_b32 v93, v95\n"
  "buffer_store_dwordx4 v[88:91], v245, s[20:23], 0 offen sc1\n"
  "ds_read_b64_tr_b16 a[150:151], v169 offset:4608\n"
  "ds_read_b64_tr_b16 a[152:153], v170 offset:4096\n"
  "ds_read_b64_tr_b16 a[154:155], v170 offset:4608\n"
  "buffer_store_dwordx4 v[92:95], v245, s[20:23], s52 offen sc1\n"
  "ds_read_b64_tr_b16 a[156:157], v171 offset:4096\n"
  "ds_read_b64_tr_b16 a[158:159], v171 offset:4608\n"
  "s_lshl3_add_u32 s87, s67, s64\n"
  "s_add_u32 s18, s18, 2097152\n"
  "s_add_u32 s19, s19, 2097152\n"
  "s_lshl3_add_u32 s88, s63, s64\n"
  "s_lshr_b32 s94, s88, 3\n"
  "s_and_b32 s95, s88, 7\n"
  "s_lshl_b32 s94, s94, 9\n"
  "s_lshl3_add_u32 s94, s95, s94\n"
  "s_add_u32 s14, s94, 65536\n"
  "s_lshr_b32 s94, s64, 1\n"
  "s_lshl2_add_u32 s94, s67, s94\n"
  "s_and_b32 s95, s64, 1\n"
  "s_add_u32 m0, s8, 2048\n"
  "s_mov_b32 s9, 1433600\n"
  "buffer_load_dwordx4 v232, s[36:39], s9 offen lds\n"
  "s_lshl2_add_u32 s94, s94, s95\n"
  "s_lshl_b32 s94, s94, 7\n"
  "s_add_u32 s87, s94, 1835008\n"
  "v_lshl_add_u32 v245, v2, 3, s87\n"
  "v_bfe_u32 v45, v0, 4, 1\n"
  "v_lshl_add_u32 v245, v45, 2, v245\n"
  "v_lshrrev_b32 v45, 5, v0\n"
  "v_lshl_add_u32 v245, v45, 30, v245\n"
  "s_add_u32 m0, s8, 3072\n"
  "s_mov_b32 s9, 1462272\n"
  "buffer_load_dwordx4 v233, s[36:39], s9 offen lds\n"
  "s_waitcnt vmcnt(10) lgkmcnt(0)\n"
  "ds_read_b64_tr_b16 a[160:161], v168 offset:8192\n"
  "ds_read_b64_tr_b16 a[162:163], v168 offset:8704\n"
  "ds_read_b64_tr_b16 a[164:165], v169 offset:8192\n"
  "ds_read_b64_tr_b16 a[166:167], v169 offset:8704\n"
  "ds_read_b64_tr_b16 a[168:169], v170 offset:8192\n"
  "ds_read_b64_tr_b16 a[170:171], v170 offset:8704\n"
  "ds_read_b64_tr_b16 a[172:173], v171 offset:8192\n"
  "ds_read_b64_tr_b16 a[174:175], v171 offset:8704\n"
  "s_add_u32 m0, s8, 4096\n"
  "s_mov_b32 s9, 1490944\n"
  "buffer_load_dwordx4 v232, s[36:39], s9 offen lds\n"
  "s_add_u32 m0, s8, 5120\n"
  "s_mov_b32 s9, 1519616\n"
  "buffer_load_dwordx4 v233, s[36:39], s9 offen lds\n"
  "s_add_u32 m0, s8, 6144\n"
  "s_mov_b32 s9, 1548288\n"
  "buffer_load_dwordx4 v232, s[36:39], s9 offen lds\n"
  "s_waitcnt vmcnt(9) lgkmcnt(0)\n"
  "ds_read_b64_tr_b16 a[176:177], v168 offset:12288\n"
  "ds_read_b64_tr_b16 a[178:179], v168 offset:12800\n"
  "ds_read_b64_tr_b16 a[180:181], v169 offset:12288\n"
  "ds_read_b64_tr_b16 a[182:183], v169 offset:12800\n"
  "ds_read_b64_tr_b16 a[184:185], v170 offset:12288\n"
  "ds_read_b64_tr_b16 a[186:187], v170 offset:12800\n"
  "ds_read_b64_tr_b16 a[188:189], v171 offset:12288\n"
  "ds_read_b64_tr_b16 a[190:191], v171 offset:12800\n"
  "s_waitcnt vmcnt(5) lgkmcnt(0)\n"
  "s_barrier\n"
  "buffer_store_dwordx2 v[36:37], v248, s[20:23], s12 offen sc1\n"
  "s_add_u32 m0, s8, 7168\n"
  "s_mov_b32 s9, 1576960\n"
  "buffer_load_dwordx4 v233, s[36:39], s9 offen lds\n"
  "s_add_u32 m0, s8, 8192\n"
  "s_mov_b32 s9, 1605632\n"
  "buffer_load_dwordx4 v232, s[36:39], s9 offen lds\n"
  "s_add_u32 m0, s8, 9216\n"
  "s_mov_b32 s9, 1634304\n"
  "buffer_load_dwordx4 v233, s[36:39], s9 offen lds\n"
  "s_add_u32 m0, s8, 10240\n"
  "s_mov_b32 s9, 1662976\n"
  "buffer_load_dwordx4 v232, s[36:39], s9 offen lds\n"
  "s_waitcnt vmcnt(8)\n"
  "ds_read_b64_tr_b16 a[192:193], v168 offset:0\n"
  "ds_read_b64_tr_b16 a[194:195], v168 offset:512\n"
  "ds_read_b64_tr_b16 a[196:197], v169 offset:0\n"
  "ds_read_b64_tr_b16 a[198:199], v169 offset:512\n"
  "ds_read_b64_tr_b16 a[200:201], v170 offset:0\n"
  "ds_read_b64_tr_b16 a[202:203], v170 offset:512\n"
  "ds_read_b64_tr_b16 a[204:205], v171 offset:0\n"
  "ds_read_b64_tr_b16 a[206:207], v171 offset:512\n"
  "s_add_u32 m0, s8, 11264\n"
  "s_mov_b32 s9, 1691648\n"
  "buffer_load_dwordx4 v233, s[36:39], s9 offen lds\n"
  "s_add_u32 m0, s8, 12288\n"
  "s_mov_b32 s9, 1720320\n"
  "buffer_load_dwordx4 v232, s[36:39], s9 offen lds\n"
  "s_add_u32 m0, s8, 13312\n"
  "s_mov_b32 s9, 1748992\n"
  "buffer_load_dwordx4 v233, s[36:39], s9 offen lds\n"
  "s_add_u32 m0, s8, 14336\n"
  "s_mov_b32 s9, 1777664\n"
  "buffer_load_dwordx4 v232, s[36:39], s9 offen lds\n"
  "s_add_u32 m0, s8, 15360\n"
  "s_mov_b32 s9, 1806336\n"
  "buffer_load_dwordx4 v233, s[36:39], s9 offen lds\n"
  "s_lshl2_add_u32 s88, s64, s58\n"
  "s_lshl_b32 s89, s88, 9\n"
  "s_add_u32 s89, s89, 49152\n"
  "v_min_u32 v247, 6, v0\n"
  "v_lshl_add_u32 v247, v247, 3, s89\n"
  "s_lshl3_add_u32 s88, s64, s58\n"
  "s_mul_i32 s88, s88, 28672\n"
  "s_add_u32 s88, s88, 10485760\n"
  "v_add_u32 v232, s88, v1\n"
  "v_add_u32 v233, 114688, v232\n"
  "s_waitcnt lgkmcnt(0)\n"
  "s_add_u32 m0, s8, 0\n"
  "s_mov_b32 s9, 1703936\n"
  "buffer_load_dwordx4 v240, s[40:43], s9 offen lds\n"
  "s_waitcnt vmcnt(9)\n"
  "ds_read_b64_tr_b16 a[208:209], v168 offset:4096\n"
  "ds_read_b64_tr_b16 a[210:211], v168 offset:4608\n"
  "ds_read_b64_tr_b16 a[212:213], v169 offset:4096\n"
  "ds_read_b64_tr_b16 a[214:215], v169 offset:4608\n"
  "ds_read_b64_tr_b16 a[216:217], v170 offset:4096\n"
  "ds_read_b64_tr_b16 a[218:219], v170 offset:4608\n"
  "s_add_u32 m0, s8, 1024\n"
  "s_mov_b32 s9, 1736704\n"
  "buffer_load_dwordx4 v240, s[40:43], s9 offen lds\n"
  "ds_read_b64_tr_b16 a[220:221], v171 offset:4096\n"
  "ds_read_b64_tr_b16 a[222:223], v171 offset:4608\n"
  "s_add_u32 m0, s8, 2048\n"
  "s_mov_b32 s9, 1769472\n"
  "buffer_load_dwordx4 v240, s[40:43], s9 offen lds\n"
  "s_add_u32 m0, s8, 3072\n"
  "s_mov_b32 s9, 1802240\n"
  "buffer_load_dwordx4 v240, s[40:43], s9 offen lds\n"
  "s_waitcnt lgkmcnt(0)\n"
  "s_add_u32 m0, s8, 4096\n"
  "s_mov_b32 s9, 0\n"
  "buffer_load_dwordx4 v240, s[40:43], s9 offen lds\n"
  "s_waitcnt vmcnt(9)\n"
  "ds_read_b64_tr_b16 a[224:225], v168 offset:8192\n"
  "ds_read_b64_tr_b16 a[226:227], v168 offset:8704\n"
  "ds_read_b64_tr_b16 a[228:229], v169 offset:8192\n"
  "ds_read_b64_tr_b16 a[230:231], v169 offset:8704\n"
  "ds_read_b64_tr_b16 a[232:233], v170 offset:8192\n"
  "ds_read_b64_tr_b16 a[234:235], v170 offset:8704\n"
  "s_add_u32 m0, s8, 5120\n"
  "s_mov_b32 s9, 32768\n"
  "buffer_load_dwordx4 v240, s[40:43], s9 offen lds\n"
  "ds_read_b64_tr_b16 a[236:237], v171 offset:8192\n"
  "ds_read_b64_tr_b16 a[238:239], v171 offset:8704\n"
  "s_add_u32 m0, s8, 6144\n"
  "s_mov_b32 s9, 65536\n"
  "buffer_load_dwordx4 v240, s[40:43], s9 offen lds\n"
  "s_waitcnt vmcnt(7) lgkmcnt(0)\n"
  "ds_read_b64_tr_b16 a[240:241], v168 offset:12288\n"
  "ds_read_b64_tr_b16 a[242:243], v168 offset:12800\n"
  "ds_read_b64_tr_b16 a[244:245], v169 offset:12288\n"
  "ds_read_b64_tr_b16 a[246:247], v169 offset:12800\n"
  "ds_read_b64_tr_b16 a[248:249], v170 offset:12288\n"
  "ds_read_b64_tr_b16 a[250:251], v170 offset:12800\n"
  "ds_read_b64_tr_b16 a[252:253], v171 offset:12288\n"
  "ds_read_b64_tr_b16 a[254:255], v171 offset:12800\n"
  "s_sleep 2\n"
  "buffer_load_dwordx2 v[206:207], v246, s[20:23], 0 offen sc1\n"
  "s_add_u32 m0, s8, 7168\n"
  "s_mov_b32 s9, 98304\n"
  "buffer_load_dwordx4 v240, s[40:43], s9 offen lds\n"
  "s_add_u32 m0, s8, 8192\n"
  "s_mov_b32 s9, 131072\n"
  "buffer_load_dwordx4 v240, s[40:43], s9 offen lds\n"
  "s_add_u32 m0, s8, 9216\n"
  "s_mov_b32 s9, 163840\n"
  "buffer_load_dwordx4 v240, s[40:43], s9 offen lds\n"
  "s_waitcnt vmcnt(7) lgkmcnt(0)\n"
  "ds_read_b64_tr_b16 v[176:177], v234 offset:0\n"
  "ds_read_b64_tr_b16 v[178:179], v234 offset:2048\n"
  "ds_read_b64_tr_b16 v[200:201], v235 offset:0\n"
  "ds_read_b64_tr_b16 v[202:203], v235 offset:2048\n"
  "ds_read_b64_tr_b16 v[196:197], v236 offset:0\n"
  "ds_read_b64_tr_b16 v[198:199], v236 offset:2048\n"
  "ds_read_b64_tr_b16 v[192:193], v237 offset:0\n"
  "ds_read_b64_tr_b16 v[194:195], v237 offset:2048\n"
  "s_add_u32 m0, s8, 10240\n"
  "s_mov_b32 s9, 196608\n"
  "buffer_load_dwordx4 v240, s[40:43], s9 offen lds\n"
  "s_add_u32 m0, s8, 11264\n"
  "s_mov_b32 s9, 229376\n"
  "buffer_load_dwordx4 v240, s[40:43], s9 offen lds\n"
  "s_add_u32 m0, s8, 12288\n"
  "s_mov_b32 s9, 262144\n"
  "buffer_load_dwordx4 v240, s[40:43], s9 offen lds\n"
  "s_add_u32 m0, s8, 13312\n"
  "s_mov_b32 s9, 294912\n"
  "buffer_load_dwordx4 v240, s[40:43], s9 offen lds\n"
  "s_add_u32 m0, s8, 14336\n"
  "s_mov_b32 s9, 327680\n"
  "buffer_load_dwordx4 v240, s[40:43], s9 offen lds\n"
  "s_add_u32 m0, s8, 15360\n"
  "s_mov_b32 s9, 360448\n"
  "buffer_load_dwordx4 v240, s[40:43], s9 offen lds\n"
  "s_waitcnt vmcnt(9) lgkmcnt(0)\n"
  "v_cmp_ne_u64 vcc, s[56:57], v[206:207]\n"
  "s_cmp_eq_u64 vcc, 0\n"
  "s_cbranch_scc0 .Lfb14_%=\n"
  ".Lbk14_%=:\n"
  "s_lshl2_add_u32 s88, s71, s58\n"
  "s_lshl_b32 s89, s88, 9\n"
  "s_add_u32 s89, s89, 98304\n"
  "v_and_b32 v246, 7, v0\n"
  "v_lshl_add_u32 v246, v246, 3, s89\n"
  "buffer_load_dwordx4 v[112:115], v238, s[20:23], 0 offen sc1\n"
  "buffer_load_dwordx4 v[116:119], v239, s[20:23], 0 offen sc1\n"
  "buffer_load_dwordx4 v[120:123], v238, s[20:23], 0 offen offset:2048 sc1\n"
  "buffer_load_dwordx4 v[124:127], v239, s[20:23], 0 offen offset:2048 sc1\n"
  "buffer_load_dwordx4 v[128:131], v238, s[20:23], s72 offen sc1\n"
  "buffer_load_dwordx4 v[132:135], v239, s[20:23], s72 offen sc1\n"
  "buffer_load_dwordx4 v[136:139], v238, s[20:23], s72 offen offset:2048 sc1\n"
  "buffer_load_dwordx4 v[140:143], v239, s[20:23], s72 offen offset:2048 sc1\n"
  "buffer_load_dwordx4 v[144:147], v238, s[20:23], s73 offen sc1\n"
  "buffer_load_dwordx4 v[148:151], v239, s[20:23], s73 offen sc1\n"
  "buffer_load_dwordx4 v[152:155], v238, s[20:23], s73 offen offset:2048 sc1\n"
  "buffer_load_dwordx4 v[156:159], v239, s[20:23], s73 offen offset:2048 sc1\n"
  "buffer_load_dwordx4 v[160:163], v238, s[20:23], s74 offen sc1\n"
  "s_waitcnt vmcnt(12)\n"
  "v_mfma_f32_16x16x32_bf16 v[48:51], a[0:3], v[112:115], v[48:51]\n"
  "v_mfma_f32_16x16x32_bf16 v[52:55], a[4:7], v[112:115], v[52:55]\n"
  "v_mfma_f32_16x16x32_bf16 v[64:67], a[8:11], v[112:115], 0\n"
  "buffer_load_dwordx4 v[164:167], v239, s[20:23], s74 offen sc1\n"
  "v_mfma_f32_16x16x32_bf16 v[68:71], a[12:15], v[112:115], 0\n"
  "s_waitcnt vmcnt(12)\n"
  "v_mfma_f32_16x16x32_bf16 v[56:59], a[0:3], v[116:119], 0\n"
  "v_mfma_f32_16x16x32_bf16 v[60:63], a[4:7], v[116:119], 0\n"
  "v_mfma_f32_16x16x32_bf16 v[72:75], a[8:11], v[116:119], 0\n"
  "buffer_load_dwordx4 v[168:171], v238, s[20:23], s74 offen offset:2048 sc1\n"
  "v_mfma_f32_16x16x32_bf16 v[76:79], a[12:15], v[116:119], 0\n"
  "s_waitcnt vmcnt(12)\n"
  "v_mfma_f32_16x16x32_bf16 v[48:51], a[16:19], v[120:123], v[48:51]\n"
  "v_mfma_f32_16x16x32_bf16 v[52:55], a[20:23], v[120:123], v[52:55]\n"
  "buffer_load_dwordx4 v[172:175], v239, s[20:23], s74 offen offset:2048 sc1\n"
  "v_mfma_f32_16x16x32_bf16 v[64:67], a[24:27], v[120:123], v[64:67]\n"
  "v_mfma_f32_16x16x32_bf16 v[68:71], a[28:31], v[120:123], v[68:71]\n"
  "s_waitcnt vmcnt(12)\n"
  "v_mfma_f32_16x16x32_bf16 v[56:59], a[16:19], v[124:127], v[56:59]\n"
  "v_mfma_f32_16x16x32_bf16 v[60:63], a[20:23], v[124:127], v[60:63]\n"
  "buffer_load_dwordx4 v[180:183], v238, s[20:23], s75 offen sc1\n"
  "v_mfma_f32_16x16x32_bf16 v[72:75], a[24:27], v[124:127], v[72:75]\n"
  "v_mfma_f32_16x16x32_bf16 v[76:79], a[28:31], v[124:127], v[76:79]\n"
  "s_waitcnt vmcnt(12)\n"
  "v_mfma_f32_16x16x32_bf16 v[48:51], a[32:35], v[128:131], v[48:51]\n"
  "v_mfma_f32_16x16x32_bf16 v[52:55], a[36:39], v[128:131], v[52:55]\n"
  "buffer_load_dwordx4 v[188:191], v239, s[20:23], s75 offen sc1\n"
  "v_mfma_f32_16x16x32_bf16 v[64:67], a[40:43], v[128:131], v[64:67]\n"
  "v_mfma_f32_16x16x32_bf16 v[68:71], a[44:47], v[128:131], v[68:71]\n"
  "s_waitcnt vmcnt(12)\n"
  "v_mfma_f32_16x16x32_bf16 v[56:59], a[32:35], v[132:135], v[56:59]\n"
  "v_mfma_f32_16x16x32_bf16 v[60:63], a[36:39], v[132:135], v[60:63]\n"
  "buffer_load_dwordx4 v[112:115], v238, s[20:23], s75 offen offset:2048 sc1\n"
  "v_mfma_f32_16x16x32_bf16 v[72:75], a[40:43], v[132:135], v[72:75]\n"
  "v_mfma_f32_16x16x32_bf16 v[76:79], a[44:47], v[132:135], v[76:79]\n"
  "s_waitcnt vmcnt(12)\n"
  "v_mfma_f32_16x16x32_bf16 v[48:51], a[48:51], v[136:139], v[48:51]\n"
  "buffer_load_dwordx4 v[116:119], v239, s[20:23], s75 offen offset:2048 sc1\n"
  "v_mfma_f32_16x16x32_bf16 v[52:55], a[52:55], v[136:139], v[52:55]\n"
  "v_mfma_f32_16x16x32_bf16 v[64:67], a[56:59], v[136:139], v[64:67]\n"
  "v_mfma_f32_16x16x32_bf16 v[68:71], a[60:63], v[136:139], v[68:71]\n"
  "s_waitcnt vmcnt(12)\n"
  "v_mfma_f32_16x16x32_bf16 v[56:59], a[48:51], v[140:143], v[56:59]\n"
  "buffer_load_dwordx4 v[120:123], v238, s[20:23], s76 offen sc1\n"
  "v_mfma_f32_16x16x32_bf16 v[60:63], a[52:55], v[140:143], v[60:63]\n"
  "v_mfma_f32_16x16x32_bf16 v[72:75], a[56:59], v[140:143], v[72:75]\n"
  "v_mfma_f32_16x16x32_bf16 v[76:79], a[60:63], v[140:143], v[76:79]\n"
  "s_waitcnt vmcnt(12)\n"
  "v_mfma_f32_16x16x32_bf16 v[48:51], a[64:67], v[144:147], v[48:51]\n"
  "buffer_load_dwordx4 v[124:127], v239, s[20:23], s76 offen sc1\n"
  "v_mfma_f32_16x16x32_bf16 v[52:55], a[68:71], v[144:147], v[52:55]\n"
  "v_mfma_f32_16x16x32_bf16 v[64:67], a[72:75], v[144:147], v[64:67]\n"
  "v_mfma_f32_16x16x32_bf16 v[68:71], a[76:79], v[144:147], v[68:71]\n"
  "s_waitcnt vmcnt(12)\n"
  "v_mfma_f32_16x16x32_bf16 v[56:59], a[64:67], v[148:151], v[56:59]\n"
  "buffer_load_dwordx4 v[128:131], v238, s[20:23], s76 offen offset:2048 sc1\n"
  "v_mfma_f32_16x16x32_bf16 v[60:63], a[68:71], v[148:151], v[60:63]\n"
  "v_mfma_f32_16x16x32_bf16 v[72:75], a[72:75], v[148:151], v[72:75]\n"
  "v_mfma_f32_16x16x32_bf16 v[76:79], a[76:79], v[148:151], v[76:79]\n"
  "s_waitcnt vmcnt(12)\n"
  "v_mfma_f32_16x16x32_bf16 v[48:51], a[80:83], v[152:155], v[48:51]\n"
  "buffer_load_dwordx4 v[132:135], v239, s[20:23], s76 offen offset:2048 sc1\n"
  "v_mfma_f32_16x16x32_bf16 v[52:55], a[84:87], v[152:155], v[52:55]\n"
  "v_mfma_f32_16x16x32_bf16 v[64:67], a[88:91], v[152:155], v[64:67]\n"
  "v_mfma_f32_16x16x32_bf16 v[68:71], a[92:95], v[152:155], v[68:71]\n"
  "buffer_load_dwordx4 v[136:139], v238, s[20:23], s77 offen sc1\n"
  "s_waitcnt vmcnt(13)\n"
  "v_mfma_f32_16x16x32_bf16 v[56:59], a[80:83], v[156:159], v[56:59]\n"
  "v_mfma_f32_16x16x32_bf16 v[60:63], a[84:87], v[156:159], v[60:63]\n"
  "v_mfma_f32_16x16x32_bf16 v[72:75], a[88:91], v[156:159], v[72:75]\n"
  "v_mfma_f32_16x16x32_bf16 v[76:79], a[92:95], v[156:159], v[76:79]\n"
  "buffer_load_dwordx4 v[140:143], v239, s[20:23], s77 offen sc1\n"
  "s_waitcnt vmcnt(13)\n"
  "v_mfma_f32_16x16x32_bf16 v[48:51], a[96:99], v[160:163], v[48:51]\n"
  "v_mfma_f32_16x16x32_bf16 v[52:55], a[100:103], v[160:163], v[52:55]\n"
  "v_mfma_f32_16x16x32_bf16 v[64:67], a[104:107], v[160:163], v[64:67]\n"
  "v_mfma_f32_16x16x32_bf16 v[68:71], a[108:111], v[160:163], v[68:71]\n"
  "buffer_load_dwordx4 v[144:147], v238, s[20:23], s77 offen offset:2048 sc1\n"
  "s_waitcnt vmcnt(13)\n"
  "v_mfma_f32_16x16x32_bf16 v[56:59], a[96:99], v[164:167], v[56:59]\n"
  "v_mfma_f32_16x16x32_bf16 v[60:63], a[100:103], v[164:167], v[60:63]\n"
  "v_mfma_f32_16x16x32_bf16 v[72:75], a[104:107], v[164:167], v[72:75]\n"
  "v_mfma_f32_16x16x32_bf16 v[76:79], a[108:111], v[164:167], v[76:79]\n"
  "buffer_load_dwordx4 v[148:151], v239, s[20:23], s77 offen offset:2048 sc1\n"
  "s_waitcnt vmcnt(13)\n"
  "v_mfma_f32_16x16x32_bf16 v[48:51], a[112:115], v[168:171], v[48:51]\n"
  "v_mfma_f32_16x16x32_bf16 v[52:55], a[116:119], v[168:171], v[52:55]\n"
  "v_mfma_f32_16x16x32_bf16 v[64:67], a[120:123], v[168:171], v[64:67]\n"
  "buffer_load_dwordx4 v[152:155], v238, s[20:23], s78 offen sc1\n"
  "v_mfma_f32_16x16x32_bf16 v[68:71], a[124:127], v[168:171], v[68:71]\n"
  "s_waitcnt vmcnt(13)\n"
  "v_mfma_f32_16x16x32_bf16 v[56:59], a[112:115], v[172:175], v[56:59]\n"
  "v_mfma_f32_16x16x32_bf16 v[60:63], a[116:119], v[172:175], v[60:63]\n"
  "v_mfma_f32_16x16x32_bf16 v[72:75], a[120:123], v[172:175], v[72:75]\n"
  "buffer_load_dwordx4 v[156:159], v239, s[20:23], s78 offen sc1\n"
  "v_mfma_f32_16x16x32_bf16 v[76:79], a[124:127], v[172:175], v[76:79]\n"
  "s_waitcnt vmcnt(13)\n"
  "v_mfma_f32_16x16x32_bf16 v[48:51], a[128:131], v[180:183], v[48:51]\n"
  "v_mfma_f32_16x16x32_bf16 v[52:55], a[132:135], v[180:183], v[52:55]\n"
  "v_mfma_f32_16x16x32_bf16 v[64:67], a[136:139], v[180:183], v[64:67]\n"
  "buffer_load_dwordx4 v[160:163], v238, s[20:23], s78 offen offset:2048 sc1\n"
  "v_mfma_f32_16x16x32_bf16 v[68:71], a[140:143], v[180:183], v[68:71]\n"
  "s_waitcnt vmcnt(13)\n"
  "v_mfma_f32_16x16x32_bf16 v[56:59], a[128:131], v[188:191], v[56:59]\n"
  "v_mfma_f32_16x16x32_bf16 v[60:63], a[132:135], v[188:191], v[60:63]\n"
  "v_mfma_f32_16x16x32_bf16 v[72:75], a[136:139], v[188:191], v[72:75]\n"
  "buffer_load_dwordx4 v[164:167], v239, s[20:23], s78 offen offset:2048 sc1\n"
  "s_lshl3_add_u32 s88, s71, s58\n"
  "v_mfma_f32_16x16x32_bf16 v[76:79], a[140:143], v[188:191], v[76:79]\n"
  "s_lshl_b32 s88, s88, 15\n"
  "s_add_u32 s88, s88, 8388608\n"
  "s_waitcnt vmcnt(13)\n"
  "v_mfma_f32_16x16x32_bf16 v[48:51], a[144:147], v[112:115], v[48:51]\n"
  "s_or_b32 s88, s88, s70\n"
  "s_lshl_b32 s89, s60, 10\n"
  "v_mfma_f32_16x16x32_bf16 v[52:55], a[148:151], v[112:115], v[52:55]\n"
  "s_add_u32 s88, s88, s89\n"
  "buffer_load_dwordx4 v[168:171], v251, s[20:23], 0 offen sc1\n"
  "v_mfma_f32_16x16x32_bf16 v[64:67], a[152:155], v[112:115], v[64:67]\n"
  "v_add_u32 v238, s88, v1\n"
  "v_xor_b32 v239, 0x400, v238\n"
  "v_mfma_f32_16x16x32_bf16 v[68:71], a[156:159], v[112:115], v[68:71]\n"
  "s_waitcnt vmcnt(13)\n"
  "v_mfma_f32_16x16x32_bf16 v[56:59], a[144:147], v[116:119], v[56:59]\n"
  "v_mfma_f32_16x16x32_bf16 v[60:63], a[148:151], v[116:119], v[60:63]\n"
  "buffer_load_dwordx4 v[172:175], v252, s[20:23], 0 offen sc1\n"
  "v_mfma_f32_16x16x32_bf16 v[72:75], a[152:155], v[116:119], v[72:75]\n"
  "v_mfma_f32_16x16x32_bf16 v[76:79], a[156:159], v[116:119], v[76:79]\n"
  "s_waitcnt vmcnt(13)\n"
  "v_mfma_f32_16x16x32_bf16 v[48:51], a[160:163], v[120:123], v[48:51]\n"
  "v_mfma_f32_16x16x32_bf16 v[52:55], a[164:167], v[120:123], v[52:55]\n"
  "buffer_load_dwordx4 v[180:183], v251, s[20:23], 0 offen offset:2048 sc1\n"
  "v_mfma_f32_16x16x32_bf16 v[64:67], a[168:171], v[120:123], v[64:67]\n"
  "v_mfma_f32_16x16x32_bf16 v[68:71], a[172:175], v[120:123], v[68:71]\n"
  "s_waitcnt vmcnt(13)\n"
  "v_mfma_f32_16x16x32_bf16 v[56:59], a[160:163], v[124:127], v[56:59]\n"
  "v_mfma_f32_16x16x32_bf16 v[60:63], a[164:167], v[124:127], v[60:63]\n"
  "buffer_load_dwordx4 v[188:191], v252, s[20:23], 0 offen offset:2048 sc1\n"
  "v_mfma_f32_16x16x32_bf16 v[72:75], a[168:171], v[124:127], v[72:75]\n"
  "v_mfma_f32_16x16x32_bf16 v[76:79], a[172:175], v[124:127], v[76:79]\n"
  "s_waitcnt vmcnt(13)\n"
  "v_mfma_f32_16x16x32_bf16 v[48:51], a[176:179], v[128:131], v[48:51]\n"
  "buffer_load_dwordx4 v[112:115], v251, s[20:23], s72 offen sc1\n"
  "v_mfma_f32_16x16x32_bf16 v[52:55], a[180:183], v[128:131], v[52:55]\n"
  "v_mfma_f32_16x16x32_bf16 v[64:67], a[184:187], v[128:131], v[64:67]\n"
  "v_mfma_f32_16x16x32_bf16 v[68:71], a[188:191], v[128:131], v[68:71]\n"
  "s_waitcnt vmcnt(13)\n"
  "v_mfma_f32_16x16x32_bf16 v[56:59], a[176:179], v[132:135], v[56:59]\n"
  "buffer_load_dwordx4 v[116:119], v252, s[20:23], s72 offen sc1\n"
  "v_mfma_f32_16x16x32_bf16 v[60:63], a[180:183], v[132:135], v[60:63]\n"
  "v_mfma_f32_16x16x32_bf16 v[72:75], a[184:187], v[132:135], v[72:75]\n"
  "v_mfma_f32_16x16x32_bf16 v[76:79], a[188:191], v[132:135], v[76:79]\n"
  "s_waitcnt vmcnt(13)\n"
  "v_mfma_f32_16x16x32_bf16 v[48:51], a[192:195], v[136:139], v[48:51]\n"
  "buffer_load_dwordx4 v[120:123], v251, s[20:23], s72 offen offset:2048 sc1\n"
  "v_mfma_f32_16x16x32_bf16 v[52:55], a[196:199], v[136:139], v[52:55]\n"
  "v_mfma_f32_16x16x32_bf16 v[64:67], a[200:203], v[136:139], v[64:67]\n"
  "v_mfma_f32_16x16x32_bf16 v[68:71], a[204:207], v[136:139], v[68:71]\n"
  "s_waitcnt vmcnt(13)\n"
  "v_mfma_f32_16x16x32_bf16 v[56:59], a[192:195], v[140:143], v[56:59]\n"
  "buffer_load_dwordx4 v[124:127], v252, s[20:23], s72 offen offset:2048 sc1\n"
  "v_mfma_f32_16x16x32_bf16 v[60:63], a[196:199], v[140:143], v[60:63]\n"
  "v_mfma_f32_16x16x32_bf16 v[72:75], a[200:203], v[140:143], v[72:75]\n"
  "v_mfma_f32_16x16x32_bf16 v[76:79], a[204:207], v[140:143], v[76:79]\n"
  "s_waitcnt vmcnt(13)\n"
  "v_mfma_f32_16x16x32_bf16 v[48:51], a[208:211], v[144:147], v[48:51]\n"
  "buffer_load_dwordx4 v[128:131], v251, s[20:23], s73 offen sc1\n"
  "v_mfma_f32_16x16x32_bf16 v[52:55], a[212:215], v[144:147], v[52:55]\n"
  "v_mfma_f32_16x16x32_bf16 v[64:67], a[216:219], v[144:147], v[64:67]\n"
  "v_mfma_f32_16x16x32_bf16 v[68:71], a[220:223], v[144:147], v[68:71]\n"
  "buffer_load_dwordx4 v[132:135], v252, s[20:23], s73 offen sc1\n"
  "s_waitcnt vmcnt(14)\n"
  "v_mfma_f32_16x16x32_bf16 v[56:59], a[208:211], v[148:151], v[56:59]\n"
  "v_mfma_f32_16x16x32_bf16 v[60:63], a[212:215], v[148:151], v[60:63]\n"
  "v_mfma_f32_16x16x32_bf16 v[72:75], a[216:219], v[148:151], v[72:75]\n"
  "v_mfma_f32_16x16x32_bf16 v[76:79], a[220:223], v[148:151], v[76:79]\n"
  "buffer_load_dwordx4 v[136:139], v251, s[20:23], s73 offen offset:2048 sc1\n"
  "s_waitcnt vmcnt(14)\n"
  "v_mfma_f32_16x16x32_bf16 v[48:51], a[224:227], v[152:155], v[48:51]\n"
  "v_mfma_f32_16x16x32_bf16 v[52:55], a[228:231], v[152:155], v[52:55]\n"
  "v_mfma_f32_16x16x32_bf16 v[64:67], a[232:235], v[152:155], v[64:67]\n"
  "v_mfma_f32_16x16x32_bf16 v[68:71], a[236:239], v[152:155], v[68:71]\n"
  "buffer_load_dwordx4 v[140:143], v252, s[20:23], s73 offen offset:2048 sc1\n"
  "s_waitcnt vmcnt(14)\n"
  "v_mfma_f32_16x16x32_bf16 v[56:59], a[224:227], v[156:159], v[56:59]\n"
  "v_mfma_f32_16x16x32_bf16 v[60:63], a[228:231], v[156:159], v[60:63]\n"
  "v_mfma_f32_16x16x32_bf16 v[72:75], a[232:235], v[156:159], v[72:75]\n"
  "v_mfma_f32_16x16x32_bf16 v[76:79], a[236:239], v[156:159], v[76:79]\n"
  "buffer_load_dwordx4 v[144:147], v251, s[20:23], s74 offen sc1\n"
  "s_waitcnt vmcnt(14)\n"
  "v_mfma_f32_16x16x32_bf16 v[48:51], a[240:243], v[160:163], v[48:51]\n"
  "v_mfma_f32_16x16x32_bf16 v[52:55], a[244:247], v[160:163], v[52:55]\n"
  "v_mfma_f32_16x16x32_bf16 v[64:67], a[248:251], v[160:163], v[64:67]\n"
  "buffer_load_dwordx4 v[148:151], v252, s[20:23], s74 offen sc1\n"
  "v_mfma_f32_16x16x32_bf16 v[68:71], a[252:255], v[160:163], v[68:71]\n"
  "s_waitcnt vmcnt(14)\n"
  "v_mfma_f32_16x16x32_bf16 v[56:59], a[240:243], v[164:167], v[56:59]\n"
  "s_nop 3\n"
  "ds_write_b64 v243, v[64:65] offset:0\n"
  "v_mfma_f32_16x16x32_bf16 v[60:63], a[244:247], v[164:167], v[60:63]\n"
  "ds_write_b64 v243, v[66:67] offset:8\n"
  "v_mfma_f32_16x16x32_bf16 v[72:75], a[248:251], v[164:167], v[72:75]\n"
  "buffer_load_dwordx4 v[152:155], v251, s[20:23], s74 offen offset:2048 sc1\n"
  "v_mfma_f32_16x16x32_bf16 v[76:79], a[252:255], v[164:167], v[76:79]\n"
  "ds_write_b64 v242, v[56:57] offset:0\n"
  "s_waitcnt vmcnt(14)\n"
  "v_mfma_f32_16x16x32_bf16 v[80:83], a[0:3], v[168:171], v[80:83]\n"
  "ds_write_b64 v242, v[58:59] offset:8\n"
  "v_mfma_f32_16x16x32_bf16 v[84:87], a[4:7], v[168:171], v[84:87]\n"
  "ds_write_b64 v242, v[60:61] offset:1024\n"
  "v_mfma_f32_16x16x32_bf16 v[96:99], a[8:11], v[168:171], 0\n"
  "buffer_load_dwordx4 v[156:159], v252, s[20:23], s74 offen offset:2048 sc1\n"
  "v_mfma_f32_16x16x32_bf16 v[100:103], a[12:15], v[168:171], 0\n"
  "ds_write_b64 v242, v[62:63] offset:1032\n"
  "s_waitcnt vmcnt(14) lgkmcnt(5)\n"
  "v_mfma_f32_16x16x32_bf16 v[88:91], a[0:3], v[172:175], 0\n"
  "ds_write_b64 v243, v[68:69] offset:1024\n"
  "v_mfma_f32_16x16x32_bf16 v[92:95], a[4:7], v[172:175], 0\n"
  "buffer_load_dwordx4 v[160:163], v251, s[20:23], s75 offen sc1\n"
  "ds_write_b64 v243, v[70:71] offset:1032\n"
  "v_mfma_f32_16x16x32_bf16 v[104:107], a[8:11], v[172:175], 0\n"
  "ds_read_b64_tr_b16 a[0:1], v234 offset:4096\n"
  "ds_write_b64 v244, v[72:73] offset:0\n"
  "v_mfma_f32_16x16x32_bf16 v[108:111], a[12:15], v[172:175], 0\n"
  "ds_read_b64_tr_b16 a[2:3], v234 offset:6144\n"
  "ds_write_b64 v244, v[74:75] offset:8\n"
  "s_waitcnt vmcnt(14) lgkmcnt(7)\n"
  "v_mfma_f32_16x16x32_bf16 v[80:83], a[16:19], v[180:183], v[80:83]\n"
  "ds_write_b64 v244, v[76:77] offset:1024\n"
  "v_mfma_f32_16x16x32_bf16 v[84:87], a[20:23], v[180:183], v[84:87]\n"
  "buffer_load_dwordx4 v[164:167], v252, s[20:23], s75 offen sc1\n"
  "ds_write_b64 v244, v[78:79] offset:1032\n"
  "v_mfma_f32_16x16x32_bf16 v[96:99], a[24:27], v[180:183], v[96:99]\n"
  "ds_read_b64_tr_b16 a[4:5], v235 offset:4096\n"
  "v_mfma_f32_16x16x32_bf16 v[100:103], a[28:31], v[180:183], v[100:103]\n"
  "ds_read_b64_tr_b16 a[6:7], v235 offset:6144\n"
  "s_waitcnt vmcnt(14) lgkmcnt(8)\n"
  "v_mfma_f32_16x16x32_bf16 v[88:91], a[16:19], v[188:191], v[88:91]\n"
  "buffer_load_dwordx4 v[168:171], v251, s[20:23], s75 offen offset:2048 sc1\n"
  "ds_read_b64_tr_b16 a[8:9], v236 offset:4096\n"
  "v_mfma_f32_16x16x32_bf16 v[92:95], a[20:23], v[188:191], v[92:95]\n"
  "ds_read_b64_tr_b16 a[10:11], v236 offset:6144\n"
  "v_mfma_f32_16x16x32_bf16 v[104:107], a[24:27], v[188:191], v[104:107]\n"
  "ds_read_b64_tr_b16 a[12:13], v237 offset:4096\n"
  "s_waitcnt lgkmcnt(5)\n"
  "s_barrier\n"
  "v_mfma_f32_16x16x32_bf16 v[108:111], a[28:31], v[188:191], v[108:111]\n"
  "ds_read_b128 v[56:59], v241 offset:0\n"
  "ds_read_b128 v[64:67], v241 offset:2048\n"
  "s_waitcnt vmcnt(14)\n"
  "v_mfma_f32_16x16x32_bf16 v[80:83], a[32:35], v[112:115], v[80:83]\n"
  "ds_read_b128 v[72:75], v241 offset:4096\n"
  "buffer_load_dwordx4 v[172:175], v252, s[20:23], s75 offen offset:2048 sc1\n"
  "v_mfma_f32_16x16x32_bf16 v[84:87], a[36:39], v[112:115], v[84:87]\n"
  "ds_read_b128 v[60:63], v241 offset:1024\n"
  "v_mfma_f32_16x16x32_bf16 v[96:99], a[40:43], v[112:115], v[96:99]\n"
  "ds_read_b128 v[68:71], v241 offset:3072\n"
  "v_mfma_f32_16x16x32_bf16 v[100:103], a[44:47], v[112:115], v[100:103]\n"
  "ds_read_b128 v[76:79], v241 offset:5120\n"
  "buffer_load_dwordx4 v[180:183], v251, s[20:23], s76 offen sc1\n"
  "s_waitcnt vmcnt(15) lgkmcnt(9)\n"
  "v_mfma_f32_16x16x32_bf16 v[88:91], a[32:35], v[116:119], v[88:91]\n"
  "ds_read_b64_tr_b16 a[14:15], v237 offset:6144\n"
  "v_mfma_f32_16x16x32_bf16 v[92:95], a[36:39], v[116:119], v[92:95]\n"
  "s_waitcnt lgkmcnt(6)\n"
  "v_add_f32 v48, v48, v56\n"
  "v_mfma_f32_16x16x32_bf16 v[104:107], a[40:43], v[116:119], v[104:107]\n"
  "v_add_f32 v49, v49, v57\n"
  "v_add_f32 v50, v50, v58\n"
  "v_mfma_f32_16x16x32_bf16 v[108:111], a[44:47], v[116:119], v[108:111]\n"
  "s_waitcnt lgkmcnt(4)\n"
  "v_add_f32 v64, v64, v72\n"
  "s_waitcnt vmcnt(14)\n"
  "v_mfma_f32_16x16x32_bf16 v[80:83], a[48:51], v[120:123], v[80:83]\n"
  "v_add_f32 v65, v65, v73\n"
  "v_add_f32 v66, v66, v74\n"
  "v_mfma_f32_16x16x32_bf16 v[84:87], a[52:55], v[120:123], v[84:87]\n"
  "v_add_f32 v51, v51, v59\n"
  "v_add_f32 v67, v67, v75\n"
  "v_mfma_f32_16x16x32_bf16 v[96:99], a[56:59], v[120:123], v[96:99]\n"
  "buffer_load_dwordx4 v[188:191], v252, s[20:23], s76 offen sc1\n"
  "s_waitcnt lgkmcnt(2)\n"
  "v_add_f32 v52, v52, v60\n"
  "v_mfma_f32_16x16x32_bf16 v[100:103], a[60:63], v[120:123], v[100:103]\n"
  "s_waitcnt lgkmcnt(1)\n"
  "v_add_f32 v68, v68, v76\n"
  "s_waitcnt vmcnt(14)\n"
  "v_mfma_f32_16x16x32_bf16 v[88:91], a[48:51], v[124:127], v[88:91]\n"
  "v_add_f32 v53, v53, v61\n"
  "v_add_f32 v69, v69, v77\n"
  "v_mfma_f32_16x16x32_bf16 v[92:95], a[52:55], v[124:127], v[92:95]\n"
  "v_add_f32 v54, v54, v62\n"
  "v_add_f32 v70, v70, v78\n"
  "v_mfma_f32_16x16x32_bf16 v[104:107], a[56:59], v[124:127], v[104:107]\n"
  "buffer_load_dwordx4 v[112:115], v251, s[20:23], s76 offen offset:2048 sc1\n"
  "v_add_f32 v55, v55, v63\n"
  "v_mfma_f32_16x16x32_bf16 v[108:111], a[60:63], v[124:127], v[108:111]\n"
  "v_add_f32 v71, v71, v79\n"
  "v_add_f32 v48, v48, v64\n"
  "s_waitcnt vmcnt(14) lgkmcnt(0)\n"
  "v_mfma_f32_16x16x32_bf16 v[80:83], a[64:67], v[128:131], v[80:83]\n"
  "v_add_f32 v49, v49, v65\n"
  "v_add_f32 v50, v50, v66\n"
  "v_mfma_f32_16x16x32_bf16 v[84:87], a[68:71], v[128:131], v[84:87]\n"
  "v_add_f32 v51, v51, v67\n"
  "buffer_load_dwordx4 v[116:119], v252, s[20:23], s76 offen offset:2048 sc1\n"
  "v_mfma_f32_16x16x32_bf16 v[96:99], a[72:75], v[128:131], v[96:99]\n"
  "v_add_f32 v52, v52, v68\n"
  "v_add_f32 v53, v53, v69\n"
  "v_mfma_f32_16x16x32_bf16 v[100:103], a[76:79], v[128:131], v[100:103]\n"
  "v_add_f32 v54, v54, v70\n"
  "v_add_f32 v55, v55, v71\n"
  "s_waitcnt vmcnt(14)\n"
  "v_mfma_f32_16x16x32_bf16 v[88:91], a[64:67], v[132:135], v[88:91]\n"
  "v_mul_f32 v56, v48, v48\n"
  "v_mul_f32 v57, v49, v49\n"
  "v_mfma_f32_16x16x32_bf16 v[92:95], a[68:71], v[132:135], v[92:95]\n"
  "buffer_load_dwordx4 v[120:123], v251, s[20:23], s77 offen sc1\n"
  "v_mul_f32 v58, v50, v50\n"
  "v_mfma_f32_16x16x32_bf16 v[104:107], a[72:75], v[132:135], v[104:107]\n"
  "v_mul_f32 v59, v51, v51\n"
  "v_mul_f32 v60, v52, v52\n"
  "v_mfma_f32_16x16x32_bf16 v[108:111], a[76:79], v[132:135], v[108:111]\n"
  "v_mul_f32 v61, v53, v53\n"
  "v_mul_f32 v62, v54, v54\n"
  "s_waitcnt vmcnt(14)\n"
  "v_mfma_f32_16x16x32_bf16 v[80:83], a[80:83], v[136:139], v[80:83]\n"
  "v_mul_f32 v63, v55, v55\n"
  "v_fma_f32 v56, v56, s7, v205\n"
  "v_mfma_f32_16x16x32_bf16 v[84:87], a[84:87], v[136:139], v[84:87]\n"
  "buffer_load_dwordx4 v[124:127], v252, s[20:23], s77 offen sc1\n"
  "v_fma_f32 v57, v57, s7, v205\n"
  "v_mfma_f32_16x16x32_bf16 v[96:99], a[88:91], v[136:139], v[96:99]\n"
  "v_fma_f32 v58, v58, s7, v205\n"
  "v_fma_f32 v59, v59, s7, v205\n"
  "v_mfma_f32_16x16x32_bf16 v[100:103], a[92:95], v[136:139], v[100:103]\n"
  "v_fma_f32 v60, v60, s7, v205\n"
  "v_fma_f32 v61, v61, s7, v205\n"
  "s_waitcnt vmcnt(14)\n"
  "v_mfma_f32_16x16x32_bf16 v[88:91], a[80:83], v[140:143], v[88:91]\n"
  "v_fma_f32 v62, v62, s7, v205\n"
  "v_fma_f32 v63, v63, s7, v205\n"
  "v_mfma_f32_16x16x32_bf16 v[92:95], a[84:87], v[140:143], v[92:95]\n"
  "buffer_load_dwordx4 v[128:131], v251, s[20:23], s77 offen offset:2048 sc1\n"
  "v_mul_f32 v56, v56, v48\n"
  "v_mfma_f32_16x16x32_bf16 v[104:107], a[88:91], v[140:143], v[104:107]\n"
  "v_mul_f32 v57, v57, v49\n"
  "v_mul_f32 v58, v58, v50\n"
  "v_mfma_f32_16x16x32_bf16 v[108:111], a[92:95], v[140:143], v[108:111]\n"
  "v_mul_f32 v59, v59, v51\n"
  "v_mul_f32 v60, v60, v52\n"
  "s_waitcnt vmcnt(14)\n"
  "v_mfma_f32_16x16x32_bf16 v[80:83], a[96:99], v[144:147], v[80:83]\n"
  "v_mul_f32 v61, v61, v53\n"
  "buffer_load_dwordx4 v[132:135], v252, s[20:23], s77 offen offset:2048 sc1\n"
  "v_mfma_f32_16x16x32_bf16 v[84:87], a[100:103], v[144:147], v[84:87]\n"
  "v_mul_f32 v62, v62, v54\n"
  "v_mul_f32 v63, v63, v55\n"
  "v_mfma_f32_16x16x32_bf16 v[96:99], a[104:107], v[144:147], v[96:99]\n"
  "v_exp_f32 v56, v56\n"
  "v_mfma_f32_16x16x32_bf16 v[100:103], a[108:111], v[144:147], v[100:103]\n"
  "v_exp_f32 v57, v57\n"
  "s_waitcnt vmcnt(14)\n"
  "v_mfma_f32_16x16x32_bf16 v[88:91], a[96:99], v[148:151], v[88:91]\n"
  "buffer_load_dwordx4 v[136:139], v251, s[20:23], s78 offen sc1\n"
  "v_exp_f32 v58, v58\n"
  "v_mfma_f32_16x16x32_bf16 v[92:95], a[100:103], v[148:151], v[92:95]\n"
  "v_exp_f32 v59, v59\n"
  "v_mfma_f32_16x16x32_bf16 v[104:107], a[104:107], v[148:151], v[104:107]\n"
  "v_exp_f32 v60, v60\n"
  "v_mfma_f32_16x16x32_bf16 v[108:111], a[108:111], v[148:151], v[108:111]\n"
  "v_exp_f32 v61, v61\n"
  "s_waitcnt vmcnt(14)\n"
  "v_mfma_f32_16x16x32_bf16 v[80:83], a[112:115], v[152:155], v[80:83]\n"
  "buffer_load_dwordx4 v[140:143], v252, s[20:23], s78 offen sc1\n"
  "v_exp_f32 v62, v62\n"
  "v_mfma_f32_16x16x32_bf16 v[84:87], a[116:119], v[152:155], v[84:87]\n"
  "v_exp_f32 v63, v63\n"
  "v_mfma_f32_16x16x32_bf16 v[96:99], a[120:123], v[152:155], v[96:99]\n"
  "v_add_f32 v56, 1.0, v56\n"
  "v_add_f32 v57, 1.0, v57\n"
  "v_mfma_f32_16x16x32_bf16 v[100:103], a[124:127], v[152:155], v[100:103]\n"
  "v_add_f32 v58, 1.0, v58\n"
  "buffer_load_dwordx4 v[144:147], v251, s[20:23], s78 offen offset:2048 sc1\n"
  "s_waitcnt vmcnt(15)\n"
  "v_mfma_f32_16x16x32_bf16 v[88:91], a[112:115], v[156:159], v[88:91]\n"
  "v_add_f32 v59, 1.0, v59\n"
  "v_add_f32 v60, 1.0, v60\n"
  "v_mfma_f32_16x16x32_bf16 v[92:95], a[116:119], v[156:159], v[92:95]\n"
  "v_add_f32 v61, 1.0, v61\n"
  "v_add_f32 v62, 1.0, v62\n"
  "v_mfma_f32_16x16x32_bf16 v[104:107], a[120:123], v[156:159], v[104:107]\n"
  "v_add_f32 v63, 1.0, v63\n"
  "v_rcp_f32 v56, v56\n"
  "v_mfma_f32_16x16x32_bf16 v[108:111], a[124:127], v[156:159], v[108:111]\n"
  "buffer_load_dwordx4 v[148:151], v252, s[20:23], s78 offen offset:2048 sc1\n"
  "v_rcp_f32 v57, v57\n"
  "s_waitcnt vmcnt(15)\n"
  "v_mfma_f32_16x16x32_bf16 v[80:83], a[128:131], v[160:163], v[80:83]\n"
  "v_rcp_f32 v58, v58\n"
  "v_mfma_f32_16x16x32_bf16 v[84:87], a[132:135], v[160:163], v[84:87]\n"
  "v_rcp_f32 v59, v59\n"
  "v_mfma_f32_16x16x32_bf16 v[96:99], a[136:139], v[160:163], v[96:99]\n"
  "v_rcp_f32 v60, v60\n"
  "v_mfma_f32_16x16x32_bf16 v[100:103], a[140:143], v[160:163], v[100:103]\n"
  "s_add_u32 m0, s8, 0\n"
  "s_mov_b32 s9, 393216\n"
  "buffer_load_dwordx4 v240, s[40:43], s9 offen lds\n"
  "s_waitcnt vmcnt(15)\n"
  "v_mfma_f32_16x16x32_bf16 v[88:91], a[128:131], v[164:167], v[88:91]\n"
  "v_rcp_f32 v61, v61\n"
  "v_mfma_f32_16x16x32_bf16 v[92:95], a[132:135], v[164:167], v[92:95]\n"
  "v_rcp_f32 v62, v62\n"
  "v_mfma_f32_16x16x32_bf16 v[104:107], a[136:139], v[164:167], v[104:107]\n"
  "v_rcp_f32 v63, v63\n"
  "v_mfma_f32_16x16x32_bf16 v[108:111], a[140:143], v[164:167], v[108:111]\n"
  "s_add_u32 m0, s8, 1024\n"
  "s_mov_b32 s9, 425984\n"
  "buffer_load_dwordx4 v240, s[40:43], s9 offen lds\n"
  "s_waitcnt vmcnt(15)\n"
  "v_mfma_f32_16x16x32_bf16 v[80:83], a[144:147], v[168:171], v[80:83]\n"
  "v_mul_f32 v56, v56, v48\n"
  "v_mul_f32 v57, v57, v49\n"
  "v_mfma_f32_16x16x32_bf16 v[84:87], a[148:151], v[168:171], v[84:87]\n"
  "v_mul_f32 v58, v58, v50\n"
  "v_mul_f32 v59, v59, v51\n"
  "v_mfma_f32_16x16x32_bf16 v[96:99], a[152:155], v[168:171], v[96:99]\n"
  "v_mul_f32 v60, v60, v52\n"
  "v_mul_f32 v61, v61, v53\n"
  "v_mfma_f32_16x16x32_bf16 v[100:103], a[156:159], v[168:171], v[100:103]\n"
  "v_mul_f32 v62, v62, v54\n"
  "v_mul_f32 v63, v63, v55\n"
  "s_waitcnt vmcnt(14)\n"
  "v_mfma_f32_16x16x32_bf16 v[88:91], a[144:147], v[172:175], v[88:91]\n"
  "s_add_u32 m0, s8, 2048\n"
  "s_mov_b32 s9, 458752\n"
  "buffer_load_dwordx4 v240, s[40:43], s9 offen lds\n"
  "v_mfma_f32_16x16x32_bf16 v[92:95], a[148:151], v[172:175], v[92:95]\n"
  "v_cvt_pk_bf16_f32 v64, v56, v57\n"
  "v_cvt_pk_bf16_f32 v65, v58, v59\n"
  "v_mfma_f32_16x16x32_bf16 v[104:107], a[152:155], v[172:175], v[104:107]\n"
  "v_cvt_pk_bf16_f32 v66, v60, v61\n"
  "v_cvt_pk_bf16_f32 v67, v62, v63\n"
  "v_mfma_f32_16x16x32_bf16 v[108:111], a[156:159], v[172:175], v[108:111]\n"
  "buffer_store_dwordx4 v[64:67], v1, s[20:23], s17 offen sc1\n"
  "ds_read_b64_tr_b16 a[16:17], v234 offset:8192\n"
  "s_waitcnt vmcnt(15)\n"
  "v_mfma_f32_16x16x32_bf16 v[80:83], a[160:163], v[180:183], v[80:83]\n"
  "ds_read_b64_tr_b16 a[18:19], v234 offset:10240\n"
  "s_add_u32 m0, s8, 3072\n"
  "s_mov_b32 s9, 491520\n"
  "buffer_load_dwordx4 v240, s[40:43], s9 offen lds\n"
  "v_mfma_f32_16x16x32_bf16 v[84:87], a[164:167], v[180:183], v[84:87]\n"
  "ds_read_b64_tr_b16 a[20:21], v235 offset:8192\n"
  "v_mov_b32 v48, v47\n"
  "v_mfma_f32_16x16x32_bf16 v[96:99], a[168:171], v[180:183], v[96:99]\n"
  "ds_read_b64_tr_b16 a[22:23], v235 offset:10240\n"
  "v_mov_b32 v49, v47\n"
  "v_mfma_f32_16x16x32_bf16 v[100:103], a[172:175], v[180:183], v[100:103]\n"
  "ds_read_b64_tr_b16 a[24:25], v236 offset:8192\n"
  "v_mov_b32 v50, v47\n"
  "s_waitcnt vmcnt(15)\n"
  "v_mfma_f32_16x16x32_bf16 v[88:91], a[160:163], v[188:191], v[88:91]\n"
  "ds_read_b64_tr_b16 a[26:27], v236 offset:10240\n"
  "v_mov_b32 v51, v47\n"
  "v_mfma_f32_16x16x32_bf16 v[92:95], a[164:167], v[188:191], v[92:95]\n"
  "s_add_u32 m0, s8, 4096\n"
  "s_mov_b32 s9, 524288\n"
  "buffer_load_dwordx4 v240, s[40:43], s9 offen lds\n"
  "v_mfma_f32_16x16x32_bf16 v[104:107], a[168:171], v[188:191], v[104:107]\n"
  "ds_read_b64_tr_b16 a[28:29], v237 offset:8192\n"
  "v_mov_b32 v52, v47\n"
  "v_mfma_f32_16x16x32_bf16 v[108:111], a[172:175], v[188:191], v[108:111]\n"
  "ds_read_b64_tr_b16 a[30:31], v237 offset:10240\n"
  "v_mov_b32 v53, v47\n"
  "s_waitcnt vmcnt(15) lgkmcnt(6)\n"
  "v_mfma_f32_16x16x32_bf16 v[80:83], a[176:179], v[112:115], v[80:83]\n"
  "ds_read_b64_tr_b16 a[32:33], v234 offset:12288\n"
  "v_mov_b32 v54, v47\n"
  "v_mfma_f32_16x16x32_bf16 v[84:87], a[180:183], v[112:115], v[84:87]\n"
  "s_add_u32 m0, s8, 5120\n"
  "s_mov_b32 s9, 557056\n"
  "buffer_load_dwordx4 v240, s[40:43], s9 offen lds\n"
  "v_mfma_f32_16x16x32_bf16 v[96:99], a[184:187], v[112:115], v[96:99]\n"
  "ds_read_b64_tr_b16 a[34:35], v234 offset:14336\n"
  "v_mov_b32 v55, v47\n"
  "v_mfma_f32_16x16x32_bf16 v[100:103], a[188:191], v[112:115], v[100:103]\n"
  "ds_read_b64_tr_b16 a[36:37], v235 offset:12288\n"
  "s_lshr_b32 s94, s64, 1\n"
  "s_waitcnt vmcnt(15) lgkmcnt(5)\n"
  "v_mfma_f32_16x16x32_bf16 v[88:91], a[176:179], v[116:119], v[88:91]\n"
  "ds_read_b64_tr_b16 a[38:39], v235 offset:14336\n"
  "s_lshl2_add_u32 s94, s67, s94\n"
  "v_mfma_f32_16x16x32_bf16 v[92:95], a[180:183], v[116:119], v[92:95]\n"
  "ds_read_b64_tr_b16 a[40:41], v236 offset:12288\n"
  "s_and_b32 s95, s64, 1\n"
  "v_mfma_f32_16x16x32_bf16 v[104:107], a[184:187], v[116:119], v[104:107]\n"
  "s_add_u32 m0, s8, 6144\n"
  "s_mov_b32 s9, 589824\n"
  "buffer_load_dwordx4 v240, s[40:43], s9 offen lds\n"
  "v_mfma_f32_16x16x32_bf16 v[108:111], a[188:191], v[116:119], v[108:111]\n"
  "ds_read_b64_tr_b16 a[42:43], v236 offset:14336\n"
  "s_lshl2_add_u32 s94, s94, s95\n"
  "s_waitcnt vmcnt(15) lgkmcnt(5)\n"
  "v_mfma_f32_16x16x32_bf16 v[80:83], a[192:195], v[120:123], v[80:83]\n"
  "ds_read_b64_tr_b16 a[44:45], v237 offset:12288\n"
  "s_lshl_b32 s94, s94, 7\n"
  "v_mfma_f32_16x16x32_bf16 v[84:87], a[196:199], v[120:123], v[84:87]\n"
  "ds_read_b64_tr_b16 a[46:47], v237 offset:14336\n"
  "s_add_u32 s87, s94, 1310720\n"
  "v_mfma_f32_16x16x32_bf16 v[96:99], a[200:203], v[120:123], v[96:99]\n"
  "v_lshl_add_u32 v251, v0, 1, s87\n"
  "s_add_u32 m0, s8, 7168\n"
  "s_mov_b32 s9, 622592\n"
  "buffer_load_dwordx4 v240, s[40:43], s9 offen lds\n"
  "v_mfma_f32_16x16x32_bf16 v[100:103], a[204:207], v[120:123], v[100:103]\n"
  "v_and_b32 v45, 3, v0\n"
  "v_lshl_add_u32 v251, v45, 28, v251\n"
  "s_waitcnt vmcnt(15) lgkmcnt(4)\n"
  "v_mfma_f32_16x16x32_bf16 v[88:91], a[192:195], v[124:127], v[88:91]\n"
  "s_lshl_b32 s87, s67, 11\n"
  "s_add_u32 s87, s87, 1310720\n"
  "v_mfma_f32_16x16x32_bf16 v[92:95], a[196:199], v[124:127], v[92:95]\n"
  "v_lshl_add_u32 v252, v3, 9, s87\n"
  "v_lshl_add_u32 v252, v2, 3, v252\n"
  "v_mfma_f32_16x16x32_bf16 v[104:107], a[200:203], v[124:127], v[104:107]\n"
  "v_mfma_f32_16x16x32_bf16 v[108:111], a[204:207], v[124:127], v[108:111]\n"
  "s_waitcnt vmcnt(14) lgkmcnt(2)\n"
  "v_mfma_f32_16x16x32_bf16 v[80:83], a[208:211], v[128:131], v[80:83]\n"
  "v_mfma_f32_16x16x32_bf16 v[84:87], a[212:215], v[128:131], v[84:87]\n"
  "v_mfma_f32_16x16x32_bf16 v[96:99], a[216:219], v[128:131], v[96:99]\n"
  "v_mfma_f32_16x16x32_bf16 v[100:103], a[220:223], v[128:131], v[100:103]\n"
  "s_waitcnt vmcnt(13) lgkmcnt(0)\n"
  "v_mfma_f32_16x16x32_bf16 v[88:91], a[208:211], v[132:135], v[88:91]\n"
  "v_mfma_f32_16x16x32_bf16 v[92:95], a[212:215], v[132:135], v[92:95]\n"
  "v_mfma_f32_16x16x32_bf16 v[104:107], a[216:219], v[132:135], v[104:107]\n"
  "v_mfma_f32_16x16x32_bf16 v[108:111], a[220:223], v[132:135], v[108:111]\n"
  "s_waitcnt vmcnt(12)\n"
  "v_mfma_f32_16x16x32_bf16 v[80:83], a[224:227], v[136:139], v[80:83]\n"
  "v_mfma_f32_16x16x32_bf16 v[84:87], a[228:231], v[136:139], v[84:87]\n"
  "v_mfma_f32_16x16x32_bf16 v[96:99], a[232:235], v[136:139], v[96:99]\n"
  "v_mfma_f32_16x16x32_bf16 v[100:103], a[236:239], v[136:139], v[100:103]\n"
  "s_waitcnt vmcnt(11)\n"
  "v_mfma_f32_16x16x32_bf16 v[88:91], a[224:227], v[140:143], v[88:91]\n"
  "v_mfma_f32_16x16x32_bf16 v[92:95], a[228:231], v[140:143], v[92:95]\n"
  "v_mfma_f32_16x16x32_bf16 v[104:107], a[232:235], v[140:143], v[104:107]\n"
  "v_mfma_f32_16x16x32_bf16 v[108:111], a[236:239], v[140:143], v[108:111]\n"
  "s_waitcnt vmcnt(10)\n"
  "v_mfma_f32_16x16x32_bf16 v[80:83], a[240:243], v[144:147], v[80:83]\n"
  "s_waitcnt vmcnt(5)\n"
  "s_barrier\n"
  "buffer_store_dwordx2 v[36:37], v248, s[20:23], s13 offen sc1\n"
  "v_mfma_f32_16x16x32_bf16 v[84:87], a[244:247], v[144:147], v[84:87]\n"
  "s_add_u32 m0, s8, 8192\n"
  "s_mov_b32 s9, 655360\n"
  "buffer_load_dwordx4 v240, s[40:43], s9 offen lds\n"
  "v_mfma_f32_16x16x32_bf16 v[96:99], a[248:251], v[144:147], v[96:99]\n"
  "s_add_u32 m0, s8, 9216\n"
  "s_mov_b32 s9, 688128\n"
  "buffer_load_dwordx4 v240, s[40:43], s9 offen lds\n"
  "v_mfma_f32_16x16x32_bf16 v[100:103], a[252:255], v[144:147], v[100:103]\n"
  "s_add_u32 m0, s8, 10240\n"
  "s_mov_b32 s9, 720896\n"
  "buffer_load_dwordx4 v240, s[40:43], s9 offen lds\n"
  "v_mfma_f32_16x16x32_bf16 v[88:91], a[240:243], v[148:151], v[88:91]\n"
  "s_waitcnt vmcnt(8)\n"
  "ds_read_b64_tr_b16 a[48:49], v234 offset:0\n"
  "v_mfma_f32_16x16x32_bf16 v[92:95], a[244:247], v[148:151], v[92:95]\n"
  "ds_read_b64_tr_b16 a[50:51], v234 offset:2048\n"
  "v_mfma_f32_16x16x32_bf16 v[104:107], a[248:251], v[148:151], v[104:107]\n"
  "ds_read_b64_tr_b16 a[52:53], v235 offset:0\n"
  "v_mfma_f32_16x16x32_bf16 v[108:111], a[252:255], v[148:151], v[108:111]\n"
  "s_nop 0\n"
  "ds_write_b128 v242, v[88:91] offset:24576\n"
  "v_mfma_f32_16x16x16_bf16 v[48:51], v[22:23], v[40:41], v[48:51]\n"
  "s_add_u32 m0, s8, 11264\n"
  "s_mov_b32 s9, 753664\n"
  "buffer_load_dwordx4 v240, s[40:43], s9 offen lds\n"
  "v_mfma_f32_16x16x16_bf16 v[52:55], v[24:25], v[40:41], v[52:55]\n"
  "ds_read_b64_tr_b16 a[54:55], v235 offset:2048\n"
  "ds_write_b128 v243, v[96:99] offset:24576\n"
  "ds_read_b64_tr_b16 a[56:57], v236 offset:0\n"
  "ds_read_b64_tr_b16 a[58:59], v236 offset:2048\n"
  "ds_read_b64_tr_b16 a[60:61], v237 offset:0\n"
  "ds_read_b64_tr_b16 a[62:63], v237 offset:2048\n"
  "s_add_u32 m0, s8, 12288\n"
  "s_mov_b32 s9, 786432\n"
  "buffer_load_dwordx4 v240, s[40:43], s9 offen lds\n"
  "ds_write_b128 v242, v[92:95] offset:25600\n"
  "ds_write_b128 v243, v[100:103] offset:25600\n"
  "s_add_u32 m0, s8, 13312\n"
  "s_mov_b32 s9, 819200\n"
  "buffer_load_dwordx4 v240, s[40:43], s9 offen lds\n"
  "ds_write_b128 v244, v[104:107] offset:24576\n"
  "ds_write_b128 v244, v[108:111] offset:25600\n"
  "s_add_u32 m0, s8, 14336\n"
  "s_mov_b32 s9, 851968\n"
  "buffer_load_dwordx4 v240, s[40:43], s9 offen lds\n"
  "s_waitcnt vmcnt(8) lgkmcnt(2)\n"
  "ds_read_b64_tr_b16 a[64:65], v234 offset:4096\n"
  "ds_read_b64_tr_b16 a[66:67], v234 offset:6144\n"
  "ds_read_b64_tr_b16 a[68:69], v235 offset:4096\n"
  "ds_read_b64_tr_b16 a[70:71], v235 offset:6144\n"
  "ds_read_b64_tr_b16 a[72:73], v236 offset:4096\n"
  "ds_read_b64_tr_b16 a[74:75], v236 offset:6144\n"
  "ds_read_b64_tr_b16 a[76:77], v237 offset:4096\n"
  "ds_read_b64_tr_b16 a[78:79], v237 offset:6144\n"
  "s_add_u32 m0, s8, 15360\n"
  "s_mov_b32 s9, 884736\n"
  "buffer_load_dwordx4 v240, s[40:43], s9 offen lds\n"
  "s_waitcnt lgkmcnt(8)\n"
  "s_barrier\n"
  "ds_read_b128 v[88:91], v241 offset:24576\n"
  "ds_read_b128 v[96:99], v241 offset:26624\n"
  "ds_read_b128 v[104:107], v241 offset:28672\n"
  "ds_read_b128 v[92:95], v241 offset:25600\n"
  "ds_read_b128 v[100:103], v241 offset:27648\n"
  "ds_read_b128 v[108:111], v241 offset:29696\n"
  "s_add_u32 m0, s8, 0\n"
  "s_mov_b32 s9, 917504\n"
  "buffer_load_dwordx4 v240, s[40:43], s9 offen lds\n"
  "s_waitcnt lgkmcnt(5)\n"
  "v_pk_add_f32 v[80:81], v[80:81], v[88:89]\n"
  "v_pk_add_f32 v[82:83], v[82:83], v[90:91]\n"
  "s_waitcnt lgkmcnt(3)\n"
  "v_pk_add_f32 v[96:97], v[96:97], v[104:105]\n"
  "v_pk_add_f32 v[98:99], v[98:99], v[106:107]\n"
  "v_pk_add_f32 v[80:81], v[80:81], v[96:97]\n"
  "s_waitcnt lgkmcnt(2)\n"
  "v_pk_add_f32 v[84:85], v[84:85], v[92:93]\n"
  "v_pk_add_f32 v[86:87], v[86:87], v[94:95]\n"
  "v_pk_add_f32 v[82:83], v[82:83], v[98:99]\n"
  "v_pk_mul_f32 v[88:89], v[80:81], v[80:81]\n"
  "v_pk_mul_f32 v[90:91], v[82:83], v[82:83]\n"
  "v_pk_fma_f32 v[88:89], v[88:89], s[6:7], v[204:205] op_sel:[0,1,1]\n"
  "v_pk_fma_f32 v[90:91], v[90:91], s[6:7], v[204:205] op_sel:[0,1,1]\n"
  "s_waitcnt lgkmcnt(0)\n"
  "v_pk_add_f32 v[100:101], v[100:101], v[108:109]\n"
  "v_pk_add_f32 v[102:103], v[102:103], v[110:111]\n"
  "v_pk_add_f32 v[84:85], v[84:85], v[100:101]\n"
  "v_pk_add_f32 v[86:87], v[86:87], v[102:103]\n"
  "v_pk_mul_f32 v[92:93], v[84:85], v[84:85]\n"
  "v_pk_mul_f32 v[94:95], v[86:87], v[86:87]\n"
  "v_pk_fma_f32 v[92:93], v[92:93], s[6:7], v[204:205] op_sel:[0,1,1]\n"
  "v_pk_fma_f32 v[94:95], v[94:95], s[6:7], v[204:205] op_sel:[0,1,1]\n"
  "v_pk_mul_f32 v[88:89], v[88:89], v[80:81]\n"
  "v_pk_mul_f32 v[90:91], v[90:91], v[82:83]\n"
  "v_pk_mul_f32 v[92:93], v[92:93], v[84:85]\n"
  "v_pk_mul_f32 v[94:95], v[94:95], v[86:87]\n"
  "s_add_u32 m0, s8, 1024\n"
  "s_mov_b32 s9, 950272\n"
  "buffer_load_dwordx4 v240, s[40:43], s9 offen lds\n"
  "v_exp_f32 v88, v88\n"
  "v_exp_f32 v89, v89\n"
  "v_exp_f32 v90, v90\n"
  "v_exp_f32 v91, v91\n"
  "v_exp_f32 v92, v92\n"
  "v_exp_f32 v93, v93\n"
  "v_exp_f32 v94, v94\n"
  "v_exp_f32 v95, v95\n"
  "v_pk_add_f32 v[88:89], v[88:89], 1.0 op_sel_hi:[1,0]\n"
  "v_pk_add_f32 v[90:91], v[90:91], 1.0 op_sel_hi:[1,0]\n"
  "v_pk_add_f32 v[92:93], v[92:93], 1.0 op_sel_hi:[1,0]\n"
  "v_pk_add_f32 v[94:95], v[94:95], 1.0 op_sel_hi:[1,0]\n"
  "s_add_u32 m0, s8, 2048\n"
  "s_mov_b32 s9, 983040\n"
  "buffer_load_dwordx4 v240, s[40:43], s9 offen lds\n"
  "v_rcp_f32 v88, v88\n"
  "v_rcp_f32 v89, v89\n"
  "v_rcp_f32 v90, v90\n"
  "v_rcp_f32 v91, v91\n"
  "v_rcp_f32 v92, v92\n"
  "v_rcp_f32 v93, v93\n"
  "v_rcp_f32 v94, v94\n"
  "s_add_u32 m0, s8, 3072\n"
  "s_mov_b32 s9, 1015808\n"
  "buffer_load_dwordx4 v240, s[40:43], s9 offen lds\n"
  "v_rcp_f32 v95, v95\n"
  "v_pk_mul_f32 v[88:89], v[88:89], v[80:81]\n"
  "v_pk_mul_f32 v[90:91], v[90:91], v[82:83]\n"
  "v_pk_mul_f32 v[92:93], v[92:93], v[84:85]\n"
  "v_pk_mul_f32 v[94:95], v[94:95], v[86:87]\n"
  "v_cvt_pk_bf16_f32 v96, v88, v89\n"
  "v_cvt_pk_bf16_f32 v97, v90, v91\n"
  "v_cvt_pk_bf16_f32 v98, v92, v93\n"
  "v_cvt_pk_bf16_f32 v99, v94, v95\n"
  "buffer_store_dwordx4 v[96:99], v1, s[20:23], s53 offen sc1\n"
  "v_mov_b32 v80, v47\n"
  "v_mov_b32 v81, v47\n"
  "v_mov_b32 v82, v47\n"
  "s_waitcnt vmcnt(9)\n"
  "ds_read_b64_tr_b16 a[80:81], v234 offset:8192\n"
  "ds_read_b64_tr_b16 a[82:83], v234 offset:10240\n"
  "s_add_u32 m0, s8, 4096\n"
  "s_mov_b32 s9, 1048576\n"
  "buffer_load_dwordx4 v240, s[40:43], s9 offen lds\n"
  "ds_read_b64_tr_b16 a[84:85], v235 offset:8192\n"
  "ds_read_b64_tr_b16 a[86:87], v235 offset:10240\n"
  "ds_read_b64_tr_b16 a[88:89], v236 offset:8192\n"
  "ds_read_b64_tr_b16 a[90:91], v236 offset:10240\n"
  "ds_read_b64_tr_b16 a[92:93], v237 offset:8192\n"
  "v_mov_b32 v83, v47\n"
  "ds_read_b64_tr_b16 a[94:95], v237 offset:10240\n"
  "v_mov_b32 v84, v47\n"
  "v_mfma_f32_16x16x16_bf16 v[80:83], v[26:27], v[40:41], v[80:83]\n"
  "v_mov_b32 v85, v47\n"
  "v_mov_b32 v86, v47\n"
  "v_mov_b32 v87, v47\n"
  "s_lshl1_add_u32 s87, s61, s59\n"
  "s_add_u32 s88, s87, s60\n"
  "v_mfma_f32_16x16x16_bf16 v[84:87], v[28:29], v[40:41], v[84:87]\n"
  "s_add_u32 m0, s8, 5120\n"
  "s_mov_b32 s9, 1081344\n"
  "buffer_load_dwordx4 v240, s[40:43], s9 offen lds\n"
  "s_sub_u32 s88, s88, 1\n"
  "s_max_i32 s88, s88, 0\n"
  "s_sub_u32 s88, s88, s87\n"
  "s_lshl_b32 s88, s88, 6\n"
  "s_lshl_b32 s87, s87, 9\n"
  "s_add_u32 s87, s87, 65536\n"
  "v_and_b32 v204, 8, v0\n"
  "v_mul_lo_u32 v204, v204, s88\n"
  "v_and_b32 v45, 7, v0\n"
  "v_lshl_add_u32 v204, v45, 3, v204\n"
  "v_add_u32 v204, s87, v204\n"
  "s_add_u32 m0, s8, 6144\n"
  "s_mov_b32 s9, 1114112\n"
  "buffer_load_dwordx4 v240, s[40:43], s9 offen lds\n"
  "s_add_u32 m0, s8, 7168\n"
  "s_mov_b32 s9, 1146880\n"
  "buffer_load_dwordx4 v240, s[40:43], s9 offen lds\n"
  "s_waitcnt vmcnt(9) lgkmcnt(0)\n"
  "ds_read_b64_tr_b16 a[96:97], v234 offset:12288\n"
  "ds_read_b64_tr_b16 a[98:99], v234 offset:14336\n"
  "ds_read_b64_tr_b16 a[100:101], v235 offset:12288\n"
  "ds_read_b64_tr_b16 a[102:103], v235 offset:14336\n"
  "ds_read_b64_tr_b16 a[104:105], v236 offset:12288\n"
  "ds_read_b64_tr_b16 a[106:107], v236 offset:14336\n"
  "ds_read_b64_tr_b16 a[108:109], v237 offset:12288\n"
  "ds_read_b64_tr_b16 a[110:111], v237 offset:14336\n"
  "s_sleep 3\n"
  "buffer_load_dwordx2 v[206:207], v247, s[20:23], 0 offen sc1\n"
  "s_waitcnt vmcnt(6) lgkmcnt(0)\n"
  "ds_read_b64_tr_b16 a[112:113], v234 offset:0\n"
  "ds_read_b64_tr_b16 a[114:115], v234 offset:2048\n"
  "ds_read_b64_tr_b16 a[116:117], v235 offset:0\n"
  "ds_read_b64_tr_b16 a[118:119], v235 offset:2048\n"
  "ds_read_b64_tr_b16 a[120:121], v236 offset:0\n"
  "ds_read_b64_tr_b16 a[122:123], v236 offset:2048\n"
  "ds_read_b64_tr_b16 a[124:125], v237 offset:0\n"
  "ds_read_b64_tr_b16 a[126:127], v237 offset:2048\n"
  "s_waitcnt vmcnt(5)\n"
  "s_barrier\n"
  "buffer_store_dwordx2 v[36:37], v248, s[20:23], s13 offen offset:128 sc1\n"
  "s_add_u32 m0, s8, 8192\n"
  "s_mov_b32 s9, 1179648\n"
  "buffer_load_dwordx4 v240, s[40:43], s9 offen lds\n"
  "s_add_u32 m0, s8, 9216\n"
  "s_mov_b32 s9, 1212416\n"
  "buffer_load_dwordx4 v240, s[40:43], s9 offen lds\n"
  "s_mul_i32 s87, s71, 56\n"
  "s_add_u32 s87, s87, s65\n"
  "s_lshl2_add_u32 s87, s87, s58\n"
  "s_lshl_b32 s87, s87, 10\n"
  "s_add_u32 s87, s87, 12582912\n"
  "s_cmp_eq_u32 s70, 0\n"
  "s_cselect_b32 s17, s87, 786432\n"
  "s_add_u32 m0, s8, 10240\n"
  "s_mov_b32 s9, 1245184\n"
  "buffer_load_dwordx4 v240, s[40:43], s9 offen lds\n"
  "s_add_u32 s53, s17, 114688\n"
  "s_lshl_b32 s88, s71, 5\n"
  "s_add_u32 s88, s88, s69\n"
  "s_lshr_b32 s94, s88, 3\n"
  "s_and_b32 s95, s88, 7\n"
  "s_lshl_b32 s94, s94, 9\n"
  "s_lshl3_add_u32 s94, s95, s94\n"
  "s_add_u32 s13, s94, 114688\n"
  "s_add_u32 m0, s8, 11264\n"
  "s_mov_b32 s9, 1277952\n"
  "buffer_load_dwordx4 v240, s[40:43], s9 offen lds\n"
  "s_add_u32 m0, s8, 12288\n"
  "s_mov_b32 s9, 1310720\n"
  "buffer_load_dwordx4 v240, s[40:43], s9 offen lds\n"
  "s_add_u32 m0, s8, 13312\n"
  "s_mov_b32 s9, 1343488\n"
  "buffer_load_dwordx4 v240, s[40:43], s9 offen lds\n"
  "s_waitcnt vmcnt(8) lgkmcnt(0)\n"
  "ds_read_b64_tr_b16 a[128:129], v234 offset:4096\n"
  "ds_read_b64_tr_b16 a[130:131], v234 offset:6144\n"
  "ds_read_b64_tr_b16 a[132:133], v235 offset:4096\n"
  "ds_read_b64_tr_b16 a[134:135], v235 offset:6144\n"
  "ds_read_b64_tr_b16 a[136:137], v236 offset:4096\n"
  "ds_read_b64_tr_b16 a[138:139], v236 offset:6144\n"
  "ds_read_b64_tr_b16 a[140:141], v237 offset:4096\n"
  "ds_read_b64_tr_b16 a[142:143], v237 offset:6144\n"
  "s_add_u32 m0, s8, 14336\n"
  "s_mov_b32 s9, 1376256\n"
  "buffer_load_dwordx4 v240, s[40:43], s9 offen lds\n"
  "s_add_u32 m0, s8, 15360\n"
  "s_mov_b32 s9, 1409024\n"
  "buffer_load_dwordx4 v240, s[40:43], s9 offen lds\n"
  "s_waitcnt vmcnt(9) lgkmcnt(0)\n"
  "v_cmp_ne_u64 vcc, s[56:57], v[206:207]\n"
  "s_cmp_eq_u64 vcc, 0\n"
  "s_cbranch_scc0 .Lfb15_%=\n"
  ".Lbk15_%=:\n"
  "buffer_load_dwordx4 v[152:155], v232, s[20:23], 0 offen sc1\n"
  "buffer_load_dwordx4 v[156:159], v232, s[20:23], 0 offen offset:1024 sc1\n"
  "buffer_load_dwordx4 v[160:163], v232, s[20:23], 0 offen offset:2048 sc1\n"
  "buffer_load_dwordx4 v[164:167], v232, s[20:23], 0 offen offset:3072 sc1\n"
  "buffer_load_dwordx4 v[168:171], v232, s[20:23], s72 offen sc1\n"
  "buffer_load_dwordx4 v[172:175], v232, s[20:23], s72 offen offset:1024 sc1\n"
  "buffer_load_dwordx4 v[180:183], v232, s[20:23], s72 offen offset:2048 sc1\n"
  "s_waitcnt vmcnt(11)\n"
  "ds_read_b64_tr_b16 a[144:145], v234 offset:8192\n"
  "ds_read_b64_tr_b16 a[146:147], v234 offset:10240\n"
  "ds_read_b64_tr_b16 a[148:149], v235 offset:8192\n"
  "ds_read_b64_tr_b16 a[150:151], v235 offset:10240\n"
  "ds_read_b64_tr_b16 a[152:153], v236 offset:8192\n"
  "ds_read_b64_tr_b16 a[154:155], v236 offset:10240\n"
  "ds_read_b64_tr_b16 a[156:157], v237 offset:8192\n"
  "ds_read_b64_tr_b16 a[158:159], v237 offset:10240\n"
  "buffer_load_dwordx4 v[188:191], v232, s[20:23], s72 offen offset:3072 sc1\n"
  "buffer_load_dwordx4 v[112:115], v232, s[20:23], s73 offen sc1\n"
  "buffer_load_dwordx4 v[116:119], v232, s[20:23], s73 offen offset:1024 sc1\n"
  "buffer_load_dwordx4 v[120:123], v232, s[20:23], s73 offen offset:2048 sc1\n"
  "buffer_load_dwordx4 v[124:127], v232, s[20:23], s73 offen offset:3072 sc1\n"
  "s_waitcnt vmcnt(12) lgkmcnt(0)\n"
  "ds_read_b64_tr_b16 a[160:161], v234 offset:12288\n"
  "ds_read_b64_tr_b16 a[162:163], v234 offset:14336\n"
  "ds_read_b64_tr_b16 a[164:165], v235 offset:12288\n"
  "ds_read_b64_tr_b16 a[166:167], v235 offset:14336\n"
  "ds_read_b64_tr_b16 a[168:169], v236 offset:12288\n"
  "ds_read_b64_tr_b16 a[170:171], v236 offset:14336\n"
  "buffer_load_dwordx4 v[128:131], v232, s[20:23], s74 offen sc1\n"
  "ds_read_b64_tr_b16 a[172:173], v237 offset:12288\n"
  "ds_read_b64_tr_b16 a[174:175], v237 offset:14336\n"
  "s_waitcnt vmcnt(12)\n"
  "v_mfma_f32_16x16x32_bf16 v[48:51], v[152:155], a[0:3], v[48:51]\n"
  "v_mfma_f32_16x16x32_bf16 v[56:59], v[152:155], a[4:7], 0\n"
  "v_mfma_f32_16x16x32_bf16 v[64:67], v[152:155], a[8:11], 0\n"
  "buffer_load_dwordx4 v[132:135], v232, s[20:23], s74 offen offset:1024 sc1\n"
  "v_mfma_f32_16x16x32_bf16 v[72:75], v[152:155], a[12:15], 0\n"
  "s_waitcnt vmcnt(12)\n"
  "v_mfma_f32_16x16x32_bf16 v[52:55], v[156:159], a[0:3], v[52:55]\n"
  "v_mfma_f32_16x16x32_bf16 v[60:63], v[156:159], a[4:7], 0\n"
  "v_mfma_f32_16x16x32_bf16 v[68:71], v[156:159], a[8:11], 0\n"
  "buffer_load_dwordx4 v[136:139], v232, s[20:23], s74 offen offset:2048 sc1\n"
  "v_mfma_f32_16x16x32_bf16 v[76:79], v[156:159], a[12:15], 0\n"
  "s_waitcnt vmcnt(12) lgkmcnt(0)\n"
  "v_mfma_f32_16x16x32_bf16 v[48:51], v[160:163], a[16:19], v[48:51]\n"
  "v_mfma_f32_16x16x32_bf16 v[56:59], v[160:163], a[20:23], v[56:59]\n"
  "buffer_load_dwordx4 v[140:143], v232, s[20:23], s74 offen offset:3072 sc1\n"
  "v_mfma_f32_16x16x32_bf16 v[64:67], v[160:163], a[24:27], v[64:67]\n"
  "v_mfma_f32_16x16x32_bf16 v[72:75], v[160:163], a[28:31], v[72:75]\n"
  "s_waitcnt vmcnt(12)\n"
  "v_mfma_f32_16x16x32_bf16 v[52:55], v[164:167], a[16:19], v[52:55]\n"
  "v_mfma_f32_16x16x32_bf16 v[60:63], v[164:167], a[20:23], v[60:63]\n"
  "buffer_load_dwordx4 v[144:147], v232, s[20:23], s75 offen sc1\n"
  "buffer_load_dwordx2 v[206:207], v247, s[20:23], 0 offen offset:128 sc1\n"
  "v_mfma_f32_16x16x32_bf16 v[68:71], v[164:167], a[24:27], v[68:71]\n"
  "v_mfma_f32_16x16x32_bf16 v[76:79], v[164:167], a[28:31], v[76:79]\n"
  "s_waitcnt vmcnt(13)\n"
  "v_mfma_f32_16x16x32_bf16 v[48:51], v[168:171], a[32:35], v[48:51]\n"
  "v_mfma_f32_16x16x32_bf16 v[56:59], v[168:171], a[36:39], v[56:59]\n"
  "v_mfma_f32_16x16x32_bf16 v[64:67], v[168:171], a[40:43], v[64:67]\n"
  "v_mfma_f32_16x16x32_bf16 v[72:75], v[168:171], a[44:47], v[72:75]\n"
  "s_waitcnt vmcnt(12)\n"
  "v_mfma_f32_16x16x32_bf16 v[52:55], v[172:175], a[32:35], v[52:55]\n"
  "buffer_load_dwordx4 v[148:151], v232, s[20:23], s75 offen offset:1024 sc1\n"
  "v_mfma_f32_16x16x32_bf16 v[60:63], v[172:175], a[36:39], v[60:63]\n"
  "v_mfma_f32_16x16x32_bf16 v[68:71], v[172:175], a[40:43], v[68:71]\n"
  "v_mfma_f32_16x16x32_bf16 v[76:79], v[172:175], a[44:47], v[76:79]\n"
  "s_waitcnt vmcnt(12)\n"
  "v_mfma_f32_16x16x32_bf16 v[48:51], v[180:183], a[48:51], v[48:51]\n"
  "buffer_load_dwordx4 v[152:155], v232, s[20:23], s75 offen offset:2048 sc1\n"
  "v_mfma_f32_16x16x32_bf16 v[56:59], v[180:183], a[52:55], v[56:59]\n"
  "v_mfma_f32_16x16x32_bf16 v[64:67], v[180:183], a[56:59], v[64:67]\n"
  "v_mfma_f32_16x16x32_bf16 v[72:75], v[180:183], a[60:63], v[72:75]\n"
  "s_waitcnt vmcnt(12)\n"
  "v_mfma_f32_16x16x32_bf16 v[52:55], v[188:191], a[48:51], v[52:55]\n"
  "buffer_load_dwordx4 v[156:159], v232, s[20:23], s75 offen offset:3072 sc1\n"
  "v_mfma_f32_16x16x32_bf16 v[60:63], v[188:191], a[52:55], v[60:63]\n"
  "v_mfma_f32_16x16x32_bf16 v[68:71], v[188:191], a[56:59], v[68:71]\n"
  "v_mfma_f32_16x16x32_bf16 v[76:79], v[188:191], a[60:63], v[76:79]\n"
  "s_waitcnt vmcnt(12)\n"
  "v_mfma_f32_16x16x32_bf16 v[48:51], v[112:115], a[64:67], v[48:51]\n"
  "buffer_load_dwordx4 v[160:163], v232, s[20:23], s76 offen sc1\n"
  "v_mfma_f32_16x16x32_bf16 v[56:59], v[112:115], a[68:71], v[56:59]\n"
  "v_mfma_f32_16x16x32_bf16 v[64:67], v[112:115], a[72:75], v[64:67]\n"
  "v_mfma_f32_16x16x32_bf16 v[72:75], v[112:115], a[76:79], v[72:75]\n"
  "s_waitcnt vmcnt(12)\n"
  "v_mfma_f32_16x16x32_bf16 v[52:55], v[116:119], a[64:67], v[52:55]\n"
  "s_add_u32 m0, s8, 0\n"
  "s_mov_b32 s9, 1441792\n"
  "buffer_load_dwordx4 v240, s[40:43], s9 offen lds\n"
  "v_mfma_f32_16x16x32_bf16 v[60:63], v[116:119], a[68:71], v[60:63]\n"
  "v_mfma_f32_16x16x32_bf16 v[68:71], v[116:119], a[72:75], v[68:71]\n"
  "v_mfma_f32_16x16x32_bf16 v[76:79], v[116:119], a[76:79], v[76:79]\n"
  "s_waitcnt vmcnt(12)\n"
  "v_mfma_f32_16x16x32_bf16 v[48:51], v[120:123], a[80:83], v[48:51]\n"
  "s_add_u32 m0, s8, 1024\n"
  "s_mov_b32 s9, 1474560\n"
  "buffer_load_dwordx4 v240, s[40:43], s9 offen lds\n"
  "v_mfma_f32_16x16x32_bf16 v[56:59], v[120:123], a[84:87], v[56:59]\n"
  "v_mfma_f32_16x16x32_bf16 v[64:67], v[120:123], a[88:91], v[64:67]\n"
  "v_mfma_f32_16x16x32_bf16 v[72:75], v[120:123], a[92:95], v[72:75]\n"
  "s_waitcnt vmcnt(12)\n"
  "v_mfma_f32_16x16x32_bf16 v[52:55], v[124:127], a[80:83], v[52:55]\n"
  "v_mfma_f32_16x16x32_bf16 v[60:63], v[124:127], a[84:87], v[60:63]\n"
  "s_add_u32 m0, s8, 2048\n"
  "s_mov_b32 s9, 1507328\n"
  "buffer_load_dwordx4 v240, s[40:43], s9 offen lds\n"
  "v_mfma_f32_16x16x32_bf16 v[68:71], v[124:127], a[88:91], v[68:71]\n"
  "v_mfma_f32_16x16x32_bf16 v[76:79], v[124:127], a[92:95], v[76:79]\n"
  "s_waitcnt vmcnt(12)\n"
  "v_mfma_f32_16x16x32_bf16 v[48:51], v[128:131], a[96:99], v[48:51]\n"
  "v_mfma_f32_16x16x32_bf16 v[56:59], v[128:131], a[100:103], v[56:59]\n"
  "v_mfma_f32_16x16x32_bf16 v[64:67], v[128:131], a[104:107], v[64:67]\n"
  "s_add_u32 m0, s8, 3072\n"
  "s_mov_b32 s9, 1540096\n"
  "buffer_load_dwordx4 v240, s[40:43], s9 offen lds\n"
  "v_mfma_f32_16x16x32_bf16 v[72:75], v[128:131], a[108:111], v[72:75]\n"
  "s_waitcnt vmcnt(12)\n"
  "v_mfma_f32_16x16x32_bf16 v[52:55], v[132:135], a[96:99], v[52:55]\n"
  "v_mfma_f32_16x16x32_bf16 v[60:63], v[132:135], a[100:103], v[60:63]\n"
  "v_mfma_f32_16x16x32_bf16 v[68:71], v[132:135], a[104:107], v[68:71]\n"
  "buffer_load_dwordx4 v[164:167], v232, s[20:23], s76 offen offset:1024 sc1\n"
  "v_mfma_f32_16x16x32_bf16 v[76:79], v[132:135], a[108:111], v[76:79]\n"
  "s_waitcnt vmcnt(12)\n"
  "v_mfma_f32_16x16x32_bf16 v[48:51], v[136:139], a[112:115], v[48:51]\n"
  "v_mfma_f32_16x16x32_bf16 v[56:59], v[136:139], a[116:119], v[56:59]\n"
  "v_mfma_f32_16x16x32_bf16 v[64:67], v[136:139], a[120:123], v[64:67]\n"
  "buffer_load_dwordx4 v[168:171], v232, s[20:23], s76 offen offset:2048 sc1\n"
  "v_mfma_f32_16x16x32_bf16 v[72:75], v[136:139], a[124:127], v[72:75]\n"
  "s_waitcnt vmcnt(12)\n"
  "v_mfma_f32_16x16x32_bf16 v[52:55], v[140:143], a[112:115], v[52:55]\n"
  "v_mfma_f32_16x16x32_bf16 v[60:63], v[140:143], a[116:119], v[60:63]\n"
  "v_mfma_f32_16x16x32_bf16 v[68:71], v[140:143], a[120:123], v[68:71]\n"
  "s_waitcnt vmcnt(10)\n"
  "v_cmp_ne_u64 vcc, s[56:57], v[206:207]\n"
  "s_cmp_eq_u64 vcc, 0\n"
  "s_cbranch_scc0 .Lfb16_%=\n"
  ".Lbk16_%=:\n"
  "v_mfma_f32_16x16x32_bf16 v[76:79], v[140:143], a[124:127], v[76:79]\n"
  "s_add_u32 m0, s8, 4096\n"
  "s_mov_b32 s9, 1572864\n"
  "buffer_load_dwordx4 v240, s[40:43], s9 offen lds\n"
  "v_mfma_f32_16x16x32_bf16 v[48:51], v[144:147], a[128:131], v[48:51]\n"
  "v_mfma_f32_16x16x32_bf16 v[56:59], v[144:147], a[132:135], v[56:59]\n"
  "v_mfma_f32_16x16x32_bf16 v[64:67], v[144:147], a[136:139], v[64:67]\n"
  "s_add_u32 m0, s8, 5120\n"
  "s_mov_b32 s9, 1605632\n"
  "buffer_load_dwordx4 v240, s[40:43], s9 offen lds\n"
  "v_mfma_f32_16x16x32_bf16 v[72:75], v[144:147], a[140:143], v[72:75]\n"
  "s_waitcnt vmcnt(11)\n"
  "v_mfma_f32_16x16x32_bf16 v[52:55], v[148:151], a[128:131], v[52:55]\n"
  "v_mfma_f32_16x16x32_bf16 v[60:63], v[148:151], a[132:135], v[60:63]\n"
  "v_mfma_f32_16x16x32_bf16 v[68:71], v[148:151], a[136:139], v[68:71]\n"
  "v_mfma_f32_16x16x32_bf16 v[76:79], v[148:151], a[140:143], v[76:79]\n"
  "s_add_u32 m0, s8, 6144\n"
  "s_mov_b32 s9, 1638400\n"
  "buffer_load_dwordx4 v240, s[40:43], s9 offen lds\n"
  "s_waitcnt vmcnt(11)\n"
  "v_mfma_f32_16x16x32_bf16 v[48:51], v[152:155], a[144:147], v[48:51]\n"
  "v_mfma_f32_16x16x32_bf16 v[56:59], v[152:155], a[148:151], v[56:59]\n"
  "v_mfma_f32_16x16x32_bf16 v[64:67], v[152:155], a[152:155], v[64:67]\n"
  "v_mfma_f32_16x16x32_bf16 v[72:75], v[152:155], a[156:159], v[72:75]\n"
  "s_waitcnt vmcnt(10)\n"
  "v_mfma_f32_16x16x32_bf16 v[52:55], v[156:159], a[144:147], v[52:55]\n"
  "s_add_u32 m0, s8, 7168\n"
  "s_mov_b32 s9, 1671168\n"
  "buffer_load_dwordx4 v240, s[40:43], s9 offen lds\n"
  "v_mfma_f32_16x16x32_bf16 v[60:63], v[156:159], a[148:151], v[60:63]\n"
  "v_mfma_f32_16x16x32_bf16 v[68:71], v[156:159], a[152:155], v[68:71]\n"
  "v_mfma_f32_16x16x32_bf16 v[76:79], v[156:159], a[156:159], v[76:79]\n"
  "s_waitcnt vmcnt(10)\n"
  "v_mfma_f32_16x16x32_bf16 v[48:51], v[160:163], a[160:163], v[48:51]\n"
  "buffer_load_dwordx4 v[172:175], v232, s[20:23], s76 offen offset:3072 sc1\n"
  "v_mfma_f32_16x16x32_bf16 v[56:59], v[160:163], a[164:167], v[56:59]\n"
  "v_mfma_f32_16x16x32_bf16 v[64:67], v[160:163], a[168:171], v[64:67]\n"
  "v_mfma_f32_16x16x32_bf16 v[72:75], v[160:163], a[172:175], v[72:75]\n"
  "buffer_load_dwordx4 v[180:183], v232, s[20:23], s77 offen sc1\n"
  "buffer_load_dwordx4 v[188:191], v232, s[20:23], s77 offen offset:1024 sc1\n"
  "buffer_load_dwordx4 v[112:115], v232, s[20:23], s77 offen offset:2048 sc1\n"
  "buffer_load_dwordx4 v[116:119], v232, s[20:23], s77 offen offset:3072 sc1\n"
  "s_waitcnt vmcnt(11)\n"
  "ds_read_b64_tr_b16 a[176:177], v234 offset:0\n"
  "ds_read_b64_tr_b16 a[178:179], v234 offset:2048\n"
  "ds_read_b64_tr_b16 a[180:181], v235 offset:0\n"
  "ds_read_b64_tr_b16 a[182:183], v235 offset:2048\n"
  "ds_read_b64_tr_b16 a[184:185], v236 offset:0\n"
  "ds_read_b64_tr_b16 a[186:187], v236 offset:2048\n"
  "ds_read_b64_tr_b16 a[188:189], v237 offset:0\n"
  "ds_read_b64_tr_b16 a[190:191], v237 offset:2048\n"
  "buffer_load_dwordx4 v[120:123], v233, s[20:23], 0 offen sc1\n"
  "s_waitcnt vmcnt(11)\n"
  "v_mfma_f32_16x16x32_bf16 v[52:55], v[164:167], a[160:163], v[52:55]\n"
  "v_mfma_f32_16x16x32_bf16 v[60:63], v[164:167], a[164:167], v[60:63]\n"
  "v_mfma_f32_16x16x32_bf16 v[68:71], v[164:167], a[168:171], v[68:71]\n"
  "v_mfma_f32_16x16x32_bf16 v[76:79], v[164:167], a[172:175], v[76:79]\n"
  "buffer_load_dwordx4 v[124:127], v233, s[20:23], 0 offen offset:1024 sc1\n"
  "s_waitcnt vmcnt(11) lgkmcnt(6)\n"
  "v_mfma_f32_16x16x32_bf16 v[48:51], v[168:171], a[176:179], v[48:51]\n"
  "s_waitcnt lgkmcnt(4)\n"
  "v_mfma_f32_16x16x32_bf16 v[56:59], v[168:171], a[180:183], v[56:59]\n"
  "s_waitcnt lgkmcnt(2)\n"
  "v_mfma_f32_16x16x32_bf16 v[64:67], v[168:171], a[184:187], v[64:67]\n"
  "buffer_load_dwordx4 v[128:131], v233, s[20:23], 0 offen offset:2048 sc1\n"
  "s_waitcnt lgkmcnt(0)\n"
  "v_mfma_f32_16x16x32_bf16 v[72:75], v[168:171], a[188:191], v[72:75]\n"
  "buffer_load_dwordx4 v[132:135], v233, s[20:23], 0 offen offset:3072 sc1\n"
  "buffer_load_dwordx4 v[136:139], v233, s[20:23], s72 offen sc1\n"
  "buffer_load_dwordx4 v[140:143], v233, s[20:23], s72 offen offset:1024 sc1\n"
  "buffer_load_dwordx4 v[144:147], v233, s[20:23], s72 offen offset:2048 sc1\n"
  "s_waitcnt vmcnt(12)\n"
  "ds_read_b64_tr_b16 a[192:193], v234 offset:4096\n"
  "ds_read_b64_tr_b16 a[194:195], v234 offset:6144\n"
  "ds_read_b64_tr_b16 a[196:197], v235 offset:4096\n"
  "ds_read_b64_tr_b16 a[198:199], v235 offset:6144\n"
  "ds_read_b64_tr_b16 a[200:201], v236 offset:4096\n"
  "ds_read_b64_tr_b16 a[202:203], v236 offset:6144\n"
  "buffer_load_dwordx4 v[148:151], v233, s[20:23], s72 offen offset:3072 sc1\n"
  "ds_read_b64_tr_b16 a[204:205], v237 offset:4096\n"
  "ds_read_b64_tr_b16 a[206:207], v237 offset:6144\n"
  "s_waitcnt vmcnt(12)\n"
  "v_mfma_f32_16x16x32_bf16 v[52:55], v[172:175], a[176:179], v[52:55]\n"
  "v_mfma_f32_16x16x32_bf16 v[60:63], v[172:175], a[180:183], v[60:63]\n"
  "v_mfma_f32_16x16x32_bf16 v[68:71], v[172:175], a[184:187], v[68:71]\n"
  "buffer_load_dwordx4 v[152:155], v233, s[20:23], s73 offen sc1\n"
  "v_mfma_f32_16x16x32_bf16 v[76:79], v[172:175], a[188:191], v[76:79]\n"
  "s_waitcnt vmcnt(12) lgkmcnt(6)\n"
  "v_mfma_f32_16x16x32_bf16 v[48:51], v[180:183], a[192:195], v[48:51]\n"
  "s_waitcnt lgkmcnt(4)\n"
  "v_mfma_f32_16x16x32_bf16 v[56:59], v[180:183], a[196:199], v[56:59]\n"
  "buffer_load_dwordx4 v[156:159], v233, s[20:23], s73 offen offset:1024 sc1\n"
  "s_waitcnt lgkmcnt(2)\n"
  "v_mfma_f32_16x16x32_bf16 v[64:67], v[180:183], a[200:203], v[64:67]\n"
  "s_waitcnt lgkmcnt(0)\n"
  "v_mfma_f32_16x16x32_bf16 v[72:75], v[180:183], a[204:207], v[72:75]\n"
  "s_waitcnt vmcnt(12)\n"
  "v_mfma_f32_16x16x32_bf16 v[52:55], v[188:191], a[192:195], v[52:55]\n"
  "v_mfma_f32_16x16x32_bf16 v[60:63], v[188:191], a[196:199], v[60:63]\n"
  "buffer_load_dwordx4 v[160:163], v233, s[20:23], s73 offen offset:2048 sc1\n"
  "v_mfma_f32_16x16x32_bf16 v[68:71], v[188:191], a[200:203], v[68:71]\n"
  "v_mfma_f32_16x16x32_bf16 v[76:79], v[188:191], a[204:207], v[76:79]\n"
  "s_waitcnt vmcnt(12)\n"
  "v_mfma_f32_16x16x32_bf16 v[48:51], v[112:115], v[176:179], v[48:51]\n"
  "buffer_load_dwordx4 v[164:167], v233, s[20:23], s73 offen offset:3072 sc1\n"
  "v_mfma_f32_16x16x32_bf16 v[56:59], v[112:115], v[200:203], v[56:59]\n"
  "v_mfma_f32_16x16x32_bf16 v[64:67], v[112:115], v[196:199], v[64:67]\n"
  "v_mfma_f32_16x16x32_bf16 v[72:75], v[112:115], v[192:195], v[72:75]\n"
  "s_nop 5\n"
  "ds_write_b64 v242, v[56:57] offset:0\n"
  "s_waitcnt vmcnt(12)\n"
  "v_mfma_f32_16x16x32_bf16 v[52:55], v[116:119], v[176:179], v[52:55]\n"
  "ds_write_b64 v242, v[58:59] offset:8\n"
  "v_mfma_f32_16x16x32_bf16 v[60:63], v[116:119], v[200:203], v[60:63]\n"
  "buffer_load_dwordx4 v[168:171], v233, s[20:23], s74 offen sc1\n"
  "v_mfma_f32_16x16x32_bf16 v[68:71], v[116:119], v[196:199], v[68:71]\n"
  "ds_write_b64 v243, v[64:65] offset:0\n"
  "v_mfma_f32_16x16x32_bf16 v[76:79], v[116:119], v[192:195], v[76:79]\n"
  "s_nop 3\n"
  "ds_write_b64 v242, v[60:61] offset:1024\n"
  "s_waitcnt vmcnt(12)\n"
  "v_mfma_f32_16x16x32_bf16 v[80:83], v[120:123], a[0:3], v[80:83]\n"
  "ds_write_b64 v242, v[62:63] offset:1032\n"
  "v_mfma_f32_16x16x32_bf16 v[88:91], v[120:123], a[4:7], 0\n"
  "buffer_load_dwordx4 v[172:175], v233, s[20:23], s74 offen offset:1024 sc1\n"
  "ds_write_b64 v243, v[66:67] offset:8\n"
  "v_mfma_f32_16x16x32_bf16 v[96:99], v[120:123], a[8:11], 0\n"
  "ds_write_b64 v243, v[68:69] offset:1024\n"
  "v_mfma_f32_16x16x32_bf16 v[104:107], v[120:123], a[12:15], 0\n"
  "buffer_load_dwordx4 v[180:183], v233, s[20:23], s74 offen offset:2048 sc1\n"
  "ds_write_b64 v243, v[70:71] offset:1032\n"
  "s_waitcnt vmcnt(13) lgkmcnt(6)\n"
  "v_mfma_f32_16x16x32_bf16 v[84:87], v[124:127], a[0:3], v[84:87]\n"
  "ds_write_b64 v244, v[72:73] offset:0\n"
  "v_mfma_f32_16x16x32_bf16 v[92:95], v[124:127], a[4:7], 0\n"
  "ds_write_b64 v244, v[74:75] offset:8\n"
  "v_mfma_f32_16x16x32_bf16 v[100:103], v[124:127], a[8:11], 0\n"
  "ds_write_b64 v244, v[76:77] offset:1024\n"
  "v_mfma_f32_16x16x32_bf16 v[108:111], v[124:127], a[12:15], 0\n"
  "buffer_load_dwordx4 v[188:191], v233, s[20:23], s74 offen offset:3072 sc1\n"
  "ds_write_b64 v244, v[78:79] offset:1032\n"
  "s_waitcnt vmcnt(13) lgkmcnt(6)\n"
  "v_mfma_f32_16x16x32_bf16 v[80:83], v[128:131], a[16:19], v[80:83]\n"
  "v_mfma_f32_16x16x32_bf16 v[88:91], v[128:131], a[20:23], v[88:91]\n"
  "v_mfma_f32_16x16x32_bf16 v[96:99], v[128:131], a[24:27], v[96:99]\n"
  "buffer_load_dwordx4 v[112:115], v233, s[20:23], s75 offen sc1\n"
  "v_mfma_f32_16x16x32_bf16 v[104:107], v[128:131], a[28:31], v[104:107]\n"
  "s_waitcnt vmcnt(13) lgkmcnt(3)\n"
  "v_mfma_f32_16x16x32_bf16 v[84:87], v[132:135], a[16:19], v[84:87]\n"
  "s_waitcnt lgkmcnt(0)\n"
  "s_barrier\n"
  "v_mfma_f32_16x16x32_bf16 v[92:95], v[132:135], a[20:23], v[92:95]\n"
  "ds_read_b128 v[56:59], v241 offset:0\n"
  "ds_read_b128 v[64:67], v241 offset:2048\n"
  "v_mfma_f32_16x16x32_bf16 v[100:103], v[132:135], a[24:27], v[100:103]\n"
  "ds_read_b128 v[72:75], v241 offset:4096\n"
  "buffer_load_dwordx4 v[116:119], v233, s[20:23], s75 offen offset:1024 sc1\n"
  "v_mfma_f32_16x16x32_bf16 v[108:111], v[132:135], a[28:31], v[108:111]\n"
  "ds_read_b128 v[60:63], v241 offset:1024\n"
  "s_waitcnt vmcnt(13)\n"
  "v_mfma_f32_16x16x32_bf16 v[80:83], v[136:139], a[32:35], v[80:83]\n"
  "ds_read_b128 v[68:71], v241 offset:3072\n"
  "v_mfma_f32_16x16x32_bf16 v[88:91], v[136:139], a[36:39], v[88:91]\n"
  "ds_read_b128 v[76:79], v241 offset:5120\n"
  "buffer_load_dwordx4 v[120:123], v233, s[20:23], s75 offen offset:2048 sc1\n"
  "v_mfma_f32_16x16x32_bf16 v[96:99], v[136:139], a[40:43], v[96:99]\n"
  "v_mfma_f32_16x16x32_bf16 v[104:107], v[136:139], a[44:47], v[104:107]\n"
  "s_waitcnt lgkmcnt(5)\n"
  "v_add_f32 v48, v48, v56\n"
  "s_waitcnt vmcnt(13)\n"
  "v_mfma_f32_16x16x32_bf16 v[84:87], v[140:143], a[32:35], v[84:87]\n"
  "s_waitcnt lgkmcnt(3)\n"
  "v_add_f32 v64, v64, v72\n"
  "v_mfma_f32_16x16x32_bf16 v[92:95], v[140:143], a[36:39], v[92:95]\n"
  "v_add_f32 v49, v49, v57\n"
  "v_add_f32 v65, v65, v73\n"
  "v_mfma_f32_16x16x32_bf16 v[100:103], v[140:143], a[40:43], v[100:103]\n"
  "v_add_f32 v50, v50, v58\n"
  "v_add_f32 v66, v66, v74\n"
  "v_mfma_f32_16x16x32_bf16 v[108:111], v[140:143], a[44:47], v[108:111]\n"
  "v_add_f32 v51, v51, v59\n"
  "v_add_f32 v67, v67, v75\n"
  "s_waitcnt vmcnt(12) lgkmcnt(2)\n"
  "v_mfma_f32_16x16x32_bf16 v[80:83], v[144:147], a[48:51], v[80:83]\n"
  "buffer_load_dwordx4 v[124:127], v233, s[20:23], s75 offen offset:3072 sc1\n"
  "v_add_f32 v52, v52, v60\n"
  "v_mfma_f32_16x16x32_bf16 v[88:91], v[144:147], a[52:55], v[88:91]\n"
  "s_waitcnt lgkmcnt(0)\n"
  "v_add_f32 v68, v68, v76\n"
  "v_mfma_f32_16x16x32_bf16 v[96:99], v[144:147], a[56:59], v[96:99]\n"
  "v_add_f32 v53, v53, v61\n"
  "v_add_f32 v69, v69, v77\n"
  "v_mfma_f32_16x16x32_bf16 v[104:107], v[144:147], a[60:63], v[104:107]\n"
  "v_add_f32 v54, v54, v62\n"
  "v_add_f32 v70, v70, v78\n"
  "s_waitcnt vmcnt(12)\n"
  "v_mfma_f32_16x16x32_bf16 v[84:87], v[148:151], a[48:51], v[84:87]\n"
  "buffer_load_dwordx4 v[128:131], v233, s[20:23], s76 offen sc1\n"
  "v_add_f32 v55, v55, v63\n"
  "v_mfma_f32_16x16x32_bf16 v[92:95], v[148:151], a[52:55], v[92:95]\n"
  "v_add_f32 v71, v71, v79\n"
  "v_add_f32 v48, v48, v64\n"
  "v_mfma_f32_16x16x32_bf16 v[100:103], v[148:151], a[56:59], v[100:103]\n"
  "v_add_f32 v49, v49, v65\n"
  "v_add_f32 v50, v50, v66\n"
  "v_mfma_f32_16x16x32_bf16 v[108:111], v[148:151], a[60:63], v[108:111]\n"
  "v_add_f32 v51, v51, v67\n"
  "v_add_f32 v52, v52, v68\n"
  "s_waitcnt vmcnt(12)\n"
  "v_mfma_f32_16x16x32_bf16 v[80:83], v[152:155], a[64:67], v[80:83]\n"
  "buffer_load_dwordx4 v[132:135], v233, s[20:23], s76 offen offset:1024 sc1\n"
  "v_add_f32 v53, v53, v69\n"
  "v_mfma_f32_16x16x32_bf16 v[88:91], v[152:155], a[68:71], v[88:91]\n"
  "v_add_f32 v54, v54, v70\n"
  "v_add_f32 v55, v55, v71\n"
  "v_mfma_f32_16x16x32_bf16 v[96:99], v[152:155], a[72:75], v[96:99]\n"
  "v_cvt_pk_bf16_f32 v22, v48, v49\n"
  "v_cvt_pk_bf16_f32 v23, v50, v51\n"
  "v_mfma_f32_16x16x32_bf16 v[104:107], v[152:155], a[76:79], v[104:107]\n"
  "v_cvt_pk_bf16_f32 v24, v52, v53\n"
  "buffer_load_dwordx4 v[136:139], v233, s[20:23], s76 offen offset:2048 sc1\n"
  "s_waitcnt vmcnt(13)\n"
  "v_mfma_f32_16x16x32_bf16 v[84:87], v[156:159], a[64:67], v[84:87]\n"
  "v_cvt_pk_bf16_f32 v25, v54, v55\n"
  "buffer_store_dwordx4 v[22:25], v1, s[20:23], s18 offen sc1\n"
  "v_mfma_f32_16x16x32_bf16 v[92:95], v[156:159], a[68:71], v[92:95]\n"
  "v_lshlrev_b32 v64, 16, v22\n"
  "v_and_b32 v65, 0xffff0000, v22\n"
  "v_mfma_f32_16x16x32_bf16 v[100:103], v[156:159], a[72:75], v[100:103]\n"
  "v_lshlrev_b32 v66, 16, v23\n"
  "v_and_b32 v67, 0xffff0000, v23\n"
  "v_mfma_f32_16x16x32_bf16 v[108:111], v[156:159], a[76:79], v[108:111]\n"
  "buffer_load_dwordx4 v[140:143], v233, s[20:23], s76 offen offset:3072 sc1\n"
  "v_mul_f32 v58, v64, v64\n"
  "s_waitcnt vmcnt(14)\n"
  "v_mfma_f32_16x16x32_bf16 v[80:83], v[160:163], a[80:83], v[80:83]\n"
  "v_mul_f32 v59, v65, v65\n"
  "v_lshlrev_b32 v68, 16, v24\n"
  "v_mfma_f32_16x16x32_bf16 v[88:91], v[160:163], a[84:87], v[88:91]\n"
  "v_and_b32 v69, 0xffff0000, v24\n"
  "v_add_f32 v56, v64, v66\n"
  "v_mfma_f32_16x16x32_bf16 v[96:99], v[160:163], a[88:91], v[96:99]\n"
  "v_add_f32 v57, v65, v67\n"
  "v_fmac_f32 v58, v66, v66\n"
  "v_mfma_f32_16x16x32_bf16 v[104:107], v[160:163], a[92:95], v[104:107]\n"
  "buffer_load_dwordx4 v[144:147], v233, s[20:23], s77 offen sc1\n"
  "v_fmac_f32 v59, v67, v67\n"
  "s_waitcnt vmcnt(14)\n"
  "v_mfma_f32_16x16x32_bf16 v[84:87], v[164:167], a[80:83], v[84:87]\n"
  "v_lshlrev_b32 v70, 16, v25\n"
  "v_and_b32 v71, 0xffff0000, v25\n"
  "v_mfma_f32_16x16x32_bf16 v[92:95], v[164:167], a[84:87], v[92:95]\n"
  "v_add_f32 v56, v56, v68\n"
  "v_fmac_f32 v58, v68, v68\n"
  "v_mfma_f32_16x16x32_bf16 v[100:103], v[164:167], a[88:91], v[100:103]\n"
  "v_add_f32 v57, v57, v69\n"
  "v_fmac_f32 v59, v69, v69\n"
  "v_mfma_f32_16x16x32_bf16 v[108:111], v[164:167], a[92:95], v[108:111]\n"
  "buffer_load_dwordx4 v[148:151], v233, s[20:23], s77 offen offset:1024 sc1\n"
  "v_add_f32 v56, v56, v70\n"
  "s_waitcnt vmcnt(14)\n"
  "v_mfma_f32_16x16x32_bf16 v[80:83], v[168:171], a[96:99], v[80:83]\n"
  "v_fmac_f32 v58, v70, v70\n"
  "v_add_f32 v57, v57, v71\n"
  "v_mfma_f32_16x16x32_bf16 v[88:91], v[168:171], a[100:103], v[88:91]\n"
  "v_fmac_f32 v59, v71, v71\n"
  "v_mfma_f32_16x16x32_bf16 v[96:99], v[168:171], a[104:107], v[96:99]\n"
  "buffer_load_dwordx4 v[152:155], v233, s[20:23], s77 offen offset:2048 sc1\n"
  "v_mfma_f32_16x16x32_bf16 v[104:107], v[168:171], a[108:111], v[104:107]\n"
  "s_waitcnt vmcnt(14)\n"
  "v_mfma_f32_16x16x32_bf16 v[84:87], v[172:175], a[96:99], v[84:87]\n"
  "v_mfma_f32_16x16x32_bf16 v[92:95], v[172:175], a[100:103], v[92:95]\n"
  "v_mfma_f32_16x16x32_bf16 v[100:103], v[172:175], a[104:107], v[100:103]\n"
  "buffer_load_dwordx4 v[156:159], v233, s[20:23], s77 offen offset:3072 sc1\n"
  "s_mul_i32 s87, s58, 1835008\n"
  "v_mfma_f32_16x16x32_bf16 v[108:111], v[172:175], a[108:111], v[108:111]\n"
  "s_lshl_b32 s88, s65, 7\n"
  "s_add_u32 s87, s87, s88\n"
  "s_waitcnt vmcnt(14)\n"
  "v_mfma_f32_16x16x32_bf16 v[80:83], v[180:183], a[112:115], v[80:83]\n"
  "s_or_b32 s87, s87, s70\n"
  "v_lshrrev_b32 v45, 3, v0\n"
  "v_mfma_f32_16x16x32_bf16 v[88:91], v[180:183], a[116:119], v[88:91]\n"
  "v_mul_u32_u24 v232, 3584, v45\n"
  "v_and_b32 v45, 2, v45\n"
  "v_mfma_f32_16x16x32_bf16 v[96:99], v[180:183], a[120:123], v[96:99]\n"
  "s_add_u32 m0, s8, 0\n"
  "s_nop 0\n"
  "buffer_load_dwordx4 v187, s[20:23], 0 offen sc1 lds\n"
  "v_mfma_f32_16x16x32_bf16 v[104:107], v[180:183], a[124:127], v[104:107]\n"
  "v_xor_b32 v45, v45, v0\n"
  "v_and_b32 v45, 7, v45\n"
  "s_waitcnt vmcnt(14)\n"
  "v_mfma_f32_16x16x32_bf16 v[84:87], v[188:191], a[112:115], v[84:87]\n"
  "v_lshl_add_u32 v232, v45, 4, v232\n"
  "v_add_u32 v232, s87, v232\n"
  "v_mfma_f32_16x16x32_bf16 v[92:95], v[188:191], a[116:119], v[92:95]\n"
  "v_xor_b32 v233, 64, v232\n"
  "v_mfma_f32_16x16x32_bf16 v[100:103], v[188:191], a[120:123], v[100:103]\n"
  "v_mfma_f32_16x16x32_bf16 v[108:111], v[188:191], a[124:127], v[108:111]\n"
  "s_waitcnt vmcnt(13)\n"
  "v_mfma_f32_16x16x32_bf16 v[80:83], v[112:115], a[128:131], v[80:83]\n"
  "v_mfma_f32_16x16x32_bf16 v[88:91], v[112:115], a[132:135], v[88:91]\n"
  "v_mfma_f32_16x16x32_bf16 v[96:99], v[112:115], a[136:139], v[96:99]\n"
  "v_mfma_f32_16x16x32_bf16 v[104:107], v[112:115], a[140:143], v[104:107]\n"
  "s_waitcnt vmcnt(12)\n"
  "v_mfma_f32_16x16x32_bf16 v[84:87], v[116:119], a[128:131], v[84:87]\n"
  "v_mfma_f32_16x16x32_bf16 v[92:95], v[116:119], a[132:135], v[92:95]\n"
  "v_mfma_f32_16x16x32_bf16 v[100:103], v[116:119], a[136:139], v[100:103]\n"
  "v_mfma_f32_16x16x32_bf16 v[108:111], v[116:119], a[140:143], v[108:111]\n"
  "s_waitcnt vmcnt(11)\n"
  "v_mfma_f32_16x16x32_bf16 v[80:83], v[120:123], a[144:147], v[80:83]\n"
  "v_mfma_f32_16x16x32_bf16 v[88:91], v[120:123], a[148:151], v[88:91]\n"
  "v_mfma_f32_16x16x32_bf16 v[96:99], v[120:123], a[152:155], v[96:99]\n"
  "v_mfma_f32_16x16x32_bf16 v[104:107], v[120:123], a[156:159], v[104:107]\n"
  "s_waitcnt vmcnt(10)\n"
  "v_mfma_f32_16x16x32_bf16 v[84:87], v[124:127], a[144:147], v[84:87]\n"
  "v_mfma_f32_16x16x32_bf16 v[92:95], v[124:127], a[148:151], v[92:95]\n"
  "v_mfma_f32_16x16x32_bf16 v[100:103], v[124:127], a[152:155], v[100:103]\n"
  "v_mfma_f32_16x16x32_bf16 v[108:111], v[124:127], a[156:159], v[108:111]\n"
  "s_waitcnt vmcnt(9)\n"
  "v_mfma_f32_16x16x32_bf16 v[80:83], v[128:131], a[160:163], v[80:83]\n"
  "v_mfma_f32_16x16x32_bf16 v[88:91], v[128:131], a[164:167], v[88:91]\n"
  "s_waitcnt vmcnt(6)\n"
  "s_barrier\n"
  "buffer_store_dwordx2 v[36:37], v248, s[20:23], s14 offen sc1\n"
  "v_mfma_f32_16x16x32_bf16 v[96:99], v[128:131], a[168:171], v[96:99]\n"
  "s_add_u32 m0, s8, 0\n"
  "s_nop 0\n"
  "buffer_load_dwordx4 v187, s[20:23], 0 offen offset:1024 sc1 lds\n"
  "v_mfma_f32_16x16x32_bf16 v[104:107], v[128:131], a[172:175], v[104:107]\n"
  "s_add_u32 m0, s8, 0\n"
  "s_nop 0\n"
  "buffer_load_dwordx4 v187, s[20:23], 0 offen offset:2048 sc1 lds\n"
  "v_mfma_f32_16x16x32_bf16 v[84:87], v[132:135], a[160:163], v[84:87]\n"
  "s_add_u32 m0, s8, 0\n"
  "s_nop 0\n"
  "buffer_load_dwordx4 v187, s[20:23], 0 offen offset:3072 sc1 lds\n"
  "v_mfma_f32_16x16x32_bf16 v[92:95], v[132:135], a[164:167], v[92:95]\n"
  "v_mfma_f32_16x16x32_bf16 v[100:103], v[132:135], a[168:171], v[100:103]\n"
  "v_mfma_f32_16x16x32_bf16 v[108:111], v[132:135], a[172:175], v[108:111]\n"
  "v_mfma_f32_16x16x32_bf16 v[80:83], v[136:139], a[176:179], v[80:83]\n"
  "v_mfma_f32_16x16x32_bf16 v[88:91], v[136:139], a[180:183], v[88:91]\n"
  "s_add_u32 m0, s8, 4096\n"
  "s_nop 0\n"
  "buffer_load_dwordx4 v187, s[20:23], s72 offen sc1 lds\n"
  "v_mfma_f32_16x16x32_bf16 v[96:99], v[136:139], a[184:187], v[96:99]\n"
  "v_mfma_f32_16x16x32_bf16 v[104:107], v[136:139], a[188:191], v[104:107]\n"
  "s_waitcnt vmcnt(10)\n"
  "v_mfma_f32_16x16x32_bf16 v[84:87], v[140:143], a[176:179], v[84:87]\n"
  "v_mfma_f32_16x16x32_bf16 v[92:95], v[140:143], a[180:183], v[92:95]\n"
  "v_mfma_f32_16x16x32_bf16 v[100:103], v[140:143], a[184:187], v[100:103]\n"
  "s_add_u32 m0, s8, 4096\n"
  "s_nop 0\n"
  "buffer_load_dwordx4 v187, s[20:23], s72 offen offset:1024 sc1 lds\n"
  "v_mfma_f32_16x16x32_bf16 v[108:111], v[140:143], a[188:191], v[108:111]\n"
  "s_waitcnt vmcnt(10)\n"
  "v_mfma_f32_16x16x32_bf16 v[80:83], v[144:147], a[192:195], v[80:83]\n"
  "v_mfma_f32_16x16x32_bf16 v[88:91], v[144:147], a[196:199], v[88:91]\n"
  "v_mfma_f32_16x16x32_bf16 v[96:99], v[144:147], a[200:203], v[96:99]\n"
  "v_mfma_f32_16x16x32_bf16 v[104:107], v[144:147], a[204:207], v[104:107]\n"
  "s_add_u32 m0, s8, 4096\n"
  "s_nop 0\n"
  "buffer_load_dwordx4 v187, s[20:23], s72 offen offset:2048 sc1 lds\n"
  "s_waitcnt vmcnt(10)\n"
  "v_mfma_f32_16x16x32_bf16 v[84:87], v[148:151], a[192:195], v[84:87]\n"
  "v_mfma_f32_16x16x32_bf16 v[92:95], v[148:151], a[196:199], v[92:95]\n"
  "v_mfma_f32_16x16x32_bf16 v[100:103], v[148:151], a[200:203], v[100:103]\n"
  "v_mfma_f32_16x16x32_bf16 v[108:111], v[148:151], a[204:207], v[108:111]\n"
  "s_add_u32 m0, s8, 4096\n"
  "s_nop 0\n"
  "buffer_load_dwordx4 v187, s[20:23], s72 offen offset:3072 sc1 lds\n"
  "s_waitcnt vmcnt(10)\n"
  "v_mfma_f32_16x16x32_bf16 v[80:83], v[152:155], v[176:179], v[80:83]\n"
  "v_mfma_f32_16x16x32_bf16 v[88:91], v[152:155], v[200:203], v[88:91]\n"
  "v_mfma_f32_16x16x32_bf16 v[96:99], v[152:155], v[196:199], v[96:99]\n"
  "v_mfma_f32_16x16x32_bf16 v[104:107], v[152:155], v[192:195], v[104:107]\n"
  "s_waitcnt vmcnt(9)\n"
  "v_mfma_f32_16x16x32_bf16 v[84:87], v[156:159], v[176:179], v[84:87]\n"
  "s_add_u32 m0, s8, 8192\n"
  "s_nop 0\n"
  "buffer_load_dwordx4 v187, s[20:23], s73 offen sc1 lds\n"
  "v_mfma_f32_16x16x32_bf16 v[92:95], v[156:159], v[200:203], v[92:95]\n"
  "s_lshl2_add_u32 s87, s68, s58\n"
  "s_bfe_u32 s89, s99, 0x20001\n"
  "v_mfma_f32_16x16x32_bf16 v[100:103], v[156:159], v[196:199], v[100:103]\n"
  "s_and_b32 s90, s99, 1\n"
  "s_lshr_b32 s88, s87, 5\n"
  "v_mfma_f32_16x16x32_bf16 v[108:111], v[156:159], v[192:195], v[108:111]\n"
  "ds_write_b128 v242, v[88:91] offset:24576\n"
  "s_mul_i32 s88, s88, 512\n"
  "s_and_b32 s87, s87, 31\n"
  "s_add_u32 m0, s8, 8192\n"
  "s_nop 0\n"
  "buffer_load_dwordx4 v187, s[20:23], s73 offen offset:1024 sc1 lds\n"
  "s_add_u32 s87, s87, s88\n"
  "s_mul_i32 s87, s87, 3584\n"
  "s_mul_i32 s89, s89, 896\n"
  "ds_write_b128 v242, v[92:95] offset:25600\n"
  "s_add_u32 s87, s87, s89\n"
  "v_lshrrev_b32 v45, 3, v0\n"
  "v_add_u32 v45, 0, v45\n"
  "v_mul_u32_u24 v45, 114688, v45\n"
  "v_and_b32 v46, 7, v0\n"
  "v_min_u32 v46, 6, v46\n"
  "v_lshl_add_u32 v45, v46, 7, v45\n"
  "v_add_u32 v45, s87, v45\n"
  "v_mov_b32 v200, v45\n"
  "ds_write_b128 v243, v[96:99] offset:24576\n"
  "s_add_u32 m0, s8, 8192\n"
  "s_nop 0\n"
  "buffer_load_dwordx4 v187, s[20:23], s73 offen offset:2048 sc1 lds\n"
  "s_lshl2_add_u32 s87, s68, s58\n"
  "s_bfe_u32 s89, s99, 0x20001\n"
  "s_mul_i32 s89, s89, 7\n"
  "s_or_b32 s89, s89, 3\n"
  "s_lshr_b32 s90, s89, 2\n"
  "s_mul_i32 s87, s87, 57344\n"
  "ds_write_b128 v243, v[100:103] offset:25600\n"
  "s_lshl_b32 s89, s89, 7\n"
  "s_add_u32 s87, s87, s89\n"
  "v_xor_b32 v46, s90, v0\n"
  "v_and_b32 v46, 1, v46\n"
  "v_mul_u32_u24 v45, 3584, v2\n"
  "v_lshl_add_u32 v45, v46, 9, v45\n"
  "s_add_u32 m0, s8, 8192\n"
  "s_nop 0\n"
  "buffer_load_dwordx4 v187, s[20:23], s73 offen offset:3072 sc1 lds\n"
  "ds_write_b128 v244, v[104:107] offset:24576\n"
  "v_add_u32 v45, s87, v45\n"
  "v_lshl_add_u32 v45, v3, 5, v45\n"
  "v_mov_b32 v201, v45\n"
  "ds_write_b128 v244, v[108:111] offset:25600\n"
  "s_add_u32 m0, s8, 12288\n"
  "s_nop 0\n"
  "buffer_load_dwordx4 v187, s[20:23], s74 offen sc1 lds\n"
  "s_waitcnt lgkmcnt(0)\n"
  "s_barrier\n"
  "ds_read_b128 v[88:91], v241 offset:24576\n"
  "ds_read_b128 v[96:99], v241 offset:26624\n"
  "s_add_u32 m0, s8, 12288\n"
  "s_nop 0\n"
  "buffer_load_dwordx4 v187, s[20:23], s74 offen offset:1024 sc1 lds\n"
  "ds_read_b128 v[104:107], v241 offset:28672\n"
  "ds_read_b128 v[92:95], v241 offset:25600\n"
  "ds_read_b128 v[100:103], v241 offset:27648\n"
  "ds_read_b128 v[108:111], v241 offset:29696\n"
  "s_add_u32 m0, s8, 12288\n"
  "s_nop 0\n"
  "buffer_load_dwordx4 v187, s[20:23], s74 offen offset:2048 sc1 lds\n"
  "s_waitcnt lgkmcnt(5)\n"
  "v_pk_add_f32 v[80:81], v[80:81], v[88:89]\n"
  "v_pk_add_f32 v[82:83], v[82:83], v[90:91]\n"
  "s_waitcnt lgkmcnt(3)\n"
  "v_pk_add_f32 v[96:97], v[96:97], v[104:105]\n"
  "v_pk_add_f32 v[80:81], v[80:81], v[96:97]\n"
  "v_pk_add_f32 v[98:99], v[98:99], v[106:107]\n"
  "v_pk_add_f32 v[82:83], v[82:83], v[98:99]\n"
  "s_waitcnt lgkmcnt(2)\n"
  "v_pk_add_f32 v[84:85], v[84:85], v[92:93]\n"
  "v_cvt_pk_bf16_f32 v26, v80, v81\n"
  "v_pk_add_f32 v[86:87], v[86:87], v[94:95]\n"
  "v_cvt_pk_bf16_f32 v27, v82, v83\n"
  "s_waitcnt lgkmcnt(0)\n"
  "v_pk_add_f32 v[100:101], v[100:101], v[108:109]\n"
  "v_pk_add_f32 v[84:85], v[84:85], v[100:101]\n"
  "v_pk_add_f32 v[102:103], v[102:103], v[110:111]\n"
  "v_lshlrev_b32 v96, 16, v26\n"
  "v_and_b32 v97, 0xffff0000, v26\n"
  "v_pk_add_f32 v[86:87], v[86:87], v[102:103]\n"
  "v_cvt_pk_bf16_f32 v28, v84, v85\n"
  "v_lshlrev_b32 v98, 16, v27\n"
  "v_and_b32 v99, 0xffff0000, v27\n"
  "v_pk_add_f32 v[56:57], v[56:57], v[96:97]\n"
  "v_pk_fma_f32 v[58:59], v[96:97], v[96:97], v[58:59]\n"
  "v_cvt_pk_bf16_f32 v29, v86, v87\n"
  "v_lshlrev_b32 v100, 16, v28\n"
  "v_and_b32 v101, 0xffff0000, v28\n"
  "v_pk_add_f32 v[56:57], v[56:57], v[98:99]\n"
  "v_pk_fma_f32 v[58:59], v[98:99], v[98:99], v[58:59]\n"
  "s_add_u32 m0, s8, 12288\n"
  "s_nop 0\n"
  "buffer_load_dwordx4 v187, s[20:23], s74 offen offset:3072 sc1 lds\n"
  "v_lshlrev_b32 v102, 16, v29\n"
  "v_and_b32 v103, 0xffff0000, v29\n"
  "v_pk_add_f32 v[56:57], v[56:57], v[100:101]\n"
  "v_pk_fma_f32 v[58:59], v[100:101], v[100:101], v[58:59]\n"
  "v_pk_add_f32 v[56:57], v[56:57], v[102:103]\n"
  "v_pk_fma_f32 v[58:59], v[102:103], v[102:103], v[58:59]\n"
  "v_add_f32 v56, v56, v57\n"
  "v_add_f32 v58, v58, v59\n"
  "buffer_store_dwordx4 v[26:29], v1, s[20:23], s19 offen sc1\n"
  "s_nop 0\n"
  "v_permlane16_swap_b32 v56, v58\n"
  "v_add_f32 v56, v56, v58\n"
  "v_mov_b32 v57, v56\n"
  "s_nop 1\n"
  "v_permlane32_swap_b32 v56, v57\n"
  "v_add_f32 v56, v56, v57\n"
  "buffer_store_dword v56, v245, s[20:23], 0 offen sc1\n"
  "s_lshl_b32 s87, s64, 5\n"
  "s_add_u32 s89, s87, s63\n"
  "s_lshr_b32 s94, s89, 3\n"
  "s_and_b32 s95, s89, 7\n"
  "s_lshl_b32 s94, s94, 9\n"
  "s_lshl3_add_u32 s94, s95, s94\n"
  "s_add_u32 s12, s94, 98304\n"
  "s_add_u32 s87, s87, s89\n"
  "s_lshl1_add_u32 s87, s87, s59\n"
  "s_lshl2_add_u32 s87, s87, s60\n"
  "s_lshl_b32 s87, s87, 9\n"
  "s_mov_b32 s52, 131072\n"
  "s_add_u32 s87, s87, 8388608\n"
  "v_lshl_add_u32 v245, v2, 4, s87\n"
  "v_and_b32 v45, 16, v0\n"
  "v_lshl_add_u32 v245, v45, 6, v245\n"
  "v_and_b32 v45, 32, v0\n"
  "v_lshl_add_u32 v245, v45, 3, v245\n"
  "s_sleep 4\n"
  "buffer_load_dwordx2 v[206:207], v204, s[20:23], 0 offen sc1\n"
  "s_waitcnt vmcnt(3)\n"
  "s_barrier\n"
  "ds_read_b64_tr_b16 v[112:113], v222 offset:1024\n"
  "ds_read_b64_tr_b16 v[114:115], v222 offset:4096\n"
  "ds_read_b64_tr_b16 v[116:117], v222 offset:2048\n"
  "ds_read_b64_tr_b16 v[118:119], v222 offset:5120\n"
  "ds_read_b64_tr_b16 v[120:121], v222 offset:0\n"
  "ds_read_b64_tr_b16 v[122:123], v222 offset:3072\n"
  "s_waitcnt vmcnt(1)\n"
  "s_barrier\n"
  "buffer_store_dwordx2 v[36:37], v248, s[20:23], s14 offen offset:128 sc1\n"
  "s_add_u32 m0, s10, 98304\n"
  "s_nop 0\n"
  "buffer_load_dwordx4 v184, s[32:35], 0 offen lds\n"
  "s_add_u32 m0, s10, 99328\n"
  "s_nop 0\n"
  "buffer_load_dwordx4 v184, s[32:35], s73 offen lds\n"
  "ds_read_b64_tr_b16 v[124:125], v223 offset:1024\n"
  "ds_read_b64_tr_b16 v[126:127], v223 offset:4096\n"
  "ds_read_b64_tr_b16 v[128:129], v223 offset:2048\n"
  "ds_read_b64_tr_b16 v[130:131], v223 offset:5120\n"
  "ds_read_b64_tr_b16 v[132:133], v223 offset:0\n"
  "s_add_u32 m0, s10, 100352\n"
  "s_nop 0\n"
  "buffer_load_dwordx4 v184, s[32:35], s75 offen lds\n"
  "ds_read_b64_tr_b16 v[134:135], v223 offset:3072\n"
  "s_add_u32 m0, s10, 101376\n"
  "s_nop 0\n"
  "buffer_load_dwordx4 v184, s[32:35], s77 offen lds\n"
  "s_add_u32 m0, s10, 102400\n"
  "s_nop 0\n"
  "buffer_load_dwordx4 v184, s[32:35], s79 offen lds\n"
  "s_add_u32 m0, s10, 103424\n"
  "s_nop 0\n"
  "buffer_load_dwordx4 v184, s[32:35], s81 offen lds\n"
  "s_waitcnt vmcnt(7) lgkmcnt(0)\n"
  "v_cmp_ne_u64 vcc, s[56:57], v[206:207]\n"
  "s_cmp_eq_u64 vcc, 0\n"
  "s_cbranch_scc0 .Lfb17_%=\n"
  ".Lbk17_%=:\n"
  "buffer_load_dwordx4 a[0:3], v227, s[20:23], 0 offen sc1\n"
  "buffer_load_dwordx4 a[4:7], v228, s[20:23], 0 offen sc1\n"
  "buffer_load_dwordx4 a[8:11], v229, s[20:23], 0 offen sc1\n"
  "buffer_load_dwordx4 a[12:15], v227, s[20:23], 0 offen offset:1024 sc1\n"
  "buffer_load_dwordx4 a[16:19], v228, s[20:23], 0 offen offset:1024 sc1\n"
  "buffer_load_dwordx4 a[20:23], v229, s[20:23], 0 offen offset:1024 sc1\n"
  "buffer_load_dword v44, v250, s[20:23], 0 offen sc1\n"
  "s_and_b32 s87, s68, 3\n"
  "s_lshl_b32 s87, s87, 10\n"
  "s_bfe_u32 s88, s68, 0x10002\n"
  "s_lshl_b32 s88, s88, 14\n"
  "s_add_u32 s87, s87, s88\n"
  "s_add_u32 s87, s87, 12288\n"
  "v_and_b32 v45, 7, v0\n"
  "v_lshl_add_u32 v45, v45, 7, s87\n"
  "v_and_b32 v46, 0xfffffc00, v228\n"
  "v_add_u32 v45, v45, v46\n"
  "v_mov_b32 v250, v45\n"
  "buffer_load_dwordx4 a[24:27], v227, s[20:23], 0 offen offset:2048 sc1\n"
  "buffer_load_dwordx4 a[28:31], v228, s[20:23], 0 offen offset:2048 sc1\n"
  "buffer_load_dwordx4 a[32:35], v229, s[20:23], 0 offen offset:2048 sc1\n"
  "buffer_load_dwordx4 a[36:39], v227, s[20:23], 0 offen offset:3072 sc1\n"
  "buffer_load_dwordx4 a[40:43], v228, s[20:23], 0 offen offset:3072 sc1\n"
  "buffer_load_dwordx4 a[44:47], v229, s[20:23], 0 offen offset:3072 sc1\n"
  "s_waitcnt vmcnt(12)\n"
  "v_mfma_f32_16x16x32_bf16 v[48:51], v[112:115], a[0:3], 0\n"
  "buffer_load_dwordx4 a[48:51], v227, s[20:23], s72 offen sc1\n"
  "s_waitcnt vmcnt(12)\n"
  "v_mfma_f32_16x16x32_bf16 v[56:59], v[112:115], a[4:7], 0\n"
  "buffer_load_dwordx4 a[52:55], v228, s[20:23], s72 offen sc1\n"
  "s_waitcnt vmcnt(12)\n"
  "v_mfma_f32_16x16x32_bf16 v[64:67], v[112:115], a[8:11], 0\n"
  "ds_read_b64_tr_b16 v[112:113], v222 offset:13312\n"
  "v_mfma_f32_16x16x32_bf16 v[72:75], a[0:3], v[116:119], 0\n"
  "ds_read_b64_tr_b16 v[114:115], v222 offset:16384\n"
  "v_mfma_f32_16x16x32_bf16 v[80:83], a[4:7], v[116:119], 0\n"
  "buffer_load_dwordx4 a[56:59], v229, s[20:23], s72 offen sc1\n"
  "v_mfma_f32_16x16x32_bf16 v[88:91], a[8:11], v[116:119], 0\n"
  "ds_read_b64_tr_b16 v[116:117], v222 offset:14336\n"
  "v_mfma_f32_16x16x32_bf16 v[96:99], v[120:123], a[4:7], 0\n"
  "ds_read_b64_tr_b16 v[118:119], v222 offset:17408\n"
  "v_mfma_f32_16x16x32_bf16 v[104:107], v[120:123], a[8:11], 0\n"
  "ds_read_b64_tr_b16 v[120:121], v222 offset:12288\n"
  "v_mfma_f32_16x16x32_bf16 v[52:55], v[124:127], a[0:3], 0\n"
  "ds_read_b64_tr_b16 v[122:123], v222 offset:15360\n"
  "buffer_load_dwordx4 a[60:63], v227, s[20:23], s72 offen offset:1024 sc1\n"
  "v_mfma_f32_16x16x32_bf16 v[60:63], v[124:127], a[4:7], 0\n"
  "v_mfma_f32_16x16x32_bf16 v[68:71], v[124:127], a[8:11], 0\n"
  "ds_read_b64_tr_b16 v[124:125], v223 offset:13312\n"
  "v_mfma_f32_16x16x32_bf16 v[76:79], a[0:3], v[128:131], 0\n"
  "ds_read_b64_tr_b16 v[126:127], v223 offset:16384\n"
  "v_mfma_f32_16x16x32_bf16 v[84:87], a[4:7], v[128:131], 0\n"
  "buffer_load_dwordx4 a[64:67], v228, s[20:23], s72 offen offset:1024 sc1\n"
  "v_mfma_f32_16x16x32_bf16 v[92:95], a[8:11], v[128:131], 0\n"
  "ds_read_b64_tr_b16 v[128:129], v223 offset:14336\n"
  "v_mfma_f32_16x16x32_bf16 v[100:103], v[132:135], a[4:7], 0\n"
  "ds_read_b64_tr_b16 v[130:131], v223 offset:17408\n"
  "v_mfma_f32_16x16x32_bf16 v[108:111], v[132:135], a[8:11], 0\n"
  "ds_read_b64_tr_b16 v[132:133], v223 offset:12288\n"
  "s_waitcnt vmcnt(14) lgkmcnt(7)\n"
  "v_mfma_f32_16x16x32_bf16 v[48:51], v[112:115], a[12:15], v[48:51]\n"
  "ds_read_b64_tr_b16 v[134:135], v223 offset:15360\n"
  "buffer_load_dwordx4 a[68:71], v229, s[20:23], s72 offen offset:1024 sc1\n"
  "s_waitcnt vmcnt(14) lgkmcnt(6)\n"
  "v_mfma_f32_16x16x32_bf16 v[56:59], v[112:115], a[16:19], v[56:59]\n"
  "s_waitcnt vmcnt(13)\n"
  "v_mfma_f32_16x16x32_bf16 v[64:67], v[112:115], a[20:23], v[64:67]\n"
  "ds_read_b64_tr_b16 v[112:113], v222 offset:25600\n"
  "v_mfma_f32_16x16x32_bf16 v[72:75], a[12:15], v[116:119], v[72:75]\n"
  "ds_read_b64_tr_b16 v[114:115], v222 offset:28672\n"
  "buffer_load_dwordx4 a[72:75], v227, s[20:23], s72 offen offset:2048 sc1\n"
  "v_mfma_f32_16x16x32_bf16 v[80:83], a[16:19], v[116:119], v[80:83]\n"
  "v_mfma_f32_16x16x32_bf16 v[88:91], a[20:23], v[116:119], v[88:91]\n"
  "ds_read_b64_tr_b16 v[116:117], v222 offset:26624\n"
  "v_mfma_f32_16x16x32_bf16 v[96:99], v[120:123], a[16:19], v[96:99]\n"
  "ds_read_b64_tr_b16 v[118:119], v222 offset:29696\n"
  "v_mfma_f32_16x16x32_bf16 v[104:107], v[120:123], a[20:23], v[104:107]\n"
  "ds_read_b64_tr_b16 v[120:121], v222 offset:24576\n"
  "buffer_load_dwordx4 a[76:79], v228, s[20:23], s72 offen offset:2048 sc1\n"
  "s_waitcnt lgkmcnt(6)\n"
  "v_mfma_f32_16x16x32_bf16 v[52:55], v[124:127], a[12:15], v[52:55]\n"
  "ds_read_b64_tr_b16 v[122:123], v222 offset:27648\n"
  "v_mfma_f32_16x16x32_bf16 v[60:63], v[124:127], a[16:19], v[60:63]\n"
  "buffer_load_dwordx2 v[206:207], v204, s[20:23], 0 offen offset:128 sc1\n"
  "v_mfma_f32_16x16x32_bf16 v[68:71], v[124:127], a[20:23], v[68:71]\n"
  "ds_read_b64_tr_b16 v[124:125], v223 offset:25600\n"
  "v_mfma_f32_16x16x32_bf16 v[76:79], a[12:15], v[128:131], v[76:79]\n"
  "ds_read_b64_tr_b16 v[126:127], v223 offset:28672\n"
  "v_mfma_f32_16x16x32_bf16 v[84:87], a[16:19], v[128:131], v[84:87]\n"
  "v_mfma_f32_16x16x32_bf16 v[92:95], a[20:23], v[128:131], v[92:95]\n"
  "ds_read_b64_tr_b16 v[128:129], v223 offset:26624\n"
  "s_waitcnt lgkmcnt(6)\n"
  "v_mfma_f32_16x16x32_bf16 v[100:103], v[132:135], a[16:19], v[100:103]\n"
  "ds_read_b64_tr_b16 v[130:131], v223 offset:29696\n"
  "buffer_load_dwordx4 a[80:83], v229, s[20:23], s72 offen offset:2048 sc1\n"
  "v_mfma_f32_16x16x32_bf16 v[108:111], v[132:135], a[20:23], v[108:111]\n"
  "ds_read_b64_tr_b16 v[132:133], v223 offset:24576\n"
  "s_waitcnt vmcnt(15) lgkmcnt(6)\n"
  "v_mfma_f32_16x16x32_bf16 v[48:51], v[112:115], a[24:27], v[48:51]\n"
  "ds_read_b64_tr_b16 v[134:135], v223 offset:27648\n"
  "s_waitcnt vmcnt(14) lgkmcnt(6)\n"
  "v_mfma_f32_16x16x32_bf16 v[56:59], v[112:115], a[28:31], v[56:59]\n"
  "buffer_load_dwordx4 a[84:87], v227, s[20:23], s72 offen offset:3072 sc1\n"
  "s_waitcnt vmcnt(14)\n"
  "v_mfma_f32_16x16x32_bf16 v[64:67], v[112:115], a[32:35], v[64:67]\n"
  "ds_read_b64_tr_b16 v[112:113], v222 offset:37888\n"
  "v_mfma_f32_16x16x32_bf16 v[72:75], a[24:27], v[116:119], v[72:75]\n"
  "ds_read_b64_tr_b16 v[114:115], v222 offset:40960\n"
  "v_mfma_f32_16x16x32_bf16 v[80:83], a[28:31], v[116:119], v[80:83]\n"
  "v_mfma_f32_16x16x32_bf16 v[88:91], a[32:35], v[116:119], v[88:91]\n"
  "ds_read_b64_tr_b16 v[116:117], v222 offset:38912\n"
  "buffer_load_dwordx4 a[88:91], v228, s[20:23], s72 offen offset:3072 sc1\n"
  "v_mfma_f32_16x16x32_bf16 v[96:99], v[120:123], a[28:31], v[96:99]\n"
  "ds_read_b64_tr_b16 v[118:119], v222 offset:41984\n"
  "v_mfma_f32_16x16x32_bf16 v[104:107], v[120:123], a[32:35], v[104:107]\n"
  "ds_read_b64_tr_b16 v[120:121], v222 offset:36864\n"
  "s_waitcnt lgkmcnt(6)\n"
  "v_mfma_f32_16x16x32_bf16 v[52:55], v[124:127], a[24:27], v[52:55]\n"
  "ds_read_b64_tr_b16 v[122:123], v222 offset:39936\n"
  "v_mfma_f32_16x16x32_bf16 v[60:63], v[124:127], a[28:31], v[60:63]\n"
  "buffer_load_dwordx4 a[92:95], v229, s[20:23], s72 offen offset:3072 sc1\n"
  "v_mfma_f32_16x16x32_bf16 v[68:71], v[124:127], a[32:35], v[68:71]\n"
  "ds_read_b64_tr_b16 v[124:125], v223 offset:37888\n"
  "v_mfma_f32_16x16x32_bf16 v[76:79], a[24:27], v[128:131], v[76:79]\n"
  "ds_read_b64_tr_b16 v[126:127], v223 offset:40960\n"
  "v_mfma_f32_16x16x32_bf16 v[84:87], a[28:31], v[128:131], v[84:87]\n"
  "v_mfma_f32_16x16x32_bf16 v[92:95], a[32:35], v[128:131], v[92:95]\n"
  "ds_read_b64_tr_b16 v[128:129], v223 offset:38912\n"
  "s_add_u32 m0, s10, 104448\n"
  "s_nop 0\n"
  "buffer_load_dwordx4 v184, s[32:35], s83 offen lds\n"
  "s_waitcnt lgkmcnt(6)\n"
  "v_mfma_f32_16x16x32_bf16 v[100:103], v[132:135], a[28:31], v[100:103]\n"
  "ds_read_b64_tr_b16 v[130:131], v223 offset:41984\n"
  "v_mfma_f32_16x16x32_bf16 v[108:111], v[132:135], a[32:35], v[108:111]\n"
  "ds_read_b64_tr_b16 v[132:133], v223 offset:36864\n"
  "s_waitcnt vmcnt(16) lgkmcnt(6)\n"
  "v_mfma_f32_16x16x32_bf16 v[48:51], v[112:115], a[36:39], v[48:51]\n"
  "ds_read_b64_tr_b16 v[134:135], v223 offset:39936\n"
  "s_add_u32 m0, s10, 105472\n"
  "s_nop 0\n"
  "buffer_load_dwordx4 v184, s[32:35], s85 offen lds\n"
  "s_waitcnt vmcnt(16) lgkmcnt(6)\n"
  "v_mfma_f32_16x16x32_bf16 v[56:59], v[112:115], a[40:43], v[56:59]\n"
  "s_waitcnt vmcnt(15) lgkmcnt(5)\n"
  "v_mfma_f32_16x16x32_bf16 v[64:67], v[112:115], a[44:47], v[64:67]\n"
  "ds_read_b64_tr_b16 v[112:113], v224 offset:1024\n"
  "v_mfma_f32_16x16x32_bf16 v[72:75], a[36:39], v[116:119], v[72:75]\n"
  "ds_read_b64_tr_b16 v[114:115], v224 offset:4096\n"
  "s_add_u32 m0, s10, 106496\n"
  "s_nop 0\n"
  "buffer_load_dwordx4 v185, s[32:35], 0 offen lds\n"
  "v_mfma_f32_16x16x32_bf16 v[80:83], a[40:43], v[116:119], v[80:83]\n"
  "v_mfma_f32_16x16x32_bf16 v[88:91], a[44:47], v[116:119], v[88:91]\n"
  "ds_read_b64_tr_b16 v[116:117], v224 offset:2048\n"
  "v_mfma_f32_16x16x32_bf16 v[96:99], v[120:123], a[40:43], v[96:99]\n"
  "ds_read_b64_tr_b16 v[118:119], v224 offset:5120\n"
  "s_add_u32 m0, s10, 107520\n"
  "s_nop 0\n"
  "buffer_load_dwordx4 v185, s[32:35], s73 offen lds\n"
  "v_mfma_f32_16x16x32_bf16 v[104:107], v[120:123], a[44:47], v[104:107]\n"
  "ds_read_b64_tr_b16 v[120:121], v224 offset:0\n"
  "s_waitcnt lgkmcnt(5)\n"
  "v_mfma_f32_16x16x32_bf16 v[52:55], v[124:127], a[36:39], v[52:55]\n"
  "ds_read_b64_tr_b16 v[122:123], v224 offset:3072\n"
  "v_mfma_f32_16x16x32_bf16 v[60:63], v[124:127], a[40:43], v[60:63]\n"
  "s_add_u32 m0, s10, 108544\n"
  "s_nop 0\n"
  "buffer_load_dwordx4 v185, s[32:35], s75 offen lds\n"
  "v_mfma_f32_16x16x32_bf16 v[68:71], v[124:127], a[44:47], v[68:71]\n"
  "ds_read_b64_tr_b16 v[124:125], v225 offset:1024\n"
  "v_mfma_f32_16x16x32_bf16 v[76:79], a[36:39], v[128:131], v[76:79]\n"
  "ds_read_b64_tr_b16 v[126:127], v225 offset:4096\n"
  "v_mfma_f32_16x16x32_bf16 v[84:87], a[40:43], v[128:131], v[84:87]\n"
  "v_mfma_f32_16x16x32_bf16 v[92:95], a[44:47], v[128:131], v[92:95]\n"
  "ds_read_b64_tr_b16 v[128:129], v225 offset:2048\n"
  "s_add_u32 m0, s10, 109568\n"
  "s_nop 0\n"
  "buffer_load_dwordx4 v185, s[32:35], s77 offen lds\n"
  "v_mfma_f32_16x16x32_bf16 v[100:103], v[132:135], a[40:43], v[100:103]\n"
  "ds_read_b64_tr_b16 v[130:131], v225 offset:5120\n"
  "v_mfma_f32_16x16x32_bf16 v[108:111], v[132:135], a[44:47], v[108:111]\n"
  "ds_read_b64_tr_b16 v[132:133], v225 offset:0\n"
  "s_waitcnt vmcnt(18) lgkmcnt(6)\n"
  "v_mfma_f32_16x16x32_bf16 v[48:51], v[112:115], a[48:51], v[48:51]\n"
  "ds_read_b64_tr_b16 v[134:135], v225 offset:3072\n"
  "s_add_u32 m0, s10, 110592\n"
  "s_nop 0\n"
  "buffer_load_dwordx4 v185, s[32:35], s79 offen lds\n"
  "s_waitcnt vmcnt(18) lgkmcnt(6)\n"
  "v_mfma_f32_16x16x32_bf16 v[56:59], v[112:115], a[52:55], v[56:59]\n"
  "s_waitcnt vmcnt(17) lgkmcnt(5)\n"
  "v_mfma_f32_16x16x32_bf16 v[64:67], v[112:115], a[56:59], v[64:67]\n"
  "ds_read_b64_tr_b16 v[112:113], v224 offset:13312\n"
  "v_mfma_f32_16x16x32_bf16 v[72:75], a[48:51], v[116:119], v[72:75]\n"
  "ds_read_b64_tr_b16 v[114:115], v224 offset:16384\n"
  "s_add_u32 m0, s10, 111616\n"
  "s_nop 0\n"
  "buffer_load_dwordx4 v185, s[32:35], s81 offen lds\n"
  "v_mfma_f32_16x16x32_bf16 v[80:83], a[52:55], v[116:119], v[80:83]\n"
  "s_waitcnt vmcnt(12) lgkmcnt(5)\n"
  "v_cmp_ne_u64 vcc, s[56:57], v[206:207]\n"
  "s_cmp_eq_u64 vcc, 0\n"
  "s_cbranch_scc0 .Lfb18_%=\n"
  ".Lbk18_%=:\n"
  "v_mfma_f32_16x16x32_bf16 v[88:91], a[56:59], v[116:119], v[88:91]\n"
  "ds_read_b64_tr_b16 v[116:117], v224 offset:14336\n"
  "v_mfma_f32_16x16x32_bf16 v[96:99], v[120:123], a[52:55], v[96:99]\n"
  "buffer_load_dwordx2 v[136:137], v231, s[20:23], 0 offen sc1\n"
  "ds_read_b64_tr_b16 v[118:119], v224 offset:17408\n"
  "v_mfma_f32_16x16x32_bf16 v[104:107], v[120:123], a[56:59], v[104:107]\n"
  "ds_read_b64_tr_b16 v[120:121], v224 offset:12288\n"
  "v_mfma_f32_16x16x32_bf16 v[52:55], v[124:127], a[48:51], v[52:55]\n"
  "ds_read_b64_tr_b16 v[122:123], v224 offset:15360\n"
  "v_mfma_f32_16x16x32_bf16 v[60:63], v[124:127], a[52:55], v[60:63]\n"
  "v_mfma_f32_16x16x32_bf16 v[68:71], v[124:127], a[56:59], v[68:71]\n"
  "buffer_load_dwordx2 v[138:139], v231, s[20:23], 0 offen offset:128 sc1\n"
  "ds_read_b64_tr_b16 v[124:125], v225 offset:13312\n"
  "s_waitcnt lgkmcnt(6)\n"
  "v_mfma_f32_16x16x32_bf16 v[76:79], a[48:51], v[128:131], v[76:79]\n"
  "ds_read_b64_tr_b16 v[126:127], v225 offset:16384\n"
  "v_mfma_f32_16x16x32_bf16 v[84:87], a[52:55], v[128:131], v[84:87]\n"
  "v_mfma_f32_16x16x32_bf16 v[92:95], a[56:59], v[128:131], v[92:95]\n"
  "ds_read_b64_tr_b16 v[128:129], v225 offset:14336\n"
  "buffer_load_dwordx2 v[140:141], v230, s[20:23], 0 offen sc1\n"
  "v_mfma_f32_16x16x32_bf16 v[100:103], v[132:135], a[52:55], v[100:103]\n"
  "ds_read_b64_tr_b16 v[130:131], v225 offset:17408\n"
  "v_mfma_f32_16x16x32_bf16 v[108:111], v[132:135], a[56:59], v[108:111]\n"
  "ds_read_b64_tr_b16 v[132:133], v225 offset:12288\n"
  "s_waitcnt lgkmcnt(6)\n"
  "v_mfma_f32_16x16x32_bf16 v[48:51], v[112:115], a[60:63], v[48:51]\n"
  "ds_read_b64_tr_b16 v[134:135], v225 offset:15360\n"
  "v_mfma_f32_16x16x32_bf16 v[56:59], v[112:115], a[64:67], v[56:59]\n"
  "buffer_load_dwordx2 v[142:143], v230, s[20:23], 0 offen offset:128 sc1\n"
  "v_mfma_f32_16x16x32_bf16 v[64:67], v[112:115], a[68:71], v[64:67]\n"
  "ds_read_b64_tr_b16 v[112:113], v224 offset:25600\n"
  "v_mfma_f32_16x16x32_bf16 v[72:75], a[60:63], v[116:119], v[72:75]\n"
  "ds_read_b64_tr_b16 v[114:115], v224 offset:28672\n"
  "v_mfma_f32_16x16x32_bf16 v[80:83], a[64:67], v[116:119], v[80:83]\n"
  "v_mfma_f32_16x16x32_bf16 v[88:91], a[68:71], v[116:119], v[88:91]\n"
  "buffer_load_dwordx2 v[144:145], v230, s[20:23], 0 offen offset:2048 sc1\n"
  "ds_read_b64_tr_b16 v[116:117], v224 offset:26624\n"
  "s_waitcnt lgkmcnt(6)\n"
  "v_mfma_f32_16x16x32_bf16 v[96:99], v[120:123], a[64:67], v[96:99]\n"
  "ds_read_b64_tr_b16 v[118:119], v224 offset:29696\n"
  "v_mfma_f32_16x16x32_bf16 v[104:107], v[120:123], a[68:71], v[104:107]\n"
  "ds_read_b64_tr_b16 v[120:121], v224 offset:24576\n"
  "v_mfma_f32_16x16x32_bf16 v[52:55], v[124:127], a[60:63], v[52:55]\n"
  "ds_read_b64_tr_b16 v[122:123], v224 offset:27648\n"
  "v_mfma_f32_16x16x32_bf16 v[60:63], v[124:127], a[64:67], v[60:63]\n"
  "buffer_load_dwordx2 v[146:147], v230, s[20:23], 0 offen offset:2176 sc1\n"
  "v_mfma_f32_16x16x32_bf16 v[68:71], v[124:127], a[68:71], v[68:71]\n"
  "ds_read_b64_tr_b16 v[124:125], v225 offset:25600\n"
  "s_waitcnt lgkmcnt(6)\n"
  "v_mfma_f32_16x16x32_bf16 v[76:79], a[60:63], v[128:131], v[76:79]\n"
  "ds_read_b64_tr_b16 v[126:127], v225 offset:28672\n"
  "v_mfma_f32_16x16x32_bf16 v[84:87], a[64:67], v[128:131], v[84:87]\n"
  "buffer_load_dwordx4 a[96:99], v227, s[20:23], s73 offen sc1\n"
  "v_mfma_f32_16x16x32_bf16 v[92:95], a[68:71], v[128:131], v[92:95]\n"
  "ds_read_b64_tr_b16 v[128:129], v225 offset:26624\n"
  "v_mfma_f32_16x16x32_bf16 v[100:103], v[132:135], a[64:67], v[100:103]\n"
  "ds_read_b64_tr_b16 v[130:131], v225 offset:29696\n"
  "v_mfma_f32_16x16x32_bf16 v[108:111], v[132:135], a[68:71], v[108:111]\n"
  "ds_read_b64_tr_b16 v[132:133], v225 offset:24576\n"
  "s_waitcnt vmcnt(18) lgkmcnt(6)\n"
  "v_mfma_f32_16x16x32_bf16 v[48:51], v[112:115], a[72:75], v[48:51]\n"
  "buffer_load_dwordx4 a[100:103], v228, s[20:23], s73 offen sc1\n"
  "ds_read_b64_tr_b16 v[134:135], v225 offset:27648\n"
  "v_mfma_f32_16x16x32_bf16 v[56:59], v[112:115], a[76:79], v[56:59]\n"
  "v_mfma_f32_16x16x32_bf16 v[64:67], v[112:115], a[80:83], v[64:67]\n"
  "ds_read_b64_tr_b16 v[112:113], v224 offset:37888\n"
  "v_mfma_f32_16x16x32_bf16 v[72:75], a[72:75], v[116:119], v[72:75]\n"
  "ds_read_b64_tr_b16 v[114:115], v224 offset:40960\n"
  "v_mfma_f32_16x16x32_bf16 v[80:83], a[76:79], v[116:119], v[80:83]\n"
  "buffer_load_dwordx4 a[104:107], v229, s[20:23], s73 offen sc1\n"
  "v_mfma_f32_16x16x32_bf16 v[88:91], a[80:83], v[116:119], v[88:91]\n"
  "ds_read_b64_tr_b16 v[116:117], v224 offset:38912\n"
  "s_waitcnt vmcnt(19) lgkmcnt(6)\n"
  "v_mfma_f32_16x16x32_bf16 v[96:99], v[120:123], a[76:79], v[96:99]\n"
  "ds_read_b64_tr_b16 v[118:119], v224 offset:41984\n"
  "v_mfma_f32_16x16x32_bf16 v[104:107], v[120:123], a[80:83], v[104:107]\n"
  "ds_read_b64_tr_b16 v[120:121], v224 offset:36864\n"
  "v_mfma_f32_16x16x32_bf16 v[52:55], v[124:127], a[72:75], v[52:55]\n"
  "buffer_load_dwordx4 a[108:111], v227, s[20:23], s73 offen offset:1024 sc1\n"
  "ds_read_b64_tr_b16 v[122:123], v224 offset:39936\n"
  "v_mfma_f32_16x16x32_bf16 v[60:63], v[124:127], a[76:79], v[60:63]\n"
  "v_mfma_f32_16x16x32_bf16 v[68:71], v[124:127], a[80:83], v[68:71]\n"
  "ds_read_b64_tr_b16 v[124:125], v225 offset:37888\n"
  "s_waitcnt vmcnt(18) lgkmcnt(6)\n"
  "v_mfma_f32_16x16x32_bf16 v[76:79], a[72:75], v[128:131], v[76:79]\n"
  "ds_read_b64_tr_b16 v[126:127], v225 offset:40960\n"
  "buffer_load_dwordx4 a[112:115], v228, s[20:23], s73 offen offset:1024 sc1\n"
  "v_mfma_f32_16x16x32_bf16 v[84:87], a[76:79], v[128:131], v[84:87]\n"
  "v_mfma_f32_16x16x32_bf16 v[92:95], a[80:83], v[128:131], v[92:95]\n"
  "ds_read_b64_tr_b16 v[128:129], v225 offset:38912\n"
  "v_mfma_f32_16x16x32_bf16 v[100:103], v[132:135], a[76:79], v[100:103]\n"
  "ds_read_b64_tr_b16 v[130:131], v225 offset:41984\n"
  "v_mfma_f32_16x16x32_bf16 v[108:111], v[132:135], a[80:83], v[108:111]\n"
  "ds_read_b64_tr_b16 v[132:133], v225 offset:36864\n"
  "buffer_load_dwordx4 a[116:119], v229, s[20:23], s73 offen offset:1024 sc1\n"
  "s_waitcnt lgkmcnt(6)\n"
  "v_mfma_f32_16x16x32_bf16 v[48:51], v[112:115], a[84:87], v[48:51]\n"
  "ds_read_b64_tr_b16 v[134:135], v225 offset:39936\n"
  "v_mfma_f32_16x16x32_bf16 v[56:59], v[112:115], a[88:91], v[56:59]\n"
  "v_mfma_f32_16x16x32_bf16 v[64:67], v[112:115], a[92:95], v[64:67]\n"
  "ds_read_b64_tr_b16 v[112:113], v222 offset:7168\n"
  "v_mfma_f32_16x16x32_bf16 v[72:75], a[84:87], v[116:119], v[72:75]\n"
  "buffer_load_dword v44, v250, s[20:23], 0 offen sc1\n"
  "ds_read_b64_tr_b16 v[114:115], v222 offset:10240\n"
  "v_mfma_f32_16x16x32_bf16 v[80:83], a[88:91], v[116:119], v[80:83]\n"
  "s_lshl_b32 s88, s61, 9\n"
  "s_add_u32 s88, s88, 81920\n"
  "v_mfma_f32_16x16x32_bf16 v[88:91], a[92:95], v[116:119], v[88:91]\n"
  "ds_read_b64_tr_b16 v[116:117], v222 offset:8192\n"
  "v_lshl_add_u32 v249, v2, 3, s88\n"
  "s_waitcnt lgkmcnt(6)\n"
  "v_mfma_f32_16x16x32_bf16 v[96:99], v[120:123], a[88:91], v[96:99]\n"
  "ds_read_b64_tr_b16 v[118:119], v222 offset:11264\n"
  "s_lshl_b32 s88, s67, 14\n"
  "v_mfma_f32_16x16x32_bf16 v[104:107], v[120:123], a[92:95], v[104:107]\n"
  "buffer_load_dwordx4 a[120:123], v227, s[20:23], s73 offen offset:2048 sc1\n"
  "ds_read_b64_tr_b16 v[120:121], v222 offset:6144\n"
  "v_mfma_f32_16x16x32_bf16 v[52:55], v[124:127], a[84:87], v[52:55]\n"
  "ds_read_b64_tr_b16 v[122:123], v222 offset:9216\n"
  "s_add_u32 s88, s88, 4194304\n"
  "v_mfma_f32_16x16x32_bf16 v[60:63], v[124:127], a[88:91], v[60:63]\n"
  "s_waitcnt vmcnt(12) lgkmcnt(6)\n"
  "v_add_f32 v136, v136, v138\n"
  "v_mfma_f32_16x16x32_bf16 v[68:71], v[124:127], a[92:95], v[68:71]\n"
  "ds_read_b64_tr_b16 v[124:125], v223 offset:7168\n"
  "buffer_load_dwordx4 a[124:127], v228, s[20:23], s73 offen offset:2048 sc1\n"
  "v_mfma_f32_16x16x32_bf16 v[76:79], a[84:87], v[128:131], v[76:79]\n"
  "ds_read_b64_tr_b16 v[126:127], v223 offset:10240\n"
  "v_add_f32 v137, v137, v139\n"
  "v_mfma_f32_16x16x32_bf16 v[84:87], a[88:91], v[128:131], v[84:87]\n"
  "s_nop 0\n"
  "v_permlane16_swap_b32 v136, v137\n"
  "v_mfma_f32_16x16x32_bf16 v[92:95], a[92:95], v[128:131], v[92:95]\n"
  "ds_read_b64_tr_b16 v[128:129], v223 offset:8192\n"
  "v_add_f32 v136, v136, v137\n"
  "v_mfma_f32_16x16x32_bf16 v[100:103], v[132:135], a[88:91], v[100:103]\n"
  "buffer_load_dwordx4 a[128:131], v229, s[20:23], s73 offen offset:2048 sc1\n"
  "v_mfma_f32_16x16x32_bf16 v[108:111], v[132:135], a[92:95], v[108:111]\n"
  "ds_read_b64_tr_b16 v[130:131], v223 offset:11264\n"
  "ds_read_b64_tr_b16 v[132:133], v223 offset:6144\n"
  "ds_read_b64_tr_b16 v[134:135], v223 offset:9216\n"
  "s_waitcnt vmcnt(12) lgkmcnt(7)\n"
  "v_add_f32 v140, v140, v142\n"
  "v_add_f32 v141, v141, v143\n"
  "v_mov_b32 v137, v136\n"
  "v_add_u32 v250, s88, v1\n"
  "v_permlane16_swap_b32 v140, v141\n"
  "v_add_f32 v140, v140, v141\n"
  "v_mov_b32 v141, v140\n"
  "buffer_load_dwordx4 a[132:135], v227, s[20:23], s73 offen offset:3072 sc1\n"
  "v_permlane32_swap_b32 v136, v137\n"
  "v_permlane32_swap_b32 v140, v141\n"
  "v_add_f32 v136, v136, v137\n"
  "v_add_f32 v140, v140, v141\n"
  "v_mov_b32 v137, v136\n"
  "v_mov_b32 v141, v140\n"
  "s_nop 0\n"
  "v_permlane16_swap_b32 v136, v137\n"
  "v_permlane16_swap_b32 v140, v141\n"
  "v_mul_f32 v138, s4, v136\n"
  "v_mul_f32 v142, s4, v140\n"
  "buffer_load_dwordx4 a[136:139], v228, s[20:23], s73 offen offset:3072 sc1\n"
  "v_mul_f32 v139, v138, v138\n"
  "v_mul_f32 v143, v142, v142\n"
  "v_fma_f32 v137, v137, s4, -v139\n"
  "v_fma_f32 v141, v141, s4, -v143\n"
  "v_max_f32 v137, 0, v137\n"
  "s_waitcnt vmcnt(12) lgkmcnt(3)\n"
  "v_add_f32 v144, v144, v146\n"
  "v_add_f32 v145, v145, v147\n"
  "v_max_f32 v141, 0, v141\n"
  "v_add_f32 v137, s5, v137\n"
  "v_permlane16_swap_b32 v144, v145\n"
  "v_add_f32 v144, v144, v145\n"
  "v_mov_b32 v145, v144\n"
  "v_add_f32 v141, s5, v141\n"
  "buffer_load_dwordx4 a[140:143], v229, s[20:23], s73 offen offset:3072 sc1\n"
  "v_permlane32_swap_b32 v144, v145\n"
  "v_add_f32 v144, v144, v145\n"
  "v_mov_b32 v145, v144\n"
  "s_waitcnt vmcnt(12) lgkmcnt(0)\n"
  "v_mfma_f32_16x16x32_bf16 v[48:51], v[112:115], a[96:99], v[48:51]\n"
  "v_permlane16_swap_b32 v144, v145\n"
  "v_mul_f32 v146, s4, v144\n"
  "v_mul_f32 v147, v146, v146\n"
  "v_fma_f32 v145, v145, s4, -v147\n"
  "v_max_f32 v145, 0, v145\n"
  "v_add_f32 v145, s5, v145\n"
  "buffer_load_dwordx4 a[144:147], v227, s[20:23], s74 offen sc1\n"
  "v_rsq_f32 v139, v137\n"
  "v_rsq_f32 v143, v141\n"
  "ds_bpermute_b32 v148, v226, v138 offset:0\n"
  "s_waitcnt vmcnt(12)\n"
  "v_mfma_f32_16x16x32_bf16 v[56:59], v[112:115], a[100:103], v[56:59]\n"
  "v_rsq_f32 v147, v145\n"
  "ds_bpermute_b32 v149, v226, v138 offset:4\n"
  "ds_bpermute_b32 v150, v226, v138 offset:8\n"
  "ds_bpermute_b32 v151, v226, v138 offset:12\n"
  "ds_bpermute_b32 v156, v226, v142 offset:0\n"
  "buffer_load_dwordx4 a[148:151], v228, s[20:23], s74 offen sc1\n"
  "ds_bpermute_b32 v157, v226, v142 offset:4\n"
  "ds_bpermute_b32 v158, v226, v142 offset:8\n"
  "v_mul_f32 v140, s6, v143\n"
  "ds_bpermute_b32 v159, v226, v142 offset:12\n"
  "s_waitcnt vmcnt(12)\n"
  "v_mfma_f32_16x16x32_bf16 v[64:67], v[112:115], a[104:107], v[64:67]\n"
  "ds_read_b64_tr_b16 v[112:113], v222 offset:19456\n"
  "v_mul_f32 v144, s6, v147\n"
  "v_mfma_f32_16x16x32_bf16 v[72:75], a[96:99], v[116:119], v[72:75]\n"
  "ds_read_b64_tr_b16 v[114:115], v222 offset:22528\n"
  "v_mfma_f32_16x16x32_bf16 v[80:83], a[100:103], v[116:119], v[80:83]\n"
  "buffer_load_dwordx4 a[152:155], v229, s[20:23], s74 offen sc1\n"
  "ds_bpermute_b32 v152, v226, v139 offset:0\n"
  "v_mfma_f32_16x16x32_bf16 v[88:91], a[104:107], v[116:119], v[88:91]\n"
  "ds_read_b64_tr_b16 v[116:117], v222 offset:20480\n"
  "v_mfma_f32_16x16x32_bf16 v[96:99], v[120:123], a[100:103], v[96:99]\n"
  "ds_read_b64_tr_b16 v[118:119], v222 offset:23552\n"
  "v_mfma_f32_16x16x32_bf16 v[104:107], v[120:123], a[104:107], v[104:107]\n"
  "ds_read_b64_tr_b16 v[120:121], v222 offset:18432\n"
  "v_mfma_f32_16x16x32_bf16 v[52:55], v[124:127], a[96:99], v[52:55]\n"
  "ds_read_b64_tr_b16 v[122:123], v222 offset:21504\n"
  "buffer_load_dwordx4 a[156:159], v227, s[20:23], s74 offen offset:1024 sc1\n"
  "v_mfma_f32_16x16x32_bf16 v[60:63], v[124:127], a[100:103], v[60:63]\n"
  "ds_bpermute_b32 v153, v226, v139 offset:4\n"
  "v_mfma_f32_16x16x32_bf16 v[68:71], v[124:127], a[104:107], v[68:71]\n"
  "ds_read_b64_tr_b16 v[124:125], v223 offset:19456\n"
  "v_mfma_f32_16x16x32_bf16 v[76:79], a[96:99], v[128:131], v[76:79]\n"
  "ds_read_b64_tr_b16 v[126:127], v223 offset:22528\n"
  "v_mfma_f32_16x16x32_bf16 v[84:87], a[100:103], v[128:131], v[84:87]\n"
  "buffer_load_dwordx4 a[160:163], v228, s[20:23], s74 offen offset:1024 sc1\n"
  "ds_bpermute_b32 v154, v226, v139 offset:8\n"
  "v_mfma_f32_16x16x32_bf16 v[92:95], a[104:107], v[128:131], v[92:95]\n"
  "ds_read_b64_tr_b16 v[128:129], v223 offset:20480\n"
  "v_mfma_f32_16x16x32_bf16 v[100:103], v[132:135], a[100:103], v[100:103]\n"
  "ds_read_b64_tr_b16 v[130:131], v223 offset:23552\n"
  "v_mfma_f32_16x16x32_bf16 v[108:111], v[132:135], a[104:107], v[108:111]\n"
  "ds_read_b64_tr_b16 v[132:133], v223 offset:18432\n"
  "s_waitcnt vmcnt(14) lgkmcnt(9)\n"
  "v_mfma_f32_16x16x32_bf16 v[48:51], v[112:115], a[108:111], v[48:51]\n"
  "ds_read_b64_tr_b16 v[134:135], v223 offset:21504\n"
  "buffer_load_dwordx4 a[164:167], v229, s[20:23], s74 offen offset:1024 sc1\n"
  "s_waitcnt vmcnt(14) lgkmcnt(8)\n"
  "v_mfma_f32_16x16x32_bf16 v[56:59], v[112:115], a[112:115], v[56:59]\n"
  "ds_bpermute_b32 v155, v226, v139 offset:12\n"
  "s_waitcnt vmcnt(13) lgkmcnt(8)\n"
  "v_mfma_f32_16x16x32_bf16 v[64:67], v[112:115], a[116:119], v[64:67]\n"
  "ds_read_b64_tr_b16 v[112:113], v222 offset:31744\n"
  "v_mfma_f32_16x16x32_bf16 v[72:75], a[108:111], v[116:119], v[72:75]\n"
  "ds_read_b64_tr_b16 v[114:115], v222 offset:34816\n"
  "buffer_load_dwordx4 a[168:171], v227, s[20:23], s74 offen offset:2048 sc1\n"
  "v_mfma_f32_16x16x32_bf16 v[80:83], a[112:115], v[116:119], v[80:83]\n"
  "ds_bpermute_b32 v160, v226, v143 offset:0\n"
  "v_mfma_f32_16x16x32_bf16 v[88:91], a[116:119], v[116:119], v[88:91]\n"
  "ds_read_b64_tr_b16 v[116:117], v222 offset:32768\n"
  "v_mfma_f32_16x16x32_bf16 v[96:99], v[120:123], a[112:115], v[96:99]\n"
  "ds_read_b64_tr_b16 v[118:119], v222 offset:35840\n"
  "v_mfma_f32_16x16x32_bf16 v[104:107], v[120:123], a[116:119], v[104:107]\n"
  "ds_read_b64_tr_b16 v[120:121], v222 offset:30720\n"
  "buffer_load_dwordx4 a[172:175], v228, s[20:23], s74 offen offset:2048 sc1\n"
  "s_waitcnt lgkmcnt(8)\n"
  "v_mfma_f32_16x16x32_bf16 v[52:55], v[124:127], a[108:111], v[52:55]\n"
  "ds_read_b64_tr_b16 v[122:123], v222 offset:33792\n"
  "v_mfma_f32_16x16x32_bf16 v[60:63], v[124:127], a[112:115], v[60:63]\n"
  "ds_bpermute_b32 v161, v226, v143 offset:4\n"
  "v_mfma_f32_16x16x32_bf16 v[68:71], v[124:127], a[116:119], v[68:71]\n"
  "ds_read_b64_tr_b16 v[124:125], v223 offset:31744\n"
  "buffer_load_dwordx4 a[176:179], v229, s[20:23], s74 offen offset:2048 sc1\n"
  "v_mfma_f32_16x16x32_bf16 v[76:79], a[108:111], v[128:131], v[76:79]\n"
  "ds_read_b64_tr_b16 v[126:127], v223 offset:34816\n"
  "v_mfma_f32_16x16x32_bf16 v[84:87], a[112:115], v[128:131], v[84:87]\n"
  "ds_bpermute_b32 v162, v226, v143 offset:8\n"
  "v_mfma_f32_16x16x32_bf16 v[92:95], a[116:119], v[128:131], v[92:95]\n"
  "ds_read_b64_tr_b16 v[128:129], v223 offset:32768\n"
  "s_waitcnt lgkmcnt(8)\n"
  "v_mfma_f32_16x16x32_bf16 v[100:103], v[132:135], a[112:115], v[100:103]\n"
  "ds_read_b64_tr_b16 v[130:131], v223 offset:35840\n"
  "buffer_load_dwordx4 a[180:183], v227, s[20:23], s74 offen offset:3072 sc1\n"
  "v_mfma_f32_16x16x32_bf16 v[108:111], v[132:135], a[116:119], v[108:111]\n"
  "ds_read_b64_tr_b16 v[132:133], v223 offset:30720\n"
  "s_waitcnt vmcnt(15) lgkmcnt(8)\n"
  "v_mfma_f32_16x16x32_bf16 v[48:51], v[112:115], a[120:123], v[48:51]\n"
  "ds_read_b64_tr_b16 v[134:135], v223 offset:33792\n"
  "s_waitcnt vmcnt(14) lgkmcnt(8)\n"
  "v_mfma_f32_16x16x32_bf16 v[56:59], v[112:115], a[124:127], v[56:59]\n"
  "ds_bpermute_b32 v163, v226, v143 offset:12\n"
  "buffer_load_dwordx4 a[184:187], v228, s[20:23], s74 offen offset:3072 sc1\n"
  "s_waitcnt vmcnt(14) lgkmcnt(8)\n"
  "v_mfma_f32_16x16x32_bf16 v[64:67], v[112:115], a[128:131], v[64:67]\n"
  "ds_read_b64_tr_b16 v[112:113], v222 offset:44032\n"
  "v_mfma_f32_16x16x32_bf16 v[72:75], a[120:123], v[116:119], v[72:75]\n"
  "ds_read_b64_tr_b16 v[114:115], v222 offset:47104\n"
  "v_mfma_f32_16x16x32_bf16 v[80:83], a[124:127], v[116:119], v[80:83]\n"
  "ds_bpermute_b32 v164, v226, v146 offset:0\n"
  "v_mfma_f32_16x16x32_bf16 v[88:91], a[128:131], v[116:119], v[88:91]\n"
  "ds_read_b64_tr_b16 v[116:117], v222 offset:45056\n"
  "buffer_load_dwordx4 a[188:191], v229, s[20:23], s74 offen offset:3072 sc1\n"
  "v_mfma_f32_16x16x32_bf16 v[96:99], v[120:123], a[124:127], v[96:99]\n"
  "ds_read_b64_tr_b16 v[118:119], v222 offset:48128\n"
  "v_mfma_f32_16x16x32_bf16 v[104:107], v[120:123], a[128:131], v[104:107]\n"
  "ds_read_b64_tr_b16 v[120:121], v222 offset:43008\n"
  "s_waitcnt lgkmcnt(8)\n"
  "v_mfma_f32_16x16x32_bf16 v[52:55], v[124:127], a[120:123], v[52:55]\n"
  "ds_read_b64_tr_b16 v[122:123], v222 offset:46080\n"
  "v_mfma_f32_16x16x32_bf16 v[60:63], v[124:127], a[124:127], v[60:63]\n"
  "ds_bpermute_b32 v165, v226, v146 offset:4\n"
  "s_add_u32 m0, s10, 112640\n"
  "s_nop 0\n"
  "buffer_load_dwordx4 v185, s[32:35], s83 offen lds\n"
  "v_mfma_f32_16x16x32_bf16 v[68:71], v[124:127], a[128:131], v[68:71]\n"
  "ds_read_b64_tr_b16 v[124:125], v223 offset:44032\n"
  "v_mfma_f32_16x16x32_bf16 v[76:79], a[120:123], v[128:131], v[76:79]\n"
  "ds_read_b64_tr_b16 v[126:127], v223 offset:47104\n"
  "v_mfma_f32_16x16x32_bf16 v[84:87], a[124:127], v[128:131], v[84:87]\n"
  "ds_bpermute_b32 v166, v226, v146 offset:8\n"
  "s_add_u32 m0, s10, 113664\n"
  "s_nop 0\n"
  "buffer_load_dwordx4 v185, s[32:35], s85 offen lds\n"
  "v_mfma_f32_16x16x32_bf16 v[92:95], a[128:131], v[128:131], v[92:95]\n"
  "ds_read_b64_tr_b16 v[128:129], v223 offset:45056\n"
  "s_waitcnt lgkmcnt(7)\n"
  "v_mfma_f32_16x16x32_bf16 v[100:103], v[132:135], a[124:127], v[100:103]\n"
  "ds_read_b64_tr_b16 v[130:131], v223 offset:48128\n"
  "v_mfma_f32_16x16x32_bf16 v[108:111], v[132:135], a[128:131], v[108:111]\n"
  "ds_read_b64_tr_b16 v[132:133], v223 offset:43008\n"
  "buffer_load_dword v44, v200, s[36:39], 0 offen\n"
  "s_waitcnt vmcnt(17) lgkmcnt(7)\n"
  "v_mfma_f32_16x16x32_bf16 v[48:51], v[112:115], a[132:135], v[48:51]\n"
  "ds_read_b64_tr_b16 v[134:135], v223 offset:46080\n"
  "s_waitcnt vmcnt(16) lgkmcnt(7)\n"
  "v_mfma_f32_16x16x32_bf16 v[56:59], v[112:115], a[136:139], v[56:59]\n"
  "ds_bpermute_b32 v167, v226, v146 offset:12\n"
  "s_waitcnt vmcnt(15) lgkmcnt(7)\n"
  "v_mfma_f32_16x16x32_bf16 v[64:67], v[112:115], a[140:143], v[64:67]\n"
  "ds_read_b64_tr_b16 v[112:113], v224 offset:7168\n"
  "v_mfma_f32_16x16x32_bf16 v[72:75], a[132:135], v[116:119], v[72:75]\n"
  "ds_read_b64_tr_b16 v[114:115], v224 offset:10240\n"
  "v_mfma_f32_16x16x32_bf16 v[80:83], a[136:139], v[116:119], v[80:83]\n"
  "ds_bpermute_b32 v168, v226, v147 offset:0\n"
  "v_mfma_f32_16x16x32_bf16 v[88:91], a[140:143], v[116:119], v[88:91]\n"
  "ds_read_b64_tr_b16 v[116:117], v224 offset:8192\n"
  "v_mfma_f32_16x16x32_bf16 v[96:99], v[120:123], a[136:139], v[96:99]\n"
  "ds_read_b64_tr_b16 v[118:119], v224 offset:11264\n"
  "v_mfma_f32_16x16x32_bf16 v[104:107], v[120:123], a[140:143], v[104:107]\n"
  "ds_read_b64_tr_b16 v[120:121], v224 offset:6144\n"
  "s_waitcnt lgkmcnt(8)\n"
  "v_mfma_f32_16x16x32_bf16 v[52:55], v[124:127], a[132:135], v[52:55]\n"
  "ds_read_b64_tr_b16 v[122:123], v224 offset:9216\n"
  "v_mfma_f32_16x16x32_bf16 v[60:63], v[124:127], a[136:139], v[60:63]\n"
  "ds_bpermute_b32 v169, v226, v147 offset:4\n"
  "v_mfma_f32_16x16x32_bf16 v[68:71], v[124:127], a[140:143], v[68:71]\n"
  "ds_read_b64_tr_b16 v[124:125], v225 offset:7168\n"
  "v_mfma_f32_16x16x32_bf16 v[76:79], a[132:135], v[128:131], v[76:79]\n"
  "buffer_load_dword v44, v201, s[36:39], 0 offen\n"
  "ds_read_b64_tr_b16 v[126:127], v225 offset:10240\n"
  "v_mfma_f32_16x16x32_bf16 v[84:87], a[136:139], v[128:131], v[84:87]\n"
  "ds_bpermute_b32 v170, v226, v147 offset:8\n"
  "v_mfma_f32_16x16x32_bf16 v[92:95], a[140:143], v[128:131], v[92:95]\n"
  "ds_read_b64_tr_b16 v[128:129], v225 offset:8192\n"
  "s_waitcnt lgkmcnt(8)\n"
  "v_mfma_f32_16x16x32_bf16 v[100:103], v[132:135], a[136:139], v[100:103]\n"
  "ds_read_b64_tr_b16 v[130:131], v225 offset:11264\n"
  "v_mfma_f32_16x16x32_bf16 v[108:111], v[132:135], a[140:143], v[108:111]\n"
  "ds_read_b64_tr_b16 v[132:133], v225 offset:6144\n"
  "s_waitcnt vmcnt(15) lgkmcnt(8)\n"
  "v_mfma_f32_16x16x32_bf16 v[48:51], v[112:115], a[144:147], v[48:51]\n"
  "ds_read_b64_tr_b16 v[134:135], v225 offset:9216\n"
  "s_waitcnt vmcnt(14) lgkmcnt(8)\n"
  "v_mfma_f32_16x16x32_bf16 v[56:59], v[112:115], a[148:151], v[56:59]\n"
  "ds_bpermute_b32 v171, v226, v147 offset:12\n"
  "s_waitcnt vmcnt(13) lgkmcnt(8)\n"
  "v_mfma_f32_16x16x32_bf16 v[64:67], v[112:115], a[152:155], v[64:67]\n"
  "ds_read_b64_tr_b16 v[112:113], v224 offset:19456\n"
  "v_mfma_f32_16x16x32_bf16 v[72:75], a[144:147], v[116:119], v[72:75]\n"
  "ds_read_b64_tr_b16 v[114:115], v224 offset:22528\n"
  "v_mfma_f32_16x16x32_bf16 v[80:83], a[148:151], v[116:119], v[80:83]\n"
  "v_mfma_f32_16x16x32_bf16 v[88:91], a[152:155], v[116:119], v[88:91]\n"
  "ds_read_b64_tr_b16 v[116:117], v224 offset:20480\n"
  "v_mfma_f32_16x16x32_bf16 v[96:99], v[120:123], a[148:151], v[96:99]\n"
  "ds_read_b64_tr_b16 v[118:119], v224 offset:23552\n"
  "v_mfma_f32_16x16x32_bf16 v[104:107], v[120:123], a[152:155], v[104:107]\n"
  "ds_read_b64_tr_b16 v[120:121], v224 offset:18432\n"
  "s_waitcnt lgkmcnt(7)\n"
  "v_mfma_f32_16x16x32_bf16 v[52:55], v[124:127], a[144:147], v[52:55]\n"
  "ds_read_b64_tr_b16 v[122:123], v224 offset:21504\n"
  "v_mfma_f32_16x16x32_bf16 v[60:63], v[124:127], a[148:151], v[60:63]\n"
  "v_mfma_f32_16x16x32_bf16 v[68:71], v[124:127], a[152:155], v[68:71]\n"
  "ds_read_b64_tr_b16 v[124:125], v225 offset:19456\n"
  "v_mfma_f32_16x16x32_bf16 v[76:79], a[144:147], v[128:131], v[76:79]\n"
  "ds_read_b64_tr_b16 v[126:127], v225 offset:22528\n"
  "v_mfma_f32_16x16x32_bf16 v[84:87], a[148:151], v[128:131], v[84:87]\n"
  "v_mfma_f32_16x16x32_bf16 v[92:95], a[152:155], v[128:131], v[92:95]\n"
  "ds_read_b64_tr_b16 v[128:129], v225 offset:20480\n"
  "s_waitcnt vmcnt(12) lgkmcnt(6)\n"
  "v_mfma_f32_16x16x32_bf16 v[100:103], v[132:135], a[148:151], v[100:103]\n"
  "ds_read_b64_tr_b16 v[130:131], v225 offset:23552\n"
  "v_mfma_f32_16x16x32_bf16 v[108:111], v[132:135], a[152:155], v[108:111]\n"
  "ds_read_b64_tr_b16 v[132:133], v225 offset:18432\n"
  "v_mfma_f32_16x16x32_bf16 v[48:51], v[112:115], a[156:159], v[48:51]\n"
  "ds_read_b64_tr_b16 v[134:135], v225 offset:21504\n"
  "s_waitcnt vmcnt(11) lgkmcnt(6)\n"
  "v_mfma_f32_16x16x32_bf16 v[56:59], v[112:115], a[160:163], v[56:59]\n"
  "s_waitcnt vmcnt(10)\n"
  "v_mfma_f32_16x16x32_bf16 v[64:67], v[112:115], a[164:167], v[64:67]\n"
  "ds_read_b64_tr_b16 v[112:113], v224 offset:31744\n"
  "v_mfma_f32_16x16x32_bf16 v[72:75], a[156:159], v[116:119], v[72:75]\n"
  "ds_read_b64_tr_b16 v[114:115], v224 offset:34816\n"
  "v_mfma_f32_16x16x32_bf16 v[80:83], a[160:163], v[116:119], v[80:83]\n"
  "v_mfma_f32_16x16x32_bf16 v[88:91], a[164:167], v[116:119], v[88:91]\n"
  "ds_read_b64_tr_b16 v[116:117], v224 offset:32768\n"
  "v_mfma_f32_16x16x32_bf16 v[96:99], v[120:123], a[160:163], v[96:99]\n"
  "ds_read_b64_tr_b16 v[118:119], v224 offset:35840\n"
  "v_mfma_f32_16x16x32_bf16 v[104:107], v[120:123], a[164:167], v[104:107]\n"
  "ds_read_b64_tr_b16 v[120:121], v224 offset:30720\n"
  "s_waitcnt vmcnt(9) lgkmcnt(6)\n"
  "v_mfma_f32_16x16x32_bf16 v[52:55], v[124:127], a[156:159], v[52:55]\n"
  "ds_read_b64_tr_b16 v[122:123], v224 offset:33792\n"
  "v_mfma_f32_16x16x32_bf16 v[60:63], v[124:127], a[160:163], v[60:63]\n"
  "v_mfma_f32_16x16x32_bf16 v[68:71], v[124:127], a[164:167], v[68:71]\n"
  "ds_read_b64_tr_b16 v[124:125], v225 offset:31744\n"
  "v_mfma_f32_16x16x32_bf16 v[76:79], a[156:159], v[128:131], v[76:79]\n"
  "ds_read_b64_tr_b16 v[126:127], v225 offset:34816\n"
  "v_mfma_f32_16x16x32_bf16 v[84:87], a[160:163], v[128:131], v[84:87]\n"
  "v_mfma_f32_16x16x32_bf16 v[92:95], a[164:167], v[128:131], v[92:95]\n"
  "ds_read_b64_tr_b16 v[128:129], v225 offset:32768\n"
  "s_waitcnt vmcnt(8) lgkmcnt(6)\n"
  "v_mfma_f32_16x16x32_bf16 v[100:103], v[132:135], a[160:163], v[100:103]\n"
  "ds_read_b64_tr_b16 v[130:131], v225 offset:35840\n"
  "v_mfma_f32_16x16x32_bf16 v[108:111], v[132:135], a[164:167], v[108:111]\n"
  "ds_read_b64_tr_b16 v[132:133], v225 offset:30720\n"
  "v_mfma_f32_16x16x32_bf16 v[48:51], v[112:115], a[168:171], v[48:51]\n"
  "ds_read_b64_tr_b16 v[134:135], v225 offset:33792\n"
  "v_mfma_f32_16x16x32_bf16 v[56:59], v[112:115], a[172:175], v[56:59]\n"
  "s_waitcnt vmcnt(7) lgkmcnt(6)\n"
  "v_mfma_f32_16x16x32_bf16 v[64:67], v[112:115], a[176:179], v[64:67]\n"
  "ds_read_b64_tr_b16 v[112:113], v224 offset:44032\n"
  "v_mfma_f32_16x16x32_bf16 v[72:75], a[168:171], v[116:119], v[72:75]\n"
  "ds_read_b64_tr_b16 v[114:115], v224 offset:47104\n"
  "v_mfma_f32_16x16x32_bf16 v[80:83], a[172:175], v[116:119], v[80:83]\n"
  "v_mfma_f32_16x16x32_bf16 v[88:91], a[176:179], v[116:119], v[88:91]\n"
  "ds_read_b64_tr_b16 v[116:117], v224 offset:45056\n"
  "v_mfma_f32_16x16x32_bf16 v[96:99], v[120:123], a[172:175], v[96:99]\n"
  "ds_read_b64_tr_b16 v[118:119], v224 offset:48128\n"
  "v_mfma_f32_16x16x32_bf16 v[104:107], v[120:123], a[176:179], v[104:107]\n"
  "ds_read_b64_tr_b16 v[120:121], v224 offset:43008\n"
  "s_waitcnt vmcnt(5) lgkmcnt(6)\n"
  "v_mfma_f32_16x16x32_bf16 v[52:55], v[124:127], a[168:171], v[52:55]\n"
  "ds_read_b64_tr_b16 v[122:123], v224 offset:46080\n"
  "v_mfma_f32_16x16x32_bf16 v[60:63], v[124:127], a[172:175], v[60:63]\n"
  "v_mfma_f32_16x16x32_bf16 v[68:71], v[124:127], a[176:179], v[68:71]\n"
  "ds_read_b64_tr_b16 v[124:125], v225 offset:44032\n"
  "v_mfma_f32_16x16x32_bf16 v[76:79], a[168:171], v[128:131], v[76:79]\n"
  "ds_read_b64_tr_b16 v[126:127], v225 offset:47104\n"
  "v_mfma_f32_16x16x32_bf16 v[84:87], a[172:175], v[128:131], v[84:87]\n"
  "v_mfma_f32_16x16x32_bf16 v[92:95], a[176:179], v[128:131], v[92:95]\n"
  "ds_read_b64_tr_b16 v[128:129], v225 offset:45056\n"
  "s_waitcnt vmcnt(4) lgkmcnt(6)\n"
  "v_mfma_f32_16x16x32_bf16 v[100:103], v[132:135], a[172:175], v[100:103]\n"
  "ds_read_b64_tr_b16 v[130:131], v225 offset:48128\n"
  "v_mfma_f32_16x16x32_bf16 v[108:111], v[132:135], a[176:179], v[108:111]\n"
  "ds_read_b64_tr_b16 v[132:133], v225 offset:43008\n"
  "v_mfma_f32_16x16x32_bf16 v[48:51], v[112:115], a[180:183], v[48:51]\n"
  "ds_read_b64_tr_b16 v[134:135], v225 offset:46080\n"
  "v_mfma_f32_16x16x32_bf16 v[56:59], v[112:115], a[184:187], v[56:59]\n"
  "v_mfma_f32_16x16x32_bf16 v[64:67], v[112:115], a[188:191], v[64:67]\n"
  "s_waitcnt lgkmcnt(5)\n"
  "v_mfma_f32_16x16x32_bf16 v[72:75], a[180:183], v[116:119], v[72:75]\n"
  "v_mfma_f32_16x16x32_bf16 v[80:83], a[184:187], v[116:119], v[80:83]\n"
  "v_mfma_f32_16x16x32_bf16 v[88:91], a[188:191], v[116:119], v[88:91]\n"
  "s_waitcnt lgkmcnt(0)\n"
  "s_barrier\n"
  "v_mfma_f32_16x16x32_bf16 v[96:99], v[120:123], a[184:187], v[96:99]\n"
  "s_add_u32 m0, s8, 0\n"
  "s_mov_b32 s9, 0\n"
  "buffer_load_dwordx4 v232, s[36:39], s9 offen lds\n"
  "v_mfma_f32_16x16x32_bf16 v[104:107], v[120:123], a[188:191], v[104:107]\n"
  "s_add_u32 m0, s8, 1024\n"
  "s_mov_b32 s9, 28672\n"
  "buffer_load_dwordx4 v233, s[36:39], s9 offen lds\n"
  "v_mfma_f32_16x16x32_bf16 v[52:55], v[124:127], a[180:183], v[52:55]\n"
  "s_add_u32 m0, s8, 2048\n"
  "s_mov_b32 s9, 57344\n"
  "buffer_load_dwordx4 v232, s[36:39], s9 offen lds\n"
  "v_mfma_f32_16x16x32_bf16 v[60:63], v[124:127], a[184:187], v[60:63]\n"
  "v_mfma_f32_16x16x32_bf16 v[68:71], v[124:127], a[188:191], v[68:71]\n"
  "v_mfma_f32_16x16x32_bf16 v[76:79], a[180:183], v[128:131], v[76:79]\n"
  "v_mfma_f32_16x16x32_bf16 v[84:87], a[184:187], v[128:131], v[84:87]\n"
  "v_mfma_f32_16x16x32_bf16 v[92:95], a[188:191], v[128:131], v[92:95]\n"
  "s_add_u32 m0, s8, 3072\n"
  "s_mov_b32 s9, 86016\n"
  "buffer_load_dwordx4 v233, s[36:39], s9 offen lds\n"
  "v_mfma_f32_16x16x32_bf16 v[100:103], v[132:135], a[184:187], v[100:103]\n"
  "v_mfma_f32_16x16x32_bf16 v[108:111], v[132:135], a[188:191], v[108:111]\n"
  "v_pk_fma_f32 v[48:49], v[12:13], v[138:139], v[48:49] op_sel_hi:[1,0,1] neg_lo:[0,1,0] neg_hi:[0,1,0]\n"
  "v_pk_fma_f32 v[52:53], v[16:17], v[138:139], v[52:53] op_sel_hi:[1,0,1] neg_lo:[0,1,0] neg_hi:[0,1,0]\n"
  "v_pk_fma_f32 v[56:57], v[12:13], v[142:143], v[56:57] op_sel_hi:[1,0,1] neg_lo:[0,1,0] neg_hi:[0,1,0]\n"
  "v_pk_fma_f32 v[60:61], v[16:17], v[142:143], v[60:61] op_sel_hi:[1,0,1] neg_lo:[0,1,0] neg_hi:[0,1,0]\n"
  "v_pk_fma_f32 v[64:65], v[12:13], v[146:147], v[64:65] op_sel_hi:[1,0,1] neg_lo:[0,1,0] neg_hi:[0,1,0]\n"
  "v_pk_fma_f32 v[68:69], v[16:17], v[146:147], v[68:69] op_sel_hi:[1,0,1] neg_lo:[0,1,0] neg_hi:[0,1,0]\n"
  "v_pk_mul_f32 v[48:49], v[48:49], v[138:139] op_sel:[0,1]\n"
  "v_pk_fma_f32 v[50:51], v[14:15], v[138:139], v[50:51] op_sel_hi:[1,0,1] neg_lo:[0,1,0] neg_hi:[0,1,0]\n"
  "v_pk_mul_f32 v[52:53], v[52:53], v[138:139] op_sel:[0,1]\n"
  "v_pk_fma_f32 v[54:55], v[18:19], v[138:139], v[54:55] op_sel_hi:[1,0,1] neg_lo:[0,1,0] neg_hi:[0,1,0]\n"
  "v_pk_mul_f32 v[56:57], v[56:57], v[142:143] op_sel:[0,1]\n"
  "v_pk_fma_f32 v[58:59], v[14:15], v[142:143], v[58:59] op_sel_hi:[1,0,1] neg_lo:[0,1,0] neg_hi:[0,1,0]\n"
  "v_pk_fma_f32 v[96:97], v[4:5], v[142:143], v[96:97] op_sel_hi:[1,0,1] neg_lo:[0,1,0] neg_hi:[0,1,0]\n"
  "v_pk_mul_f32 v[60:61], v[60:61], v[142:143] op_sel:[0,1]\n"
  "v_pk_fma_f32 v[62:63], v[18:19], v[142:143], v[62:63] op_sel_hi:[1,0,1] neg_lo:[0,1,0] neg_hi:[0,1,0]\n"
  "v_pk_fma_f32 v[100:101], v[8:9], v[142:143], v[100:101] op_sel_hi:[1,0,1] neg_lo:[0,1,0] neg_hi:[0,1,0]\n"
  "v_pk_mul_f32 v[64:65], v[64:65], v[146:147] op_sel:[0,1]\n"
  "v_pk_fma_f32 v[66:67], v[14:15], v[146:147], v[66:67] op_sel_hi:[1,0,1] neg_lo:[0,1,0] neg_hi:[0,1,0]\n"
  "v_pk_fma_f32 v[104:105], v[4:5], v[146:147], v[104:105] op_sel_hi:[1,0,1] neg_lo:[0,1,0] neg_hi:[0,1,0]\n"
  "v_pk_mul_f32 v[68:69], v[68:69], v[146:147] op_sel:[0,1]\n"
  "v_pk_fma_f32 v[70:71], v[18:19], v[146:147], v[70:71] op_sel_hi:[1,0,1] neg_lo:[0,1,0] neg_hi:[0,1,0]\n"
  "v_pk_fma_f32 v[108:109], v[8:9], v[146:147], v[108:109] op_sel_hi:[1,0,1] neg_lo:[0,1,0] neg_hi:[0,1,0]\n"
  "v_pk_mul_f32 v[50:51], v[50:51], v[138:139] op_sel:[0,1]\n"
  "s_nop 1\n"
  "v_mfma_f32_16x16x32_bf16 v[184:187], v[26:29], v[40:43], 0\n"
  "v_cvt_pk_bf16_f32 v48, v48, v49\n"
  "v_cvt_pk_bf16_f32 v52, v52, v53\n"
  "v_pk_mul_f32 v[54:55], v[54:55], v[138:139] op_sel:[0,1]\n"
  "v_pk_mul_f32 v[58:59], v[58:59], v[142:143] op_sel:[0,1]\n"
  "s_add_u32 m0, s8, 4096\n"
  "s_mov_b32 s9, 114688\n"
  "buffer_load_dwordx4 v232, s[36:39], s9 offen lds\n"
  "v_cvt_pk_bf16_f32 v56, v56, v57\n"
  "v_pk_fma_f32 v[98:99], v[6:7], v[142:143], v[98:99] op_sel_hi:[1,0,1] neg_lo:[0,1,0] neg_hi:[0,1,0]\n"
  "v_cvt_pk_bf16_f32 v96, v96, v97\n"
  "v_pk_mul_f32 v[62:63], v[62:63], v[142:143] op_sel:[0,1]\n"
  "v_cvt_pk_bf16_f32 v60, v60, v61\n"
  "v_pk_fma_f32 v[102:103], v[10:11], v[142:143], v[102:103] op_sel_hi:[1,0,1] neg_lo:[0,1,0] neg_hi:[0,1,0]\n"
  "v_cvt_pk_bf16_f32 v100, v100, v101\n"
  "v_pk_mul_f32 v[66:67], v[66:67], v[146:147] op_sel:[0,1]\n"
  "v_cvt_pk_bf16_f32 v64, v64, v65\n"
  "v_pk_fma_f32 v[106:107], v[6:7], v[146:147], v[106:107] op_sel_hi:[1,0,1] neg_lo:[0,1,0] neg_hi:[0,1,0]\n"
  "v_cvt_pk_bf16_f32 v104, v104, v105\n"
  "v_pk_mul_f32 v[70:71], v[70:71], v[146:147] op_sel:[0,1]\n"
  "v_cvt_pk_bf16_f32 v68, v68, v69\n"
  "v_pk_fma_f32 v[110:111], v[10:11], v[146:147], v[110:111] op_sel_hi:[1,0,1] neg_lo:[0,1,0] neg_hi:[0,1,0]\n"
  "v_cvt_pk_bf16_f32 v108, v108, v109\n"
  "v_cvt_pk_bf16_f32 v49, v50, v51\n"
  "s_add_u32 m0, s8, 5120\n"
  "s_mov_b32 s9, 143360\n"
  "buffer_load_dwordx4 v233, s[36:39], s9 offen lds\n"
  "v_cvt_pk_bf16_f32 v53, v54, v55\n"
  "v_cvt_pk_bf16_f32 v57, v58, v59\n"
  "v_cvt_pk_bf16_f32 v97, v98, v99\n"
  "v_cvt_pk_bf16_f32 v61, v62, v63\n"
  "v_cvt_pk_bf16_f32 v101, v102, v103\n"
  "v_mfma_f32_16x16x16_bf16 v[112:115], v[48:49], v[96:97], v[216:219]\n"
  "v_cvt_pk_bf16_f32 v65, v66, v67\n"
  "v_cvt_pk_bf16_f32 v105, v106, v107\n"
  "v_mfma_f32_16x16x16_bf16 v[116:119], v[56:57], v[96:97], v[208:211]\n"
  "v_cvt_pk_bf16_f32 v69, v70, v71\n"
  "v_cvt_pk_bf16_f32 v109, v110, v111\n"
  "v_mfma_f32_16x16x16_bf16 v[120:123], v[52:53], v[100:101], v[216:219]\n"
  "s_add_u32 m0, s8, 6144\n"
  "s_mov_b32 s9, 172032\n"
  "buffer_load_dwordx4 v232, s[36:39], s9 offen lds\n"
  "v_mfma_f32_16x16x16_bf16 v[124:127], v[60:61], v[100:101], v[208:211]\n"
  "s_nop 0\n"
  "v_max_f32 v112, v112, v116\n"
  "v_max_f32 v113, v113, v117\n"
  "v_mfma_f32_16x16x16_bf16 v[128:131], v[56:57], v[104:105], v[212:215]\n"
  "v_max_f32 v114, v114, v118\n"
  "v_max_f32 v115, v115, v119\n"
  "v_mfma_f32_16x16x16_bf16 v[132:135], v[64:65], v[104:105], v[208:211]\n"
  "s_nop 0\n"
  "v_max_f32 v120, v120, v124\n"
  "v_max_f32 v121, v121, v125\n"
  "v_mfma_f32_16x16x16_bf16 v[188:191], v[60:61], v[108:109], v[212:215]\n"
  "v_max_f32 v122, v122, v126\n"
  "v_max3_f32 v50, v112, v113, v114\n"
  "v_mfma_f32_16x16x16_bf16 v[192:195], v[68:69], v[108:109], v[208:211]\n"
  "s_nop 0\n"
  "v_max_f32 v128, v128, v132\n"
  "v_max_f32 v129, v129, v133\n"
  "v_max_f32 v130, v130, v134\n"
  "s_add_u32 m0, s8, 7168\n"
  "s_mov_b32 s9, 200704\n"
  "buffer_load_dwordx4 v233, s[36:39], s9 offen lds\n"
  "v_max_f32 v123, v123, v127\n"
  "v_max3_f32 v54, v120, v121, v122\n"
  "v_max_f32 v188, v188, v192\n"
  "v_max_f32 v189, v189, v193\n"
  "v_max_f32 v190, v190, v194\n"
  "v_max_f32 v131, v131, v135\n"
  "v_max3_f32 v66, v128, v129, v130\n"
  "v_max_f32 v191, v191, v195\n"
  "v_max3_f32 v70, v188, v189, v190\n"
  "v_max_f32 v50, v50, v115\n"
  "v_max_f32 v54, v54, v123\n"
  "v_max_f32 v66, v66, v131\n"
  "v_max_f32 v70, v70, v191\n"
  "v_mov_b32 v51, v50\n"
  "v_mov_b32 v55, v54\n"
  "v_mov_b32 v67, v66\n"
  "v_mov_b32 v71, v70\n"
  "v_permlane16_swap_b32 v50, v51\n"
  "s_add_u32 m0, s8, 8192\n"
  "s_mov_b32 s9, 229376\n"
  "buffer_load_dwordx4 v232, s[36:39], s9 offen lds\n"
  "v_permlane16_swap_b32 v54, v55\n"
  "v_permlane16_swap_b32 v66, v67\n"
  "v_permlane16_swap_b32 v70, v71\n"
  "v_max_f32 v50, v50, v51\n"
  "v_max_f32 v54, v54, v55\n"
  "v_max_f32 v66, v66, v67\n"
  "v_max_f32 v70, v70, v71\n"
  "v_mov_b32 v51, v50\n"
  "v_mov_b32 v55, v54\n"
  "v_mov_b32 v67, v66\n"
  "v_mov_b32 v71, v70\n"
  "v_permlane32_swap_b32 v50, v51\n"
  "v_permlane32_swap_b32 v54, v55\n"
  "s_add_u32 m0, s8, 9216\n"
  "s_mov_b32 s9, 258048\n"
  "buffer_load_dwordx4 v233, s[36:39], s9 offen lds\n"
  "v_permlane32_swap_b32 v66, v67\n"
  "v_permlane32_swap_b32 v70, v71\n"
  "v_max_f32 v50, v50, v51\n"
  "v_max_f32 v54, v54, v55\n"
  "v_max_f32 v66, v66, v67\n"
  "v_max_f32 v70, v70, v71\n"
  "v_mul_f32 v50, v50, v140\n"
  "v_mul_f32 v54, v54, v140\n"
  "v_mul_f32 v66, v66, v144\n"
  "v_mul_f32 v70, v70, v144\n"
  "v_pk_fma_f32 v[112:113], v[112:113], v[140:141], v[50:51] op_sel_hi:[1,0,0] neg_lo:[0,0,1] neg_hi:[0,0,1]\n"
  "v_pk_fma_f32 v[114:115], v[114:115], v[140:141], v[50:51] op_sel_hi:[1,0,0] neg_lo:[0,0,1] neg_hi:[0,0,1]\n"
  "v_pk_fma_f32 v[120:121], v[120:121], v[140:141], v[54:55] op_sel_hi:[1,0,0] neg_lo:[0,0,1] neg_hi:[0,0,1]\n"
  "v_pk_fma_f32 v[122:123], v[122:123], v[140:141], v[54:55] op_sel_hi:[1,0,0] neg_lo:[0,0,1] neg_hi:[0,0,1]\n"
  "v_pk_fma_f32 v[128:129], v[128:129], v[144:145], v[66:67] op_sel_hi:[1,0,0] neg_lo:[0,0,1] neg_hi:[0,0,1]\n"
  "v_pk_fma_f32 v[130:131], v[130:131], v[144:145], v[66:67] op_sel_hi:[1,0,0] neg_lo:[0,0,1] neg_hi:[0,0,1]\n"
  "v_pk_fma_f32 v[188:189], v[188:189], v[144:145], v[70:71] op_sel_hi:[1,0,0] neg_lo:[0,0,1] neg_hi:[0,0,1]\n"
  "v_pk_fma_f32 v[190:191], v[190:191], v[144:145], v[70:71] op_sel_hi:[1,0,0] neg_lo:[0,0,1] neg_hi:[0,0,1]\n"
  "s_add_u32 m0, s8, 10240\n"
  "s_mov_b32 s9, 286720\n"
  "buffer_load_dwordx4 v232, s[36:39], s9 offen lds\n"
  "v_exp_f32 v112, v112\n"
  "v_exp_f32 v113, v113\n"
  "v_exp_f32 v114, v114\n"
  "v_exp_f32 v115, v115\n"
  "v_exp_f32 v120, v120\n"
  "v_exp_f32 v121, v121\n"
  "v_exp_f32 v122, v122\n"
  "v_exp_f32 v123, v123\n"
  "s_add_u32 m0, s8, 11264\n"
  "s_mov_b32 s9, 315392\n"
  "buffer_load_dwordx4 v233, s[36:39], s9 offen lds\n"
  "v_exp_f32 v128, v128\n"
  "v_exp_f32 v129, v129\n"
  "v_exp_f32 v130, v130\n"
  "v_exp_f32 v131, v131\n"
  "v_exp_f32 v188, v188\n"
  "v_exp_f32 v189, v189\n"
  "v_exp_f32 v190, v190\n"
  "v_exp_f32 v191, v191\n"
  "v_cvt_pk_bf16_f32 v98, v112, v113\n"
  "v_cvt_pk_bf16_f32 v99, v114, v115\n"
  "v_cvt_pk_bf16_f32 v102, v120, v121\n"
  "s_add_u32 m0, s8, 12288\n"
  "s_mov_b32 s9, 344064\n"
  "buffer_load_dwordx4 v232, s[36:39], s9 offen lds\n"
  "v_cvt_pk_bf16_f32 v103, v122, v123\n"
  "v_cvt_pk_bf16_f32 v106, v128, v129\n"
  "v_cvt_pk_bf16_f32 v107, v130, v131\n"
  "v_cvt_pk_bf16_f32 v110, v188, v189\n"
  "v_cvt_pk_bf16_f32 v111, v190, v191\n"
  "v_and_b32 v116, v220, v98\n"
  "v_and_b32 v117, v221, v99\n"
  "v_and_b32 v124, v220, v102\n"
  "v_and_b32 v125, v221, v103\n"
  "v_and_b32 v132, v220, v106\n"
  "v_and_b32 v133, v221, v107\n"
  "v_and_b32 v192, v220, v110\n"
  "v_and_b32 v193, v221, v111\n"
  "v_pk_fma_f32 v[72:73], v[148:149], v[20:21], v[72:73] op_sel_hi:[1,0,1] neg_lo:[1,0,0] neg_hi:[1,0,0]\n"
  "v_pk_fma_f32 v[74:75], v[150:151], v[20:21], v[74:75] op_sel_hi:[1,0,1] neg_lo:[1,0,0] neg_hi:[1,0,0]\n"
  "v_pk_fma_f32 v[76:77], v[148:149], v[20:21], v[76:77] op_sel:[0,1,0] neg_lo:[1,0,0] neg_hi:[1,0,0]\n"
  "v_pk_fma_f32 v[78:79], v[150:151], v[20:21], v[78:79] op_sel:[0,1,0] neg_lo:[1,0,0] neg_hi:[1,0,0]\n"
  "v_pk_fma_f32 v[80:81], v[156:157], v[20:21], v[80:81] op_sel_hi:[1,0,1] neg_lo:[1,0,0] neg_hi:[1,0,0]\n"
  "v_pk_fma_f32 v[82:83], v[158:159], v[20:21], v[82:83] op_sel_hi:[1,0,1] neg_lo:[1,0,0] neg_hi:[1,0,0]\n"
  "v_pk_fma_f32 v[84:85], v[156:157], v[20:21], v[84:85] op_sel:[0,1,0] neg_lo:[1,0,0] neg_hi:[1,0,0]\n"
  "v_pk_fma_f32 v[86:87], v[158:159], v[20:21], v[86:87] op_sel:[0,1,0] neg_lo:[1,0,0] neg_hi:[1,0,0]\n"
  "s_add_u32 m0, s8, 13312\n"
  "s_mov_b32 s9, 372736\n"
  "buffer_load_dwordx4 v233, s[36:39], s9 offen lds\n"
  "v_xor_b32 v118, v116, v98\n"
  "v_xor_b32 v119, v117, v99\n"
  "v_xor_b32 v126, v124, v102\n"
  "v_xor_b32 v127, v125, v103\n"
  "v_xor_b32 v134, v132, v106\n"
  "v_xor_b32 v135, v133, v107\n"
  "v_xor_b32 v194, v192, v110\n"
  "v_xor_b32 v195, v193, v111\n"
  "v_pk_mul_f32 v[72:73], v[72:73], v[152:153]\n"
  "v_pk_mul_f32 v[74:75], v[74:75], v[154:155]\n"
  "v_pk_mul_f32 v[76:77], v[76:77], v[152:153]\n"
  "v_pk_mul_f32 v[78:79], v[78:79], v[154:155]\n"
  "v_pk_mul_f32 v[80:81], v[80:81], v[160:161]\n"
  "v_pk_mul_f32 v[82:83], v[82:83], v[162:163]\n"
  "v_pk_mul_f32 v[84:85], v[84:85], v[160:161]\n"
  "v_pk_mul_f32 v[86:87], v[86:87], v[162:163]\n"
  "v_pk_fma_f32 v[88:89], v[164:165], v[20:21], v[88:89] op_sel_hi:[1,0,1] neg_lo:[1,0,0] neg_hi:[1,0,0]\n"
  "v_pk_fma_f32 v[90:91], v[166:167], v[20:21], v[90:91] op_sel_hi:[1,0,1] neg_lo:[1,0,0] neg_hi:[1,0,0]\n"
  "v_pk_fma_f32 v[92:93], v[164:165], v[20:21], v[92:93] op_sel:[0,1,0] neg_lo:[1,0,0] neg_hi:[1,0,0]\n"
  "v_pk_fma_f32 v[94:95], v[166:167], v[20:21], v[94:95] op_sel:[0,1,0] neg_lo:[1,0,0] neg_hi:[1,0,0]\n"
  "s_add_u32 m0, s8, 14336\n"
  "s_mov_b32 s9, 401408\n"
  "buffer_load_dwordx4 v232, s[36:39], s9 offen lds\n"
  "v_cvt_pk_bf16_f32 v172, v72, v73\n"
  "v_cvt_pk_bf16_f32 v173, v74, v75\n"
  "v_mfma_f32_16x16x32_bf16 v[72:75], v[30:33], v[116:119], 0\n"
  "v_cvt_pk_bf16_f32 v178, v76, v77\n"
  "v_cvt_pk_bf16_f32 v179, v78, v79\n"
  "v_mfma_f32_16x16x32_bf16 v[76:79], v[30:33], v[124:127], 0\n"
  "v_cvt_pk_bf16_f32 v174, v80, v81\n"
  "v_cvt_pk_bf16_f32 v175, v82, v83\n"
  "v_mfma_f32_16x16x32_bf16 v[80:83], v[30:33], v[132:135], 0\n"
  "v_cvt_pk_bf16_f32 v180, v84, v85\n"
  "s_add_u32 m0, s8, 15360\n"
  "s_mov_b32 s9, 430080\n"
  "buffer_load_dwordx4 v233, s[36:39], s9 offen lds\n"
  "v_mfma_f32_16x16x32_bf16 v[112:115], v[172:175], v[116:119], 0\n"
  "v_cvt_pk_bf16_f32 v181, v86, v87\n"
  "s_add_u32 m0, s8, 16384\n"
  "s_mov_b32 s9, 458752\n"
  "buffer_load_dwordx4 v232, s[36:39], s9 offen lds\n"
  "v_pk_mul_f32 v[88:89], v[88:89], v[168:169]\n"
  "v_pk_mul_f32 v[90:91], v[90:91], v[170:171]\n"
  "v_pk_mul_f32 v[92:93], v[92:93], v[168:169]\n"
  "v_pk_mul_f32 v[94:95], v[94:95], v[170:171]\n"
  "s_nop 0\n"
  "v_mfma_f32_16x16x32_bf16 v[84:87], v[30:33], v[192:195], 0\n"
  "v_cvt_pk_bf16_f32 v176, v88, v89\n"
  "v_cvt_pk_bf16_f32 v177, v90, v91\n"
  "v_mfma_f32_16x16x32_bf16 v[120:123], v[178:181], v[124:127], 0\n"
  "v_cvt_pk_bf16_f32 v182, v92, v93\n"
  "v_cvt_pk_bf16_f32 v183, v94, v95\n"
  "v_mfma_f32_16x16x32_bf16 v[128:131], v[174:177], v[132:135], 0\n"
  "v_rcp_f32 v72, v72\n"
  "v_mfma_f32_16x16x32_bf16 v[188:191], v[180:183], v[192:195], 0\n"
  "v_rcp_f32 v76, v76\n"
  "v_mfma_f32_16x16x32_bf16 v[176:179], v[22:25], v[40:43], 0\n"
  "v_rcp_f32 v80, v80\n"
  "v_mfma_f32_16x16x32_bf16 v[180:183], v[22:25], v[38:41], 0\n"
  "v_rcp_f32 v84, v84\n"
  "v_pk_mul_f32 v[112:113], v[112:113], v[72:73] op_sel_hi:[1,0]\n"
  "v_pk_mul_f32 v[114:115], v[114:115], v[72:73] op_sel_hi:[1,0]\n"
  "v_pk_mul_f32 v[120:121], v[120:121], v[76:77] op_sel_hi:[1,0]\n"
  "v_pk_mul_f32 v[122:123], v[122:123], v[76:77] op_sel_hi:[1,0]\n"
  "v_pk_mul_f32 v[128:129], v[128:129], v[80:81] op_sel_hi:[1,0]\n"
  "v_pk_mul_f32 v[130:131], v[130:131], v[80:81] op_sel_hi:[1,0]\n"
  "v_pk_mul_f32 v[188:189], v[188:189], v[84:85] op_sel_hi:[1,0]\n"
  "v_pk_mul_f32 v[190:191], v[190:191], v[84:85] op_sel_hi:[1,0]\n"
  "s_add_u32 m0, s8, 17408\n"
  "s_mov_b32 s9, 487424\n"
  "buffer_load_dwordx4 v233, s[36:39], s9 offen lds\n"
  "v_cvt_pk_bf16_f32 v196, v112, v113\n"
  "v_cvt_pk_bf16_f32 v197, v114, v115\n"
  "v_cvt_pk_bf16_f32 v198, v120, v121\n"
  "v_cvt_pk_bf16_f32 v199, v122, v123\n"
  "v_cvt_pk_bf16_f32 v200, v128, v129\n"
  "v_cvt_pk_bf16_f32 v201, v130, v131\n"
  "v_cvt_pk_bf16_f32 v202, v188, v189\n"
  "v_cvt_pk_bf16_f32 v203, v190, v191\n"
  "v_mfma_f32_16x16x32_bf16 v[188:191], v[26:29], v[38:41], 0\n"
  "buffer_store_dwordx4 v[196:199], v1, s[20:23], s15 offen sc1\n"
  "v_bfe_u32 v45, v0, 3, 2\n"
  "s_lshl_b32 s87, s59, 1\n"
  "v_xor_b32 v45, s87, v45\n"
  "v_lshl_add_u32 v45, v45, 5, s8\n"
  "v_lshl_add_u32 v45, v3, 10, v45\n"
  "buffer_store_dwordx4 v[200:203], v1, s[20:23], s16 offen sc1\n"
  "v_bfe_u32 v46, v0, 2, 2\n"
  "v_lshl_add_u32 v45, v46, 7, v45\n"
  "v_and_b32 v46, 3, v0\n"
  "v_lshl_add_u32 v168, v46, 3, v45\n"
  "v_xor_b32 v169, 32, v168\n"
  "v_xor_b32 v170, 64, v168\n"
  "v_xor_b32 v171, 0x60, v168\n"
  "v_bfe_u32 v45, v0, 3, 2\n"
  "v_lshlrev_b32 v45, 5, v45\n"
  "v_lshrrev_b32 v46, 2, v0\n"
  "v_lshl_add_u32 v45, v46, 7, v45\n"
  "v_and_b32 v46, 3, v0\n"
  "v_lshl_add_u32 v45, v46, 3, v45\n"
  "v_add_u32 v200, 98304, v45\n"
  "v_xor_b32 v201, 32, v200\n"
  "v_xor_b32 v202, 64, v200\n"
  "v_xor_b32 v203, 0x60, v200\n"
  "s_add_u32 m0, s8, 18432\n"
  "s_mov_b32 s9, 516096\n"
  "buffer_load_dwordx4 v232, s[36:39], s9 offen lds\n"
  "s_waitcnt vmcnt(17)\n"
  "ds_read_b64_tr_b16 a[0:1], v168 offset:0\n"
  "ds_read_b64_tr_b16 a[2:3], v168 offset:512\n"
  "ds_read_b64_tr_b16 a[4:5], v169 offset:0\n"
  "ds_read_b64_tr_b16 a[6:7], v169 offset:512\n"
  "ds_read_b64_tr_b16 a[8:9], v170 offset:0\n"
  "ds_read_b64_tr_b16 a[10:11], v170 offset:512\n"
  "ds_read_b64_tr_b16 a[12:13], v171 offset:0\n"
  "ds_read_b64_tr_b16 a[14:15], v171 offset:512\n"
  "s_waitcnt vmcnt(13)\n"
  "ds_read_b64_tr_b16 a[16:17], v168 offset:4096\n"
  "s_add_u32 m0, s8, 19456\n"
  "s_mov_b32 s9, 544768\n"
  "buffer_load_dwordx4 v233, s[36:39], s9 offen lds\n"
  "ds_read_b64_tr_b16 a[18:19], v168 offset:4608\n"
  "ds_read_b64_tr_b16 a[20:21], v169 offset:4096\n"
  "ds_read_b64_tr_b16 a[22:23], v169 offset:4608\n"
  "ds_read_b64_tr_b16 a[24:25], v170 offset:4096\n"
  "ds_read_b64_tr_b16 a[26:27], v170 offset:4608\n"
  "ds_read_b64_tr_b16 a[28:29], v171 offset:4096\n"
  "ds_read_b64_tr_b16 a[30:31], v171 offset:4608\n"
  "s_waitcnt vmcnt(10)\n"
  "ds_read_b64_tr_b16 a[32:33], v168 offset:8192\n"
  "ds_read_b64_tr_b16 a[34:35], v168 offset:8704\n"
  "s_add_u32 m0, s8, 20480\n"
  "s_mov_b32 s9, 573440\n"
  "buffer_load_dwordx4 v232, s[36:39], s9 offen lds\n"
  "ds_read_b64_tr_b16 a[36:37], v169 offset:8192\n"
  "ds_read_b64_tr_b16 a[38:39], v169 offset:8704\n"
  "ds_read_b64_tr_b16 a[40:41], v170 offset:8192\n"
  "ds_read_b64_tr_b16 a[42:43], v170 offset:8704\n"
  "ds_read_b64_tr_b16 a[44:45], v171 offset:8192\n"
  "ds_read_b64_tr_b16 a[46:47], v171 offset:8704\n"
  "s_add_u32 m0, s8, 21504\n"
  "s_mov_b32 s9, 602112\n"
  "buffer_load_dwordx4 v233, s[36:39], s9 offen lds\n"
  "s_add_u32 m0, s8, 22528\n"
  "s_mov_b32 s9, 630784\n"
  "buffer_load_dwordx4 v232, s[36:39], s9 offen lds\n"
  "s_waitcnt vmcnt(9) lgkmcnt(0)\n"
  "ds_read_b64_tr_b16 a[48:49], v168 offset:12288\n"
  "ds_read_b64_tr_b16 a[50:51], v168 offset:12800\n"
  "ds_read_b64_tr_b16 a[52:53], v169 offset:12288\n"
  "ds_read_b64_tr_b16 a[54:55], v169 offset:12800\n"
  "ds_read_b64_tr_b16 a[56:57], v170 offset:12288\n"
  "ds_read_b64_tr_b16 a[58:59], v170 offset:12800\n"
  "ds_read_b64_tr_b16 a[60:61], v171 offset:12288\n"
  "ds_read_b64_tr_b16 a[62:63], v171 offset:12800\n"
  "s_waitcnt vmcnt(5) lgkmcnt(0)\n"
  "s_barrier\n"
  "buffer_store_dwordx2 v[36:37], v248, s[20:23], s11 offen sc1\n"
  "s_add_u32 m0, s8, 23552\n"
  "s_mov_b32 s9, 659456\n"
  "buffer_load_dwordx4 v233, s[36:39], s9 offen lds\n"
  "ds_read_b64_tr_b16 v[112:113], v200 offset:0\n"
  "ds_read_b64_tr_b16 v[114:115], v200 offset:2048\n"
  "ds_read_b64_tr_b16 v[116:117], v201 offset:0\n"
  "ds_read_b64_tr_b16 v[118:119], v201 offset:2048\n"
  "ds_read_b64_tr_b16 v[120:121], v202 offset:0\n"
  "s_add_u32 m0, s8, 0\n"
  "s_mov_b32 s9, 688128\n"
  "buffer_load_dwordx4 v232, s[36:39], s9 offen lds\n"
  "ds_read_b64_tr_b16 v[122:123], v202 offset:2048\n"
  "ds_read_b64_tr_b16 v[124:125], v203 offset:0\n"
  "ds_read_b64_tr_b16 v[126:127], v203 offset:2048\n"
  "s_add_u32 m0, s8, 1024\n"
  "s_mov_b32 s9, 716800\n"
  "buffer_load_dwordx4 v233, s[36:39], s9 offen lds\n"
  "ds_read_b64_tr_b16 v[128:129], v200 offset:4096\n"
  "ds_read_b64_tr_b16 v[130:131], v200 offset:6144\n"
  "ds_read_b64_tr_b16 v[132:133], v201 offset:4096\n"
  "ds_read_b64_tr_b16 v[134:135], v201 offset:6144\n"
  "ds_read_b64_tr_b16 v[136:137], v202 offset:4096\n"
  "ds_read_b64_tr_b16 v[138:139], v202 offset:6144\n"
  "ds_read_b64_tr_b16 v[140:141], v203 offset:4096\n"
  "ds_read_b64_tr_b16 v[142:143], v203 offset:6144\n"
  "s_add_u32 m0, s8, 2048\n"
  "s_mov_b32 s9, 745472\n"
  "buffer_load_dwordx4 v232, s[36:39], s9 offen lds\n"
  "ds_read_b64_tr_b16 v[144:145], v200 offset:8192\n"
  "ds_read_b64_tr_b16 v[146:147], v200 offset:10240\n"
  "ds_read_b64_tr_b16 v[148:149], v201 offset:8192\n"
  "ds_read_b64_tr_b16 v[150:151], v201 offset:10240\n"
  "ds_read_b64_tr_b16 v[152:153], v202 offset:8192\n"
  "ds_read_b64_tr_b16 v[154:155], v202 offset:10240\n"
  "ds_read_b64_tr_b16 v[156:157], v203 offset:8192\n"
  "ds_read_b64_tr_b16 v[158:159], v203 offset:10240\n"
  "ds_read_b64_tr_b16 v[160:161], v200 offset:12288\n"
  "ds_read_b64_tr_b16 v[162:163], v200 offset:14336\n"
  "s_add_u32 m0, s8, 3072\n"
  "s_mov_b32 s9, 774144\n"
  "buffer_load_dwordx4 v233, s[36:39], s9 offen lds\n"
  "ds_read_b64_tr_b16 v[164:165], v201 offset:12288\n"
  "ds_read_b64_tr_b16 v[166:167], v201 offset:14336\n"
  "ds_read_b64_tr_b16 a[176:177], v202 offset:12288\n"
  "ds_read_b64_tr_b16 a[178:179], v202 offset:14336\n"
  "ds_read_b64_tr_b16 a[180:181], v203 offset:12288\n"
  "ds_read_b64_tr_b16 a[182:183], v203 offset:14336\n"
  "ds_read_b64_tr_b16 a[184:185], v200 offset:16384\n"
  "ds_read_b64_tr_b16 a[186:187], v200 offset:18432\n"
  "ds_read_b64_tr_b16 a[188:189], v201 offset:16384\n"
  "s_add_u32 m0, s8, 4096\n"
  "s_mov_b32 s9, 802816\n"
  "buffer_load_dwordx4 v232, s[36:39], s9 offen lds\n"
  "ds_read_b64_tr_b16 a[190:191], v201 offset:18432\n"
  "ds_read_b64_tr_b16 a[192:193], v202 offset:16384\n"
  "ds_read_b64_tr_b16 a[194:195], v202 offset:18432\n"
  "ds_read_b64_tr_b16 a[196:197], v203 offset:16384\n"
  "ds_read_b64_tr_b16 a[198:199], v203 offset:18432\n"
  "ds_read_b64_tr_b16 a[200:201], v200 offset:20480\n"
  "ds_read_b64_tr_b16 a[202:203], v200 offset:22528\n"
  "ds_read_b64_tr_b16 a[204:205], v201 offset:20480\n"
  "ds_read_b64_tr_b16 a[206:207], v201 offset:22528\n"
  "s_waitcnt vmcnt(10) lgkmcnt(15)\n"
  "ds_read_b64_tr_b16 a[64:65], v168 offset:16384\n"
  "s_add_u32 m0, s8, 5120\n"
  "s_mov_b32 s9, 831488\n"
  "buffer_load_dwordx4 v233, s[36:39], s9 offen lds\n"
  "ds_read_b64_tr_b16 a[66:67], v168 offset:16896\n"
  "ds_read_b64_tr_b16 a[68:69], v169 offset:16384\n"
  "ds_read_b64_tr_b16 a[70:71], v169 offset:16896\n"
  "ds_read_b64_tr_b16 a[72:73], v170 offset:16384\n"
  "ds_read_b64_tr_b16 a[74:75], v170 offset:16896\n"
  "ds_read_b64_tr_b16 a[76:77], v171 offset:16384\n"
  "ds_read_b64_tr_b16 a[78:79], v171 offset:16896\n"
  "s_add_u32 m0, s8, 6144\n"
  "s_mov_b32 s9, 860160\n"
  "buffer_load_dwordx4 v232, s[36:39], s9 offen lds\n"
  "s_add_u32 m0, s8, 7168\n"
  "s_mov_b32 s9, 888832\n"
  "buffer_load_dwordx4 v233, s[36:39], s9 offen lds\n"
  "s_add_u32 m0, s8, 8192\n"
  "s_mov_b32 s9, 917504\n"
  "buffer_load_dwordx4 v232, s[36:39], s9 offen lds\n"
  "s_add_u32 m0, s8, 9216\n"
  "s_mov_b32 s9, 946176\n"
  "buffer_load_dwordx4 v233, s[36:39], s9 offen lds\n"
  "s_waitcnt vmcnt(10) lgkmcnt(0)\n"
  "ds_read_b64_tr_b16 a[80:81], v168 offset:20480\n"
  "ds_read_b64_tr_b16 a[82:83], v168 offset:20992\n"
  "ds_read_b64_tr_b16 a[84:85], v169 offset:20480\n"
  "ds_read_b64_tr_b16 a[86:87], v169 offset:20992\n"
  "ds_read_b64_tr_b16 a[88:89], v170 offset:20480\n"
  "ds_read_b64_tr_b16 a[90:91], v170 offset:20992\n"
  "ds_read_b64_tr_b16 a[92:93], v171 offset:20480\n"
  "ds_read_b64_tr_b16 a[94:95], v171 offset:20992\n"
  "s_add_u32 m0, s8, 10240\n"
  "s_mov_b32 s9, 974848\n"
  "buffer_load_dwordx4 v232, s[36:39], s9 offen lds\n"
  "s_add_u32 m0, s8, 11264\n"
  "s_mov_b32 s9, 1003520\n"
  "buffer_load_dwordx4 v233, s[36:39], s9 offen lds\n"
  "s_add_u32 m0, s8, 12288\n"
  "s_mov_b32 s9, 1032192\n"
  "buffer_load_dwordx4 v232, s[36:39], s9 offen lds\n"
  "s_add_u32 m0, s8, 13312\n"
  "s_mov_b32 s9, 1060864\n"
  "buffer_load_dwordx4 v233, s[36:39], s9 offen lds\n"
  "s_waitcnt vmcnt(10) lgkmcnt(0)\n"
  "ds_read_b64_tr_b16 a[96:97], v168 offset:0\n"
  "ds_read_b64_tr_b16 a[98:99], v168 offset:512\n"
  "ds_read_b64_tr_b16 a[100:101], v169 offset:0\n"
  "ds_read_b64_tr_b16 a[102:103], v169 offset:512\n"
  "ds_read_b64_tr_b16 a[104:105], v170 offset:0\n"
  "ds_read_b64_tr_b16 a[106:107], v170 offset:512\n"
  "ds_read_b64_tr_b16 a[108:109], v171 offset:0\n"
  "ds_read_b64_tr_b16 a[110:111], v171 offset:512\n"
  "s_add_u32 m0, s8, 14336\n"
  "s_mov_b32 s9, 1089536\n"
  "buffer_load_dwordx4 v232, s[36:39], s9 offen lds\n"
  "s_waitcnt vmcnt(7) lgkmcnt(0)\n"
  "ds_read_b64_tr_b16 a[112:113], v168 offset:4096\n"
  "ds_read_b64_tr_b16 a[114:115], v168 offset:4608\n"
  "ds_read_b64_tr_b16 a[116:117], v169 offset:4096\n"
  "ds_read_b64_tr_b16 a[118:119], v169 offset:4608\n"
  "ds_read_b64_tr_b16 a[120:121], v170 offset:4096\n"
  "ds_read_b64_tr_b16 a[122:123], v170 offset:4608\n"
  "ds_read_b64_tr_b16 a[124:125], v171 offset:4096\n"
  "ds_read_b64_tr_b16 a[126:127], v171 offset:4608\n"
  "s_sleep 1\n"
  "buffer_load_dwordx2 v[206:207], v249, s[20:23], 0 offen sc1\n"
  "s_add_u32 m0, s8, 15360\n"
  "s_mov_b32 s9, 1118208\n"
  "buffer_load_dwordx4 v233, s[36:39], s9 offen lds\n"
  "s_add_u32 m0, s8, 16384\n"
  "s_mov_b32 s9, 1146880\n"
  "buffer_load_dwordx4 v232, s[36:39], s9 offen lds\n"
  "s_add_u32 m0, s8, 17408\n"
  "s_mov_b32 s9, 1175552\n"
  "buffer_load_dwordx4 v233, s[36:39], s9 offen lds\n"
  "s_waitcnt vmcnt(7) lgkmcnt(0)\n"
  "ds_read_b64_tr_b16 a[128:129], v168 offset:8192\n"
  "ds_read_b64_tr_b16 a[130:131], v168 offset:8704\n"
  "ds_read_b64_tr_b16 a[132:133], v169 offset:8192\n"
  "s_add_u32 m0, s8, 18432\n"
  "s_mov_b32 s9, 1204224\n"
  "buffer_load_dwordx4 v232, s[36:39], s9 offen lds\n"
  "ds_read_b64_tr_b16 a[134:135], v169 offset:8704\n"
  "ds_read_b64_tr_b16 a[136:137], v170 offset:8192\n"
  "ds_read_b64_tr_b16 a[138:139], v170 offset:8704\n"
  "ds_read_b64_tr_b16 a[140:141], v171 offset:8192\n"
  "ds_read_b64_tr_b16 a[142:143], v171 offset:8704\n"
  "s_add_u32 m0, s8, 19456\n"
  "s_mov_b32 s9, 1232896\n"
  "buffer_load_dwordx4 v233, s[36:39], s9 offen lds\n"
  "s_add_u32 m0, s8, 20480\n"
  "s_mov_b32 s9, 1261568\n"
  "buffer_load_dwordx4 v232, s[36:39], s9 offen lds\n"
  "s_add_u32 m0, s8, 21504\n"
  "s_mov_b32 s9, 1290240\n"
  "buffer_load_dwordx4 v233, s[36:39], s9 offen lds\n"
  "s_add_u32 m0, s8, 22528\n"
  "s_mov_b32 s9, 1318912\n"
  "buffer_load_dwordx4 v232, s[36:39], s9 offen lds\n"
  "s_add_u32 m0, s8, 23552\n"
  "s_mov_b32 s9, 1347584\n"
  "buffer_load_dwordx4 v233, s[36:39], s9 offen lds\n"
  "s_waitcnt vmcnt(9) lgkmcnt(0)\n"
  "v_cmp_ne_u64 vcc, s[56:57], v[206:207]\n"
  "s_cmp_eq_u64 vcc, 0\n"
  "s_cbranch_scc0 .Lfb19_%=\n"
  ".Lbk19_%=:\n"
  "buffer_load_dwordx4 v[48:51], v250, s[20:23], 0 offen sc1\n"
  "buffer_load_dwordx4 v[52:55], v250, s[20:23], 0 offen offset:1024 sc1\n"
  "s_waitcnt vmcnt(10)\n"
  "ds_read_b64_tr_b16 a[144:145], v168 offset:12288\n"
  "ds_read_b64_tr_b16 a[146:147], v168 offset:12800\n"
  "ds_read_b64_tr_b16 a[148:149], v169 offset:12288\n"
  "ds_read_b64_tr_b16 a[150:151], v169 offset:12800\n"
  "ds_read_b64_tr_b16 a[152:153], v170 offset:12288\n"
  "buffer_load_dwordx4 v[56:59], v250, s[20:23], 0 offen offset:2048 sc1\n"
  "ds_read_b64_tr_b16 a[154:155], v170 offset:12800\n"
  "ds_read_b64_tr_b16 a[156:157], v171 offset:12288\n"
  "ds_read_b64_tr_b16 a[158:159], v171 offset:12800\n"
  "buffer_load_dwordx4 v[60:63], v250, s[20:23], 0 offen offset:3072 sc1\n"
  "buffer_load_dwordx4 v[64:67], v250, s[20:23], s72 offen sc1\n"
  "buffer_load_dwordx4 v[68:71], v250, s[20:23], s72 offen offset:1024 sc1\n"
  "buffer_load_dwordx4 v[72:75], v250, s[20:23], s72 offen offset:2048 sc1\n"
  "s_waitcnt vmcnt(11) lgkmcnt(0)\n"
  "ds_read_b64_tr_b16 a[160:161], v168 offset:16384\n"
  "buffer_load_dwordx4 v[76:79], v250, s[20:23], s72 offen offset:3072 sc1\n"
  "ds_read_b64_tr_b16 a[162:163], v168 offset:16896\n"
  "ds_read_b64_tr_b16 a[164:165], v169 offset:16384\n"
  "ds_read_b64_tr_b16 a[166:167], v169 offset:16896\n"
  "ds_read_b64_tr_b16 a[168:169], v170 offset:16384\n"
  "ds_read_b64_tr_b16 a[170:171], v170 offset:16896\n"
  "ds_read_b64_tr_b16 a[172:173], v171 offset:16384\n"
  "ds_read_b64_tr_b16 a[174:175], v171 offset:16896\n"
  "buffer_load_dwordx4 v[80:83], v250, s[20:23], s73 offen sc1\n"
  "buffer_load_dwordx4 v[84:87], v250, s[20:23], s73 offen offset:1024 sc1\n"
  "buffer_load_dwordx4 v[88:91], v250, s[20:23], s73 offen offset:2048 sc1\n"
  "buffer_load_dwordx4 v[92:95], v250, s[20:23], s73 offen offset:3072 sc1\n"
  "buffer_load_dwordx4 v[96:99], v250, s[20:23], s74 offen sc1\n"
  "s_waitcnt vmcnt(12) lgkmcnt(0)\n"
  "v_mfma_f32_16x16x32_bf16 v[176:179], v[48:51], v[112:115], v[176:179]\n"
  "ds_read_b64_tr_b16 v[112:113], v202 offset:20480\n"
  "v_mfma_f32_16x16x32_bf16 v[180:183], v[48:51], v[116:119], v[180:183]\n"
  "ds_read_b64_tr_b16 v[114:115], v202 offset:22528\n"
  "v_mfma_f32_16x16x32_bf16 v[184:187], v[48:51], v[120:123], v[184:187]\n"
  "buffer_load_dwordx4 v[100:103], v250, s[20:23], s74 offen offset:1024 sc1\n"
  "ds_read_b64_tr_b16 v[116:117], v203 offset:20480\n"
  "v_mfma_f32_16x16x32_bf16 v[188:191], v[48:51], v[124:127], v[188:191]\n"
  "ds_read_b64_tr_b16 v[118:119], v203 offset:22528\n"
  "s_lshl1_add_u32 s87, s65, s59\n"
  "s_waitcnt vmcnt(12)\n"
  "v_mfma_f32_16x16x32_bf16 v[176:179], v[52:55], v[128:131], v[176:179]\n"
  "ds_read_b64_tr_b16 v[120:121], v200 offset:24576\n"
  "s_lshl_b32 s87, s87, 6\n"
  "v_mfma_f32_16x16x32_bf16 v[180:183], v[52:55], v[132:135], v[180:183]\n"
  "ds_read_b64_tr_b16 v[122:123], v200 offset:26624\n"
  "buffer_load_dwordx4 v[104:107], v250, s[20:23], s74 offen offset:2048 sc1\n"
  "v_mfma_f32_16x16x32_bf16 v[184:187], v[52:55], v[136:139], v[184:187]\n"
  "ds_read_b64_tr_b16 v[124:125], v201 offset:24576\n"
  "s_or_b32 s87, s87, s70\n"
  "v_mfma_f32_16x16x32_bf16 v[188:191], v[52:55], v[140:143], v[188:191]\n"
  "ds_read_b64_tr_b16 v[126:127], v201 offset:26624\n"
  "v_lshl_add_u32 v48, v3, 3, s87\n"
  "s_waitcnt vmcnt(12)\n"
  "v_mfma_f32_16x16x32_bf16 v[176:179], v[56:59], v[144:147], v[176:179]\n"
  "ds_read_b64_tr_b16 v[128:129], v202 offset:24576\n"
  "v_mfma_f32_16x16x32_bf16 v[180:183], v[56:59], v[148:151], v[180:183]\n"
  "ds_read_b64_tr_b16 v[130:131], v202 offset:26624\n"
  "buffer_load_dwordx4 v[108:111], v250, s[20:23], s74 offen offset:3072 sc1\n"
  "v_mfma_f32_16x16x32_bf16 v[184:187], v[56:59], v[152:155], v[184:187]\n"
  "ds_read_b64_tr_b16 v[132:133], v203 offset:24576\n"
  "v_mfma_f32_16x16x32_bf16 v[188:191], v[56:59], v[156:159], v[188:191]\n"
  "ds_read_b64_tr_b16 v[134:135], v203 offset:26624\n"
  "s_waitcnt vmcnt(12) lgkmcnt(8)\n"
  "v_mfma_f32_16x16x32_bf16 v[176:179], v[60:63], v[160:163], v[176:179]\n"
  "ds_read_b64_tr_b16 v[136:137], v200 offset:28672\n"
  "v_mfma_f32_16x16x32_bf16 v[180:183], v[60:63], v[164:167], v[180:183]\n"
  "ds_read_b64_tr_b16 v[138:139], v200 offset:30720\n"
  "buffer_load_dwordx2 v[50:51], v48, s[44:47], 0 offen\n"
  "v_mfma_f32_16x16x32_bf16 v[184:187], v[60:63], a[176:179], v[184:187]\n"
  "ds_read_b64_tr_b16 v[140:141], v201 offset:28672\n"
  "v_mfma_f32_16x16x32_bf16 v[188:191], v[60:63], a[180:183], v[188:191]\n"
  "ds_read_b64_tr_b16 v[142:143], v201 offset:30720\n"
  "s_waitcnt vmcnt(12) lgkmcnt(8)\n"
  "v_mfma_f32_16x16x32_bf16 v[176:179], v[64:67], a[184:187], v[176:179]\n"
  "ds_read_b64_tr_b16 v[144:145], v202 offset:28672\n"
  "s_add_u32 m0, s8, 0\n"
  "s_mov_b32 s9, 1376256\n"
  "buffer_load_dwordx4 v232, s[36:39], s9 offen lds\n"
  "v_mfma_f32_16x16x32_bf16 v[180:183], v[64:67], a[188:191], v[180:183]\n"
  "ds_read_b64_tr_b16 v[146:147], v202 offset:30720\n"
  "v_mfma_f32_16x16x32_bf16 v[184:187], v[64:67], a[192:195], v[184:187]\n"
  "ds_read_b64_tr_b16 v[148:149], v203 offset:28672\n"
  "v_mfma_f32_16x16x32_bf16 v[188:191], v[64:67], a[196:199], v[188:191]\n"
  "ds_read_b64_tr_b16 v[150:151], v203 offset:30720\n"
  "s_waitcnt vmcnt(12) lgkmcnt(8)\n"
  "v_mfma_f32_16x16x32_bf16 v[176:179], v[68:71], a[200:203], v[176:179]\n"
  "ds_read_b64_tr_b16 v[152:153], v200 offset:32768\n"
  "v_mfma_f32_16x16x32_bf16 v[180:183], v[68:71], a[204:207], v[180:183]\n"
  "ds_read_b64_tr_b16 v[154:155], v200 offset:34816\n"
  "buffer_load_dwordx2 v[54:55], v48, s[44:47], 0 offen offset:32\n"
  "v_mfma_f32_16x16x32_bf16 v[184:187], v[68:71], v[112:115], v[184:187]\n"
  "ds_read_b64_tr_b16 v[156:157], v201 offset:32768\n"
  "v_mfma_f32_16x16x32_bf16 v[188:191], v[68:71], v[116:119], v[188:191]\n"
  "ds_read_b64_tr_b16 v[158:159], v201 offset:34816\n"
  "s_waitcnt vmcnt(12) lgkmcnt(8)\n"
  "v_mfma_f32_16x16x32_bf16 v[176:179], v[72:75], v[120:123], v[176:179]\n"
  "ds_read_b64_tr_b16 v[160:161], v202 offset:32768\n"
  "v_mfma_f32_16x16x32_bf16 v[180:183], v[72:75], v[124:127], v[180:183]\n"
  "ds_read_b64_tr_b16 v[162:163], v202 offset:34816\n"
  "s_add_u32 m0, s8, 1024\n"
  "s_mov_b32 s9, 1404928\n"
  "buffer_load_dwordx4 v233, s[36:39], s9 offen lds\n"
  "v_mfma_f32_16x16x32_bf16 v[184:187], v[72:75], v[128:131], v[184:187]\n"
  "ds_read_b64_tr_b16 v[164:165], v203 offset:32768\n"
  "v_mfma_f32_16x16x32_bf16 v[188:191], v[72:75], v[132:135], v[188:191]\n"
  "ds_read_b64_tr_b16 v[166:167], v203 offset:34816\n"
  "s_waitcnt vmcnt(12) lgkmcnt(8)\n"
  "v_mfma_f32_16x16x32_bf16 v[176:179], v[76:79], v[136:139], v[176:179]\n"
  "ds_read_b64_tr_b16 a[176:177], v200 offset:36864\n"
  "v_mfma_f32_16x16x32_bf16 v[180:183], v[76:79], v[140:143], v[180:183]\n"
  "ds_read_b64_tr_b16 a[178:179], v200 offset:38912\n"
  "s_add_u32 m0, s8, 2048\n"
  "s_mov_b32 s9, 1433600\n"
  "buffer_load_dwordx4 v232, s[36:39], s9 offen lds\n"
  "v_mfma_f32_16x16x32_bf16 v[184:187], v[76:79], v[144:147], v[184:187]\n"
  "ds_read_b64_tr_b16 a[180:181], v201 offset:36864\n"
  "ds_read_b64_tr_b16 a[182:183], v201 offset:38912\n"
  "v_mfma_f32_16x16x32_bf16 v[188:191], v[76:79], v[148:151], v[188:191]\n"
  "ds_read_b64_tr_b16 a[184:185], v202 offset:36864\n"
  "ds_read_b64_tr_b16 a[186:187], v202 offset:38912\n"
  "s_waitcnt vmcnt(12) lgkmcnt(9)\n"
  "v_mfma_f32_16x16x32_bf16 v[176:179], v[80:83], v[152:155], v[176:179]\n"
  "ds_read_b64_tr_b16 a[188:189], v203 offset:36864\n"
  "ds_read_b64_tr_b16 a[190:191], v203 offset:38912\n"
  "v_mfma_f32_16x16x32_bf16 v[180:183], v[80:83], v[156:159], v[180:183]\n"
  "ds_read_b64_tr_b16 a[192:193], v200 offset:40960\n"
  "ds_read_b64_tr_b16 a[194:195], v200 offset:43008\n"
  "s_waitcnt lgkmcnt(11)\n"
  "v_mfma_f32_16x16x32_bf16 v[184:187], v[80:83], v[160:163], v[184:187]\n"
  "ds_read_b64_tr_b16 a[196:197], v201 offset:40960\n"
  "ds_read_b64_tr_b16 a[198:199], v201 offset:43008\n"
  "s_waitcnt lgkmcnt(12)\n"
  "v_mfma_f32_16x16x32_bf16 v[188:191], v[80:83], v[164:167], v[188:191]\n"
  "ds_read_b64_tr_b16 a[200:201], v202 offset:40960\n"
  "ds_read_b64_tr_b16 a[202:203], v202 offset:43008\n"
  "s_waitcnt vmcnt(11) lgkmcnt(12)\n"
  "v_mfma_f32_16x16x32_bf16 v[176:179], v[84:87], a[176:179], v[176:179]\n"
  "s_add_u32 m0, s8, 3072\n"
  "s_mov_b32 s9, 1462272\n"
  "buffer_load_dwordx4 v233, s[36:39], s9 offen lds\n"
  "s_waitcnt lgkmcnt(10)\n"
  "v_mfma_f32_16x16x32_bf16 v[180:183], v[84:87], a[180:183], v[180:183]\n"
  "ds_read_b64_tr_b16 a[204:205], v203 offset:40960\n"
  "ds_read_b64_tr_b16 a[206:207], v203 offset:43008\n"
  "s_waitcnt lgkmcnt(10)\n"
  "v_mfma_f32_16x16x32_bf16 v[184:187], v[84:87], a[184:187], v[184:187]\n"
  "ds_read_b64_tr_b16 v[112:113], v200 offset:45056\n"
  "ds_read_b64_tr_b16 v[114:115], v200 offset:47104\n"
  "s_waitcnt lgkmcnt(10)\n"
  "v_mfma_f32_16x16x32_bf16 v[188:191], v[84:87], a[188:191], v[188:191]\n"
  "ds_read_b64_tr_b16 v[116:117], v201 offset:45056\n"
  "ds_read_b64_tr_b16 v[118:119], v201 offset:47104\n"
  "s_waitcnt vmcnt(11) lgkmcnt(10)\n"
  "v_mfma_f32_16x16x32_bf16 v[176:179], v[88:91], a[192:195], v[176:179]\n"
  "s_add_u32 m0, s8, 4096\n"
  "s_mov_b32 s9, 1572864\n"
  "buffer_load_dwordx4 v240, s[40:43], s9 offen lds\n"
  "s_waitcnt lgkmcnt(8)\n"
  "v_mfma_f32_16x16x32_bf16 v[180:183], v[88:91], a[196:199], v[180:183]\n"
  "ds_read_b64_tr_b16 v[120:121], v202 offset:45056\n"
  "ds_read_b64_tr_b16 v[122:123], v202 offset:47104\n"
  "s_waitcnt lgkmcnt(8)\n"
  "v_mfma_f32_16x16x32_bf16 v[184:187], v[88:91], a[200:203], v[184:187]\n"
  "ds_read_b64_tr_b16 v[124:125], v203 offset:45056\n"
  "ds_read_b64_tr_b16 v[126:127], v203 offset:47104\n"
  "s_waitcnt lgkmcnt(8)\n"
  "v_mfma_f32_16x16x32_bf16 v[188:191], v[88:91], a[204:207], v[188:191]\n"
  "ds_read_b64_tr_b16 v[128:129], v200 offset:49152\n"
  "ds_read_b64_tr_b16 v[130:131], v200 offset:51200\n"
  "s_waitcnt vmcnt(11) lgkmcnt(8)\n"
  "v_mfma_f32_16x16x32_bf16 v[176:179], v[92:95], v[112:115], v[176:179]\n"
  "s_add_u32 m0, s8, 5120\n"
  "s_mov_b32 s9, 1605632\n"
  "buffer_load_dwordx4 v240, s[40:43], s9 offen lds\n"
  "s_waitcnt lgkmcnt(6)\n"
  "v_mfma_f32_16x16x32_bf16 v[180:183], v[92:95], v[116:119], v[180:183]\n"
  "ds_read_b64_tr_b16 v[132:133], v201 offset:49152\n"
  "ds_read_b64_tr_b16 v[134:135], v201 offset:51200\n"
  "ds_read_b64_tr_b16 v[136:137], v202 offset:49152\n"
  "ds_read_b64_tr_b16 v[138:139], v202 offset:51200\n"
  "ds_read_b64_tr_b16 v[140:141], v203 offset:49152\n"
  "s_waitcnt lgkmcnt(9)\n"
  "v_mfma_f32_16x16x32_bf16 v[184:187], v[92:95], v[120:123], v[184:187]\n"
  "ds_read_b64_tr_b16 v[142:143], v203 offset:51200\n"
  "ds_read_b64_tr_b16 v[144:145], v200 offset:53248\n"
  "s_waitcnt lgkmcnt(9)\n"
  "v_mfma_f32_16x16x32_bf16 v[188:191], v[92:95], v[124:127], v[188:191]\n"
  "s_add_u32 m0, s8, 6144\n"
  "s_mov_b32 s9, 1638400\n"
  "buffer_load_dwordx4 v240, s[40:43], s9 offen lds\n"
  "s_waitcnt vmcnt(12) lgkmcnt(7)\n"
  "v_mfma_f32_16x16x32_bf16 v[176:179], v[96:99], v[128:131], v[176:179]\n"
  "ds_read_b64_tr_b16 v[146:147], v200 offset:55296\n"
  "ds_read_b64_tr_b16 v[148:149], v201 offset:53248\n"
  "ds_read_b64_tr_b16 v[150:151], v201 offset:55296\n"
  "ds_read_b64_tr_b16 v[152:153], v202 offset:53248\n"
  "ds_read_b64_tr_b16 v[154:155], v202 offset:55296\n"
  "s_lshl2_add_u32 s88, s64, s58\n"
  "ds_read_b64_tr_b16 v[156:157], v203 offset:53248\n"
  "s_waitcnt lgkmcnt(11)\n"
  "v_mfma_f32_16x16x32_bf16 v[180:183], v[96:99], v[132:135], v[180:183]\n"
  "ds_read_b64_tr_b16 v[158:159], v203 offset:55296\n"
  "ds_read_b64_tr_b16 v[160:161], v200 offset:57344\n"
  "s_waitcnt lgkmcnt(11)\n"
  "v_mfma_f32_16x16x32_bf16 v[184:187], v[96:99], v[136:139], v[184:187]\n"
  "s_add_u32 m0, s8, 7168\n"
  "s_mov_b32 s9, 1671168\n"
  "buffer_load_dwordx4 v240, s[40:43], s9 offen lds\n"
  "s_waitcnt lgkmcnt(9)\n"
  "v_mfma_f32_16x16x32_bf16 v[188:191], v[96:99], v[140:143], v[188:191]\n"
  "ds_read_b64_tr_b16 v[162:163], v200 offset:59392\n"
  "ds_read_b64_tr_b16 v[164:165], v201 offset:57344\n"
  "ds_read_b64_tr_b16 v[166:167], v201 offset:59392\n"
  "ds_read_b64_tr_b16 a[176:177], v202 offset:57344\n"
  "s_waitcnt vmcnt(12) lgkmcnt(11)\n"
  "v_mfma_f32_16x16x32_bf16 v[176:179], v[100:103], v[144:147], v[176:179]\n"
  "ds_read_b64_tr_b16 a[178:179], v202 offset:59392\n"
  "ds_read_b64_tr_b16 a[180:181], v203 offset:57344\n"
  "s_waitcnt lgkmcnt(11)\n"
  "v_mfma_f32_16x16x32_bf16 v[180:183], v[100:103], v[148:151], v[180:183]\n"
  "s_add_u32 m0, s8, 8192\n"
  "s_mov_b32 s9, 1703936\n"
  "buffer_load_dwordx4 v240, s[40:43], s9 offen lds\n"
  "s_waitcnt lgkmcnt(9)\n"
  "v_mfma_f32_16x16x32_bf16 v[184:187], v[100:103], v[152:155], v[184:187]\n"
  "ds_read_b64_tr_b16 a[182:183], v203 offset:59392\n"
  "ds_read_b64_tr_b16 a[184:185], v200 offset:61440\n"
  "s_waitcnt lgkmcnt(9)\n"
  "v_mfma_f32_16x16x32_bf16 v[188:191], v[100:103], v[156:159], v[188:191]\n"
  "ds_read_b64_tr_b16 a[186:187], v200 offset:63488\n"
  "ds_read_b64_tr_b16 a[188:189], v201 offset:61440\n"
  "s_waitcnt vmcnt(12) lgkmcnt(9)\n"
  "v_mfma_f32_16x16x32_bf16 v[176:179], v[104:107], v[160:163], v[176:179]\n"
  "ds_read_b64_tr_b16 a[190:191], v201 offset:63488\n"
  "ds_read_b64_tr_b16 a[192:193], v202 offset:61440\n"
  "s_waitcnt lgkmcnt(9)\n"
  "v_mfma_f32_16x16x32_bf16 v[180:183], v[104:107], v[164:167], v[180:183]\n"
  "s_add_u32 m0, s8, 9216\n"
  "s_mov_b32 s9, 1736704\n"
  "buffer_load_dwordx4 v240, s[40:43], s9 offen lds\n"
  "s_waitcnt lgkmcnt(7)\n"
  "v_mfma_f32_16x16x32_bf16 v[184:187], v[104:107], a[176:179], v[184:187]\n"
  "ds_read_b64_tr_b16 a[194:195], v202 offset:63488\n"
  "ds_read_b64_tr_b16 a[196:197], v203 offset:61440\n"
  "ds_read_b64_tr_b16 a[198:199], v203 offset:63488\n"
  "ds_read_b64_tr_b16 a[176:177], v168 offset:20480\n"
  "s_waitcnt lgkmcnt(9)\n"
  "v_mfma_f32_16x16x32_bf16 v[188:191], v[104:107], a[180:183], v[188:191]\n"
  "s_waitcnt vmcnt(11)\n"
  "v_lshlrev_b32 v80, 16, v50\n"
  "s_add_u32 m0, s8, 10240\n"
  "s_mov_b32 s9, 1769472\n"
  "buffer_load_dwordx4 v240, s[40:43], s9 offen lds\n"
  "s_waitcnt lgkmcnt(7)\n"
  "v_mfma_f32_16x16x32_bf16 v[176:179], v[108:111], a[184:187], v[176:179]\n"
  "v_and_b32 v81, 0xffff0000, v50\n"
  "v_lshlrev_b32 v48, 16, v50\n"
  "s_waitcnt lgkmcnt(5)\n"
  "v_mfma_f32_16x16x32_bf16 v[180:183], v[108:111], a[188:191], v[180:183]\n"
  "v_and_b32 v49, 0xffff0000, v50\n"
  "v_lshlrev_b32 v82, 16, v51\n"
  "v_and_b32 v83, 0xffff0000, v51\n"
  "s_nop 0\n"
  "v_cvt_pk_bf16_f32 v22, v176, v177\n"
  "v_lshlrev_b32 v56, 16, v22\n"
  "v_and_b32 v57, 0xffff0000, v22\n"
  "s_add_u32 m0, s8, 11264\n"
  "s_mov_b32 s9, 1802240\n"
  "buffer_load_dwordx4 v240, s[40:43], s9 offen lds\n"
  "s_waitcnt lgkmcnt(3)\n"
  "v_mfma_f32_16x16x32_bf16 v[184:187], v[108:111], a[192:195], v[184:187]\n"
  "v_cvt_pk_bf16_f32 v24, v180, v181\n"
  "v_lshlrev_b32 v60, 16, v24\n"
  "s_waitcnt lgkmcnt(1)\n"
  "v_mfma_f32_16x16x32_bf16 v[188:191], v[108:111], a[196:199], v[188:191]\n"
  "v_and_b32 v61, 0xffff0000, v24\n"
  "v_cvt_pk_bf16_f32 v23, v178, v179\n"
  "v_pk_mul_f32 v[76:77], v[56:57], v[56:57]\n"
  "s_nop 0\n"
  "v_cvt_pk_bf16_f32 v26, v184, v185\n"
  "v_cvt_pk_bf16_f32 v27, v186, v187\n"
  "v_lshlrev_b32 v64, 16, v26\n"
  "v_and_b32 v65, 0xffff0000, v26\n"
  "v_pk_fma_f32 v[76:77], v[60:61], v[60:61], v[76:77]\n"
  "v_cvt_pk_bf16_f32 v28, v188, v189\n"
  "v_cvt_pk_bf16_f32 v29, v190, v191\n"
  "v_lshlrev_b32 v68, 16, v28\n"
  "v_and_b32 v69, 0xffff0000, v28\n"
  "v_lshlrev_b32 v58, 16, v23\n"
  "v_and_b32 v59, 0xffff0000, v23\n"
  "v_cvt_pk_bf16_f32 v25, v182, v183\n"
  "v_lshlrev_b32 v66, 16, v27\n"
  "v_and_b32 v67, 0xffff0000, v27\n"
  "s_add_u32 m0, s8, 12288\n"
  "s_mov_b32 s9, 0\n"
  "buffer_load_dwordx4 v240, s[40:43], s9 offen lds\n"
  "v_lshlrev_b32 v70, 16, v29\n"
  "v_and_b32 v71, 0xffff0000, v29\n"
  "v_pk_add_f32 v[72:73], v[56:57], v[60:61]\n"
  "v_pk_add_f32 v[88:89], v[64:65], v[68:69]\n"
  "v_pk_fma_f32 v[76:77], v[64:65], v[64:65], v[76:77]\n"
  "v_lshlrev_b32 v62, 16, v25\n"
  "v_and_b32 v63, 0xffff0000, v25\n"
  "v_pk_add_f32 v[72:73], v[72:73], v[88:89]\n"
  "v_pk_fma_f32 v[76:77], v[68:69], v[68:69], v[76:77]\n"
  "v_pk_add_f32 v[90:91], v[66:67], v[70:71]\n"
  "v_pk_mul_f32 v[78:79], v[58:59], v[58:59]\n"
  "v_pk_add_f32 v[74:75], v[58:59], v[62:63]\n"
  "v_pk_fma_f32 v[78:79], v[62:63], v[62:63], v[78:79]\n"
  "v_pk_add_f32 v[74:75], v[74:75], v[90:91]\n"
  "v_pk_fma_f32 v[78:79], v[66:67], v[66:67], v[78:79]\n"
  "v_pk_fma_f32 v[78:79], v[70:71], v[70:71], v[78:79]\n"
  "v_add_f32_dpp v88, v72, v72 row_ror:8 row_mask:0xf bank_mask:0x3\n"
  "v_add_f32_dpp v90, v76, v76 row_ror:8 row_mask:0xf bank_mask:0x3\n"
  "s_add_u32 m0, s8, 13312\n"
  "s_mov_b32 s9, 32768\n"
  "buffer_load_dwordx4 v240, s[40:43], s9 offen lds\n"
  "v_add_f32_dpp v88, v74, v74 row_ror:8 row_mask:0xf bank_mask:0xc\n"
  "v_add_f32_dpp v89, v73, v73 row_ror:8 row_mask:0xf bank_mask:0x3\n"
  "v_add_f32_dpp v90, v78, v78 row_ror:8 row_mask:0xf bank_mask:0xc\n"
  "v_add_f32_dpp v91, v77, v77 row_ror:8 row_mask:0xf bank_mask:0x3\n"
  "v_add_f32_dpp v89, v75, v75 row_ror:8 row_mask:0xf bank_mask:0xc\n"
  "v_add_f32_dpp v92, v88, v88 row_shl:4 row_mask:0xf bank_mask:0x5\n"
  "v_add_f32_dpp v91, v79, v79 row_ror:8 row_mask:0xf bank_mask:0xc\n"
  "v_add_f32_dpp v93, v90, v90 row_shl:4 row_mask:0xf bank_mask:0x5\n"
  "v_add_f32_dpp v92, v89, v89 row_shr:4 row_mask:0xf bank_mask:0xa\n"
  "v_lshlrev_b32 v50, 16, v51\n"
  "v_add_f32_dpp v93, v91, v91 row_shr:4 row_mask:0xf bank_mask:0xa\n"
  "v_add_f32_dpp v92, v92, v92 quad_perm:[1,0,3,2] row_mask:0xf bank_mask:0xf\n"
  "v_and_b32 v51, 0xffff0000, v51\n"
  "v_add_f32_dpp v93, v93, v93 quad_perm:[1,0,3,2] row_mask:0xf bank_mask:0xf\n"
  "v_add_f32_dpp v92, v92, v92 quad_perm:[2,3,0,1] row_mask:0xf bank_mask:0xf\n"
  "v_min_u32 v92, -2, v92\n"
  "v_add_f32_dpp v93, v93, v93 quad_perm:[2,3,0,1] row_mask:0xf bank_mask:0xf\n"
  "v_min_u32 v93, -2, v93\n"
  "s_add_u32 m0, s8, 14336\n"
  "s_mov_b32 s9, 65536\n"
  "buffer_load_dwordx4 v240, s[40:43], s9 offen lds\n"
  "s_waitcnt vmcnt(14) lgkmcnt(0)\n"
  "v_lshlrev_b32 v84, 16, v54\n"
  "buffer_store_dwordx2 v[92:93], v251, s[20:23], 0 offen sc1\n"
  "v_and_b32 v85, 0xffff0000, v54\n"
  "v_lshlrev_b32 v52, 16, v54\n"
  "v_and_b32 v53, 0xffff0000, v54\n"
  "v_lshlrev_b32 v86, 16, v55\n"
  "v_and_b32 v87, 0xffff0000, v55\n"
  "v_lshlrev_b32 v54, 16, v55\n"
  "v_and_b32 v55, 0xffff0000, v55\n"
  "ds_read_b64_tr_b16 a[178:179], v168 offset:20992\n"
  "ds_read_b64_tr_b16 a[180:181], v169 offset:20480\n"
  "ds_read_b64_tr_b16 a[182:183], v169 offset:20992\n"
  "ds_read_b64_tr_b16 a[184:185], v170 offset:20480\n"
  "ds_read_b64_tr_b16 a[186:187], v170 offset:20992\n"
  "v_add_u32 v251, 131072, v238\n"
  "ds_read_b64_tr_b16 a[188:189], v171 offset:20480\n"
  "s_lshl_b32 s89, s88, 9\n"
  "ds_read_b64_tr_b16 a[190:191], v171 offset:20992\n"
  "s_add_u32 s89, s89, 114688\n"
  "s_waitcnt vmcnt(12)\n"
  "ds_read_b64_tr_b16 a[192:193], v168 offset:0\n"
  "s_add_u32 m0, s8, 15360\n"
  "s_mov_b32 s9, 98304\n"
  "buffer_load_dwordx4 v240, s[40:43], s9 offen lds\n"
  "ds_read_b64_tr_b16 a[194:195], v168 offset:512\n"
  "ds_read_b64_tr_b16 a[196:197], v169 offset:0\n"
  "ds_read_b64_tr_b16 a[198:199], v169 offset:512\n"
  "ds_read_b64_tr_b16 a[200:201], v170 offset:0\n"
  "v_min_u32 v247, 6, v0\n"
  "ds_read_b64_tr_b16 a[202:203], v170 offset:512\n"
  "v_lshl_add_u32 v247, v247, 3, s89\n"
  "ds_read_b64_tr_b16 a[204:205], v171 offset:0\n"
  "s_add_u32 m0, s8, 16384\n"
  "s_mov_b32 s9, 131072\n"
  "buffer_load_dwordx4 v240, s[40:43], s9 offen lds\n"
  "ds_read_b64_tr_b16 a[206:207], v171 offset:512\n"
  "s_lshl3_add_u32 s88, s64, s58\n"
  "s_mul_i32 s88, s88, 28672\n"
  "s_add_u32 s88, s88, 12582912\n"
  "v_add_u32 v232, s88, v1\n"
  "v_add_u32 v233, 114688, v232\n"
  "s_waitcnt vmcnt(10) lgkmcnt(12)\n"
  "ds_read_b64_tr_b16 v[212:213], v234 offset:4096\n"
  "s_add_u32 m0, s8, 17408\n"
  "s_mov_b32 s9, 163840\n"
  "buffer_load_dwordx4 v240, s[40:43], s9 offen lds\n"
  "ds_read_b64_tr_b16 v[214:215], v234 offset:6144\n"
  "ds_read_b64_tr_b16 v[208:209], v235 offset:4096\n"
  "ds_read_b64_tr_b16 v[210:211], v235 offset:6144\n"
  "ds_read_b64_tr_b16 v[184:185], v236 offset:4096\n"
  "ds_read_b64_tr_b16 v[186:187], v236 offset:6144\n"
  "ds_read_b64_tr_b16 v[180:181], v237 offset:4096\n"
  "ds_read_b64_tr_b16 v[182:183], v237 offset:6144\n"
  "s_add_u32 m0, s8, 18432\n"
  "s_mov_b32 s9, 196608\n"
  "buffer_load_dwordx4 v240, s[40:43], s9 offen lds\n"
  "s_add_u32 m0, s8, 19456\n"
  "s_mov_b32 s9, 229376\n"
  "buffer_load_dwordx4 v240, s[40:43], s9 offen lds\n"
  "s_waitcnt lgkmcnt(0)\n"
  "s_add_u32 m0, s8, 20480\n"
  "s_mov_b32 s9, 262144\n"
  "buffer_load_dwordx4 v240, s[40:43], s9 offen lds\n"
  "s_waitcnt vmcnt(10)\n"
  "ds_read_b64_tr_b16 v[176:177], v234 offset:8192\n"
  "s_add_u32 m0, s8, 21504\n"
  "s_mov_b32 s9, 294912\n"
  "buffer_load_dwordx4 v240, s[40:43], s9 offen lds\n"
  "ds_read_b64_tr_b16 v[178:179], v234 offset:10240\n"
  "ds_read_b64_tr_b16 v[200:201], v235 offset:8192\n"
  "ds_read_b64_tr_b16 v[202:203], v235 offset:10240\n"
  "ds_read_b64_tr_b16 v[196:197], v236 offset:8192\n"
  "ds_read_b64_tr_b16 v[198:199], v236 offset:10240\n"
  "ds_read_b64_tr_b16 v[192:193], v237 offset:8192\n"
  "ds_read_b64_tr_b16 v[194:195], v237 offset:10240\n"
  "s_add_u32 m0, s8, 22528\n"
  "s_mov_b32 s9, 327680\n"
  "buffer_load_dwordx4 v240, s[40:43], s9 offen lds\n"
  "s_add_u32 m0, s8, 23552\n"
  "s_mov_b32 s9, 360448\n"
  "buffer_load_dwordx4 v240, s[40:43], s9 offen lds\n"
  "s_add_u32 m0, s8, 0\n"
  "s_mov_b32 s9, 393216\n"
  "buffer_load_dwordx4 v240, s[40:43], s9 offen lds\n"
  "s_sleep 6\n"
  "buffer_load_dwordx2 v[94:95], v252, s[20:23], 0 offen sc1\n"
  "buffer_load_dwordx2 v[96:97], v252, s[20:23], 0 offen offset:128 sc1\n"
  "s_add_u32 m0, s8, 1024\n"
  "s_mov_b32 s9, 425984\n"
  "buffer_load_dwordx4 v240, s[40:43], s9 offen lds\n"
  "s_add_u32 m0, s8, 2048\n"
  "s_mov_b32 s9, 458752\n"
  "buffer_load_dwordx4 v240, s[40:43], s9 offen lds\n"
  "s_add_u32 m0, s8, 3072\n"
  "s_mov_b32 s9, 491520\n"
  "buffer_load_dwordx4 v240, s[40:43], s9 offen lds\n"
  "s_add_u32 m0, s8, 4096\n"
  "s_mov_b32 s9, 524288\n"
  "buffer_load_dwordx4 v240, s[40:43], s9 offen lds\n"
  "s_add_u32 m0, s8, 5120\n"
  "s_mov_b32 s9, 557056\n"
  "buffer_load_dwordx4 v240, s[40:43], s9 offen lds\n"
  "s_add_u32 m0, s8, 6144\n"
  "s_mov_b32 s9, 589824\n"
  "buffer_load_dwordx4 v240, s[40:43], s9 offen lds\n"
  "s_add_u32 m0, s8, 7168\n"
  "s_mov_b32 s9, 622592\n"
  "buffer_load_dwordx4 v240, s[40:43], s9 offen lds\n"
  "s_waitcnt lgkmcnt(0)\n"
  "s_add_u32 m0, s8, 8192\n"
  "s_mov_b32 s9, 655360\n"
  "buffer_load_dwordx4 v240, s[40:43], s9 offen lds\n"
  "s_add_u32 m0, s8, 9216\n"
  "s_mov_b32 s9, 688128\n"
  "buffer_load_dwordx4 v240, s[40:43], s9 offen lds\n"
  "s_waitcnt vmcnt(9)\n"
  "v_max3_u32 v98, v94, v95, v96\n"
  "v_max_u32 v98, v98, v97\n"
  "v_cmp_eq_u32 vcc, -1, v98\n"
  "s_cmp_eq_u64 vcc, 0\n"
  "s_cbranch_scc0 .Lfb20_%=\n"
  ".Lbk20_%=:\n"
  "v_pk_add_f32 v[94:95], v[94:95], v[96:97]\n"
  "v_add_u32 v252, 131072, v239\n"
  "s_nop 0\n"
  "v_mfma_f32_16x16x4_f32 v[100:103], v35, v94, 0\n"
  "s_add_u32 m0, s8, 10240\n"
  "s_mov_b32 s9, 720896\n"
  "buffer_load_dwordx4 v240, s[40:43], s9 offen lds\n"
  "v_mfma_f32_16x16x4_f32 v[104:107], v35, v95, 0\n"
  "s_nop 5\n"
  "v_mul_f32 v101, s4, v100\n"
  "v_mul_f32 v103, v101, v101\n"
  "s_nop 0\n"
  "v_mfma_f32_16x16x4_f32 v[108:111], v101, v34, 0\n"
  "v_fma_f32 v105, v104, s4, -v103\n"
  "v_max_f32 v105, 0, v105\n"
  "v_add_f32 v102, s5, v105\n"
  "v_rsq_f32 v102, v102\n"
  "s_add_u32 m0, s8, 11264\n"
  "s_mov_b32 s9, 753664\n"
  "buffer_load_dwordx4 v240, s[40:43], s9 offen lds\n"
  "v_mul_f32 v105, v105, v102\n"
  "v_fma_f32 v103, v105, v102, s5\n"
  "v_rsq_f32 v103, v103\n"
  "v_pk_add_f32 v[56:57], v[56:57], v[108:109] neg_lo:[0,1] neg_hi:[0,1]\n"
  "v_mul_f32 v102, v102, v103\n"
  "v_pk_add_f32 v[58:59], v[58:59], v[110:111] neg_lo:[0,1] neg_hi:[0,1]\n"
  "v_pk_add_f32 v[60:61], v[60:61], v[108:109] neg_lo:[0,1] neg_hi:[0,1]\n"
  "s_nop 0\n"
  "v_mfma_f32_16x16x4_f32 v[72:75], v102, v34, 0\n"
  "v_pk_add_f32 v[62:63], v[62:63], v[110:111] neg_lo:[0,1] neg_hi:[0,1]\n"
  "v_pk_add_f32 v[64:65], v[64:65], v[108:109] neg_lo:[0,1] neg_hi:[0,1]\n"
  "v_pk_add_f32 v[66:67], v[66:67], v[110:111] neg_lo:[0,1] neg_hi:[0,1]\n"
  "v_pk_add_f32 v[68:69], v[68:69], v[108:109] neg_lo:[0,1] neg_hi:[0,1]\n"
  "v_pk_add_f32 v[70:71], v[70:71], v[110:111] neg_lo:[0,1] neg_hi:[0,1]\n"
  "s_nop 4\n"
  "v_pk_mul_f32 v[56:57], v[56:57], v[72:73]\n"
  "v_pk_mul_f32 v[58:59], v[58:59], v[74:75]\n"
  "v_pk_mul_f32 v[60:61], v[60:61], v[72:73]\n"
  "v_pk_mul_f32 v[62:63], v[62:63], v[74:75]\n"
  "v_pk_mul_f32 v[64:65], v[64:65], v[72:73]\n"
  "v_pk_mul_f32 v[66:67], v[66:67], v[74:75]\n"
  "v_pk_mul_f32 v[68:69], v[68:69], v[72:73]\n"
  "v_pk_mul_f32 v[70:71], v[70:71], v[74:75]\n"
  "v_cvt_pk_bf16_f32 v88, v56, v57\n"
  "v_cvt_pk_bf16_f32 v89, v58, v59\n"
  "v_cvt_pk_bf16_f32 v90, v60, v61\n"
  "v_cvt_pk_bf16_f32 v91, v62, v63\n"
  "v_cvt_pk_bf16_f32 v92, v64, v65\n"
  "v_cvt_pk_bf16_f32 v93, v66, v67\n"
  "v_cvt_pk_bf16_f32 v94, v68, v69\n"
  "v_cvt_pk_bf16_f32 v95, v70, v71\n"
  "v_permlane16_swap_b32 v88, v90\n"
  "v_permlane16_swap_b32 v89, v91\n"
  "v_permlane16_swap_b32 v92, v94\n"
  "v_permlane16_swap_b32 v93, v95\n"
  "buffer_store_dwordx4 v[88:91], v245, s[20:23], 0 offen sc1\n"
  "buffer_store_dwordx4 v[92:95], v245, s[20:23], s52 offen sc1\n"
  "s_waitcnt vmcnt(0)\n"
  "s_barrier\n"
  "buffer_store_dwordx2 v[36:37], v248, s[20:23], s12 offen sc1\n"
  "s_sleep 24\n"
  "buffer_load_dwordx2 v[206:207], v246, s[20:23], 0 offen sc1\n"
  "s_waitcnt vmcnt(0)\n"
  "v_cmp_ne_u64 vcc, s[56:57], v[206:207]\n"
  "s_cmp_eq_u64 vcc, 0\n"
  "s_cbranch_scc0 .Lfb21_%=\n"
  ".Lbk21_%=:\n"
  "buffer_load_dwordx4 v[112:115], v238, s[20:23], 0 offen sc1\n"
  "buffer_load_dwordx4 v[116:119], v239, s[20:23], 0 offen sc1\n"
  "buffer_load_dwordx4 v[120:123], v238, s[20:23], 0 offen offset:2048 sc1\n"
  "buffer_load_dwordx4 v[124:127], v239, s[20:23], 0 offen offset:2048 sc1\n"
  "buffer_load_dwordx4 v[128:131], v238, s[20:23], s72 offen sc1\n"
  "buffer_load_dwordx4 v[132:135], v239, s[20:23], s72 offen sc1\n"
  "buffer_load_dwordx4 v[136:139], v238, s[20:23], s72 offen offset:2048 sc1\n"
  "buffer_load_dwordx4 v[140:143], v239, s[20:23], s72 offen offset:2048 sc1\n"
  "buffer_load_dwordx4 v[144:147], v238, s[20:23], s73 offen sc1\n"
  "buffer_load_dwordx4 v[148:151], v239, s[20:23], s73 offen sc1\n"
  "buffer_load_dwordx4 v[152:155], v238, s[20:23], s73 offen offset:2048 sc1\n"
  "buffer_load_dwordx4 v[156:159], v239, s[20:23], s73 offen offset:2048 sc1\n"
  "buffer_load_dwordx4 v[160:163], v238, s[20:23], s74 offen sc1\n"
  "s_waitcnt vmcnt(12)\n"
  "v_mfma_f32_16x16x32_bf16 v[48:51], a[0:3], v[112:115], v[48:51]\n"
  "v_mfma_f32_16x16x32_bf16 v[52:55], a[4:7], v[112:115], v[52:55]\n"
  "v_mfma_f32_16x16x32_bf16 v[64:67], a[8:11], v[112:115], 0\n"
  "buffer_load_dwordx4 v[164:167], v239, s[20:23], s74 offen sc1\n"
  "v_mfma_f32_16x16x32_bf16 v[68:71], a[12:15], v[112:115], 0\n"
  "s_waitcnt vmcnt(12)\n"
  "v_mfma_f32_16x16x32_bf16 v[56:59], a[0:3], v[116:119], 0\n"
  "v_mfma_f32_16x16x32_bf16 v[60:63], a[4:7], v[116:119], 0\n"
  "v_mfma_f32_16x16x32_bf16 v[72:75], a[8:11], v[116:119], 0\n"
  "buffer_load_dwordx4 v[168:171], v238, s[20:23], s74 offen offset:2048 sc1\n"
  "v_mfma_f32_16x16x32_bf16 v[76:79], a[12:15], v[116:119], 0\n"
  "s_waitcnt vmcnt(12)\n"
  "v_mfma_f32_16x16x32_bf16 v[48:51], a[16:19], v[120:123], v[48:51]\n"
  "v_mfma_f32_16x16x32_bf16 v[52:55], a[20:23], v[120:123], v[52:55]\n"
  "buffer_load_dwordx4 v[172:175], v239, s[20:23], s74 offen offset:2048 sc1\n"
  "v_mfma_f32_16x16x32_bf16 v[64:67], a[24:27], v[120:123], v[64:67]\n"
  "v_mfma_f32_16x16x32_bf16 v[68:71], a[28:31], v[120:123], v[68:71]\n"
  "s_waitcnt vmcnt(12)\n"
  "v_mfma_f32_16x16x32_bf16 v[56:59], a[16:19], v[124:127], v[56:59]\n"
  "v_mfma_f32_16x16x32_bf16 v[60:63], a[20:23], v[124:127], v[60:63]\n"
  "buffer_load_dwordx4 v[216:219], v238, s[20:23], s75 offen sc1\n"
  "v_mfma_f32_16x16x32_bf16 v[72:75], a[24:27], v[124:127], v[72:75]\n"
  "v_mfma_f32_16x16x32_bf16 v[76:79], a[28:31], v[124:127], v[76:79]\n"
  "s_waitcnt vmcnt(12)\n"
  "v_mfma_f32_16x16x32_bf16 v[48:51], a[32:35], v[128:131], v[48:51]\n"
  "v_mfma_f32_16x16x32_bf16 v[52:55], a[36:39], v[128:131], v[52:55]\n"
  "buffer_load_dwordx4 v[220:223], v239, s[20:23], s75 offen sc1\n"
  "v_mfma_f32_16x16x32_bf16 v[64:67], a[40:43], v[128:131], v[64:67]\n"
  "v_mfma_f32_16x16x32_bf16 v[68:71], a[44:47], v[128:131], v[68:71]\n"
  "s_waitcnt vmcnt(12)\n"
  "v_mfma_f32_16x16x32_bf16 v[56:59], a[32:35], v[132:135], v[56:59]\n"
  "v_mfma_f32_16x16x32_bf16 v[60:63], a[36:39], v[132:135], v[60:63]\n"
  "buffer_load_dwordx4 v[224:227], v238, s[20:23], s75 offen offset:2048 sc1\n"
  "v_mfma_f32_16x16x32_bf16 v[72:75], a[40:43], v[132:135], v[72:75]\n"
  "v_mfma_f32_16x16x32_bf16 v[76:79], a[44:47], v[132:135], v[76:79]\n"
  "s_waitcnt vmcnt(12)\n"
  "v_mfma_f32_16x16x32_bf16 v[48:51], a[48:51], v[136:139], v[48:51]\n"
  "buffer_load_dwordx4 v[228:231], v239, s[20:23], s75 offen offset:2048 sc1\n"
  "v_mfma_f32_16x16x32_bf16 v[52:55], a[52:55], v[136:139], v[52:55]\n"
  "v_mfma_f32_16x16x32_bf16 v[64:67], a[56:59], v[136:139], v[64:67]\n"
  "v_mfma_f32_16x16x32_bf16 v[68:71], a[60:63], v[136:139], v[68:71]\n"
  "s_waitcnt vmcnt(12)\n"
  "v_mfma_f32_16x16x32_bf16 v[56:59], a[48:51], v[140:143], v[56:59]\n"
  "buffer_load_dwordx4 v[112:115], v238, s[20:23], s76 offen sc1\n"
  "v_mfma_f32_16x16x32_bf16 v[60:63], a[52:55], v[140:143], v[60:63]\n"
  "v_mfma_f32_16x16x32_bf16 v[72:75], a[56:59], v[140:143], v[72:75]\n"
  "v_mfma_f32_16x16x32_bf16 v[76:79], a[60:63], v[140:143], v[76:79]\n"
  "s_waitcnt vmcnt(12)\n"
  "v_mfma_f32_16x16x32_bf16 v[48:51], a[64:67], v[144:147], v[48:51]\n"
  "buffer_load_dwordx4 v[116:119], v239, s[20:23], s76 offen sc1\n"
  "v_mfma_f32_16x16x32_bf16 v[52:55], a[68:71], v[144:147], v[52:55]\n"
  "v_mfma_f32_16x16x32_bf16 v[64:67], a[72:75], v[144:147], v[64:67]\n"
  "v_mfma_f32_16x16x32_bf16 v[68:71], a[76:79], v[144:147], v[68:71]\n"
  "s_waitcnt vmcnt(12)\n"
  "v_mfma_f32_16x16x32_bf16 v[56:59], a[64:67], v[148:151], v[56:59]\n"
  "buffer_load_dwordx4 v[120:123], v238, s[20:23], s76 offen offset:2048 sc1\n"
  "v_mfma_f32_16x16x32_bf16 v[60:63], a[68:71], v[148:151], v[60:63]\n"
  "v_mfma_f32_16x16x32_bf16 v[72:75], a[72:75], v[148:151], v[72:75]\n"
  "v_mfma_f32_16x16x32_bf16 v[76:79], a[76:79], v[148:151], v[76:79]\n"
  "s_waitcnt vmcnt(12)\n"
  "v_mfma_f32_16x16x32_bf16 v[48:51], a[80:83], v[152:155], v[48:51]\n"
  "buffer_load_dwordx4 v[124:127], v239, s[20:23], s76 offen offset:2048 sc1\n"
  "v_mfma_f32_16x16x32_bf16 v[52:55], a[84:87], v[152:155], v[52:55]\n"
  "v_mfma_f32_16x16x32_bf16 v[64:67], a[88:91], v[152:155], v[64:67]\n"
  "v_mfma_f32_16x16x32_bf16 v[68:71], a[92:95], v[152:155], v[68:71]\n"
  "buffer_load_dwordx4 v[128:131], v238, s[20:23], s77 offen sc1\n"
  "s_waitcnt vmcnt(13)\n"
  "v_mfma_f32_16x16x32_bf16 v[56:59], a[80:83], v[156:159], v[56:59]\n"
  "v_mfma_f32_16x16x32_bf16 v[60:63], a[84:87], v[156:159], v[60:63]\n"
  "v_mfma_f32_16x16x32_bf16 v[72:75], a[88:91], v[156:159], v[72:75]\n"
  "v_mfma_f32_16x16x32_bf16 v[76:79], a[92:95], v[156:159], v[76:79]\n"
  "buffer_load_dwordx4 v[132:135], v239, s[20:23], s77 offen sc1\n"
  "s_waitcnt vmcnt(13)\n"
  "v_mfma_f32_16x16x32_bf16 v[48:51], a[96:99], v[160:163], v[48:51]\n"
  "v_mfma_f32_16x16x32_bf16 v[52:55], a[100:103], v[160:163], v[52:55]\n"
  "v_mfma_f32_16x16x32_bf16 v[64:67], a[104:107], v[160:163], v[64:67]\n"
  "v_mfma_f32_16x16x32_bf16 v[68:71], a[108:111], v[160:163], v[68:71]\n"
  "buffer_load_dwordx4 v[136:139], v238, s[20:23], s77 offen offset:2048 sc1\n"
  "s_waitcnt vmcnt(13)\n"
  "v_mfma_f32_16x16x32_bf16 v[56:59], a[96:99], v[164:167], v[56:59]\n"
  "v_mfma_f32_16x16x32_bf16 v[60:63], a[100:103], v[164:167], v[60:63]\n"
  "v_mfma_f32_16x16x32_bf16 v[72:75], a[104:107], v[164:167], v[72:75]\n"
  "v_mfma_f32_16x16x32_bf16 v[76:79], a[108:111], v[164:167], v[76:79]\n"
  "buffer_load_dwordx4 v[140:143], v239, s[20:23], s77 offen offset:2048 sc1\n"
  "s_waitcnt vmcnt(13)\n"
  "v_mfma_f32_16x16x32_bf16 v[48:51], a[112:115], v[168:171], v[48:51]\n"
  "v_mfma_f32_16x16x32_bf16 v[52:55], a[116:119], v[168:171], v[52:55]\n"
  "v_mfma_f32_16x16x32_bf16 v[64:67], a[120:123], v[168:171], v[64:67]\n"
  "buffer_load_dwordx4 v[144:147], v238, s[20:23], s78 offen sc1\n"
  "v_mfma_f32_16x16x32_bf16 v[68:71], a[124:127], v[168:171], v[68:71]\n"
  "s_waitcnt vmcnt(13)\n"
  "v_mfma_f32_16x16x32_bf16 v[56:59], a[112:115], v[172:175], v[56:59]\n"
  "v_mfma_f32_16x16x32_bf16 v[60:63], a[116:119], v[172:175], v[60:63]\n"
  "v_mfma_f32_16x16x32_bf16 v[72:75], a[120:123], v[172:175], v[72:75]\n"
  "buffer_load_dwordx4 v[148:151], v239, s[20:23], s78 offen sc1\n"
  "v_mfma_f32_16x16x32_bf16 v[76:79], a[124:127], v[172:175], v[76:79]\n"
  "s_waitcnt vmcnt(13)\n"
  "v_mfma_f32_16x16x32_bf16 v[48:51], a[128:131], v[216:219], v[48:51]\n"
  "v_mfma_f32_16x16x32_bf16 v[52:55], a[132:135], v[216:219], v[52:55]\n"
  "v_mfma_f32_16x16x32_bf16 v[64:67], a[136:139], v[216:219], v[64:67]\n"
  "buffer_load_dwordx4 v[152:155], v238, s[20:23], s78 offen offset:2048 sc1\n"
  "v_mfma_f32_16x16x32_bf16 v[68:71], a[140:143], v[216:219], v[68:71]\n"
  "s_waitcnt vmcnt(13)\n"
  "v_mfma_f32_16x16x32_bf16 v[56:59], a[128:131], v[220:223], v[56:59]\n"
  "v_mfma_f32_16x16x32_bf16 v[60:63], a[132:135], v[220:223], v[60:63]\n"
  "v_mfma_f32_16x16x32_bf16 v[72:75], a[136:139], v[220:223], v[72:75]\n"
  "buffer_load_dwordx4 v[156:159], v239, s[20:23], s78 offen offset:2048 sc1\n"
  "v_mfma_f32_16x16x32_bf16 v[76:79], a[140:143], v[220:223], v[76:79]\n"
  "s_waitcnt vmcnt(13)\n"
  "v_mfma_f32_16x16x32_bf16 v[48:51], a[144:147], v[224:227], v[48:51]\n"
  "v_mfma_f32_16x16x32_bf16 v[52:55], a[148:151], v[224:227], v[52:55]\n"
  "buffer_load_dwordx4 v[160:163], v251, s[20:23], 0 offen sc1\n"
  "v_mfma_f32_16x16x32_bf16 v[64:67], a[152:155], v[224:227], v[64:67]\n"
  "v_mfma_f32_16x16x32_bf16 v[68:71], a[156:159], v[224:227], v[68:71]\n"
  "s_waitcnt vmcnt(13)\n"
  "v_mfma_f32_16x16x32_bf16 v[56:59], a[144:147], v[228:231], v[56:59]\n"
  "v_mfma_f32_16x16x32_bf16 v[60:63], a[148:151], v[228:231], v[60:63]\n"
  "buffer_load_dwordx4 v[164:167], v252, s[20:23], 0 offen sc1\n"
  "v_mfma_f32_16x16x32_bf16 v[72:75], a[152:155], v[228:231], v[72:75]\n"
  "v_mfma_f32_16x16x32_bf16 v[76:79], a[156:159], v[228:231], v[76:79]\n"
  "s_waitcnt vmcnt(13)\n"
  "v_mfma_f32_16x16x32_bf16 v[48:51], a[160:163], v[112:115], v[48:51]\n"
  "v_mfma_f32_16x16x32_bf16 v[52:55], a[164:167], v[112:115], v[52:55]\n"
  "buffer_load_dwordx4 v[168:171], v251, s[20:23], 0 offen offset:2048 sc1\n"
  "v_mfma_f32_16x16x32_bf16 v[64:67], a[168:171], v[112:115], v[64:67]\n"
  "v_mfma_f32_16x16x32_bf16 v[68:71], a[172:175], v[112:115], v[68:71]\n"
  "s_waitcnt vmcnt(13)\n"
  "v_mfma_f32_16x16x32_bf16 v[56:59], a[160:163], v[116:119], v[56:59]\n"
  "v_mfma_f32_16x16x32_bf16 v[60:63], a[164:167], v[116:119], v[60:63]\n"
  "buffer_load_dwordx4 v[172:175], v252, s[20:23], 0 offen offset:2048 sc1\n"
  "v_mfma_f32_16x16x32_bf16 v[72:75], a[168:171], v[116:119], v[72:75]\n"
  "v_mfma_f32_16x16x32_bf16 v[76:79], a[172:175], v[116:119], v[76:79]\n"
  "s_waitcnt vmcnt(13)\n"
  "v_mfma_f32_16x16x32_bf16 v[48:51], a[176:179], v[120:123], v[48:51]\n"
  "buffer_load_dwordx4 v[216:219], v251, s[20:23], s72 offen sc1\n"
  "v_mfma_f32_16x16x32_bf16 v[52:55], a[180:183], v[120:123], v[52:55]\n"
  "v_mfma_f32_16x16x32_bf16 v[64:67], a[184:187], v[120:123], v[64:67]\n"
  "v_mfma_f32_16x16x32_bf16 v[68:71], a[188:191], v[120:123], v[68:71]\n"
  "s_waitcnt vmcnt(13)\n"
  "v_mfma_f32_16x16x32_bf16 v[56:59], a[176:179], v[124:127], v[56:59]\n"
  "buffer_load_dwordx4 v[220:223], v252, s[20:23], s72 offen sc1\n"
  "v_mfma_f32_16x16x32_bf16 v[60:63], a[180:183], v[124:127], v[60:63]\n"
  "v_mfma_f32_16x16x32_bf16 v[72:75], a[184:187], v[124:127], v[72:75]\n"
  "v_mfma_f32_16x16x32_bf16 v[76:79], a[188:191], v[124:127], v[76:79]\n"
  "s_waitcnt vmcnt(13)\n"
  "v_mfma_f32_16x16x32_bf16 v[48:51], a[192:195], v[128:131], v[48:51]\n"
  "buffer_load_dwordx4 v[224:227], v251, s[20:23], s72 offen offset:2048 sc1\n"
  "v_mfma_f32_16x16x32_bf16 v[52:55], a[196:199], v[128:131], v[52:55]\n"
  "v_mfma_f32_16x16x32_bf16 v[64:67], a[200:203], v[128:131], v[64:67]\n"
  "v_mfma_f32_16x16x32_bf16 v[68:71], a[204:207], v[128:131], v[68:71]\n"
  "s_waitcnt vmcnt(13)\n"
  "v_mfma_f32_16x16x32_bf16 v[56:59], a[192:195], v[132:135], v[56:59]\n"
  "buffer_load_dwordx4 v[228:231], v252, s[20:23], s72 offen offset:2048 sc1\n"
  "v_mfma_f32_16x16x32_bf16 v[60:63], a[196:199], v[132:135], v[60:63]\n"
  "v_mfma_f32_16x16x32_bf16 v[72:75], a[200:203], v[132:135], v[72:75]\n"
  "v_mfma_f32_16x16x32_bf16 v[76:79], a[204:207], v[132:135], v[76:79]\n"
  "s_waitcnt vmcnt(13)\n"
  "v_mfma_f32_16x16x32_bf16 v[48:51], a[208:211], v[136:139], v[48:51]\n"
  "buffer_load_dwordx4 v[112:115], v251, s[20:23], s73 offen sc1\n"
  "v_mfma_f32_16x16x32_bf16 v[52:55], a[212:215], v[136:139], v[52:55]\n"
  "v_mfma_f32_16x16x32_bf16 v[64:67], a[216:219], v[136:139], v[64:67]\n"
  "v_mfma_f32_16x16x32_bf16 v[68:71], a[220:223], v[136:139], v[68:71]\n"
  "buffer_load_dwordx4 v[116:119], v252, s[20:23], s73 offen sc1\n"
  "s_waitcnt vmcnt(14)\n"
  "v_mfma_f32_16x16x32_bf16 v[56:59], a[208:211], v[140:143], v[56:59]\n"
  "v_mfma_f32_16x16x32_bf16 v[60:63], a[212:215], v[140:143], v[60:63]\n"
  "v_mfma_f32_16x16x32_bf16 v[72:75], a[216:219], v[140:143], v[72:75]\n"
  "v_mfma_f32_16x16x32_bf16 v[76:79], a[220:223], v[140:143], v[76:79]\n"
  "buffer_load_dwordx4 v[120:123], v251, s[20:23], s73 offen offset:2048 sc1\n"
  "s_waitcnt vmcnt(14)\n"
  "v_mfma_f32_16x16x32_bf16 v[48:51], a[224:227], v[144:147], v[48:51]\n"
  "v_mfma_f32_16x16x32_bf16 v[52:55], a[228:231], v[144:147], v[52:55]\n"
  "v_mfma_f32_16x16x32_bf16 v[64:67], a[232:235], v[144:147], v[64:67]\n"
  "v_mfma_f32_16x16x32_bf16 v[68:71], a[236:239], v[144:147], v[68:71]\n"
  "buffer_load_dwordx4 v[124:127], v252, s[20:23], s73 offen offset:2048 sc1\n"
  "s_waitcnt vmcnt(14)\n"
  "v_mfma_f32_16x16x32_bf16 v[56:59], a[224:227], v[148:151], v[56:59]\n"
  "v_mfma_f32_16x16x32_bf16 v[60:63], a[228:231], v[148:151], v[60:63]\n"
  "v_mfma_f32_16x16x32_bf16 v[72:75], a[232:235], v[148:151], v[72:75]\n"
  "v_mfma_f32_16x16x32_bf16 v[76:79], a[236:239], v[148:151], v[76:79]\n"
  "buffer_load_dwordx4 v[128:131], v251, s[20:23], s74 offen sc1\n"
  "s_waitcnt vmcnt(14)\n"
  "v_mfma_f32_16x16x32_bf16 v[48:51], a[240:243], v[152:155], v[48:51]\n"
  "v_mfma_f32_16x16x32_bf16 v[52:55], a[244:247], v[152:155], v[52:55]\n"
  "v_mfma_f32_16x16x32_bf16 v[64:67], a[248:251], v[152:155], v[64:67]\n"
  "buffer_load_dwordx4 v[132:135], v252, s[20:23], s74 offen sc1\n"
  "v_mfma_f32_16x16x32_bf16 v[68:71], a[252:255], v[152:155], v[68:71]\n"
  "s_waitcnt vmcnt(14)\n"
  "v_mfma_f32_16x16x32_bf16 v[56:59], a[240:243], v[156:159], v[56:59]\n"
  "s_nop 3\n"
  "ds_write_b64 v243, v[64:65] offset:0\n"
  "v_mfma_f32_16x16x32_bf16 v[60:63], a[244:247], v[156:159], v[60:63]\n"
  "ds_write_b64 v243, v[66:67] offset:8\n"
  "v_mfma_f32_16x16x32_bf16 v[72:75], a[248:251], v[156:159], v[72:75]\n"
  "buffer_load_dwordx4 v[136:139], v251, s[20:23], s74 offen offset:2048 sc1\n"
  "v_mfma_f32_16x16x32_bf16 v[76:79], a[252:255], v[156:159], v[76:79]\n"
  "ds_write_b64 v242, v[56:57] offset:0\n"
  "s_waitcnt vmcnt(14)\n"
  "v_mfma_f32_16x16x32_bf16 v[80:83], a[0:3], v[160:163], v[80:83]\n"
  "ds_write_b64 v242, v[58:59] offset:8\n"
  "v_mfma_f32_16x16x32_bf16 v[84:87], a[4:7], v[160:163], v[84:87]\n"
  "ds_write_b64 v242, v[60:61] offset:1024\n"
  "v_mfma_f32_16x16x32_bf16 v[96:99], a[8:11], v[160:163], 0\n"
  "buffer_load_dwordx4 v[140:143], v252, s[20:23], s74 offen offset:2048 sc1\n"
  "v_mfma_f32_16x16x32_bf16 v[100:103], a[12:15], v[160:163], 0\n"
  "ds_write_b64 v242, v[62:63] offset:1032\n"
  "s_waitcnt vmcnt(14) lgkmcnt(5)\n"
  "v_mfma_f32_16x16x32_bf16 v[88:91], a[0:3], v[164:167], 0\n"
  "ds_write_b64 v243, v[68:69] offset:1024\n"
  "v_mfma_f32_16x16x32_bf16 v[92:95], a[4:7], v[164:167], 0\n"
  "buffer_load_dwordx4 v[144:147], v251, s[20:23], s75 offen sc1\n"
  "ds_write_b64 v243, v[70:71] offset:1032\n"
  "v_mfma_f32_16x16x32_bf16 v[104:107], a[8:11], v[164:167], 0\n"
  "ds_read_b64_tr_b16 a[0:1], v234 offset:12288\n"
  "ds_write_b64 v244, v[72:73] offset:0\n"
  "v_mfma_f32_16x16x32_bf16 v[108:111], a[12:15], v[164:167], 0\n"
  "ds_read_b64_tr_b16 a[2:3], v234 offset:14336\n"
  "ds_write_b64 v244, v[74:75] offset:8\n"
  "s_waitcnt vmcnt(14) lgkmcnt(7)\n"
  "v_mfma_f32_16x16x32_bf16 v[80:83], a[16:19], v[168:171], v[80:83]\n"
  "ds_write_b64 v244, v[76:77] offset:1024\n"
  "v_mfma_f32_16x16x32_bf16 v[84:87], a[20:23], v[168:171], v[84:87]\n"
  "buffer_load_dwordx4 v[148:151], v252, s[20:23], s75 offen sc1\n"
  "ds_write_b64 v244, v[78:79] offset:1032\n"
  "v_mfma_f32_16x16x32_bf16 v[96:99], a[24:27], v[168:171], v[96:99]\n"
  "ds_read_b64_tr_b16 a[4:5], v235 offset:12288\n"
  "v_mfma_f32_16x16x32_bf16 v[100:103], a[28:31], v[168:171], v[100:103]\n"
  "ds_read_b64_tr_b16 a[6:7], v235 offset:14336\n"
  "s_waitcnt vmcnt(14) lgkmcnt(8)\n"
  "v_mfma_f32_16x16x32_bf16 v[88:91], a[16:19], v[172:175], v[88:91]\n"
  "buffer_load_dwordx4 v[152:155], v251, s[20:23], s75 offen offset:2048 sc1\n"
  "ds_read_b64_tr_b16 a[8:9], v236 offset:12288\n"
  "v_mfma_f32_16x16x32_bf16 v[92:95], a[20:23], v[172:175], v[92:95]\n"
  "ds_read_b64_tr_b16 a[10:11], v236 offset:14336\n"
  "v_mfma_f32_16x16x32_bf16 v[104:107], a[24:27], v[172:175], v[104:107]\n"
  "ds_read_b64_tr_b16 a[12:13], v237 offset:12288\n"
  "s_waitcnt lgkmcnt(5)\n"
  "s_barrier\n"
  "v_mfma_f32_16x16x32_bf16 v[108:111], a[28:31], v[172:175], v[108:111]\n"
  "ds_read_b128 v[56:59], v241 offset:0\n"
  "ds_read_b128 v[64:67], v241 offset:2048\n"
  "s_waitcnt vmcnt(14)\n"
  "v_mfma_f32_16x16x32_bf16 v[80:83], a[32:35], v[216:219], v[80:83]\n"
  "ds_read_b128 v[72:75], v241 offset:4096\n"
  "buffer_load_dwordx4 v[156:159], v252, s[20:23], s75 offen offset:2048 sc1\n"
  "v_mfma_f32_16x16x32_bf16 v[84:87], a[36:39], v[216:219], v[84:87]\n"
  "ds_read_b128 v[60:63], v241 offset:1024\n"
  "v_mfma_f32_16x16x32_bf16 v[96:99], a[40:43], v[216:219], v[96:99]\n"
  "ds_read_b128 v[68:71], v241 offset:3072\n"
  "v_mfma_f32_16x16x32_bf16 v[100:103], a[44:47], v[216:219], v[100:103]\n"
  "ds_read_b128 v[76:79], v241 offset:5120\n"
  "buffer_load_dwordx4 v[160:163], v251, s[20:23], s76 offen sc1\n"
  "s_waitcnt vmcnt(15) lgkmcnt(9)\n"
  "v_mfma_f32_16x16x32_bf16 v[88:91], a[32:35], v[220:223], v[88:91]\n"
  "ds_read_b64_tr_b16 a[14:15], v237 offset:14336\n"
  "v_mfma_f32_16x16x32_bf16 v[92:95], a[36:39], v[220:223], v[92:95]\n"
  "s_waitcnt lgkmcnt(6)\n"
  "v_add_f32 v48, v48, v56\n"
  "v_mfma_f32_16x16x32_bf16 v[104:107], a[40:43], v[220:223], v[104:107]\n"
  "v_add_f32 v49, v49, v57\n"
  "v_add_f32 v50, v50, v58\n"
  "v_mfma_f32_16x16x32_bf16 v[108:111], a[44:47], v[220:223], v[108:111]\n"
  "s_waitcnt lgkmcnt(4)\n"
  "v_add_f32 v64, v64, v72\n"
  "s_waitcnt vmcnt(14)\n"
  "v_mfma_f32_16x16x32_bf16 v[80:83], a[48:51], v[224:227], v[80:83]\n"
  "v_add_f32 v65, v65, v73\n"
  "v_add_f32 v66, v66, v74\n"
  "v_mfma_f32_16x16x32_bf16 v[84:87], a[52:55], v[224:227], v[84:87]\n"
  "v_add_f32 v51, v51, v59\n"
  "v_add_f32 v67, v67, v75\n"
  "v_mfma_f32_16x16x32_bf16 v[96:99], a[56:59], v[224:227], v[96:99]\n"
  "buffer_load_dwordx4 v[164:167], v252, s[20:23], s76 offen sc1\n"
  "s_waitcnt lgkmcnt(2)\n"
  "v_add_f32 v52, v52, v60\n"
  "v_mfma_f32_16x16x32_bf16 v[100:103], a[60:63], v[224:227], v[100:103]\n"
  "s_waitcnt lgkmcnt(1)\n"
  "v_add_f32 v68, v68, v76\n"
  "s_waitcnt vmcnt(14)\n"
  "v_mfma_f32_16x16x32_bf16 v[88:91], a[48:51], v[228:231], v[88:91]\n"
  "v_add_f32 v53, v53, v61\n"
  "v_add_f32 v69, v69, v77\n"
  "v_mfma_f32_16x16x32_bf16 v[92:95], a[52:55], v[228:231], v[92:95]\n"
  "v_add_f32 v54, v54, v62\n"
  "v_add_f32 v70, v70, v78\n"
  "v_mfma_f32_16x16x32_bf16 v[104:107], a[56:59], v[228:231], v[104:107]\n"
  "buffer_load_dwordx4 v[168:171], v251, s[20:23], s76 offen offset:2048 sc1\n"
  "v_add_f32 v55, v55, v63\n"
  "v_mfma_f32_16x16x32_bf16 v[108:111], a[60:63], v[228:231], v[108:111]\n"
  "v_add_f32 v71, v71, v79\n"
  "v_add_f32 v48, v48, v64\n"
  "s_waitcnt vmcnt(14) lgkmcnt(0)\n"
  "v_mfma_f32_16x16x32_bf16 v[80:83], a[64:67], v[112:115], v[80:83]\n"
  "v_add_f32 v49, v49, v65\n"
  "v_add_f32 v50, v50, v66\n"
  "v_mfma_f32_16x16x32_bf16 v[84:87], a[68:71], v[112:115], v[84:87]\n"
  "v_add_f32 v51, v51, v67\n"
  "buffer_load_dwordx4 v[172:175], v252, s[20:23], s76 offen offset:2048 sc1\n"
  "v_mfma_f32_16x16x32_bf16 v[96:99], a[72:75], v[112:115], v[96:99]\n"
  "v_add_f32 v52, v52, v68\n"
  "v_add_f32 v53, v53, v69\n"
  "v_mfma_f32_16x16x32_bf16 v[100:103], a[76:79], v[112:115], v[100:103]\n"
  "v_add_f32 v54, v54, v70\n"
  "v_add_f32 v55, v55, v71\n"
  "s_waitcnt vmcnt(14)\n"
  "v_mfma_f32_16x16x32_bf16 v[88:91], a[64:67], v[116:119], v[88:91]\n"
  "v_mul_f32 v56, v48, v48\n"
  "v_mul_f32 v57, v49, v49\n"
  "v_mfma_f32_16x16x32_bf16 v[92:95], a[68:71], v[116:119], v[92:95]\n"
  "buffer_load_dwordx4 v[216:219], v251, s[20:23], s77 offen sc1\n"
  "v_mul_f32 v58, v50, v50\n"
  "v_mfma_f32_16x16x32_bf16 v[104:107], a[72:75], v[116:119], v[104:107]\n"
  "v_mul_f32 v59, v51, v51\n"
  "v_mul_f32 v60, v52, v52\n"
  "v_mfma_f32_16x16x32_bf16 v[108:111], a[76:79], v[116:119], v[108:111]\n"
  "v_mul_f32 v61, v53, v53\n"
  "v_mul_f32 v62, v54, v54\n"
  "s_waitcnt vmcnt(14)\n"
  "v_mfma_f32_16x16x32_bf16 v[80:83], a[80:83], v[120:123], v[80:83]\n"
  "v_mul_f32 v63, v55, v55\n"
  "v_fma_f32 v56, v56, s7, v205\n"
  "v_mfma_f32_16x16x32_bf16 v[84:87], a[84:87], v[120:123], v[84:87]\n"
  "buffer_load_dwordx4 v[220:223], v252, s[20:23], s77 offen sc1\n"
  "v_fma_f32 v57, v57, s7, v205\n"
  "v_mfma_f32_16x16x32_bf16 v[96:99], a[88:91], v[120:123], v[96:99]\n"
  "v_fma_f32 v58, v58, s7, v205\n"
  "v_fma_f32 v59, v59, s7, v205\n"
  "v_mfma_f32_16x16x32_bf16 v[100:103], a[92:95], v[120:123], v[100:103]\n"
  "v_fma_f32 v60, v60, s7, v205\n"
  "v_fma_f32 v61, v61, s7, v205\n"
  "s_waitcnt vmcnt(14)\n"
  "v_mfma_f32_16x16x32_bf16 v[88:91], a[80:83], v[124:127], v[88:91]\n"
  "v_fma_f32 v62, v62, s7, v205\n"
  "v_fma_f32 v63, v63, s7, v205\n"
  "v_mfma_f32_16x16x32_bf16 v[92:95], a[84:87], v[124:127], v[92:95]\n"
  "buffer_load_dwordx4 v[224:227], v251, s[20:23], s77 offen offset:2048 sc1\n"
  "v_mul_f32 v56, v56, v48\n"
  "v_mfma_f32_16x16x32_bf16 v[104:107], a[88:91], v[124:127], v[104:107]\n"
  "v_mul_f32 v57, v57, v49\n"
  "v_mul_f32 v58, v58, v50\n"
  "v_mfma_f32_16x16x32_bf16 v[108:111], a[92:95], v[124:127], v[108:111]\n"
  "v_mul_f32 v59, v59, v51\n"
  "v_mul_f32 v60, v60, v52\n"
  "s_waitcnt vmcnt(14)\n"
  "v_mfma_f32_16x16x32_bf16 v[80:83], a[96:99], v[128:131], v[80:83]\n"
  "v_mul_f32 v61, v61, v53\n"
  "buffer_load_dwordx4 v[228:231], v252, s[20:23], s77 offen offset:2048 sc1\n"
  "v_mfma_f32_16x16x32_bf16 v[84:87], a[100:103], v[128:131], v[84:87]\n"
  "v_mul_f32 v62, v62, v54\n"
  "v_mul_f32 v63, v63, v55\n"
  "v_mfma_f32_16x16x32_bf16 v[96:99], a[104:107], v[128:131], v[96:99]\n"
  "v_exp_f32 v56, v56\n"
  "v_mfma_f32_16x16x32_bf16 v[100:103], a[108:111], v[128:131], v[100:103]\n"
  "v_exp_f32 v57, v57\n"
  "s_waitcnt vmcnt(14)\n"
  "v_mfma_f32_16x16x32_bf16 v[88:91], a[96:99], v[132:135], v[88:91]\n"
  "buffer_load_dwordx4 v[112:115], v251, s[20:23], s78 offen sc1\n"
  "v_exp_f32 v58, v58\n"
  "v_mfma_f32_16x16x32_bf16 v[92:95], a[100:103], v[132:135], v[92:95]\n"
  "v_exp_f32 v59, v59\n"
  "v_mfma_f32_16x16x32_bf16 v[104:107], a[104:107], v[132:135], v[104:107]\n"
  "v_exp_f32 v60, v60\n"
  "v_mfma_f32_16x16x32_bf16 v[108:111], a[108:111], v[132:135], v[108:111]\n"
  "v_exp_f32 v61, v61\n"
  "s_waitcnt vmcnt(14)\n"
  "v_mfma_f32_16x16x32_bf16 v[80:83], a[112:115], v[136:139], v[80:83]\n"
  "buffer_load_dwordx4 v[116:119], v252, s[20:23], s78 offen sc1\n"
  "v_exp_f32 v62, v62\n"
  "v_mfma_f32_16x16x32_bf16 v[84:87], a[116:119], v[136:139], v[84:87]\n"
  "v_exp_f32 v63, v63\n"
  "v_mfma_f32_16x16x32_bf16 v[96:99], a[120:123], v[136:139], v[96:99]\n"
  "v_add_f32 v56, 1.0, v56\n"
  "v_add_f32 v57, 1.0, v57\n"
  "v_mfma_f32_16x16x32_bf16 v[100:103], a[124:127], v[136:139], v[100:103]\n"
  "v_add_f32 v58, 1.0, v58\n"
  "buffer_load_dwordx4 v[120:123], v251, s[20:23], s78 offen offset:2048 sc1\n"
  "s_waitcnt vmcnt(15)\n"
  "v_mfma_f32_16x16x32_bf16 v[88:91], a[112:115], v[140:143], v[88:91]\n"
  "v_add_f32 v59, 1.0, v59\n"
  "v_add_f32 v60, 1.0, v60\n"
  "v_mfma_f32_16x16x32_bf16 v[92:95], a[116:119], v[140:143], v[92:95]\n"
  "v_add_f32 v61, 1.0, v61\n"
  "v_add_f32 v62, 1.0, v62\n"
  "v_mfma_f32_16x16x32_bf16 v[104:107], a[120:123], v[140:143], v[104:107]\n"
  "v_add_f32 v63, 1.0, v63\n"
  "v_rcp_f32 v56, v56\n"
  "v_mfma_f32_16x16x32_bf16 v[108:111], a[124:127], v[140:143], v[108:111]\n"
  "buffer_load_dwordx4 v[124:127], v252, s[20:23], s78 offen offset:2048 sc1\n"
  "v_rcp_f32 v57, v57\n"
  "s_waitcnt vmcnt(15)\n"
  "v_mfma_f32_16x16x32_bf16 v[80:83], a[128:131], v[144:147], v[80:83]\n"
  "v_rcp_f32 v58, v58\n"
  "v_mfma_f32_16x16x32_bf16 v[84:87], a[132:135], v[144:147], v[84:87]\n"
  "v_rcp_f32 v59, v59\n"
  "v_mfma_f32_16x16x32_bf16 v[96:99], a[136:139], v[144:147], v[96:99]\n"
  "v_rcp_f32 v60, v60\n"
  "v_mfma_f32_16x16x32_bf16 v[100:103], a[140:143], v[144:147], v[100:103]\n"
  "s_add_u32 m0, s8, 12288\n"
  "s_mov_b32 s9, 786432\n"
  "buffer_load_dwordx4 v240, s[40:43], s9 offen lds\n"
  "s_waitcnt vmcnt(15)\n"
  "v_mfma_f32_16x16x32_bf16 v[88:91], a[128:131], v[148:151], v[88:91]\n"
  "v_rcp_f32 v61, v61\n"
  "v_mfma_f32_16x16x32_bf16 v[92:95], a[132:135], v[148:151], v[92:95]\n"
  "v_rcp_f32 v62, v62\n"
  "v_mfma_f32_16x16x32_bf16 v[104:107], a[136:139], v[148:151], v[104:107]\n"
  "v_rcp_f32 v63, v63\n"
  "v_mfma_f32_16x16x32_bf16 v[108:111], a[140:143], v[148:151], v[108:111]\n"
  "s_add_u32 m0, s8, 13312\n"
  "s_mov_b32 s9, 819200\n"
  "buffer_load_dwordx4 v240, s[40:43], s9 offen lds\n"
  "s_waitcnt vmcnt(15)\n"
  "v_mfma_f32_16x16x32_bf16 v[80:83], a[144:147], v[152:155], v[80:83]\n"
  "v_mul_f32 v56, v56, v48\n"
  "v_mul_f32 v57, v57, v49\n"
  "v_mfma_f32_16x16x32_bf16 v[84:87], a[148:151], v[152:155], v[84:87]\n"
  "v_mul_f32 v58, v58, v50\n"
  "v_mul_f32 v59, v59, v51\n"
  "v_mfma_f32_16x16x32_bf16 v[96:99], a[152:155], v[152:155], v[96:99]\n"
  "v_mul_f32 v60, v60, v52\n"
  "v_mul_f32 v61, v61, v53\n"
  "v_mfma_f32_16x16x32_bf16 v[100:103], a[156:159], v[152:155], v[100:103]\n"
  "v_mul_f32 v62, v62, v54\n"
  "v_mul_f32 v63, v63, v55\n"
  "s_waitcnt vmcnt(14)\n"
  "v_mfma_f32_16x16x32_bf16 v[88:91], a[144:147], v[156:159], v[88:91]\n"
  "s_add_u32 m0, s8, 14336\n"
  "s_mov_b32 s9, 851968\n"
  "buffer_load_dwordx4 v240, s[40:43], s9 offen lds\n"
  "v_mfma_f32_16x16x32_bf16 v[92:95], a[148:151], v[156:159], v[92:95]\n"
  "v_cvt_pk_bf16_f32 v64, v56, v57\n"
  "v_cvt_pk_bf16_f32 v65, v58, v59\n"
  "v_mfma_f32_16x16x32_bf16 v[104:107], a[152:155], v[156:159], v[104:107]\n"
  "v_cvt_pk_bf16_f32 v66, v60, v61\n"
  "v_cvt_pk_bf16_f32 v67, v62, v63\n"
  "v_mfma_f32_16x16x32_bf16 v[108:111], a[156:159], v[156:159], v[108:111]\n"
  "buffer_store_dwordx4 v[64:67], v1, s[20:23], s17 offen sc1\n"
  "v_mov_b32 v48, v47\n"
  "s_waitcnt vmcnt(15)\n"
  "v_mfma_f32_16x16x32_bf16 v[80:83], a[160:163], v[160:163], v[80:83]\n"
  "v_mov_b32 v49, v47\n"
  "v_mov_b32 v50, v47\n"
  "v_mfma_f32_16x16x32_bf16 v[84:87], a[164:167], v[160:163], v[84:87]\n"
  "v_mov_b32 v51, v47\n"
  "s_add_u32 m0, s8, 15360\n"
  "s_mov_b32 s9, 884736\n"
  "buffer_load_dwordx4 v240, s[40:43], s9 offen lds\n"
  "v_mfma_f32_16x16x32_bf16 v[96:99], a[168:171], v[160:163], v[96:99]\n"
  "ds_read_b64_tr_b16 a[16:17], v234 offset:16384\n"
  "v_mov_b32 v52, v47\n"
  "v_mfma_f32_16x16x32_bf16 v[100:103], a[172:175], v[160:163], v[100:103]\n"
  "ds_read_b64_tr_b16 a[18:19], v234 offset:18432\n"
  "v_mov_b32 v53, v47\n"
  "s_waitcnt vmcnt(15)\n"
  "v_mfma_f32_16x16x32_bf16 v[88:91], a[160:163], v[164:167], v[88:91]\n"
  "ds_read_b64_tr_b16 a[20:21], v235 offset:16384\n"
  "v_mov_b32 v54, v47\n"
  "v_mfma_f32_16x16x32_bf16 v[92:95], a[164:167], v[164:167], v[92:95]\n"
  "ds_read_b64_tr_b16 a[22:23], v235 offset:18432\n"
  "v_mov_b32 v55, v47\n"
  "v_mfma_f32_16x16x32_bf16 v[104:107], a[168:171], v[164:167], v[104:107]\n"
  "ds_read_b64_tr_b16 a[24:25], v236 offset:16384\n"
  "v_mfma_f32_16x16x32_bf16 v[108:111], a[172:175], v[164:167], v[108:111]\n"
  "ds_read_b64_tr_b16 a[26:27], v236 offset:18432\n"
  "s_waitcnt vmcnt(14)\n"
  "v_mfma_f32_16x16x32_bf16 v[80:83], a[176:179], v[168:171], v[80:83]\n"
  "ds_read_b64_tr_b16 a[28:29], v237 offset:16384\n"
  "v_mfma_f32_16x16x32_bf16 v[84:87], a[180:183], v[168:171], v[84:87]\n"
  "ds_read_b64_tr_b16 a[30:31], v237 offset:18432\n"
  "v_mfma_f32_16x16x32_bf16 v[96:99], a[184:187], v[168:171], v[96:99]\n"
  "ds_read_b64_tr_b16 a[32:33], v234 offset:20480\n"
  "v_mfma_f32_16x16x32_bf16 v[100:103], a[188:191], v[168:171], v[100:103]\n"
  "ds_read_b64_tr_b16 a[34:35], v234 offset:22528\n"
  "s_waitcnt vmcnt(13) lgkmcnt(8)\n"
  "v_mfma_f32_16x16x32_bf16 v[88:91], a[176:179], v[172:175], v[88:91]\n"
  "ds_read_b64_tr_b16 a[36:37], v235 offset:20480\n"
  "v_mfma_f32_16x16x32_bf16 v[92:95], a[180:183], v[172:175], v[92:95]\n"
  "ds_read_b64_tr_b16 a[38:39], v235 offset:22528\n"
  "v_mfma_f32_16x16x32_bf16 v[104:107], a[184:187], v[172:175], v[104:107]\n"
  "ds_read_b64_tr_b16 a[40:41], v236 offset:20480\n"
  "s_waitcnt lgkmcnt(5)\n"
  "s_add_u32 m0, s8, 16384\n"
  "s_mov_b32 s9, 917504\n"
  "buffer_load_dwordx4 v240, s[40:43], s9 offen lds\n"
  "v_mfma_f32_16x16x32_bf16 v[108:111], a[188:191], v[172:175], v[108:111]\n"
  "s_add_u32 m0, s8, 17408\n"
  "s_mov_b32 s9, 950272\n"
  "buffer_load_dwordx4 v240, s[40:43], s9 offen lds\n"
  "s_waitcnt vmcnt(14)\n"
  "v_mfma_f32_16x16x32_bf16 v[80:83], a[192:195], v[216:219], v[80:83]\n"
  "s_add_u32 m0, s8, 18432\n"
  "s_mov_b32 s9, 983040\n"
  "buffer_load_dwordx4 v240, s[40:43], s9 offen lds\n"
  "v_mfma_f32_16x16x32_bf16 v[84:87], a[196:199], v[216:219], v[84:87]\n"
  "ds_read_b64_tr_b16 a[42:43], v236 offset:22528\n"
  "v_mfma_f32_16x16x32_bf16 v[96:99], a[200:203], v[216:219], v[96:99]\n"
  "ds_read_b64_tr_b16 a[44:45], v237 offset:20480\n"
  "v_mfma_f32_16x16x32_bf16 v[100:103], a[204:207], v[216:219], v[100:103]\n"
  "ds_read_b64_tr_b16 a[46:47], v237 offset:22528\n"
  "s_waitcnt vmcnt(14) lgkmcnt(5)\n"
  "v_mfma_f32_16x16x32_bf16 v[88:91], a[192:195], v[220:223], v[88:91]\n"
  "ds_read_b64_tr_b16 a[48:49], v234 offset:0\n"
  "v_mfma_f32_16x16x32_bf16 v[92:95], a[196:199], v[220:223], v[92:95]\n"
  "ds_read_b64_tr_b16 a[50:51], v234 offset:2048\n"
  "v_mfma_f32_16x16x32_bf16 v[104:107], a[200:203], v[220:223], v[104:107]\n"
  "ds_read_b64_tr_b16 a[52:53], v235 offset:0\n"
  "v_mfma_f32_16x16x32_bf16 v[108:111], a[204:207], v[220:223], v[108:111]\n"
  "ds_read_b64_tr_b16 a[54:55], v235 offset:2048\n"
  "s_waitcnt vmcnt(13) lgkmcnt(7)\n"
  "v_mfma_f32_16x16x32_bf16 v[80:83], a[208:211], v[224:227], v[80:83]\n"
  "ds_read_b64_tr_b16 a[56:57], v236 offset:0\n"
  "v_mfma_f32_16x16x32_bf16 v[84:87], a[212:215], v[224:227], v[84:87]\n"
  "ds_read_b64_tr_b16 a[58:59], v236 offset:2048\n"
  "v_mfma_f32_16x16x32_bf16 v[96:99], a[216:219], v[224:227], v[96:99]\n"
  "ds_read_b64_tr_b16 a[60:61], v237 offset:0\n"
  "v_mfma_f32_16x16x32_bf16 v[100:103], a[220:223], v[224:227], v[100:103]\n"
  "ds_read_b64_tr_b16 a[62:63], v237 offset:2048\n"
  "s_waitcnt vmcnt(12) lgkmcnt(8)\n"
  "v_mfma_f32_16x16x32_bf16 v[88:91], a[208:211], v[228:231], v[88:91]\n"
  "ds_read_b64_tr_b16 a[64:65], v234 offset:4096\n"
  "v_mfma_f32_16x16x32_bf16 v[92:95], a[212:215], v[228:231], v[92:95]\n"
  "ds_read_b64_tr_b16 a[66:67], v234 offset:6144\n"
  "v_mfma_f32_16x16x32_bf16 v[104:107], a[216:219], v[228:231], v[104:107]\n"
  "ds_read_b64_tr_b16 a[68:69], v235 offset:4096\n"
  "v_mfma_f32_16x16x32_bf16 v[108:111], a[220:223], v[228:231], v[108:111]\n"
  "ds_read_b64_tr_b16 a[70:71], v235 offset:6144\n"
  "s_waitcnt vmcnt(11) lgkmcnt(8)\n"
  "v_mfma_f32_16x16x32_bf16 v[80:83], a[224:227], v[112:115], v[80:83]\n"
  "ds_read_b64_tr_b16 a[72:73], v236 offset:4096\n"
  "v_mfma_f32_16x16x32_bf16 v[84:87], a[228:231], v[112:115], v[84:87]\n"
  "ds_read_b64_tr_b16 a[74:75], v236 offset:6144\n"
  "v_mfma_f32_16x16x32_bf16 v[96:99], a[232:235], v[112:115], v[96:99]\n"
  "ds_read_b64_tr_b16 a[76:77], v237 offset:4096\n"
  "v_mfma_f32_16x16x32_bf16 v[100:103], a[236:239], v[112:115], v[100:103]\n"
  "ds_read_b64_tr_b16 a[78:79], v237 offset:6144\n"
  "s_waitcnt vmcnt(10) lgkmcnt(8)\n"
  "v_mfma_f32_16x16x32_bf16 v[88:91], a[224:227], v[116:119], v[88:91]\n"
  "ds_read_b64_tr_b16 a[80:81], v234 offset:8192\n"
  "v_mfma_f32_16x16x32_bf16 v[92:95], a[228:231], v[116:119], v[92:95]\n"
  "ds_read_b64_tr_b16 a[82:83], v234 offset:10240\n"
  "v_mfma_f32_16x16x32_bf16 v[104:107], a[232:235], v[116:119], v[104:107]\n"
  "ds_read_b64_tr_b16 a[84:85], v235 offset:8192\n"
  "v_mfma_f32_16x16x32_bf16 v[108:111], a[236:239], v[116:119], v[108:111]\n"
  "ds_read_b64_tr_b16 a[86:87], v235 offset:10240\n"
  "s_waitcnt vmcnt(9) lgkmcnt(8)\n"
  "v_mfma_f32_16x16x32_bf16 v[80:83], a[240:243], v[120:123], v[80:83]\n"
  "ds_read_b64_tr_b16 a[88:89], v236 offset:8192\n"
  "v_mfma_f32_16x16x32_bf16 v[84:87], a[244:247], v[120:123], v[84:87]\n"
  "s_waitcnt vmcnt(4) lgkmcnt(8)\n"
  "s_barrier\n"
  "buffer_store_dwordx2 v[36:37], v248, s[20:23], s13 offen sc1\n"
  "v_mfma_f32_16x16x32_bf16 v[96:99], a[248:251], v[120:123], v[96:99]\n"
  "s_add_u32 m0, s8, 19456\n"
  "s_mov_b32 s9, 1015808\n"
  "buffer_load_dwordx4 v240, s[40:43], s9 offen lds\n"
  "v_mfma_f32_16x16x32_bf16 v[100:103], a[252:255], v[120:123], v[100:103]\n"
  "s_add_u32 m0, s8, 20480\n"
  "s_mov_b32 s9, 1048576\n"
  "buffer_load_dwordx4 v240, s[40:43], s9 offen lds\n"
  "v_mfma_f32_16x16x32_bf16 v[88:91], a[240:243], v[124:127], v[88:91]\n"
  "ds_read_b64_tr_b16 a[90:91], v236 offset:10240\n"
  "v_mfma_f32_16x16x32_bf16 v[92:95], a[244:247], v[124:127], v[92:95]\n"
  "s_add_u32 m0, s8, 21504\n"
  "s_mov_b32 s9, 1081344\n"
  "buffer_load_dwordx4 v240, s[40:43], s9 offen lds\n"
  "v_mfma_f32_16x16x32_bf16 v[104:107], a[248:251], v[124:127], v[104:107]\n"
  "ds_read_b64_tr_b16 a[92:93], v237 offset:8192\n"
  "v_mfma_f32_16x16x32_bf16 v[108:111], a[252:255], v[124:127], v[108:111]\n"
  "ds_write_b128 v242, v[88:91] offset:24576\n"
  "v_mfma_f32_16x16x16_bf16 v[48:51], v[22:23], v[40:41], v[48:51]\n"
  "ds_read_b64_tr_b16 a[94:95], v237 offset:10240\n"
  "v_mfma_f32_16x16x16_bf16 v[52:55], v[24:25], v[40:41], v[52:55]\n"
  "s_add_u32 m0, s8, 22528\n"
  "s_mov_b32 s9, 1114112\n"
  "buffer_load_dwordx4 v240, s[40:43], s9 offen lds\n"
  "ds_write_b128 v242, v[92:95] offset:25600\n"
  "s_waitcnt vmcnt(8) lgkmcnt(5)\n"
  "ds_read_b64_tr_b16 a[96:97], v234 offset:12288\n"
  "ds_read_b64_tr_b16 a[98:99], v234 offset:14336\n"
  "ds_read_b64_tr_b16 a[100:101], v235 offset:12288\n"
  "ds_read_b64_tr_b16 a[102:103], v235 offset:14336\n"
  "ds_read_b64_tr_b16 a[104:105], v236 offset:12288\n"
  "ds_read_b64_tr_b16 a[106:107], v236 offset:14336\n"
  "ds_write_b128 v243, v[96:99] offset:24576\n"
  "s_add_u32 m0, s8, 23552\n"
  "s_mov_b32 s9, 1146880\n"
  "buffer_load_dwordx4 v240, s[40:43], s9 offen lds\n"
  "ds_read_b64_tr_b16 a[108:109], v237 offset:12288\n"
  "ds_read_b64_tr_b16 a[110:111], v237 offset:14336\n"
  "ds_write_b128 v243, v[100:103] offset:25600\n"
  "s_add_u32 m0, s8, 0\n"
  "s_mov_b32 s9, 1179648\n"
  "buffer_load_dwordx4 v240, s[40:43], s9 offen lds\n"
  "ds_write_b128 v244, v[104:107] offset:24576\n"
  "ds_write_b128 v244, v[108:111] offset:25600\n"
  "s_add_u32 m0, s8, 1024\n"
  "s_mov_b32 s9, 1212416\n"
  "buffer_load_dwordx4 v240, s[40:43], s9 offen lds\n"
  "s_waitcnt lgkmcnt(0)\n"
  "s_barrier\n"
  "ds_read_b128 v[88:91], v241 offset:24576\n"
  "ds_read_b128 v[96:99], v241 offset:26624\n"
  "s_add_u32 m0, s8, 2048\n"
  "s_mov_b32 s9, 1245184\n"
  "buffer_load_dwordx4 v240, s[40:43], s9 offen lds\n"
  "ds_read_b128 v[104:107], v241 offset:28672\n"
  "ds_read_b128 v[92:95], v241 offset:25600\n"
  "ds_read_b128 v[100:103], v241 offset:27648\n"
  "ds_read_b128 v[108:111], v241 offset:29696\n"
  "s_add_u32 m0, s8, 3072\n"
  "s_mov_b32 s9, 1277952\n"
  "buffer_load_dwordx4 v240, s[40:43], s9 offen lds\n"
  "s_waitcnt lgkmcnt(5)\n"
  "v_pk_add_f32 v[80:81], v[80:81], v[88:89]\n"
  "v_pk_add_f32 v[82:83], v[82:83], v[90:91]\n"
  "s_waitcnt lgkmcnt(3)\n"
  "v_pk_add_f32 v[96:97], v[96:97], v[104:105]\n"
  "v_pk_add_f32 v[98:99], v[98:99], v[106:107]\n"
  "s_waitcnt lgkmcnt(2)\n"
  "v_pk_add_f32 v[84:85], v[84:85], v[92:93]\n"
  "v_pk_add_f32 v[86:87], v[86:87], v[94:95]\n"
  "v_pk_add_f32 v[80:81], v[80:81], v[96:97]\n"
  "v_pk_add_f32 v[82:83], v[82:83], v[98:99]\n"
  "v_pk_mul_f32 v[88:89], v[80:81], v[80:81]\n"
  "v_pk_mul_f32 v[90:91], v[82:83], v[82:83]\n"
  "s_waitcnt lgkmcnt(0)\n"
  "v_pk_add_f32 v[100:101], v[100:101], v[108:109]\n"
  "v_pk_add_f32 v[102:103], v[102:103], v[110:111]\n"
  "v_pk_add_f32 v[84:85], v[84:85], v[100:101]\n"
  "v_pk_add_f32 v[86:87], v[86:87], v[102:103]\n"
  "v_pk_mul_f32 v[92:93], v[84:85], v[84:85]\n"
  "v_pk_mul_f32 v[94:95], v[86:87], v[86:87]\n"
  "v_pk_fma_f32 v[88:89], v[88:89], s[6:7], v[204:205] op_sel:[0,1,1]\n"
  "v_pk_fma_f32 v[90:91], v[90:91], s[6:7], v[204:205] op_sel:[0,1,1]\n"
  "v_pk_fma_f32 v[92:93], v[92:93], s[6:7], v[204:205] op_sel:[0,1,1]\n"
  "v_pk_fma_f32 v[94:95], v[94:95], s[6:7], v[204:205] op_sel:[0,1,1]\n"
  "v_pk_mul_f32 v[88:89], v[88:89], v[80:81]\n"
  "v_pk_mul_f32 v[90:91], v[90:91], v[82:83]\n"
  "v_pk_mul_f32 v[92:93], v[92:93], v[84:85]\n"
  "v_pk_mul_f32 v[94:95], v[94:95], v[86:87]\n"
  "v_exp_f32 v88, v88\n"
  "s_add_u32 m0, s8, 4096\n"
  "s_mov_b32 s9, 1310720\n"
  "buffer_load_dwordx4 v240, s[40:43], s9 offen lds\n"
  "v_exp_f32 v89, v89\n"
  "v_exp_f32 v90, v90\n"
  "v_exp_f32 v91, v91\n"
  "v_exp_f32 v92, v92\n"
  "v_exp_f32 v93, v93\n"
  "v_exp_f32 v94, v94\n"
  "v_exp_f32 v95, v95\n"
  "v_pk_add_f32 v[88:89], v[88:89], 1.0 op_sel_hi:[1,0]\n"
  "v_pk_add_f32 v[90:91], v[90:91], 1.0 op_sel_hi:[1,0]\n"
  "v_pk_add_f32 v[92:93], v[92:93], 1.0 op_sel_hi:[1,0]\n"
  "v_pk_add_f32 v[94:95], v[94:95], 1.0 op_sel_hi:[1,0]\n"
  "v_rcp_f32 v88, v88\n"
  "s_add_u32 m0, s8, 5120\n"
  "s_mov_b32 s9, 1343488\n"
  "buffer_load_dwordx4 v240, s[40:43], s9 offen lds\n"
  "v_rcp_f32 v89, v89\n"
  "v_rcp_f32 v90, v90\n"
  "v_rcp_f32 v91, v91\n"
  "v_rcp_f32 v92, v92\n"
  "v_rcp_f32 v93, v93\n"
  "v_rcp_f32 v94, v94\n"
  "v_rcp_f32 v95, v95\n"
  "v_pk_mul_f32 v[88:89], v[88:89], v[80:81]\n"
  "v_pk_mul_f32 v[90:91], v[90:91], v[82:83]\n"
  "v_pk_mul_f32 v[92:93], v[92:93], v[84:85]\n"
  "v_pk_mul_f32 v[94:95], v[94:95], v[86:87]\n"
  "s_add_u32 m0, s8, 6144\n"
  "s_mov_b32 s9, 1376256\n"
  "buffer_load_dwordx4 v240, s[40:43], s9 offen lds\n"
  "v_cvt_pk_bf16_f32 v96, v88, v89\n"
  "v_cvt_pk_bf16_f32 v97, v90, v91\n"
  "v_cvt_pk_bf16_f32 v98, v92, v93\n"
  "v_cvt_pk_bf16_f32 v99, v94, v95\n"
  "buffer_store_dwordx4 v[96:99], v1, s[20:23], s53 offen sc1\n"
  "s_waitcnt vmcnt(12)\n"
  "ds_read_b64_tr_b16 a[112:113], v234 offset:16384\n"
  "ds_read_b64_tr_b16 a[114:115], v234 offset:18432\n"
  "ds_read_b64_tr_b16 a[116:117], v235 offset:16384\n"
  "ds_read_b64_tr_b16 a[118:119], v235 offset:18432\n"
  "ds_read_b64_tr_b16 a[120:121], v236 offset:16384\n"
  "v_mov_b32 v80, v47\n"
  "ds_read_b64_tr_b16 a[122:123], v236 offset:18432\n"
  "v_mov_b32 v81, v47\n"
  "ds_read_b64_tr_b16 a[124:125], v237 offset:16384\n"
  "s_add_u32 m0, s8, 7168\n"
  "s_mov_b32 s9, 1409024\n"
  "buffer_load_dwordx4 v240, s[40:43], s9 offen lds\n"
  "ds_read_b64_tr_b16 a[126:127], v237 offset:18432\n"
  "v_mov_b32 v82, v47\n"
  "v_mov_b32 v83, v47\n"
  "s_lshl_b32 s87, s67, 14\n"
  "s_lshl_b32 s88, s64, 7\n"
  "v_mfma_f32_16x16x16_bf16 v[80:83], v[26:27], v[40:41], v[80:83]\n"
  "s_add_u32 s87, s87, s88\n"
  "v_lshrrev_b32 v45, 2, v0\n"
  "v_lshl_add_u32 v245, v45, 10, s87\n"
  "v_and_b32 v46, 3, v0\n"
  "v_lshl_add_u32 v245, v46, 4, v245\n"
  "v_lshlrev_b32 v204, 2, v45\n"
  "v_and_b32 v46, 1, v0\n"
  "v_lshl_add_u32 v204, v46, 7, v204\n"
  "v_mov_b32 v84, v47\n"
  "v_mov_b32 v85, v47\n"
  "v_mov_b32 v86, v47\n"
  "s_add_u32 m0, s8, 8192\n"
  "s_mov_b32 s9, 1441792\n"
  "buffer_load_dwordx4 v240, s[40:43], s9 offen lds\n"
  "v_mov_b32 v87, v47\n"
  "v_and_b32 v46, 2, v0\n"
  "v_lshl_add_u32 v204, v46, 5, v204\n"
  "v_mfma_f32_16x16x16_bf16 v[84:87], v[28:29], v[40:41], v[84:87]\n"
  "s_waitcnt vmcnt(10) lgkmcnt(3)\n"
  "ds_read_b64_tr_b16 a[128:129], v234 offset:20480\n"
  "ds_read_b64_tr_b16 a[130:131], v234 offset:22528\n"
  "ds_read_b64_tr_b16 a[132:133], v235 offset:20480\n"
  "ds_read_b64_tr_b16 a[134:135], v235 offset:22528\n"
  "ds_read_b64_tr_b16 a[136:137], v236 offset:20480\n"
  "ds_read_b64_tr_b16 a[138:139], v236 offset:22528\n"
  "ds_read_b64_tr_b16 a[140:141], v237 offset:20480\n"
  "s_add_u32 m0, s8, 9216\n"
  "s_mov_b32 s9, 1474560\n"
  "buffer_load_dwordx4 v240, s[40:43], s9 offen lds\n"
  "ds_read_b64_tr_b16 a[142:143], v237 offset:22528\n"
  "s_waitcnt vmcnt(7) lgkmcnt(0)\n"
  "ds_read_b64_tr_b16 a[144:145], v234 offset:0\n"
  "ds_read_b64_tr_b16 a[146:147], v234 offset:2048\n"
  "ds_read_b64_tr_b16 a[148:149], v235 offset:0\n"
  "ds_read_b64_tr_b16 a[150:151], v235 offset:2048\n"
  "ds_read_b64_tr_b16 a[152:153], v236 offset:0\n"
  "ds_read_b64_tr_b16 a[154:155], v236 offset:2048\n"
  "ds_read_b64_tr_b16 a[156:157], v237 offset:0\n"
  "ds_read_b64_tr_b16 a[158:159], v237 offset:2048\n"
  "s_sleep 1\n"
  "buffer_load_dwordx2 v[206:207], v247, s[20:23], 0 offen sc1\n"
  "s_waitcnt vmcnt(4) lgkmcnt(0)\n"
  "s_barrier\n"
  "buffer_store_dwordx2 v[36:37], v248, s[20:23], s13 offen offset:128 sc1\n"
  "s_add_u32 m0, s8, 10240\n"
  "s_mov_b32 s9, 1507328\n"
  "buffer_load_dwordx4 v240, s[40:43], s9 offen lds\n"
  "s_add_u32 m0, s8, 11264\n"
  "s_mov_b32 s9, 1540096\n"
  "buffer_load_dwordx4 v240, s[40:43], s9 offen lds\n"
  "s_waitcnt vmcnt(6)\n"
  "ds_read_b64_tr_b16 a[160:161], v234 offset:4096\n"
  "ds_read_b64_tr_b16 a[162:163], v234 offset:6144\n"
  "ds_read_b64_tr_b16 a[164:165], v235 offset:4096\n"
  "ds_read_b64_tr_b16 a[166:167], v235 offset:6144\n"
  "ds_read_b64_tr_b16 a[168:169], v236 offset:4096\n"
  "ds_read_b64_tr_b16 a[170:171], v236 offset:6144\n"
  "ds_read_b64_tr_b16 a[172:173], v237 offset:4096\n"
  "ds_read_b64_tr_b16 a[174:175], v237 offset:6144\n"
  "s_waitcnt vmcnt(3) lgkmcnt(0)\n"
  "v_cmp_ne_u64 vcc, s[56:57], v[206:207]\n"
  "s_cmp_eq_u64 vcc, 0\n"
  "s_cbranch_scc0 .Lfb22_%=\n"
  ".Lbk22_%=:\n"
  "buffer_load_dwordx4 v[128:131], v232, s[20:23], 0 offen sc1\n"
  "buffer_load_dwordx4 v[132:135], v232, s[20:23], 0 offen offset:1024 sc1\n"
  "buffer_load_dwordx4 v[136:139], v232, s[20:23], 0 offen offset:2048 sc1\n"
  "buffer_load_dwordx4 v[140:143], v232, s[20:23], 0 offen offset:3072 sc1\n"
  "buffer_load_dwordx4 v[144:147], v232, s[20:23], s72 offen sc1\n"
  "buffer_load_dwordx4 v[148:151], v232, s[20:23], s72 offen offset:1024 sc1\n"
  "buffer_load_dwordx4 v[152:155], v232, s[20:23], s72 offen offset:2048 sc1\n"
  "s_waitcnt vmcnt(7)\n"
  "ds_read_b64_tr_b16 a[176:177], v234 offset:8192\n"
  "ds_read_b64_tr_b16 a[178:179], v234 offset:10240\n"
  "ds_read_b64_tr_b16 a[180:181], v235 offset:8192\n"
  "ds_read_b64_tr_b16 a[182:183], v235 offset:10240\n"
  "ds_read_b64_tr_b16 a[184:185], v236 offset:8192\n"
  "ds_read_b64_tr_b16 a[186:187], v236 offset:10240\n"
  "ds_read_b64_tr_b16 a[188:189], v237 offset:8192\n"
  "ds_read_b64_tr_b16 a[190:191], v237 offset:10240\n"
  "buffer_load_dwordx4 v[156:159], v232, s[20:23], s72 offen offset:3072 sc1\n"
  "buffer_load_dwordx4 v[160:163], v232, s[20:23], s73 offen sc1\n"
  "buffer_load_dwordx4 v[164:167], v232, s[20:23], s73 offen offset:1024 sc1\n"
  "buffer_load_dwordx4 v[168:171], v232, s[20:23], s73 offen offset:2048 sc1\n"
  "buffer_load_dwordx4 v[172:175], v232, s[20:23], s73 offen offset:3072 sc1\n"
  "buffer_load_dwordx4 v[216:219], v232, s[20:23], s74 offen sc1\n"
  "s_waitcnt vmcnt(12) lgkmcnt(0)\n"
  "v_mfma_f32_16x16x32_bf16 v[48:51], v[128:131], a[0:3], v[48:51]\n"
  "v_mfma_f32_16x16x32_bf16 v[56:59], v[128:131], a[4:7], 0\n"
  "v_mfma_f32_16x16x32_bf16 v[64:67], v[128:131], a[8:11], 0\n"
  "buffer_load_dwordx4 v[220:223], v232, s[20:23], s74 offen offset:1024 sc1\n"
  "v_mfma_f32_16x16x32_bf16 v[72:75], v[128:131], a[12:15], 0\n"
  "s_waitcnt vmcnt(12)\n"
  "v_mfma_f32_16x16x32_bf16 v[52:55], v[132:135], a[0:3], v[52:55]\n"
  "v_mfma_f32_16x16x32_bf16 v[60:63], v[132:135], a[4:7], 0\n"
  "v_mfma_f32_16x16x32_bf16 v[68:71], v[132:135], a[8:11], 0\n"
  "buffer_load_dwordx4 v[224:227], v232, s[20:23], s74 offen offset:2048 sc1\n"
  "v_mfma_f32_16x16x32_bf16 v[76:79], v[132:135], a[12:15], 0\n"
  "s_waitcnt vmcnt(12)\n"
  "v_mfma_f32_16x16x32_bf16 v[48:51], v[136:139], a[16:19], v[48:51]\n"
  "v_mfma_f32_16x16x32_bf16 v[56:59], v[136:139], a[20:23], v[56:59]\n"
  "buffer_load_dwordx4 v[228:231], v232, s[20:23], s74 offen offset:3072 sc1\n"
  "v_mfma_f32_16x16x32_bf16 v[64:67], v[136:139], a[24:27], v[64:67]\n"
  "v_mfma_f32_16x16x32_bf16 v[72:75], v[136:139], a[28:31], v[72:75]\n"
  "s_waitcnt vmcnt(12)\n"
  "v_mfma_f32_16x16x32_bf16 v[52:55], v[140:143], a[16:19], v[52:55]\n"
  "v_mfma_f32_16x16x32_bf16 v[60:63], v[140:143], a[20:23], v[60:63]\n"
  "buffer_load_dwordx4 v[112:115], v232, s[20:23], s75 offen sc1\n"
  "v_mfma_f32_16x16x32_bf16 v[68:71], v[140:143], a[24:27], v[68:71]\n"
  "v_mfma_f32_16x16x32_bf16 v[76:79], v[140:143], a[28:31], v[76:79]\n"
  "s_waitcnt vmcnt(12)\n"
  "v_mfma_f32_16x16x32_bf16 v[48:51], v[144:147], a[32:35], v[48:51]\n"
  "v_mfma_f32_16x16x32_bf16 v[56:59], v[144:147], a[36:39], v[56:59]\n"
  "buffer_load_dwordx4 v[116:119], v232, s[20:23], s75 offen offset:1024 sc1\n"
  "v_mfma_f32_16x16x32_bf16 v[64:67], v[144:147], a[40:43], v[64:67]\n"
  "v_mfma_f32_16x16x32_bf16 v[72:75], v[144:147], a[44:47], v[72:75]\n"
  "s_waitcnt vmcnt(12)\n"
  "v_mfma_f32_16x16x32_bf16 v[52:55], v[148:151], a[32:35], v[52:55]\n"
  "v_mfma_f32_16x16x32_bf16 v[60:63], v[148:151], a[36:39], v[60:63]\n"
  "buffer_load_dwordx2 v[206:207], v247, s[20:23], 0 offen offset:128 sc1\n"
  "v_mfma_f32_16x16x32_bf16 v[68:71], v[148:151], a[40:43], v[68:71]\n"
  "v_mfma_f32_16x16x32_bf16 v[76:79], v[148:151], a[44:47], v[76:79]\n"
  "s_waitcnt vmcnt(12)\n"
  "v_mfma_f32_16x16x32_bf16 v[48:51], v[152:155], a[48:51], v[48:51]\n"
  "buffer_load_dwordx4 v[120:123], v232, s[20:23], s75 offen offset:2048 sc1\n"
  "v_mfma_f32_16x16x32_bf16 v[56:59], v[152:155], a[52:55], v[56:59]\n"
  "v_mfma_f32_16x16x32_bf16 v[64:67], v[152:155], a[56:59], v[64:67]\n"
  "v_mfma_f32_16x16x32_bf16 v[72:75], v[152:155], a[60:63], v[72:75]\n"
  "s_waitcnt vmcnt(12)\n"
  "v_mfma_f32_16x16x32_bf16 v[52:55], v[156:159], a[48:51], v[52:55]\n"
  "buffer_load_dwordx4 v[124:127], v232, s[20:23], s75 offen offset:3072 sc1\n"
  "v_mfma_f32_16x16x32_bf16 v[60:63], v[156:159], a[52:55], v[60:63]\n"
  "v_mfma_f32_16x16x32_bf16 v[68:71], v[156:159], a[56:59], v[68:71]\n"
  "v_mfma_f32_16x16x32_bf16 v[76:79], v[156:159], a[60:63], v[76:79]\n"
  "s_waitcnt vmcnt(12)\n"
  "v_mfma_f32_16x16x32_bf16 v[48:51], v[160:163], a[64:67], v[48:51]\n"
  "buffer_load_dwordx4 v[128:131], v232, s[20:23], s76 offen sc1\n"
  "v_mfma_f32_16x16x32_bf16 v[56:59], v[160:163], a[68:71], v[56:59]\n"
  "v_mfma_f32_16x16x32_bf16 v[64:67], v[160:163], a[72:75], v[64:67]\n"
  "v_mfma_f32_16x16x32_bf16 v[72:75], v[160:163], a[76:79], v[72:75]\n"
  "s_waitcnt vmcnt(12)\n"
  "v_mfma_f32_16x16x32_bf16 v[52:55], v[164:167], a[64:67], v[52:55]\n"
  "buffer_load_dwordx4 v[132:135], v232, s[20:23], s76 offen offset:1024 sc1\n"
  "v_mfma_f32_16x16x32_bf16 v[60:63], v[164:167], a[68:71], v[60:63]\n"
  "v_mfma_f32_16x16x32_bf16 v[68:71], v[164:167], a[72:75], v[68:71]\n"
  "v_mfma_f32_16x16x32_bf16 v[76:79], v[164:167], a[76:79], v[76:79]\n"
  "s_waitcnt vmcnt(12)\n"
  "v_mfma_f32_16x16x32_bf16 v[48:51], v[168:171], a[80:83], v[48:51]\n"
  "buffer_load_dwordx4 v[136:139], v232, s[20:23], s76 offen offset:2048 sc1\n"
  "v_mfma_f32_16x16x32_bf16 v[56:59], v[168:171], a[84:87], v[56:59]\n"
  "v_mfma_f32_16x16x32_bf16 v[64:67], v[168:171], a[88:91], v[64:67]\n"
  "v_mfma_f32_16x16x32_bf16 v[72:75], v[168:171], a[92:95], v[72:75]\n"
  "buffer_load_dwordx4 v[140:143], v232, s[20:23], s76 offen offset:3072 sc1\n"
  "s_waitcnt vmcnt(13)\n"
  "v_mfma_f32_16x16x32_bf16 v[52:55], v[172:175], a[80:83], v[52:55]\n"
  "v_mfma_f32_16x16x32_bf16 v[60:63], v[172:175], a[84:87], v[60:63]\n"
  "v_mfma_f32_16x16x32_bf16 v[68:71], v[172:175], a[88:91], v[68:71]\n"
  "v_mfma_f32_16x16x32_bf16 v[76:79], v[172:175], a[92:95], v[76:79]\n"
  "buffer_load_dwordx4 v[144:147], v232, s[20:23], s77 offen sc1\n"
  "s_waitcnt vmcnt(13)\n"
  "v_mfma_f32_16x16x32_bf16 v[48:51], v[216:219], a[96:99], v[48:51]\n"
  "v_mfma_f32_16x16x32_bf16 v[56:59], v[216:219], a[100:103], v[56:59]\n"
  "v_mfma_f32_16x16x32_bf16 v[64:67], v[216:219], a[104:107], v[64:67]\n"
  "v_mfma_f32_16x16x32_bf16 v[72:75], v[216:219], a[108:111], v[72:75]\n"
  "buffer_load_dwordx4 v[148:151], v232, s[20:23], s77 offen offset:1024 sc1\n"
  "s_waitcnt vmcnt(13)\n"
  "v_mfma_f32_16x16x32_bf16 v[52:55], v[220:223], a[96:99], v[52:55]\n"
  "v_mfma_f32_16x16x32_bf16 v[60:63], v[220:223], a[100:103], v[60:63]\n"
  "v_mfma_f32_16x16x32_bf16 v[68:71], v[220:223], a[104:107], v[68:71]\n"
  "v_mfma_f32_16x16x32_bf16 v[76:79], v[220:223], a[108:111], v[76:79]\n"
  "buffer_load_dwordx4 v[152:155], v232, s[20:23], s77 offen offset:2048 sc1\n"
  "s_waitcnt vmcnt(13)\n"
  "v_mfma_f32_16x16x32_bf16 v[48:51], v[224:227], a[112:115], v[48:51]\n"
  "v_mfma_f32_16x16x32_bf16 v[56:59], v[224:227], a[116:119], v[56:59]\n"
  "v_mfma_f32_16x16x32_bf16 v[64:67], v[224:227], a[120:123], v[64:67]\n"
  "buffer_load_dwordx4 v[156:159], v232, s[20:23], s77 offen offset:3072 sc1\n"
  "v_mfma_f32_16x16x32_bf16 v[72:75], v[224:227], a[124:127], v[72:75]\n"
  "s_waitcnt vmcnt(13)\n"
  "v_mfma_f32_16x16x32_bf16 v[52:55], v[228:231], a[112:115], v[52:55]\n"
  "v_mfma_f32_16x16x32_bf16 v[60:63], v[228:231], a[116:119], v[60:63]\n"
  "v_mfma_f32_16x16x32_bf16 v[68:71], v[228:231], a[120:123], v[68:71]\n"
  "v_mfma_f32_16x16x32_bf16 v[76:79], v[228:231], a[124:127], v[76:79]\n"
  "s_waitcnt vmcnt(12)\n"
  "v_mfma_f32_16x16x32_bf16 v[48:51], v[112:115], a[128:131], v[48:51]\n"
  "v_mfma_f32_16x16x32_bf16 v[56:59], v[112:115], a[132:135], v[56:59]\n"
  "v_mfma_f32_16x16x32_bf16 v[64:67], v[112:115], a[136:139], v[64:67]\n"
  "v_mfma_f32_16x16x32_bf16 v[72:75], v[112:115], a[140:143], v[72:75]\n"
  "s_waitcnt vmcnt(10)\n"
  "v_cmp_ne_u64 vcc, s[56:57], v[206:207]\n"
  "s_cmp_eq_u64 vcc, 0\n"
  "s_cbranch_scc0 .Lfb23_%=\n"
  ".Lbk23_%=:\n"
  "v_mfma_f32_16x16x32_bf16 v[52:55], v[116:119], a[128:131], v[52:55]\n"
  "buffer_load_dwordx4 v[160:163], v233, s[20:23], 0 offen sc1\n"
  "buffer_load_dwordx4 v[164:167], v233, s[20:23], 0 offen offset:1024 sc1\n"
  "v_mfma_f32_16x16x32_bf16 v[60:63], v[116:119], a[132:135], v[60:63]\n"
  "buffer_load_dwordx4 v[168:171], v233, s[20:23], 0 offen offset:2048 sc1\n"
  "v_mfma_f32_16x16x32_bf16 v[68:71], v[116:119], a[136:139], v[68:71]\n"
  "v_mfma_f32_16x16x32_bf16 v[76:79], v[116:119], a[140:143], v[76:79]\n"
  "s_waitcnt vmcnt(12)\n"
  "v_mfma_f32_16x16x32_bf16 v[48:51], v[120:123], a[144:147], v[48:51]\n"
  "v_mfma_f32_16x16x32_bf16 v[56:59], v[120:123], a[148:151], v[56:59]\n"
  "buffer_load_dwordx4 v[172:175], v233, s[20:23], 0 offen offset:3072 sc1\n"
  "v_mfma_f32_16x16x32_bf16 v[64:67], v[120:123], a[152:155], v[64:67]\n"
  "v_mfma_f32_16x16x32_bf16 v[72:75], v[120:123], a[156:159], v[72:75]\n"
  "s_waitcnt vmcnt(12)\n"
  "v_mfma_f32_16x16x32_bf16 v[52:55], v[124:127], a[144:147], v[52:55]\n"
  "v_mfma_f32_16x16x32_bf16 v[60:63], v[124:127], a[148:151], v[60:63]\n"
  "buffer_load_dwordx4 v[216:219], v233, s[20:23], s72 offen sc1\n"
  "v_mfma_f32_16x16x32_bf16 v[68:71], v[124:127], a[152:155], v[68:71]\n"
  "v_mfma_f32_16x16x32_bf16 v[76:79], v[124:127], a[156:159], v[76:79]\n"
  "s_waitcnt vmcnt(12)\n"
  "v_mfma_f32_16x16x32_bf16 v[48:51], v[128:131], a[160:163], v[48:51]\n"
  "buffer_load_dwordx4 v[220:223], v233, s[20:23], s72 offen offset:1024 sc1\n"
  "v_mfma_f32_16x16x32_bf16 v[56:59], v[128:131], a[164:167], v[56:59]\n"
  "v_mfma_f32_16x16x32_bf16 v[64:67], v[128:131], a[168:171], v[64:67]\n"
  "v_mfma_f32_16x16x32_bf16 v[72:75], v[128:131], a[172:175], v[72:75]\n"
  "s_waitcnt vmcnt(12)\n"
  "v_mfma_f32_16x16x32_bf16 v[52:55], v[132:135], a[160:163], v[52:55]\n"
  "buffer_load_dwordx4 v[224:227], v233, s[20:23], s72 offen offset:2048 sc1\n"
  "v_mfma_f32_16x16x32_bf16 v[60:63], v[132:135], a[164:167], v[60:63]\n"
  "v_mfma_f32_16x16x32_bf16 v[68:71], v[132:135], a[168:171], v[68:71]\n"
  "v_mfma_f32_16x16x32_bf16 v[76:79], v[132:135], a[172:175], v[76:79]\n"
  "s_waitcnt vmcnt(12)\n"
  "v_mfma_f32_16x16x32_bf16 v[48:51], v[136:139], a[176:179], v[48:51]\n"
  "buffer_load_dwordx4 v[228:231], v233, s[20:23], s72 offen offset:3072 sc1\n"
  "v_mfma_f32_16x16x32_bf16 v[56:59], v[136:139], a[180:183], v[56:59]\n"
  "v_mfma_f32_16x16x32_bf16 v[64:67], v[136:139], a[184:187], v[64:67]\n"
  "v_mfma_f32_16x16x32_bf16 v[72:75], v[136:139], a[188:191], v[72:75]\n"
  "s_waitcnt vmcnt(12)\n"
  "v_mfma_f32_16x16x32_bf16 v[52:55], v[140:143], a[176:179], v[52:55]\n"
  "buffer_load_dwordx4 v[112:115], v233, s[20:23], s73 offen sc1\n"
  "v_mfma_f32_16x16x32_bf16 v[60:63], v[140:143], a[180:183], v[60:63]\n"
  "v_mfma_f32_16x16x32_bf16 v[68:71], v[140:143], a[184:187], v[68:71]\n"
  "v_mfma_f32_16x16x32_bf16 v[76:79], v[140:143], a[188:191], v[76:79]\n"
  "s_waitcnt vmcnt(12)\n"
  "v_mfma_f32_16x16x32_bf16 v[48:51], v[144:147], v[212:215], v[48:51]\n"
  "buffer_load_dwordx4 v[116:119], v233, s[20:23], s73 offen offset:1024 sc1\n"
  "v_mfma_f32_16x16x32_bf16 v[56:59], v[144:147], v[208:211], v[56:59]\n"
  "v_mfma_f32_16x16x32_bf16 v[64:67], v[144:147], v[184:187], v[64:67]\n"
  "v_mfma_f32_16x16x32_bf16 v[72:75], v[144:147], v[180:183], v[72:75]\n"
  "buffer_load_dwordx4 v[120:123], v233, s[20:23], s73 offen offset:2048 sc1\n"
  "s_waitcnt vmcnt(13)\n"
  "v_mfma_f32_16x16x32_bf16 v[52:55], v[148:151], v[212:215], v[52:55]\n"
  "v_mfma_f32_16x16x32_bf16 v[60:63], v[148:151], v[208:211], v[60:63]\n"
  "v_mfma_f32_16x16x32_bf16 v[68:71], v[148:151], v[184:187], v[68:71]\n"
  "v_mfma_f32_16x16x32_bf16 v[76:79], v[148:151], v[180:183], v[76:79]\n"
  "buffer_load_dwordx4 v[124:127], v233, s[20:23], s73 offen offset:3072 sc1\n"
  "s_waitcnt vmcnt(13)\n"
  "v_mfma_f32_16x16x32_bf16 v[48:51], v[152:155], v[176:179], v[48:51]\n"
  "v_mfma_f32_16x16x32_bf16 v[56:59], v[152:155], v[200:203], v[56:59]\n"
  "v_mfma_f32_16x16x32_bf16 v[64:67], v[152:155], v[196:199], v[64:67]\n"
  "v_mfma_f32_16x16x32_bf16 v[72:75], v[152:155], v[192:195], v[72:75]\n"
  "s_nop 5\n"
  "ds_write_b64 v242, v[56:57] offset:0\n"
  "s_waitcnt vmcnt(12)\n"
  "v_mfma_f32_16x16x32_bf16 v[52:55], v[156:159], v[176:179], v[52:55]\n"
  "ds_write_b64 v242, v[58:59] offset:8\n"
  "v_mfma_f32_16x16x32_bf16 v[60:63], v[156:159], v[200:203], v[60:63]\n"
  "buffer_load_dwordx4 v[128:131], v233, s[20:23], s74 offen sc1\n"
  "v_mfma_f32_16x16x32_bf16 v[68:71], v[156:159], v[196:199], v[68:71]\n"
  "ds_write_b64 v243, v[64:65] offset:0\n"
  "v_mfma_f32_16x16x32_bf16 v[76:79], v[156:159], v[192:195], v[76:79]\n"
  "buffer_load_dwordx4 v[132:135], v233, s[20:23], s74 offen offset:1024 sc1\n"
  "s_nop 2\n"
  "ds_write_b64 v242, v[60:61] offset:1024\n"
  "s_waitcnt vmcnt(13)\n"
  "v_mfma_f32_16x16x32_bf16 v[80:83], v[160:163], a[0:3], v[80:83]\n"
  "ds_write_b64 v242, v[62:63] offset:1032\n"
  "v_mfma_f32_16x16x32_bf16 v[88:91], v[160:163], a[4:7], 0\n"
  "ds_write_b64 v243, v[66:67] offset:8\n"
  "v_mfma_f32_16x16x32_bf16 v[96:99], v[160:163], a[8:11], 0\n"
  "buffer_load_dwordx4 v[136:139], v233, s[20:23], s74 offen offset:2048 sc1\n"
  "ds_write_b64 v243, v[68:69] offset:1024\n"
  "v_mfma_f32_16x16x32_bf16 v[104:107], v[160:163], a[12:15], 0\n"
  "ds_write_b64 v243, v[70:71] offset:1032\n"
  "s_waitcnt vmcnt(13) lgkmcnt(6)\n"
  "v_mfma_f32_16x16x32_bf16 v[84:87], v[164:167], a[0:3], v[84:87]\n"
  "ds_write_b64 v244, v[72:73] offset:0\n"
  "v_mfma_f32_16x16x32_bf16 v[92:95], v[164:167], a[4:7], 0\n"
  "buffer_load_dwordx4 v[140:143], v233, s[20:23], s74 offen offset:3072 sc1\n"
  "ds_write_b64 v244, v[74:75] offset:8\n"
  "v_mfma_f32_16x16x32_bf16 v[100:103], v[164:167], a[8:11], 0\n"
  "ds_write_b64 v244, v[76:77] offset:1024\n"
  "v_mfma_f32_16x16x32_bf16 v[108:111], v[164:167], a[12:15], 0\n"
  "ds_write_b64 v244, v[78:79] offset:1032\n"
  "s_waitcnt vmcnt(13) lgkmcnt(6)\n"
  "v_mfma_f32_16x16x32_bf16 v[80:83], v[168:171], a[16:19], v[80:83]\n"
  "buffer_load_dwordx4 v[144:147], v233, s[20:23], s75 offen sc1\n"
  "v_mfma_f32_16x16x32_bf16 v[88:91], v[168:171], a[20:23], v[88:91]\n"
  "v_mfma_f32_16x16x32_bf16 v[96:99], v[168:171], a[24:27], v[96:99]\n"
  "v_mfma_f32_16x16x32_bf16 v[104:107], v[168:171], a[28:31], v[104:107]\n"
  "s_waitcnt vmcnt(13) lgkmcnt(3)\n"
  "v_mfma_f32_16x16x32_bf16 v[84:87], v[172:175], a[16:19], v[84:87]\n"
  "buffer_load_dwordx4 v[148:151], v233, s[20:23], s75 offen offset:1024 sc1\n"
  "s_waitcnt lgkmcnt(0)\n"
  "s_barrier\n"
  "v_mfma_f32_16x16x32_bf16 v[92:95], v[172:175], a[20:23], v[92:95]\n"
  "ds_read_b128 v[56:59], v241 offset:0\n"
  "ds_read_b128 v[64:67], v241 offset:2048\n"
  "v_mfma_f32_16x16x32_bf16 v[100:103], v[172:175], a[24:27], v[100:103]\n"
  "ds_read_b128 v[72:75], v241 offset:4096\n"
  "v_mfma_f32_16x16x32_bf16 v[108:111], v[172:175], a[28:31], v[108:111]\n"
  "ds_read_b128 v[60:63], v241 offset:1024\n"
  "buffer_load_dwordx4 v[152:155], v233, s[20:23], s75 offen offset:2048 sc1\n"
  "s_waitcnt vmcnt(14)\n"
  "v_mfma_f32_16x16x32_bf16 v[80:83], v[216:219], a[32:35], v[80:83]\n"
  "ds_read_b128 v[68:71], v241 offset:3072\n"
  "ds_read_b128 v[76:79], v241 offset:5120\n"
  "v_mfma_f32_16x16x32_bf16 v[88:91], v[216:219], a[36:39], v[88:91]\n"
  "v_mfma_f32_16x16x32_bf16 v[96:99], v[216:219], a[40:43], v[96:99]\n"
  "v_mfma_f32_16x16x32_bf16 v[104:107], v[216:219], a[44:47], v[104:107]\n"
  "buffer_load_dwordx4 v[156:159], v233, s[20:23], s75 offen offset:3072 sc1\n"
  "s_waitcnt lgkmcnt(5)\n"
  "v_add_f32 v48, v48, v56\n"
  "s_waitcnt vmcnt(14)\n"
  "v_mfma_f32_16x16x32_bf16 v[84:87], v[220:223], a[32:35], v[84:87]\n"
  "s_waitcnt lgkmcnt(3)\n"
  "v_add_f32 v64, v64, v72\n"
  "v_mfma_f32_16x16x32_bf16 v[92:95], v[220:223], a[36:39], v[92:95]\n"
  "v_add_f32 v49, v49, v57\n"
  "v_add_f32 v65, v65, v73\n"
  "v_mfma_f32_16x16x32_bf16 v[100:103], v[220:223], a[40:43], v[100:103]\n"
  "v_add_f32 v50, v50, v58\n"
  "v_add_f32 v66, v66, v74\n"
  "v_mfma_f32_16x16x32_bf16 v[108:111], v[220:223], a[44:47], v[108:111]\n"
  "v_add_f32 v51, v51, v59\n"
  "v_add_f32 v67, v67, v75\n"
  "s_waitcnt vmcnt(13) lgkmcnt(2)\n"
  "v_mfma_f32_16x16x32_bf16 v[80:83], v[224:227], a[48:51], v[80:83]\n"
  "v_add_f32 v52, v52, v60\n"
  "s_waitcnt lgkmcnt(0)\n"
  "v_add_f32 v68, v68, v76\n"
  "v_mfma_f32_16x16x32_bf16 v[88:91], v[224:227], a[52:55], v[88:91]\n"
  "v_add_f32 v53, v53, v61\n"
  "v_add_f32 v69, v69, v77\n"
  "v_mfma_f32_16x16x32_bf16 v[96:99], v[224:227], a[56:59], v[96:99]\n"
  "buffer_load_dwordx4 v[160:163], v233, s[20:23], s76 offen sc1\n"
  "v_add_f32 v54, v54, v62\n"
  "v_mfma_f32_16x16x32_bf16 v[104:107], v[224:227], a[60:63], v[104:107]\n"
  "v_add_f32 v70, v70, v78\n"
  "v_add_f32 v55, v55, v63\n"
  "s_waitcnt vmcnt(13)\n"
  "v_mfma_f32_16x16x32_bf16 v[84:87], v[228:231], a[48:51], v[84:87]\n"
  "v_add_f32 v71, v71, v79\n"
  "v_add_f32 v48, v48, v64\n"
  "v_mfma_f32_16x16x32_bf16 v[92:95], v[228:231], a[52:55], v[92:95]\n"
  "v_add_f32 v49, v49, v65\n"
  "v_add_f32 v50, v50, v66\n"
  "v_mfma_f32_16x16x32_bf16 v[100:103], v[228:231], a[56:59], v[100:103]\n"
  "buffer_load_dwordx4 v[164:167], v233, s[20:23], s76 offen offset:1024 sc1\n"
  "v_add_f32 v51, v51, v67\n"
  "v_mfma_f32_16x16x32_bf16 v[108:111], v[228:231], a[60:63], v[108:111]\n"
  "v_add_f32 v52, v52, v68\n"
  "v_add_f32 v53, v53, v69\n"
  "s_waitcnt vmcnt(13)\n"
  "v_mfma_f32_16x16x32_bf16 v[80:83], v[112:115], a[64:67], v[80:83]\n"
  "v_add_f32 v54, v54, v70\n"
  "v_add_f32 v55, v55, v71\n"
  "v_mfma_f32_16x16x32_bf16 v[88:91], v[112:115], a[68:71], v[88:91]\n"
  "v_cvt_pk_bf16_f32 v56, v48, v49\n"
  "buffer_load_dwordx4 v[168:171], v233, s[20:23], s76 offen offset:2048 sc1\n"
  "v_mfma_f32_16x16x32_bf16 v[96:99], v[112:115], a[72:75], v[96:99]\n"
  "v_cvt_pk_bf16_f32 v57, v50, v51\n"
  "v_cvt_pk_bf16_f32 v58, v52, v53\n"
  "v_mfma_f32_16x16x32_bf16 v[104:107], v[112:115], a[76:79], v[104:107]\n"
  "v_cvt_pk_bf16_f32 v59, v54, v55\n"
  "v_permlane16_swap_b32 v56, v58\n"
  "s_waitcnt vmcnt(13)\n"
  "v_mfma_f32_16x16x32_bf16 v[84:87], v[116:119], a[64:67], v[84:87]\n"
  "v_permlane16_swap_b32 v57, v59\n"
  "v_mfma_f32_16x16x32_bf16 v[92:95], v[116:119], a[68:71], v[92:95]\n"
  "buffer_load_dwordx4 v[172:175], v233, s[20:23], s76 offen offset:3072 sc1\n"
  "ds_bpermute_b32 v60, v204, v56\n"
  "v_mfma_f32_16x16x32_bf16 v[100:103], v[116:119], a[72:75], v[100:103]\n"
  "ds_bpermute_b32 v61, v204, v57\n"
  "ds_bpermute_b32 v62, v204, v58\n"
  "v_mfma_f32_16x16x32_bf16 v[108:111], v[116:119], a[76:79], v[108:111]\n"
  "ds_bpermute_b32 v63, v204, v59\n"
  "buffer_load_dwordx4 v[216:219], v233, s[20:23], s77 offen sc1\n"
  "s_waitcnt vmcnt(14)\n"
  "v_mfma_f32_16x16x32_bf16 v[80:83], v[120:123], a[80:83], v[80:83]\n"
  "buffer_load_dwordx4 v[220:223], v233, s[20:23], s77 offen offset:1024 sc1\n"
  "v_mfma_f32_16x16x32_bf16 v[88:91], v[120:123], a[84:87], v[88:91]\n"
  "v_mfma_f32_16x16x32_bf16 v[96:99], v[120:123], a[88:91], v[96:99]\n"
  "v_mfma_f32_16x16x32_bf16 v[104:107], v[120:123], a[92:95], v[104:107]\n"
  "s_waitcnt vmcnt(14)\n"
  "v_mfma_f32_16x16x32_bf16 v[84:87], v[124:127], a[80:83], v[84:87]\n"
  "buffer_load_dwordx4 v[224:227], v233, s[20:23], s77 offen offset:2048 sc1\n"
  "v_mfma_f32_16x16x32_bf16 v[92:95], v[124:127], a[84:87], v[92:95]\n"
  "v_mfma_f32_16x16x32_bf16 v[100:103], v[124:127], a[88:91], v[100:103]\n"
  "v_mfma_f32_16x16x32_bf16 v[108:111], v[124:127], a[92:95], v[108:111]\n"
  "s_waitcnt vmcnt(14) lgkmcnt(0)\n"
  "v_mfma_f32_16x16x32_bf16 v[80:83], v[128:131], a[96:99], v[80:83]\n"
  "buffer_store_dwordx4 v[60:63], v245, s[48:51], 0 offen\n"
  "v_mfma_f32_16x16x32_bf16 v[88:91], v[128:131], a[100:103], v[88:91]\n"
  "v_mfma_f32_16x16x32_bf16 v[96:99], v[128:131], a[104:107], v[96:99]\n"
  "v_mfma_f32_16x16x32_bf16 v[104:107], v[128:131], a[108:111], v[104:107]\n"
  "s_waitcnt vmcnt(14)\n"
  "v_mfma_f32_16x16x32_bf16 v[84:87], v[132:135], a[96:99], v[84:87]\n"
  "buffer_load_dwordx4 v[228:231], v233, s[20:23], s77 offen offset:3072 sc1\n"
  "v_mfma_f32_16x16x32_bf16 v[92:95], v[132:135], a[100:103], v[92:95]\n"
  "v_mfma_f32_16x16x32_bf16 v[100:103], v[132:135], a[104:107], v[100:103]\n"
  "v_mfma_f32_16x16x32_bf16 v[108:111], v[132:135], a[108:111], v[108:111]\n"
  "s_waitcnt vmcnt(14)\n"
  "v_mfma_f32_16x16x32_bf16 v[80:83], v[136:139], a[112:115], v[80:83]\n"
  "v_mfma_f32_16x16x32_bf16 v[88:91], v[136:139], a[116:119], v[88:91]\n"
  "v_mfma_f32_16x16x32_bf16 v[96:99], v[136:139], a[120:123], v[96:99]\n"
  "v_mfma_f32_16x16x32_bf16 v[104:107], v[136:139], a[124:127], v[104:107]\n"
  "s_waitcnt vmcnt(13)\n"
  "v_mfma_f32_16x16x32_bf16 v[84:87], v[140:143], a[112:115], v[84:87]\n"
  "v_mfma_f32_16x16x32_bf16 v[92:95], v[140:143], a[116:119], v[92:95]\n"
  "v_mfma_f32_16x16x32_bf16 v[100:103], v[140:143], a[120:123], v[100:103]\n"
  "v_mfma_f32_16x16x32_bf16 v[108:111], v[140:143], a[124:127], v[108:111]\n"
  "s_waitcnt vmcnt(12)\n"
  "v_mfma_f32_16x16x32_bf16 v[80:83], v[144:147], a[128:131], v[80:83]\n"
  "v_mfma_f32_16x16x32_bf16 v[88:91], v[144:147], a[132:135], v[88:91]\n"
  "v_mfma_f32_16x16x32_bf16 v[96:99], v[144:147], a[136:139], v[96:99]\n"
  "v_mfma_f32_16x16x32_bf16 v[104:107], v[144:147], a[140:143], v[104:107]\n"
  "s_waitcnt vmcnt(11)\n"
  "v_mfma_f32_16x16x32_bf16 v[84:87], v[148:151], a[128:131], v[84:87]\n"
  "v_mfma_f32_16x16x32_bf16 v[92:95], v[148:151], a[132:135], v[92:95]\n"
  "v_mfma_f32_16x16x32_bf16 v[100:103], v[148:151], a[136:139], v[100:103]\n"
  "v_mfma_f32_16x16x32_bf16 v[108:111], v[148:151], a[140:143], v[108:111]\n"
  "s_waitcnt vmcnt(10)\n"
  "v_mfma_f32_16x16x32_bf16 v[80:83], v[152:155], a[144:147], v[80:83]\n"
  "v_mfma_f32_16x16x32_bf16 v[88:91], v[152:155], a[148:151], v[88:91]\n"
  "v_mfma_f32_16x16x32_bf16 v[96:99], v[152:155], a[152:155], v[96:99]\n"
  "v_mfma_f32_16x16x32_bf16 v[104:107], v[152:155], a[156:159], v[104:107]\n"
  "s_waitcnt vmcnt(9)\n"
  "v_mfma_f32_16x16x32_bf16 v[84:87], v[156:159], a[144:147], v[84:87]\n"
  "v_mfma_f32_16x16x32_bf16 v[92:95], v[156:159], a[148:151], v[92:95]\n"
  "v_mfma_f32_16x16x32_bf16 v[100:103], v[156:159], a[152:155], v[100:103]\n"
  "v_mfma_f32_16x16x32_bf16 v[108:111], v[156:159], a[156:159], v[108:111]\n"
  "s_waitcnt vmcnt(8)\n"
  "v_mfma_f32_16x16x32_bf16 v[80:83], v[160:163], a[160:163], v[80:83]\n"
  "v_mfma_f32_16x16x32_bf16 v[88:91], v[160:163], a[164:167], v[88:91]\n"
  "v_mfma_f32_16x16x32_bf16 v[96:99], v[160:163], a[168:171], v[96:99]\n"
  "v_mfma_f32_16x16x32_bf16 v[104:107], v[160:163], a[172:175], v[104:107]\n"
  "s_waitcnt vmcnt(7)\n"
  "v_mfma_f32_16x16x32_bf16 v[84:87], v[164:167], a[160:163], v[84:87]\n"
  "v_mfma_f32_16x16x32_bf16 v[92:95], v[164:167], a[164:167], v[92:95]\n"
  "v_mfma_f32_16x16x32_bf16 v[100:103], v[164:167], a[168:171], v[100:103]\n"
  "v_mfma_f32_16x16x32_bf16 v[108:111], v[164:167], a[172:175], v[108:111]\n"
  "s_waitcnt vmcnt(6)\n"
  "v_mfma_f32_16x16x32_bf16 v[80:83], v[168:171], a[176:179], v[80:83]\n"
  "v_mfma_f32_16x16x32_bf16 v[88:91], v[168:171], a[180:183], v[88:91]\n"
  "v_mfma_f32_16x16x32_bf16 v[96:99], v[168:171], a[184:187], v[96:99]\n"
  "v_mfma_f32_16x16x32_bf16 v[104:107], v[168:171], a[188:191], v[104:107]\n"
  "s_waitcnt vmcnt(5)\n"
  "v_mfma_f32_16x16x32_bf16 v[84:87], v[172:175], a[176:179], v[84:87]\n"
  "v_mfma_f32_16x16x32_bf16 v[92:95], v[172:175], a[180:183], v[92:95]\n"
  "v_mfma_f32_16x16x32_bf16 v[100:103], v[172:175], a[184:187], v[100:103]\n"
  "v_mfma_f32_16x16x32_bf16 v[108:111], v[172:175], a[188:191], v[108:111]\n"
  "s_waitcnt vmcnt(4)\n"
  "v_mfma_f32_16x16x32_bf16 v[80:83], v[216:219], v[212:215], v[80:83]\n"
  "v_mfma_f32_16x16x32_bf16 v[88:91], v[216:219], v[208:211], v[88:91]\n"
  "v_mfma_f32_16x16x32_bf16 v[96:99], v[216:219], v[184:187], v[96:99]\n"
  "v_mfma_f32_16x16x32_bf16 v[104:107], v[216:219], v[180:183], v[104:107]\n"
  "s_waitcnt vmcnt(3)\n"
  "v_mfma_f32_16x16x32_bf16 v[84:87], v[220:223], v[212:215], v[84:87]\n"
  "v_mfma_f32_16x16x32_bf16 v[92:95], v[220:223], v[208:211], v[92:95]\n"
  "v_mfma_f32_16x16x32_bf16 v[100:103], v[220:223], v[184:187], v[100:103]\n"
  "v_mfma_f32_16x16x32_bf16 v[108:111], v[220:223], v[180:183], v[108:111]\n"
  "s_waitcnt vmcnt(2)\n"
  "v_mfma_f32_16x16x32_bf16 v[80:83], v[224:227], v[176:179], v[80:83]\n"
  "v_mfma_f32_16x16x32_bf16 v[88:91], v[224:227], v[200:203], v[88:91]\n"
  "v_mfma_f32_16x16x32_bf16 v[96:99], v[224:227], v[196:199], v[96:99]\n"
  "v_mfma_f32_16x16x32_bf16 v[104:107], v[224:227], v[192:195], v[104:107]\n"
  "s_waitcnt vmcnt(0)\n"
  "v_mfma_f32_16x16x32_bf16 v[84:87], v[228:231], v[176:179], v[84:87]\n"
  "v_mfma_f32_16x16x32_bf16 v[92:95], v[228:231], v[200:203], v[92:95]\n"
  "v_mfma_f32_16x16x32_bf16 v[100:103], v[228:231], v[196:199], v[100:103]\n"
  "v_mfma_f32_16x16x32_bf16 v[108:111], v[228:231], v[192:195], v[108:111]\n"
  "s_nop 0\n"
  "ds_write_b128 v242, v[88:91] offset:24576\n"
  "s_nop 3\n"
  "ds_write_b128 v242, v[92:95] offset:25600\n"
  "ds_write_b128 v243, v[96:99] offset:24576\n"
  "ds_write_b128 v243, v[100:103] offset:25600\n"
  "ds_write_b128 v244, v[104:107] offset:24576\n"
  "ds_write_b128 v244, v[108:111] offset:25600\n"
  "s_waitcnt lgkmcnt(0)\n"
  "s_barrier\n"
  "ds_read_b128 v[88:91], v241 offset:24576\n"
  "ds_read_b128 v[96:99], v241 offset:26624\n"
  "ds_read_b128 v[104:107], v241 offset:28672\n"
  "ds_read_b128 v[92:95], v241 offset:25600\n"
  "ds_read_b128 v[100:103], v241 offset:27648\n"
  "ds_read_b128 v[108:111], v241 offset:29696\n"
  "s_waitcnt lgkmcnt(5)\n"
  "v_pk_add_f32 v[80:81], v[80:81], v[88:89]\n"
  "v_pk_add_f32 v[82:83], v[82:83], v[90:91]\n"
  "s_waitcnt lgkmcnt(3)\n"
  "v_pk_add_f32 v[96:97], v[96:97], v[104:105]\n"
  "v_pk_add_f32 v[98:99], v[98:99], v[106:107]\n"
  "v_pk_add_f32 v[80:81], v[80:81], v[96:97]\n"
  "s_waitcnt lgkmcnt(2)\n"
  "v_pk_add_f32 v[84:85], v[84:85], v[92:93]\n"
  "v_pk_add_f32 v[86:87], v[86:87], v[94:95]\n"
  "v_pk_add_f32 v[82:83], v[82:83], v[98:99]\n"
  "v_cvt_pk_bf16_f32 v88, v80, v81\n"
  "v_cvt_pk_bf16_f32 v89, v82, v83\n"
  "s_waitcnt lgkmcnt(0)\n"
  "v_pk_add_f32 v[100:101], v[100:101], v[108:109]\n"
  "v_pk_add_f32 v[102:103], v[102:103], v[110:111]\n"
  "v_pk_add_f32 v[84:85], v[84:85], v[100:101]\n"
  "v_pk_add_f32 v[86:87], v[86:87], v[102:103]\n"
  "v_cvt_pk_bf16_f32 v90, v84, v85\n"
  "v_cvt_pk_bf16_f32 v91, v86, v87\n"
  "s_nop 0\n"
  "v_permlane16_swap_b32 v88, v90\n"
  "v_permlane16_swap_b32 v89, v91\n"
  "ds_bpermute_b32 v92, v204, v88\n"
  "ds_bpermute_b32 v93, v204, v89\n"
  "ds_bpermute_b32 v94, v204, v90\n"
  "ds_bpermute_b32 v95, v204, v91\n"
  "s_waitcnt lgkmcnt(0)\n"
  "buffer_store_dwordx4 v[92:95], v245, s[48:51], 0 offen offset:64\n"
  ".Lfin_%=:\n"
  "s_cmp_eq_u32 s99, 0\n"
  "s_cselect_b32 s87, 0, 1073741824\n"
  "v_or_b32 v48, s87, v248\n"
  "buffer_store_dwordx2 v[36:37], v48, s[20:23], 0 offen\n"
  "s_waitcnt vmcnt(0)\n"
  "s_branch .Lend_%=\n"
  ".Lfb1_%=:\n"
  "s_mov_b32 s98, 131072\n"
  ".Lp1_%=:\n"
  "buffer_load_dwordx2 v[206:207], v249, s[20:23], 0 offen sc1\n"
  "s_waitcnt vmcnt(0)\n"
  "v_cmp_ne_u64 vcc, s[56:57], v[206:207]\n"
  "s_cmp_eq_u64 vcc, 0\n"
  "s_cbranch_scc1 .Lbk1_%=\n"
  "s_sub_u32 s98, s98, 1\n"
  "s_cbranch_scc0 .Lp1_%=\n"
  "s_branch .Lbk1_%=\n"
  ".Lfb2_%=:\n"
  "s_mov_b32 s98, 131072\n"
  ".Lp2_%=:\n"
  "buffer_load_dwordx2 v[94:95], v252, s[20:23], 0 offen sc1\n"
  "buffer_load_dwordx2 v[96:97], v252, s[20:23], 0 offen offset:128 sc1\n"
  "s_waitcnt vmcnt(0)\n"
  "v_max3_u32 v98, v94, v95, v96\n"
  "v_max_u32 v98, v98, v97\n"
  "v_cmp_eq_u32 vcc, -1, v98\n"
  "s_cmp_eq_u64 vcc, 0\n"
  "s_cbranch_scc1 .Lbk2_%=\n"
  "s_sub_u32 s98, s98, 1\n"
  "s_cbranch_scc0 .Lp2_%=\n"
  "s_branch .Lbk2_%=\n"
  ".Lfb3_%=:\n"
  "s_mov_b32 s98, 131072\n"
  ".Lp3_%=:\n"
  "buffer_load_dwordx2 v[206:207], v246, s[20:23], 0 offen sc1\n"
  "s_waitcnt vmcnt(0)\n"
  "v_cmp_ne_u64 vcc, s[56:57], v[206:207]\n"
  "s_cmp_eq_u64 vcc, 0\n"
  "s_cbranch_scc1 .Lbk3_%=\n"
  "s_sub_u32 s98, s98, 1\n"
  "s_cbranch_scc0 .Lp3_%=\n"
  "s_branch .Lbk3_%=\n"
  ".Lfb4_%=:\n"
  "s_mov_b32 s98, 131072\n"
  ".Lp4_%=:\n"
  "buffer_load_dwordx2 v[206:207], v247, s[20:23], 0 offen sc1\n"
  "s_waitcnt vmcnt(0)\n"
  "v_cmp_ne_u64 vcc, s[56:57], v[206:207]\n"
  "s_cmp_eq_u64 vcc, 0\n"
  "s_cbranch_scc1 .Lbk4_%=\n"
  "s_sub_u32 s98, s98, 1\n"
  "s_cbranch_scc0 .Lp4_%=\n"
  "s_branch .Lbk4_%=\n"
  ".Lfb5_%=:\n"
  "s_mov_b32 s98, 131072\n"
  ".Lp5_%=:\n"
  "buffer_load_dwordx2 v[206:207], v247, s[20:23], 0 offen offset:128 sc1\n"
  "s_waitcnt vmcnt(0)\n"
  "v_cmp_ne_u64 vcc, s[56:57], v[206:207]\n"
  "s_cmp_eq_u64 vcc, 0\n"
  "s_cbranch_scc1 .Lbk5_%=\n"
  "s_sub_u32 s98, s98, 1\n"
  "s_cbranch_scc0 .Lp5_%=\n"
  "s_branch .Lbk5_%=\n"
  ".Lfb6_%=:\n"
  "s_mov_b32 s98, 131072\n"
  ".Lp6_%=:\n"
  "buffer_load_dwordx2 v[206:207], v204, s[20:23], 0 offen sc1\n"
  "s_waitcnt vmcnt(0)\n"
  "v_cmp_ne_u64 vcc, s[56:57], v[206:207]\n"
  "s_cmp_eq_u64 vcc, 0\n"
  "s_cbranch_scc1 .Lbk6_%=\n"
  "s_sub_u32 s98, s98, 1\n"
  "s_cbranch_scc0 .Lp6_%=\n"
  "s_branch .Lbk6_%=\n"
  ".Lfb7_%=:\n"
  "s_mov_b32 s98, 131072\n"
  ".Lp7_%=:\n"
  "buffer_load_dwordx2 v[206:207], v204, s[20:23], 0 offen offset:128 sc1\n"
  "s_waitcnt vmcnt(0)\n"
  "v_cmp_ne_u64 vcc, s[56:57], v[206:207]\n"
  "s_cmp_eq_u64 vcc, 0\n"
  "s_cbranch_scc1 .Lbk7_%=\n"
  "s_sub_u32 s98, s98, 1\n"
  "s_cbranch_scc0 .Lp7_%=\n"
  "s_branch .Lbk7_%=\n"
  ".Lfb8_%=:\n"
  "s_mov_b32 s98, 131072\n"
  ".Lp8_%=:\n"
  "buffer_load_dwordx2 v[206:207], v249, s[20:23], 0 offen sc1\n"
  "s_waitcnt vmcnt(0)\n"
  "v_cmp_ne_u64 vcc, s[56:57], v[206:207]\n"
  "s_cmp_eq_u64 vcc, 0\n"
  "s_cbranch_scc1 .Lbk8_%=\n"
  "s_sub_u32 s98, s98, 1\n"
  "s_cbranch_scc0 .Lp8_%=\n"
  "s_branch .Lbk8_%=\n"
  ".Lfb9_%=:\n"
  "s_mov_b32 s98, 131072\n"
  ".Lp9_%=:\n"
  "buffer_load_dwordx2 v[94:95], v252, s[20:23], 0 offen sc1\n"
  "buffer_load_dwordx2 v[96:97], v252, s[20:23], 0 offen offset:128 sc1\n"
  "s_waitcnt vmcnt(0)\n"
  "v_max3_u32 v98, v94, v95, v96\n"
  "v_max_u32 v98, v98, v97\n"
  "v_cmp_eq_u32 vcc, -1, v98\n"
  "s_cmp_eq_u64 vcc, 0\n"
  "s_cbranch_scc1 .Lbk9_%=\n"
  "s_sub_u32 s98, s98, 1\n"
  "s_cbranch_scc0 .Lp9_%=\n"
  "s_branch .Lbk9_%=\n"
  ".Lfb10_%=:\n"
  "s_mov_b32 s98, 131072\n"
  ".Lp10_%=:\n"
  "buffer_load_dwordx2 v[206:207], v247, s[20:23], 0 offen sc1\n"
  "s_waitcnt vmcnt(0)\n"
  "v_cmp_ne_u64 vcc, s[56:57], v[206:207]\n"
  "s_cmp_eq_u64 vcc, 0\n"
  "s_cbranch_scc1 .Lbk10_%=\n"
  "s_sub_u32 s98, s98, 1\n"
  "s_cbranch_scc0 .Lp10_%=\n"
  "s_branch .Lbk10_%=\n"
  ".Lfb11_%=:\n"
  "s_mov_b32 s98, 131072\n"
  ".Lp11_%=:\n"
  "buffer_load_dwordx2 v[206:207], v247, s[20:23], 0 offen offset:128 sc1\n"
  "s_waitcnt vmcnt(0)\n"
  "v_cmp_ne_u64 vcc, s[56:57], v[206:207]\n"
  "s_cmp_eq_u64 vcc, 0\n"
  "s_cbranch_scc1 .Lbk11_%=\n"
  "s_sub_u32 s98, s98, 1\n"
  "s_cbranch_scc0 .Lp11_%=\n"
  "s_branch .Lbk11_%=\n"
  ".Lfb12_%=:\n"
  "s_mov_b32 s98, 131072\n"
  ".Lp12_%=:\n"
  "buffer_load_dwordx2 v[206:207], v249, s[20:23], 0 offen sc1\n"
  "s_waitcnt vmcnt(0)\n"
  "v_cmp_ne_u64 vcc, s[56:57], v[206:207]\n"
  "s_cmp_eq_u64 vcc, 0\n"
  "s_cbranch_scc1 .Lbk12_%=\n"
  "s_sub_u32 s98, s98, 1\n"
  "s_cbranch_scc0 .Lp12_%=\n"
  "s_branch .Lbk12_%=\n"
  ".Lfb13_%=:\n"
  "s_mov_b32 s98, 131072\n"
  ".Lp13_%=:\n"
  "buffer_load_dwordx2 v[94:95], v252, s[20:23], 0 offen sc1\n"
  "buffer_load_dwordx2 v[96:97], v252, s[20:23], 0 offen offset:128 sc1\n"
  "s_waitcnt vmcnt(0)\n"
  "v_max3_u32 v98, v94, v95, v96\n"
  "v_max_u32 v98, v98, v97\n"
  "v_cmp_eq_u32 vcc, -1, v98\n"
  "s_cmp_eq_u64 vcc, 0\n"
  "s_cbranch_scc1 .Lbk13_%=\n"
  "s_sub_u32 s98, s98, 1\n"
  "s_cbranch_scc0 .Lp13_%=\n"
  "s_branch .Lbk13_%=\n"
  ".Lfb14_%=:\n"
  "s_mov_b32 s98, 131072\n"
  ".Lp14_%=:\n"
  "buffer_load_dwordx2 v[206:207], v246, s[20:23], 0 offen sc1\n"
  "s_waitcnt vmcnt(0)\n"
  "v_cmp_ne_u64 vcc, s[56:57], v[206:207]\n"
  "s_cmp_eq_u64 vcc, 0\n"
  "s_cbranch_scc1 .Lbk14_%=\n"
  "s_sub_u32 s98, s98, 1\n"
  "s_cbranch_scc0 .Lp14_%=\n"
  "s_branch .Lbk14_%=\n"
  ".Lfb15_%=:\n"
  "s_mov_b32 s98, 131072\n"
  ".Lp15_%=:\n"
  "buffer_load_dwordx2 v[206:207], v247, s[20:23], 0 offen sc1\n"
  "s_waitcnt vmcnt(0)\n"
  "v_cmp_ne_u64 vcc, s[56:57], v[206:207]\n"
  "s_cmp_eq_u64 vcc, 0\n"
  "s_cbranch_scc1 .Lbk15_%=\n"
  "s_sub_u32 s98, s98, 1\n"
  "s_cbranch_scc0 .Lp15_%=\n"
  "s_branch .Lbk15_%=\n"
  ".Lfb16_%=:\n"
  "s_mov_b32 s98, 131072\n"
  ".Lp16_%=:\n"
  "buffer_load_dwordx2 v[206:207], v247, s[20:23], 0 offen offset:128 sc1\n"
  "s_waitcnt vmcnt(0)\n"
  "v_cmp_ne_u64 vcc, s[56:57], v[206:207]\n"
  "s_cmp_eq_u64 vcc, 0\n"
  "s_cbranch_scc1 .Lbk16_%=\n"
  "s_sub_u32 s98, s98, 1\n"
  "s_cbranch_scc0 .Lp16_%=\n"
  "s_branch .Lbk16_%=\n"
  ".Lfb17_%=:\n"
  "s_mov_b32 s98, 131072\n"
  ".Lp17_%=:\n"
  "buffer_load_dwordx2 v[206:207], v204, s[20:23], 0 offen sc1\n"
  "s_waitcnt vmcnt(0)\n"
  "v_cmp_ne_u64 vcc, s[56:57], v[206:207]\n"
  "s_cmp_eq_u64 vcc, 0\n"
  "s_cbranch_scc1 .Lbk17_%=\n"
  "s_sub_u32 s98, s98, 1\n"
  "s_cbranch_scc0 .Lp17_%=\n"
  "s_branch .Lbk17_%=\n"
  ".Lfb18_%=:\n"
  "s_mov_b32 s98, 131072\n"
  ".Lp18_%=:\n"
  "buffer_load_dwordx2 v[206:207], v204, s[20:23], 0 offen offset:128 sc1\n"
  "s_waitcnt vmcnt(0)\n"
  "v_cmp_ne_u64 vcc, s[56:57], v[206:207]\n"
  "s_cmp_eq_u64 vcc, 0\n"
  "s_cbranch_scc1 .Lbk18_%=\n"
  "s_sub_u32 s98, s98, 1\n"
  "s_cbranch_scc0 .Lp18_%=\n"
  "s_branch .Lbk18_%=\n"
  ".Lfb19_%=:\n"
  "s_mov_b32 s98, 131072\n"
  ".Lp19_%=:\n"
  "buffer_load_dwordx2 v[206:207], v249, s[20:23], 0 offen sc1\n"
  "s_waitcnt vmcnt(0)\n"
  "v_cmp_ne_u64 vcc, s[56:57], v[206:207]\n"
  "s_cmp_eq_u64 vcc, 0\n"
  "s_cbranch_scc1 .Lbk19_%=\n"
  "s_sub_u32 s98, s98, 1\n"
  "s_cbranch_scc0 .Lp19_%=\n"
  "s_branch .Lbk19_%=\n"
  ".Lfb20_%=:\n"
  "s_mov_b32 s98, 131072\n"
  ".Lp20_%=:\n"
  "buffer_load_dwordx2 v[94:95], v252, s[20:23], 0 offen sc1\n"
  "buffer_load_dwordx2 v[96:97], v252, s[20:23], 0 offen offset:128 sc1\n"
  "s_waitcnt vmcnt(0)\n"
  "v_max3_u32 v98, v94, v95, v96\n"
  "v_max_u32 v98, v98, v97\n"
  "v_cmp_eq_u32 vcc, -1, v98\n"
  "s_cmp_eq_u64 vcc, 0\n"
  "s_cbranch_scc1 .Lbk20_%=\n"
  "s_sub_u32 s98, s98, 1\n"
  "s_cbranch_scc0 .Lp20_%=\n"
  "s_branch .Lbk20_%=\n"
  ".Lfb21_%=:\n"
  "s_mov_b32 s98, 131072\n"
  ".Lp21_%=:\n"
  "buffer_load_dwordx2 v[206:207], v246, s[20:23], 0 offen sc1\n"
  "s_waitcnt vmcnt(0)\n"
  "v_cmp_ne_u64 vcc, s[56:57], v[206:207]\n"
  "s_cmp_eq_u64 vcc, 0\n"
  "s_cbranch_scc1 .Lbk21_%=\n"
  "s_sub_u32 s98, s98, 1\n"
  "s_cbranch_scc0 .Lp21_%=\n"
  "s_branch .Lbk21_%=\n"
  ".Lfb22_%=:\n"
  "s_mov_b32 s98, 131072\n"
  ".Lp22_%=:\n"
  "buffer_load_dwordx2 v[206:207], v247, s[20:23], 0 offen sc1\n"
  "s_waitcnt vmcnt(0)\n"
  "v_cmp_ne_u64 vcc, s[56:57], v[206:207]\n"
  "s_cmp_eq_u64 vcc, 0\n"
  "s_cbranch_scc1 .Lbk22_%=\n"
  "s_sub_u32 s98, s98, 1\n"
  "s_cbranch_scc0 .Lp22_%=\n"
  "s_branch .Lbk22_%=\n"
  ".Lfb23_%=:\n"
  "s_mov_b32 s98, 131072\n"
  ".Lp23_%=:\n"
  "buffer_load_dwordx2 v[206:207], v247, s[20:23], 0 offen offset:128 sc1\n"
  "s_waitcnt vmcnt(0)\n"
  "v_cmp_ne_u64 vcc, s[56:57], v[206:207]\n"
  "s_cmp_eq_u64 vcc, 0\n"
  "s_cbranch_scc1 .Lbk23_%=\n"
  "s_sub_u32 s98, s98, 1\n"
  "s_cbranch_scc0 .Lp23_%=\n"
  "s_branch .Lbk23_%=\n"
  ".Lend_%=:\n"
  :
  : "s"(ka), "s"(bx), "s"(wave)
  : "v0", "v1", "v2", "v3", "v4", "v5", "v6", "v7", "v8", "v9", "v10", "v11", "v12", "v13", "v14", "v15", "v16", "v17", "v18", "v19", "v20", "v21", "v22", "v23", "v24", "v25", "v26", "v27", "v28", "v29", "v30", "v31", "v32", "v33", "v34", "v35", "v36", "v37", "v38", "v39", "v40", "v41", "v42", "v43", "v44", "v45", "v46", "v47", "v48", "v49", "v50", "v51", "v52", "v53", "v54", "v55", "v56", "v57", "v58", "v59", "v60", "v61", "v62", "v63", "v64", "v65", "v66", "v67", "v68", "v69", "v70", "v71", "v72", "v73", "v74", "v75", "v76", "v77", "v78", "v79", "v80", "v81", "v82", "v83", "v84", "v85", "v86", "v87", "v88", "v89", "v90", "v91", "v92", "v93", "v94", "v95", "v96", "v97", "v98", "v99", "v100", "v101", "v102", "v103", "v104", "v105", "v106", "v107", "v108", "v109", "v110", "v111", "v112", "v113", "v114", "v115", "v116", "v117", "v118", "v119", "v120", "v121", "v122", "v123", "v124", "v125", "v126", "v127", "v128", "v129", "v130", "v131", "v132", "v133", "v134", "v135", "v136", "v137", "v138", "v139", "v140", "v141", "v142", "v143", "v144", "v145", "v146", "v147", "v148", "v149", "v150", "v151", "v152", "v153", "v154", "v155", "v156", "v157", "v158", "v159", "v160", "v161", "v162", "v163", "v164", "v165", "v166", "v167", "v168", "v169", "v170", "v171", "v172", "v173", "v174", "v175", "v176", "v177", "v178", "v179", "v180", "v181", "v182", "v183", "v184", "v185", "v186", "v187", "v188", "v189", "v190", "v191", "v192", "v193", "v194", "v195", "v196", "v197", "v198", "v199", "v200", "v201", "v202", "v203", "v204", "v205", "v206", "v207", "v208", "v209", "v210", "v211", "v212", "v213", "v214", "v215", "v216", "v217", "v218", "v219", "v220", "v221", "v222", "v223", "v224", "v225", "v226", "v227", "v228", "v229", "v230", "v231", "v232", "v233", "v234", "v235", "v236", "v237", "v238", "v239", "v240", "v241", "v242", "v243", "v244", "v245", "v246", "v247", "v248", "v249", "v250", "v251", "v252", "v253", "v254", "v255", "a0", "a1", "a2", "a3", "a4", "a5", "a6", "a7", "a8", "a9", "a10", "a11", "a12", "a13", "a14", "a15", "a16", "a17", "a18", "a19", "a20", "a21", "a22", "a23", "a24", "a25", "a26", "a27", "a28", "a29", "a30", "a31", "a32", "a33", "a34", "a35", "a36", "a37", "a38", "a39", "a40", "a41", "a42", "a43", "a44", "a45", "a46", "a47", "a48", "a49", "a50", "a51", "a52", "a53", "a54", "a55", "a56", "a57", "a58", "a59", "a60", "a61", "a62", "a63", "a64", "a65", "a66", "a67", "a68", "a69", "a70", "a71", "a72", "a73", "a74", "a75", "a76", "a77", "a78", "a79", "a80", "a81", "a82", "a83", "a84", "a85", "a86", "a87", "a88", "a89", "a90", "a91", "a92", "a93", "a94", "a95", "a96", "a97", "a98", "a99", "a100", "a101", "a102", "a103", "a104", "a105", "a106", "a107", "a108", "a109", "a110", "a111", "a112", "a113", "a114", "a115", "a116", "a117", "a118", "a119", "a120", "a121", "a122", "a123", "a124", "a125", "a126", "a127", "a128", "a129", "a130", "a131", "a132", "a133", "a134", "a135", "a136", "a137", "a138", "a139", "a140", "a141", "a142", "a143", "a144", "a145", "a146", "a147", "a148", "a149", "a150", "a151", "a152", "a153", "a154", "a155", "a156", "a157", "a158", "a159", "a160", "a161", "a162", "a163", "a164", "a165", "a166", "a167", "a168", "a169", "a170", "a171", "a172", "a173", "a174", "a175", "a176", "a177", "a178", "a179", "a180", "a181", "a182", "a183", "a184", "a185", "a186", "a187", "a188", "a189", "a190", "a191", "a192", "a193", "a194", "a195", "a196", "a197", "a198", "a199", "a200", "a201", "a202", "a203", "a204", "a205", "a206", "a207", "a208", "a209", "a210", "a211", "a212", "a213", "a214", "a215", "a216", "a217", "a218", "a219", "a220", "a221", "a222", "a223", "a224", "a225", "a226", "a227", "a228", "a229", "a230", "a231", "a232", "a233", "a234", "a235", "a236", "a237", "a238", "a239", "a240", "a241", "a242", "a243", "a244", "a245", "a246", "a247", "a248", "a249", "a250", "a251", "a252", "a253", "a254", "a255", "s4", "s5", "s6", "s7", "s8", "s9", "s10", "s11", "s12", "s13", "s14", "s15", "s16", "s17", "s18", "s19", "s20", "s21", "s22", "s23", "s24", "s25", "s26", "s27", "s28", "s29", "s30", "s31", "s33", "s34", "s35", "s36", "s37", "s38", "s39", "s40", "s41", "s42", "s43", "s44", "s45", "s46", "s47", "s48", "s49", "s50", "s51", "s52", "s53", "s54", "s55", "s56", "s57", "s58", "s59", "s60", "s61", "s62", "s63", "s64", "s65", "s66", "s67", "s68", "s69", "s70", "s71", "s72", "s73", "s74", "s75", "s76", "s77", "s78", "s79", "s80", "s81", "s82", "s83", "s84", "s85", "s86", "s87", "s88", "s89", "s90", "s91", "s92", "s93", "s94", "s95", "s96", "s97", "s98", "s99", "vcc", "scc", "memory");
}

extern "C" void kernel_launch(void* const* d_in, const int* in_sizes, int n_in, void* d_out, int out_size, void* d_ws, size_t ws_size,
                              hipStream_t stream) {
  (void)hipFuncSetAttribute((const void*)fused_kernel, hipFuncAttributeMaxDynamicSharedMemorySize, LDS_BYTES);
  fused_kernel<<<256, 256, LDS_BYTES, stream>>>((const u16*)d_in[0], (const u16*)d_in[1], (const u16*)d_in[2], (const u16*)d_in[4],
                                                (const u16*)d_in[5], (const u16*)d_in[6], (const u16*)d_in[7], (u16*)d_out, (unsigned char*)d_ws);
}
```
